```python
import torch

def _softmax(x, axis=-1):
    return torch.softmax(x, dim=axis)
def _gelu(x):
    return torch.nn.functional.gelu(x, approximate="tanh")
def _silu(x):
    return torch.nn.functional.silu(x)
def _sigmoid(x):
    return torch.sigmoid(x)
def _layernorm(x, axis=-1, eps=1e-5):
    m = x.mean(dim=axis, keepdim=True)
    v = x.var(dim=axis, keepdim=True, unbiased=False)
    return (x - m) / torch.sqrt(v + eps)
def _scatter_add(tgt, idx, src, axis=0):
    return tgt.clone().index_add_(axis, idx, src)
def _segment_sum(src, seg, n):
    out = torch.zeros((n,) + src.shape[1:], dtype=src.dtype)
    return out.index_add_(0, seg, src)
def _pad(x, pw):
    flat = tuple(v for pair in reversed(pw) for v in pair)
    return torch.nn.functional.pad(x, flat)

kernel_name = 'standalone__standard_2d_1d_3d_conv__vision'

def setup_inputs():

    _w = (lambda a: (lambda t: t.to(torch.bfloat16) if t.is_floating_point() else t)(torch.as_tensor(a)))
    H = W = 384
    C = 384
    K = 1
    return {
        "x": _w((lambda *s: torch.randn(*s, dtype=torch.bfloat16) * 0.05)(1, H, W, C)),
        "w": _w((lambda *s: torch.randn(*s, dtype=torch.bfloat16) * 0.05)(K, K, C, C)),
        "b": _w((lambda *s: torch.randn(*s, dtype=torch.bfloat16) * 0.05)(C)),
    }

def reference(x, w, b):
    B, H, W, Cin = x.shape
    K = w.shape[0]
    pl = (K - 1) // 2
    pr = K // 2
    xp = _pad(x, ((0, 0), (pl, pr), (pl, pr), (0, 0)))
    out = sum(
        torch.einsum('bhwc,cd->bhwd', xp[:, i:i+H, j:j+W, :], w[i, j])
        for i in range(K) for j in range(K)
    )
    return out + b[None, None, None, :]
```

```cpp
#include <hip/hip_runtime.h>
#include <stdint.h>

#define LAS __attribute__((address_space(3)))

struct PaceTab { unsigned v[72]; };

__global__ __launch_bounds__(256, 1) void conv1x1_kernel(const uint16_t* __restrict__ x,
                                                         const uint16_t* __restrict__ w,
                                                         const uint16_t* __restrict__ bias,
                                                         uint16_t* __restrict__ out, unsigned long long* ws, PaceTab pt) {
  __shared__ __attribute__((aligned(1024))) unsigned char lds[161792];

  unsigned long long xa, wa, ba, oa, sa, rs;
  asm volatile(
      "s_load_dwordx2 s[16:17], %6, 0x8\n\t"
      "s_load_dwordx2 s[20:21], %6, 0x0\n\t"
      "s_load_dwordx2 s[36:37], %6, 0x10\n\t"
      "s_load_dwordx2 s[28:29], %6, 0x18\n\t"
      "s_load_dwordx2 s[24:25], %6, 0x20\n\t"
      "s_memrealtime s[52:53]\n\t"
      : "={s[20:21]}"(xa), "={s[16:17]}"(wa), "={s[36:37]}"(ba), "={s[28:29]}"(oa), "={s[24:25]}"(sa), "={s[52:53]}"(rs)
      : "s"(__builtin_amdgcn_kernarg_segment_ptr())
      : "memory");
  __builtin_amdgcn_sched_barrier(0);
  const unsigned tid  = threadIdx.x;
  unsigned lane = tid & 63;
  unsigned wv   = __builtin_amdgcn_readfirstlane(tid >> 6);
  const unsigned lds0 = (unsigned)(size_t)(LAS unsigned char*)lds;
  const unsigned blk  = blockIdx.x;
  const unsigned role = (blk >> 2) & 1u;
  unsigned pk = (blk & 3u) * 32u + (blk >> 3);
  const unsigned sst  = (role ^ (blk >> 4)) & 1u;
  const unsigned t0   = pk * 49152u + sst * 24576u;
  const unsigned p0   = lds0 + 3072u * wv;
  const unsigned lq = lane >> 4, ph = (lane >> 2) & 3, pl = lane & 3;
  const unsigned gs = 256u * lq + 64u * ((ph - lq) & 3) + 16u * (pl ^ ((4u - wv) & 3));
  const unsigned ga = gs + 3072u * (wv < 3u ? wv : 5u);
  const unsigned gb = gs + 3072u * (wv < 2u ? wv + 6u : wv + 1u);
  const unsigned pv = blk >> 6, ps = (blk & 63u) ^ (4u * (pv & 1u)), pgr = (ps >> 2) & 7u, pi = (ps & 3u) * 2u + (pv >> 1);
  const unsigned ppg = min(pgr * 8u, 52u) + (pi & (0x13777777u >> (4u * pgr)) & 7u);
  const unsigned pop = (ppg << 21) + ((ps & 3u) * 4u + pv) * 0x1c080u + 0x8000u;
  unsigned pfire = ((role ^ ppg) & 1u) << 13;
  const unsigned pfo = (blk >> 3) * 9216u;
  const unsigned pfv = tid * 16u;
  const unsigned ty = (blk >> 3) & 1u;
  const unsigned tidx = (blk >> 3) * 4u + wv, tsel = tidx >> 6;
  const unsigned toff = (tidx & 63u) < 54u ? (tidx & 63u) << 21 : 113246204u;
  unsigned tres, pre1;
  unsigned long long pres, ep;
  asm volatile(
      "s_add_u32 m0, s43, 0x27000\n\t"
      "s_add_u32 s51, s42, 0x1000\n\t"
      "s_waitcnt lgkmcnt(0)\n\t"
      "s_cmp_lg_u32 s41, s43\n\t"
      "s_cbranch_scc1 .Lw0pi_%=\n\t"
      "s_cmp_eq_u32 s78, 0\n\t"
      "s_cselect_b64 s[96:97], s[20:21], s[28:29]\n\t"
      "s_load_dword s92, s[96:97], s76 offset:0x0\n\t"
      "s_load_dwordx2 s[60:61], s[96:97], s76 offset:0x3300\n\t"
      ".Lw0pi_%=:\n\t"
      "buffer_load_dwordx4 v216, s[16:19], s42 offen lds\n\t"
      "buffer_load_dwordx4 v216, s[16:19], s51 offen lds\n\t"
      "s_add_u32 s51, s42, 0x2000\n\t"
      "buffer_load_dwordx4 v216, s[16:19], s51 offen lds\n\t"
      "s_mov_b32 m0, s41\n\t"
      "s_nop 0\n\t"
      "buffer_load_dwordx4 v208, s[20:23], s40 offen lds\n\t"
      "buffer_load_dwordx4 v208, s[20:23], s40 offen offset:1024 lds\n\t"
      "buffer_load_dwordx4 v208, s[20:23], s40 offen offset:2048 lds\n\t"
      "s_add_u32 m0, s41, 0x3000\n\t"
      "s_nop 0\n\t"
      "buffer_load_dwordx4 v209, s[20:23], s40 offen lds\n\t"
      "buffer_load_dwordx4 v209, s[20:23], s40 offen offset:1024 lds\n\t"
      "buffer_load_dwordx4 v209, s[20:23], s40 offen offset:2048 lds\n\t"
      "s_cmp_lg_u32 s41, s43\n\t"
      "s_cbranch_scc1 .Lw0pe_%=\n\t"
      "s_waitcnt lgkmcnt(2)\n\t"
      "s_nop 7\n\t"
      "s_getreg_b32 s51, hwreg(HW_REG_IB_STS, 8, 4)\n\t"
      "s_lshl_b32 s96, s51, 13\n\t"
      "s_xor_b32 s77, s77, s96\n\t"
      "s_and_b32 s77, s77, 0x2000\n\t"
      "s_cmp_lg_u32 s51, 0\n\t"
      "s_cselect_b32 s77, s77, 0x1000\n\t"
      ".Lw0pe_%=:\n\t"
      "s_load_dwordx2 s[62:63], s[24:25], 0x800\n\t"
      "s_cmp_eq_u32 s99, 0\n\t"
      "s_cselect_b64 s[96:97], s[20:21], s[28:29]\n\t"
      "s_load_dword s95, s[96:97], s98\n\t"
      : "={s95}"(tres), "={s92}"(pre1), "={s[60:61]}"(pres), "={s[62:63]}"(ep), "+{s77}"(pfire), "+v"(lane), "+s"(wv), "+s"(pk)
      : "{s[20:21]}"(xa), "{s[16:17]}"(wa), "{s[36:37]}"(ba), "{s[28:29]}"(oa), "{s[24:25]}"(sa), "{s[52:53]}"(rs),
        "{s[22:23]}"(0x0002000006c00000ull), "{s[26:27]}"(0x0002000000000808ull), "{s[30:31]}"(0x0002000006c00000ull),
        "{s[18:19]}"(0x0002000000048000ull), "{s[38:39]}"(0x0002000000000300ull),
        "{s40}"(t0), "{s41}"(p0), "{s43}"(lds0), "{s94}"(ty),
        "{v208}"(ga), "{v209}"(gb),
        "{s42}"(pfo), "{s98}"(toff), "{s99}"(tsel), "{s76}"(pop), "{s78}"(ps >> 5),
        "{v216}"(pfv)
      : "memory", "scc", "s51", "s96", "s97");
  __builtin_amdgcn_sched_barrier(0);
  const unsigned sp = lane < 18u ? lane : lane - 14u;
  const unsigned ob = (unsigned)(((sp < 12u ? 0x41ccd62ca418820ull >> (5u * sp) : 0x22a4c1eeull >> (5u * ((sp - 12u) & 7u)))) & 31u) * 6291456u + pk * 49152u;
  const unsigned pg = (ob + 24576u) >> 21;
  const unsigned ta = lane < 18u ? ob + 24576u * sst : 113246208u, t1 = lane < 32u ? ob + 24576u * (sst ^ 1u) : 113246208u;
  const unsigned mk = lane >= 4u && lane < 18u ? 1u << (pg < 52u ? (pg >> 3) * 4u : 28u) : 0u;
  const unsigned plm = (unsigned)__builtin_amdgcn_ballot_w64(((pg ^ role ^ sst) & 1u) != 0u), plb = wv < 2u ? plm : ~plm;
  const unsigned vgv = 0u + wv * 512u + lane * 8u;
  const unsigned vpub = lane == 0u ? 0u + (pv * 64u + ps) * 8u : 0x40000000u, vbmp = vgv == 0u ? 2048u : 0x40000000u;
  const unsigned vxr = lds0 + 159744u + lane * 16u, vxw = vxr + wv * 4u;
  const unsigned vfw = lds0 + 160768u + lane * 4u + wv * 256u, vfr = lds0 + 160768u + lane * 4u + (lane & 3u) * 256u;
  const unsigned li = lane & 15, lg = lane >> 4, rl = li & 3, hi = li >> 2;
  const unsigned rb = 768u * li + 16u * (lg ^ ((4u - hi) & 3)) - 3072u * wv;
  unsigned cfa[4], csw[3];
#pragma unroll
  for (unsigned u = 0; u < 4; ++u) cfa[u] = rb + 64u * ((u - rl) & 3);
#pragma unroll
  for (unsigned j = 0; j < 3; ++j) {
    const unsigned e = 3u * wv + j;
    csw[j] = rb + 256u * (e >> 2) + 64u * (((e & 3) + (e >> 2) - rl) & 3);
  }
  const unsigned csr = 16u * lane;
  const unsigned tbase = p0 + (4u * lg + (li >> 2)) * 192u + 16u * (li & 3);
  unsigned woff[3];
#pragma unroll
  for (unsigned j = 0; j < 3; ++j) {
    const unsigned q = 64u * j + lane, rr = q / 12u;
    woff[j] = (8u * (rr >> 2) + (rr & 3)) * 768u + 192u * wv + 16u * (q % 12u);
  }
  const unsigned boff = 192u * wv + 16u * lg;
  asm volatile(
      "s_cmp_eq_u32 s94, 1\n\t"
      "s_cbranch_scc0 .Lc0_%=\n\t"
      "s_getpc_b64 s[96:97]\n\t"
      ".Lcp_%=:\n\t"
      "s_add_u32 s98, s98, .Lcq_%=-.Lcp_%=\n\t"
      "s_min_u32 s98, s98, .Lend_%=-.Lcp_%=-68\n\t"
      "s_load_dword s99, s[96:97], s98 offset:64\n\t"
      "s_load_dword s98, s[96:97], s98\n\t"
      "s_add_u32 m0, s41, 0x6000\n\t"
      "s_add_u32 s51, s40, 0x600000\n\t"
      "buffer_load_dwordx4 v208, s[20:23], s51 offen lds\n\t"
      "buffer_load_dwordx4 v208, s[20:23], s51 offen offset:1024 lds\n\t"
      "buffer_load_dwordx4 v208, s[20:23], s51 offen offset:2048 lds\n\t"
      "s_add_u32 m0, s41, 0x9000\n\t"
      "s_add_u32 s51, s40, 0x600000\n\t"
      "buffer_load_dwordx4 v209, s[20:23], s51 offen lds\n\t"
      "buffer_load_dwordx4 v209, s[20:23], s51 offen offset:1024 lds\n\t"
      "buffer_load_dwordx4 v209, s[20:23], s51 offen offset:2048 lds\n\t"
      ".Lc0_%=:\n\t"
      "buffer_load_dwordx4 v[226:229], v215, s[36:39], 0 offen offset:0\n\t"
      "buffer_load_dwordx4 v[230:233], v215, s[36:39], 0 offen offset:64\n\t"
      "buffer_load_dwordx4 v[234:237], v215, s[36:39], 0 offen offset:128\n\t"
      "s_mov_b32 s51, 0x0\n\t"
      "s_add_u32 m0, s41, 0x1e000\n\t"
      "s_nop 0\n\t"
      "buffer_load_dwordx4 v211, s[16:19], s51 offen lds\n\t"
      "s_add_u32 m0, s41, 0x1e400\n\t"
      "s_nop 0\n\t"
      "buffer_load_dwordx4 v212, s[16:19], s51 offen lds\n\t"
      "s_add_u32 m0, s41, 0x1e800\n\t"
      "s_nop 0\n\t"
      "buffer_load_dwordx4 v213, s[16:19], s51 offen lds\n\t"
      "s_mov_b32 s51, 0xc00\n\t"
      "s_add_u32 m0, s41, 0x21000\n\t"
      "s_nop 0\n\t"
      "buffer_load_dwordx4 v211, s[16:19], s51 offen lds\n\t"
      "s_add_u32 m0, s41, 0x21400\n\t"
      "s_nop 0\n\t"
      "buffer_load_dwordx4 v212, s[16:19], s51 offen lds\n\t"
      "s_add_u32 m0, s41, 0x21800\n\t"
      "s_nop 0\n\t"
      "buffer_load_dwordx4 v213, s[16:19], s51 offen lds\n\t"
      "s_mov_b32 s51, 0x6000\n\t"
      "s_add_u32 m0, s41, 0x24000\n\t"
      "s_nop 0\n\t"
      "buffer_load_dwordx4 v211, s[16:19], s51 offen lds\n\t"
      "s_add_u32 m0, s41, 0x24400\n\t"
      "s_nop 0\n\t"
      "buffer_load_dwordx4 v212, s[16:19], s51 offen lds\n\t"
      "s_add_u32 m0, s41, 0x24800\n\t"
      "s_nop 0\n\t"
      "buffer_load_dwordx4 v213, s[16:19], s51 offen lds\n\t"
      "s_mov_b32 s51, 0x6c00\n\t"
      "s_add_u32 m0, s41, 0x12000\n\t"
      "s_nop 0\n\t"
      "buffer_load_dwordx4 v211, s[16:19], s51 offen lds\n\t"
      "s_add_u32 m0, s41, 0x12400\n\t"
      "s_nop 0\n\t"
      "buffer_load_dwordx4 v212, s[16:19], s51 offen lds\n\t"
      "s_add_u32 m0, s41, 0x12800\n\t"
      "s_nop 0\n\t"
      "buffer_load_dwordx4 v213, s[16:19], s51 offen lds\n\t"
      "s_mov_b32 s51, 0xc000\n\t"
      "s_add_u32 m0, s41, 0x15000\n\t"
      "s_nop 0\n\t"
      "buffer_load_dwordx4 v211, s[16:19], s51 offen lds\n\t"
      "s_add_u32 m0, s41, 0x15400\n\t"
      "s_nop 0\n\t"
      "buffer_load_dwordx4 v212, s[16:19], s51 offen lds\n\t"
      "s_add_u32 m0, s41, 0x15800\n\t"
      "s_nop 0\n\t"
      "buffer_load_dwordx4 v213, s[16:19], s51 offen lds\n\t"
      "s_mov_b32 s51, 0xcc00\n\t"
      "s_add_u32 m0, s41, 0x18000\n\t"
      "s_nop 0\n\t"
      "buffer_load_dwordx4 v211, s[16:19], s51 offen lds\n\t"
      "s_add_u32 m0, s41, 0x18400\n\t"
      "s_nop 0\n\t"
      "buffer_load_dwordx4 v212, s[16:19], s51 offen lds\n\t"
      "s_add_u32 m0, s41, 0x18800\n\t"
      "s_nop 0\n\t"
      "buffer_load_dwordx4 v213, s[16:19], s51 offen lds\n\t"
      "s_mov_b32 s51, 0x12000\n\t"
      "s_add_u32 m0, s41, 0x1b000\n\t"
      "s_nop 0\n\t"
      "buffer_load_dwordx4 v211, s[16:19], s51 offen lds\n\t"
      "s_add_u32 m0, s41, 0x1b400\n\t"
      "s_nop 0\n\t"
      "buffer_load_dwordx4 v212, s[16:19], s51 offen lds\n\t"
      "s_add_u32 m0, s41, 0x1b800\n\t"
      "s_nop 0\n\t"
      "buffer_load_dwordx4 v213, s[16:19], s51 offen lds\n\t"
      "s_mov_b32 s44, 0\n\t"
      "s_mov_b32 s85, 3\n\t"
      "s_add_u32 s50, s41, 0x12000\n\t"
      "s_mov_b32 s45, s41\n\t"
      "s_add_u32 s46, s41, 0x6000\n\t"
      "s_add_u32 s47, s41, 0xc000\n\t"
      "s_mov_b32 s84, 96\n\t"
      "v_add_u32_e32 v196, s41, v200\n\t"
      "v_add_u32_e32 v197, s41, v201\n\t"
      "v_add_u32_e32 v198, s41, v202\n\t"
      "v_add_u32_e32 v199, s41, v203\n\t"
      "v_add_u32_e32 v217, s41, v204\n\t"
      "v_add_u32_e32 v218, s41, v205\n\t"
      "v_add_u32_e32 v219, s41, v206\n\t"
      "v_add_u32_e32 v220, s41, v207\n\t"
      "s_waitcnt vmcnt(18)\n\t"
      "s_waitcnt lgkmcnt(0)\n\t"
      "s_barrier\n\t"
      "s_memrealtime s[68:69]\n\t"
      "v_add_u32_e32 v221, 0x1e000, v214\n\t"
      "ds_read_b64_tr_b16 a[0:1], v221 offset:0\n\t"
      "ds_read_b64_tr_b16 a[4:5], v221 offset:8\n\t"
      "ds_read_b64_tr_b16 a[8:9], v221 offset:64\n\t"
      "ds_read_b64_tr_b16 a[12:13], v221 offset:72\n\t"
      "ds_read_b64_tr_b16 a[16:17], v221 offset:128\n\t"
      "ds_read_b64_tr_b16 a[20:21], v221 offset:136\n\t"
      "ds_read_b128 v[140:143], v196 offset:0\n\t"
      "v_lshlrev_b32_e32 v8, 16, v226\n\t"
      "v_and_b32_e32 v9, 0xffff0000, v226\n\t"
      "v_lshlrev_b32_e32 v10, 16, v227\n\t"
      "v_and_b32_e32 v11, 0xffff0000, v227\n\t"
      "v_lshlrev_b32_e32 v12, 16, v228\n\t"
      "v_and_b32_e32 v13, 0xffff0000, v228\n\t"
      "v_lshlrev_b32_e32 v14, 16, v229\n\t"
      "v_and_b32_e32 v15, 0xffff0000, v229\n\t"
      "v_lshlrev_b32_e32 v16, 16, v230\n\t"
      "v_and_b32_e32 v17, 0xffff0000, v230\n\t"
      "v_lshlrev_b32_e32 v18, 16, v231\n\t"
      "v_and_b32_e32 v19, 0xffff0000, v231\n\t"
      "v_lshlrev_b32_e32 v20, 16, v232\n\t"
      "v_and_b32_e32 v21, 0xffff0000, v232\n\t"
      "v_lshlrev_b32_e32 v22, 16, v233\n\t"
      "v_and_b32_e32 v23, 0xffff0000, v233\n\t"
      "v_lshlrev_b32_e32 v24, 16, v234\n\t"
      "v_and_b32_e32 v25, 0xffff0000, v234\n\t"
      "v_lshlrev_b32_e32 v26, 16, v235\n\t"
      "v_and_b32_e32 v27, 0xffff0000, v235\n\t"
      "v_lshlrev_b32_e32 v28, 16, v236\n\t"
      "v_and_b32_e32 v29, 0xffff0000, v236\n\t"
      "v_lshlrev_b32_e32 v30, 16, v237\n\t"
      "v_and_b32_e32 v31, 0xffff0000, v237\n\t"
      "s_add_u32 s62, s62, 0x7f4a7c15\n\t"
      "s_add_u32 s63, s63, 0x9e3779b9\n\t"
      "s_waitcnt vmcnt(15)\n\t"
      "v_add_u32_e32 v221, 0x21000, v214\n\t"
      "ds_read_b64_tr_b16 a[2:3], v221 offset:0\n\t"
      "ds_read_b64_tr_b16 a[6:7], v221 offset:8\n\t"
      "ds_read_b64_tr_b16 a[10:11], v221 offset:64\n\t"
      "ds_read_b64_tr_b16 a[14:15], v221 offset:72\n\t"
      "ds_read_b64_tr_b16 a[18:19], v221 offset:128\n\t"
      "ds_read_b64_tr_b16 a[22:23], v221 offset:136\n\t"
      "ds_read_b128 v[152:155], v196 offset:12288\n\t"
      "s_waitcnt lgkmcnt(8)\n\t"
      "s_mov_b32 s51, 0x12c00\n\t"
      "s_add_u32 m0, s41, 0x1e000\n\t"
      "s_nop 0\n\t"
      "buffer_load_dwordx4 v211, s[16:19], s51 offen lds\n\t"
      "s_add_u32 m0, s41, 0x1e400\n\t"
      "s_nop 0\n\t"
      "buffer_load_dwordx4 v212, s[16:19], s51 offen lds\n\t"
      "s_add_u32 m0, s41, 0x1e800\n\t"
      "s_nop 0\n\t"
      "buffer_load_dwordx4 v213, s[16:19], s51 offen lds\n\t"
      "s_lshl_b32 s67, s68, 18\n\t"
      "s_bfe_u32 s51, s62, 0xc0000\n\t"
      "s_and_b32 s67, s67, 0xfff00000\n\t"
      "s_or_b32 s67, s67, s51\n\t"
      "s_cmp_lg_u32 s41, s43\n\t"
      "s_cbranch_scc1 .Lw0pb_%=\n\t"
      "s_or_b32 s77, s77, s67\n\t"
      "v_mov_b32_e32 v241, s63\n\t"
      "v_mov_b32_e32 v240, s77\n\t"
      "buffer_store_dwordx2 v[240:241], v244, s[24:27], 0 offen sc1\n\t"
      ".Lw0pb_%=:\n\t"
      "s_waitcnt vmcnt(15)\n\t"
      "v_add_u32_e32 v221, 0x24000, v214\n\t"
      "ds_read_b64_tr_b16 a[24:25], v221 offset:0\n\t"
      "ds_read_b64_tr_b16 a[28:29], v221 offset:8\n\t"
      "ds_read_b64_tr_b16 a[32:33], v221 offset:64\n\t"
      "ds_read_b64_tr_b16 a[36:37], v221 offset:72\n\t"
      "ds_read_b64_tr_b16 a[40:41], v221 offset:128\n\t"
      "ds_read_b64_tr_b16 a[44:45], v221 offset:136\n\t"
      "ds_read_b128 v[144:147], v197 offset:0\n\t"
      "s_waitcnt lgkmcnt(8)\n\t"
      "v_mfma_f32_16x16x32_bf16 v[80:83], a[0:3], v[140:143], v[8:11]\n\t"
      "s_mov_b32 s51, 0x18000\n\t"
      "s_add_u32 m0, s41, 0x21000\n\t"
      "s_nop 0\n\t"
      "buffer_load_dwordx4 v211, s[16:19], s51 offen lds\n\t"
      "s_add_u32 m0, s41, 0x21400\n\t"
      "s_nop 0\n\t"
      "buffer_load_dwordx4 v212, s[16:19], s51 offen lds\n\t"
      "v_mfma_f32_16x16x32_bf16 v[84:87], a[4:7], v[140:143], v[12:15]\n\t"
      "s_add_u32 m0, s41, 0x21800\n\t"
      "s_nop 0\n\t"
      "buffer_load_dwordx4 v213, s[16:19], s51 offen lds\n\t"
      "v_mfma_f32_16x16x32_bf16 v[88:91], a[8:11], v[140:143], v[16:19]\n\t"
      "s_sub_u32 s67, s67, 0x20000000\n\t"
      "s_andn2_b32 s68, s68, 3\n\t"
      "s_sub_u32 s68, s68, s52\n\t"
      "s_waitcnt vmcnt(15)\n\t"
      "v_add_u32_e32 v221, 0x12000, v214\n\t"
      "ds_read_b64_tr_b16 a[26:27], v221 offset:0\n\t"
      "v_mfma_f32_16x16x32_bf16 v[92:95], a[12:15], v[140:143], v[20:23]\n\t"
      "ds_read_b64_tr_b16 a[30:31], v221 offset:8\n\t"
      "ds_read_b64_tr_b16 a[34:35], v221 offset:64\n\t"
      "v_mfma_f32_16x16x32_bf16 v[96:99], a[16:19], v[140:143], v[24:27]\n\t"
      "ds_read_b64_tr_b16 a[38:39], v221 offset:72\n\t"
      "ds_read_b64_tr_b16 a[42:43], v221 offset:128\n\t"
      "v_mfma_f32_16x16x32_bf16 v[100:103], a[20:23], v[140:143], v[28:31]\n\t"
      "ds_read_b64_tr_b16 a[46:47], v221 offset:136\n\t"
      "ds_read_b128 v[156:159], v197 offset:12288\n\t"
      "s_waitcnt lgkmcnt(8)\n\t"
      "v_mfma_f32_16x16x32_bf16 v[104:107], a[0:3], v[152:155], v[8:11]\n\t"
      "s_mov_b32 s51, 0x18c00\n\t"
      "s_add_u32 m0, s41, 0x24000\n\t"
      "s_nop 0\n\t"
      "buffer_load_dwordx4 v211, s[16:19], s51 offen lds\n\t"
      "v_mfma_f32_16x16x32_bf16 v[108:111], a[4:7], v[152:155], v[12:15]\n\t"
      "s_add_u32 m0, s41, 0x24400\n\t"
      "s_nop 0\n\t"
      "buffer_load_dwordx4 v212, s[16:19], s51 offen lds\n\t"
      "v_mfma_f32_16x16x32_bf16 v[112:115], a[8:11], v[152:155], v[16:19]\n\t"
      "s_add_u32 m0, s41, 0x24800\n\t"
      "s_nop 0\n\t"
      "buffer_load_dwordx4 v213, s[16:19], s51 offen lds\n\t"
      "s_waitcnt vmcnt(15)\n\t"
      "v_add_u32_e32 v221, 0x15000, v214\n\t"
      "ds_read_b64_tr_b16 a[48:49], v221 offset:0\n\t"
      "v_mfma_f32_16x16x32_bf16 v[116:119], a[12:15], v[152:155], v[20:23]\n\t"
      "ds_read_b64_tr_b16 a[52:53], v221 offset:8\n\t"
      "ds_read_b64_tr_b16 a[56:57], v221 offset:64\n\t"
      "v_mfma_f32_16x16x32_bf16 v[120:123], a[16:19], v[152:155], v[24:27]\n\t"
      "ds_read_b64_tr_b16 a[60:61], v221 offset:72\n\t"
      "ds_read_b64_tr_b16 a[64:65], v221 offset:128\n\t"
      "v_mfma_f32_16x16x32_bf16 v[132:135], a[20:23], v[152:155], v[28:31]\n\t"
      "ds_read_b64_tr_b16 a[68:69], v221 offset:136\n\t"
      "ds_read_b128 v[148:151], v198 offset:0\n\t"
      "s_waitcnt lgkmcnt(8)\n\t"
      "v_mfma_f32_16x16x32_bf16 v[80:83], a[24:27], v[144:147], v[80:83]\n\t"
      "s_mov_b32 s51, 0x1e000\n\t"
      "s_add_u32 m0, s41, 0x12000\n\t"
      "s_nop 0\n\t"
      "buffer_load_dwordx4 v211, s[16:19], s51 offen lds\n\t"
      "v_mfma_f32_16x16x32_bf16 v[84:87], a[28:31], v[144:147], v[84:87]\n\t"
      "s_add_u32 m0, s41, 0x12400\n\t"
      "s_nop 0\n\t"
      "buffer_load_dwordx4 v212, s[16:19], s51 offen lds\n\t"
      "v_mfma_f32_16x16x32_bf16 v[88:91], a[32:35], v[144:147], v[88:91]\n\t"
      "s_add_u32 m0, s41, 0x12800\n\t"
      "s_nop 0\n\t"
      "buffer_load_dwordx4 v213, s[16:19], s51 offen lds\n\t"
      "s_waitcnt vmcnt(15)\n\t"
      "v_add_u32_e32 v221, 0x18000, v214\n\t"
      "ds_read_b64_tr_b16 a[50:51], v221 offset:0\n\t"
      "v_mfma_f32_16x16x32_bf16 v[92:95], a[36:39], v[144:147], v[92:95]\n\t"
      "ds_read_b64_tr_b16 a[54:55], v221 offset:8\n\t"
      "ds_read_b64_tr_b16 a[58:59], v221 offset:64\n\t"
      "v_mfma_f32_16x16x32_bf16 v[96:99], a[40:43], v[144:147], v[96:99]\n\t"
      "ds_read_b64_tr_b16 a[62:63], v221 offset:72\n\t"
      "ds_read_b64_tr_b16 a[66:67], v221 offset:128\n\t"
      "v_mfma_f32_16x16x32_bf16 v[100:103], a[44:47], v[144:147], v[100:103]\n\t"
      "ds_read_b64_tr_b16 a[70:71], v221 offset:136\n\t"
      "ds_read_b128 v[160:163], v198 offset:12288\n\t"
      "s_waitcnt lgkmcnt(8)\n\t"
      "v_mfma_f32_16x16x32_bf16 v[104:107], a[24:27], v[156:159], v[104:107]\n\t"
      "s_mov_b32 s51, 0x1ec00\n\t"
      "s_add_u32 m0, s41, 0x15000\n\t"
      "s_nop 0\n\t"
      "buffer_load_dwordx4 v211, s[16:19], s51 offen lds\n\t"
      "v_mfma_f32_16x16x32_bf16 v[108:111], a[28:31], v[156:159], v[108:111]\n\t"
      "s_add_u32 m0, s41, 0x15400\n\t"
      "s_nop 0\n\t"
      "buffer_load_dwordx4 v212, s[16:19], s51 offen lds\n\t"
      "v_mfma_f32_16x16x32_bf16 v[112:115], a[32:35], v[156:159], v[112:115]\n\t"
      "s_add_u32 m0, s41, 0x15800\n\t"
      "s_nop 0\n\t"
      "buffer_load_dwordx4 v213, s[16:19], s51 offen lds\n\t"
      "s_waitcnt vmcnt(15)\n\t"
      "v_add_u32_e32 v221, 0x1b000, v214\n\t"
      "ds_read_b64_tr_b16 a[72:73], v221 offset:0\n\t"
      "v_mfma_f32_16x16x32_bf16 v[116:119], a[36:39], v[156:159], v[116:119]\n\t"
      "ds_read_b64_tr_b16 a[76:77], v221 offset:8\n\t"
      "ds_read_b64_tr_b16 a[80:81], v221 offset:64\n\t"
      "v_mfma_f32_16x16x32_bf16 v[120:123], a[40:43], v[156:159], v[120:123]\n\t"
      "ds_read_b64_tr_b16 a[84:85], v221 offset:72\n\t"
      "ds_read_b64_tr_b16 a[88:89], v221 offset:128\n\t"
      "v_mfma_f32_16x16x32_bf16 v[132:135], a[44:47], v[156:159], v[132:135]\n\t"
      "ds_read_b64_tr_b16 a[92:93], v221 offset:136\n\t"
      "ds_read_b128 v[140:143], v199 offset:0\n\t"
      "s_waitcnt lgkmcnt(8)\n\t"
      "v_mfma_f32_16x16x32_bf16 v[80:83], a[48:51], v[148:151], v[80:83]\n\t"
      "s_mov_b32 s51, 0x24000\n\t"
      "s_add_u32 m0, s41, 0x18000\n\t"
      "s_nop 0\n\t"
      "buffer_load_dwordx4 v211, s[16:19], s51 offen lds\n\t"
      "v_mfma_f32_16x16x32_bf16 v[84:87], a[52:55], v[148:151], v[84:87]\n\t"
      "s_add_u32 m0, s41, 0x18400\n\t"
      "s_nop 0\n\t"
      "buffer_load_dwordx4 v212, s[16:19], s51 offen lds\n\t"
      "v_mfma_f32_16x16x32_bf16 v[88:91], a[56:59], v[148:151], v[88:91]\n\t"
      "s_add_u32 m0, s41, 0x18800\n\t"
      "s_nop 0\n\t"
      "buffer_load_dwordx4 v213, s[16:19], s51 offen lds\n\t"
      "s_waitcnt vmcnt(15)\n\t"
      "v_add_u32_e32 v221, 0x1e000, v214\n\t"
      "ds_read_b64_tr_b16 a[74:75], v221 offset:0\n\t"
      "v_mfma_f32_16x16x32_bf16 v[92:95], a[60:63], v[148:151], v[92:95]\n\t"
      "ds_read_b64_tr_b16 a[78:79], v221 offset:8\n\t"
      "ds_read_b64_tr_b16 a[82:83], v221 offset:64\n\t"
      "v_mfma_f32_16x16x32_bf16 v[96:99], a[64:67], v[148:151], v[96:99]\n\t"
      "ds_read_b64_tr_b16 a[86:87], v221 offset:72\n\t"
      "ds_read_b64_tr_b16 a[90:91], v221 offset:128\n\t"
      "v_mfma_f32_16x16x32_bf16 v[100:103], a[68:71], v[148:151], v[100:103]\n\t"
      "ds_read_b64_tr_b16 a[94:95], v221 offset:136\n\t"
      "ds_read_b128 v[152:155], v199 offset:12288\n\t"
      "s_waitcnt lgkmcnt(8)\n\t"
      "v_mfma_f32_16x16x32_bf16 v[104:107], a[48:51], v[160:163], v[104:107]\n\t"
      "s_mov_b32 s51, 0x24c00\n\t"
      "s_add_u32 m0, s41, 0x1b000\n\t"
      "s_nop 0\n\t"
      "buffer_load_dwordx4 v211, s[16:19], s51 offen lds\n\t"
      "v_mfma_f32_16x16x32_bf16 v[108:111], a[52:55], v[160:163], v[108:111]\n\t"
      "s_add_u32 m0, s41, 0x1b400\n\t"
      "s_nop 0\n\t"
      "buffer_load_dwordx4 v212, s[16:19], s51 offen lds\n\t"
      "v_mfma_f32_16x16x32_bf16 v[112:115], a[56:59], v[160:163], v[112:115]\n\t"
      "s_add_u32 m0, s41, 0x1b800\n\t"
      "s_nop 0\n\t"
      "buffer_load_dwordx4 v213, s[16:19], s51 offen lds\n\t"
      "s_waitcnt vmcnt(15)\n\t"
      "v_add_u32_e32 v221, 0x21000, v214\n\t"
      "ds_read_b64_tr_b16 a[96:97], v221 offset:0\n\t"
      "v_mfma_f32_16x16x32_bf16 v[116:119], a[60:63], v[160:163], v[116:119]\n\t"
      "ds_read_b64_tr_b16 a[100:101], v221 offset:8\n\t"
      "ds_read_b64_tr_b16 a[104:105], v221 offset:64\n\t"
      "v_mfma_f32_16x16x32_bf16 v[120:123], a[64:67], v[160:163], v[120:123]\n\t"
      "ds_read_b64_tr_b16 a[108:109], v221 offset:72\n\t"
      "ds_read_b64_tr_b16 a[112:113], v221 offset:128\n\t"
      "v_mfma_f32_16x16x32_bf16 v[132:135], a[68:71], v[160:163], v[132:135]\n\t"
      "ds_read_b64_tr_b16 a[116:117], v221 offset:136\n\t"
      "ds_read_b128 v[144:147], v197 offset:256\n\t"
      "s_waitcnt lgkmcnt(8)\n\t"
      "v_mfma_f32_16x16x32_bf16 v[80:83], a[72:75], v[140:143], v[80:83]\n\t"
      "s_mov_b32 s51, 0x2a000\n\t"
      "s_add_u32 m0, s41, 0x1e000\n\t"
      "s_nop 0\n\t"
      "buffer_load_dwordx4 v211, s[16:19], s51 offen lds\n\t"
      "v_mfma_f32_16x16x32_bf16 v[84:87], a[76:79], v[140:143], v[84:87]\n\t"
      "s_add_u32 m0, s41, 0x1e400\n\t"
      "s_nop 0\n\t"
      "buffer_load_dwordx4 v212, s[16:19], s51 offen lds\n\t"
      "v_mfma_f32_16x16x32_bf16 v[88:91], a[80:83], v[140:143], v[88:91]\n\t"
      "s_add_u32 m0, s41, 0x1e800\n\t"
      "s_nop 0\n\t"
      "buffer_load_dwordx4 v213, s[16:19], s51 offen lds\n\t"
      "s_waitcnt vmcnt(15)\n\t"
      "v_add_u32_e32 v221, 0x24000, v214\n\t"
      "ds_read_b64_tr_b16 a[98:99], v221 offset:0\n\t"
      "v_mfma_f32_16x16x32_bf16 v[92:95], a[84:87], v[140:143], v[92:95]\n\t"
      "ds_read_b64_tr_b16 a[102:103], v221 offset:8\n\t"
      "ds_read_b64_tr_b16 a[106:107], v221 offset:64\n\t"
      "v_mfma_f32_16x16x32_bf16 v[96:99], a[88:91], v[140:143], v[96:99]\n\t"
      "ds_read_b64_tr_b16 a[110:111], v221 offset:72\n\t"
      "ds_read_b64_tr_b16 a[114:115], v221 offset:128\n\t"
      "v_mfma_f32_16x16x32_bf16 v[100:103], a[92:95], v[140:143], v[100:103]\n\t"
      "ds_read_b64_tr_b16 a[118:119], v221 offset:136\n\t"
      "ds_read_b128 v[156:159], v197 offset:12544\n\t"
      "s_waitcnt lgkmcnt(8)\n\t"
      "v_mfma_f32_16x16x32_bf16 v[104:107], a[72:75], v[152:155], v[104:107]\n\t"
      "s_mov_b32 s51, 0x2ac00\n\t"
      "s_add_u32 m0, s41, 0x21000\n\t"
      "s_nop 0\n\t"
      "buffer_load_dwordx4 v211, s[16:19], s51 offen lds\n\t"
      "v_mfma_f32_16x16x32_bf16 v[108:111], a[76:79], v[152:155], v[108:111]\n\t"
      "s_add_u32 m0, s41, 0x21400\n\t"
      "s_nop 0\n\t"
      "buffer_load_dwordx4 v212, s[16:19], s51 offen lds\n\t"
      "v_mfma_f32_16x16x32_bf16 v[112:115], a[80:83], v[152:155], v[112:115]\n\t"
      "s_add_u32 m0, s41, 0x21800\n\t"
      "s_nop 0\n\t"
      "buffer_load_dwordx4 v213, s[16:19], s51 offen lds\n\t"
      "s_waitcnt vmcnt(15)\n\t"
      "v_add_u32_e32 v221, 0x12000, v214\n\t"
      "ds_read_b64_tr_b16 a[120:121], v221 offset:0\n\t"
      "v_mfma_f32_16x16x32_bf16 v[116:119], a[84:87], v[152:155], v[116:119]\n\t"
      "ds_read_b64_tr_b16 a[124:125], v221 offset:8\n\t"
      "ds_read_b64_tr_b16 a[128:129], v221 offset:64\n\t"
      "v_mfma_f32_16x16x32_bf16 v[120:123], a[88:91], v[152:155], v[120:123]\n\t"
      "ds_read_b64_tr_b16 a[132:133], v221 offset:72\n\t"
      "ds_read_b64_tr_b16 a[136:137], v221 offset:128\n\t"
      "v_mfma_f32_16x16x32_bf16 v[132:135], a[92:95], v[152:155], v[132:135]\n\t"
      "ds_read_b64_tr_b16 a[140:141], v221 offset:136\n\t"
      "ds_read_b128 v[148:151], v198 offset:256\n\t"
      "s_waitcnt lgkmcnt(8)\n\t"
      "v_mfma_f32_16x16x32_bf16 v[80:83], a[96:99], v[144:147], v[80:83]\n\t"
      "s_mov_b32 s51, 0x30000\n\t"
      "s_add_u32 m0, s41, 0x24000\n\t"
      "s_nop 0\n\t"
      "buffer_load_dwordx4 v211, s[16:19], s51 offen lds\n\t"
      "v_mfma_f32_16x16x32_bf16 v[84:87], a[100:103], v[144:147], v[84:87]\n\t"
      "s_add_u32 m0, s41, 0x24400\n\t"
      "s_nop 0\n\t"
      "buffer_load_dwordx4 v212, s[16:19], s51 offen lds\n\t"
      "v_mfma_f32_16x16x32_bf16 v[88:91], a[104:107], v[144:147], v[88:91]\n\t"
      "s_add_u32 m0, s41, 0x24800\n\t"
      "s_nop 0\n\t"
      "buffer_load_dwordx4 v213, s[16:19], s51 offen lds\n\t"
      "s_waitcnt vmcnt(15)\n\t"
      "v_add_u32_e32 v221, 0x15000, v214\n\t"
      "ds_read_b64_tr_b16 a[122:123], v221 offset:0\n\t"
      "v_mfma_f32_16x16x32_bf16 v[92:95], a[108:111], v[144:147], v[92:95]\n\t"
      "ds_read_b64_tr_b16 a[126:127], v221 offset:8\n\t"
      "ds_read_b64_tr_b16 a[130:131], v221 offset:64\n\t"
      "v_mfma_f32_16x16x32_bf16 v[96:99], a[112:115], v[144:147], v[96:99]\n\t"
      "ds_read_b64_tr_b16 a[134:135], v221 offset:72\n\t"
      "ds_read_b64_tr_b16 a[138:139], v221 offset:128\n\t"
      "v_mfma_f32_16x16x32_bf16 v[100:103], a[116:119], v[144:147], v[100:103]\n\t"
      "ds_read_b64_tr_b16 a[142:143], v221 offset:136\n\t"
      "ds_read_b128 v[160:163], v198 offset:12544\n\t"
      "s_waitcnt lgkmcnt(8)\n\t"
      "v_mfma_f32_16x16x32_bf16 v[104:107], a[96:99], v[156:159], v[104:107]\n\t"
      "s_mov_b32 s51, 0x30c00\n\t"
      "s_add_u32 m0, s41, 0x12000\n\t"
      "s_nop 0\n\t"
      "buffer_load_dwordx4 v211, s[16:19], s51 offen lds\n\t"
      "v_mfma_f32_16x16x32_bf16 v[108:111], a[100:103], v[156:159], v[108:111]\n\t"
      "s_add_u32 m0, s41, 0x12400\n\t"
      "s_nop 0\n\t"
      "buffer_load_dwordx4 v212, s[16:19], s51 offen lds\n\t"
      "v_mfma_f32_16x16x32_bf16 v[112:115], a[104:107], v[156:159], v[112:115]\n\t"
      "s_add_u32 m0, s41, 0x12800\n\t"
      "s_nop 0\n\t"
      "buffer_load_dwordx4 v213, s[16:19], s51 offen lds\n\t"
      "s_waitcnt vmcnt(15)\n\t"
      "v_add_u32_e32 v221, 0x18000, v214\n\t"
      "ds_read_b64_tr_b16 a[144:145], v221 offset:0\n\t"
      "v_mfma_f32_16x16x32_bf16 v[116:119], a[108:111], v[156:159], v[116:119]\n\t"
      "ds_read_b64_tr_b16 a[148:149], v221 offset:8\n\t"
      "ds_read_b64_tr_b16 a[152:153], v221 offset:64\n\t"
      "v_mfma_f32_16x16x32_bf16 v[120:123], a[112:115], v[156:159], v[120:123]\n\t"
      "ds_read_b64_tr_b16 a[156:157], v221 offset:72\n\t"
      "ds_read_b64_tr_b16 a[160:161], v221 offset:128\n\t"
      "v_mfma_f32_16x16x32_bf16 v[132:135], a[116:119], v[156:159], v[132:135]\n\t"
      "ds_read_b64_tr_b16 a[164:165], v221 offset:136\n\t"
      "ds_read_b128 v[140:143], v199 offset:256\n\t"
      "s_waitcnt lgkmcnt(8)\n\t"
      "v_mfma_f32_16x16x32_bf16 v[80:83], a[120:123], v[148:151], v[80:83]\n\t"
      "s_mov_b32 s51, 0x36000\n\t"
      "s_add_u32 m0, s41, 0x15000\n\t"
      "s_nop 0\n\t"
      "buffer_load_dwordx4 v211, s[16:19], s51 offen lds\n\t"
      "v_mfma_f32_16x16x32_bf16 v[84:87], a[124:127], v[148:151], v[84:87]\n\t"
      "s_add_u32 m0, s41, 0x15400\n\t"
      "s_nop 0\n\t"
      "buffer_load_dwordx4 v212, s[16:19], s51 offen lds\n\t"
      "v_mfma_f32_16x16x32_bf16 v[88:91], a[128:131], v[148:151], v[88:91]\n\t"
      "s_add_u32 m0, s41, 0x15800\n\t"
      "s_nop 0\n\t"
      "buffer_load_dwordx4 v213, s[16:19], s51 offen lds\n\t"
      "s_waitcnt vmcnt(15)\n\t"
      "v_add_u32_e32 v221, 0x1b000, v214\n\t"
      "ds_read_b64_tr_b16 a[146:147], v221 offset:0\n\t"
      "v_mfma_f32_16x16x32_bf16 v[92:95], a[132:135], v[148:151], v[92:95]\n\t"
      "ds_read_b64_tr_b16 a[150:151], v221 offset:8\n\t"
      "ds_read_b64_tr_b16 a[154:155], v221 offset:64\n\t"
      "v_mfma_f32_16x16x32_bf16 v[96:99], a[136:139], v[148:151], v[96:99]\n\t"
      "ds_read_b64_tr_b16 a[158:159], v221 offset:72\n\t"
      "ds_read_b64_tr_b16 a[162:163], v221 offset:128\n\t"
      "v_mfma_f32_16x16x32_bf16 v[100:103], a[140:143], v[148:151], v[100:103]\n\t"
      "ds_read_b64_tr_b16 a[166:167], v221 offset:136\n\t"
      "ds_read_b128 v[152:155], v199 offset:12544\n\t"
      "s_waitcnt lgkmcnt(8)\n\t"
      "v_mfma_f32_16x16x32_bf16 v[104:107], a[120:123], v[160:163], v[104:107]\n\t"
      "s_mov_b32 s51, 0x36c00\n\t"
      "s_add_u32 m0, s41, 0x18000\n\t"
      "s_nop 0\n\t"
      "buffer_load_dwordx4 v211, s[16:19], s51 offen lds\n\t"
      "v_mfma_f32_16x16x32_bf16 v[108:111], a[124:127], v[160:163], v[108:111]\n\t"
      "s_add_u32 m0, s41, 0x18400\n\t"
      "s_nop 0\n\t"
      "buffer_load_dwordx4 v212, s[16:19], s51 offen lds\n\t"
      "v_mfma_f32_16x16x32_bf16 v[112:115], a[128:131], v[160:163], v[112:115]\n\t"
      "s_add_u32 m0, s41, 0x18800\n\t"
      "s_nop 0\n\t"
      "buffer_load_dwordx4 v213, s[16:19], s51 offen lds\n\t"
      "s_waitcnt vmcnt(15)\n\t"
      "v_add_u32_e32 v221, 0x1e000, v214\n\t"
      "ds_read_b64_tr_b16 a[168:169], v221 offset:0\n\t"
      "v_mfma_f32_16x16x32_bf16 v[116:119], a[132:135], v[160:163], v[116:119]\n\t"
      "ds_read_b64_tr_b16 a[172:173], v221 offset:8\n\t"
      "ds_read_b64_tr_b16 a[176:177], v221 offset:64\n\t"
      "v_mfma_f32_16x16x32_bf16 v[120:123], a[136:139], v[160:163], v[120:123]\n\t"
      "ds_read_b64_tr_b16 a[180:181], v221 offset:72\n\t"
      "ds_read_b64_tr_b16 a[184:185], v221 offset:128\n\t"
      "v_mfma_f32_16x16x32_bf16 v[132:135], a[140:143], v[160:163], v[132:135]\n\t"
      "ds_read_b64_tr_b16 a[188:189], v221 offset:136\n\t"
      "ds_read_b128 v[144:147], v196 offset:256\n\t"
      "s_waitcnt lgkmcnt(8)\n\t"
      "v_mfma_f32_16x16x32_bf16 v[80:83], a[144:147], v[140:143], v[80:83]\n\t"
      "s_mov_b32 s51, 0x3c000\n\t"
      "s_add_u32 m0, s41, 0x1b000\n\t"
      "s_nop 0\n\t"
      "buffer_load_dwordx4 v211, s[16:19], s51 offen lds\n\t"
      "v_mfma_f32_16x16x32_bf16 v[84:87], a[148:151], v[140:143], v[84:87]\n\t"
      "s_add_u32 m0, s41, 0x1b400\n\t"
      "s_nop 0\n\t"
      "buffer_load_dwordx4 v212, s[16:19], s51 offen lds\n\t"
      "v_mfma_f32_16x16x32_bf16 v[88:91], a[152:155], v[140:143], v[88:91]\n\t"
      "s_add_u32 m0, s41, 0x1b800\n\t"
      "s_nop 0\n\t"
      "buffer_load_dwordx4 v213, s[16:19], s51 offen lds\n\t"
      "s_waitcnt vmcnt(15)\n\t"
      "v_add_u32_e32 v221, 0x21000, v214\n\t"
      "ds_read_b64_tr_b16 a[170:171], v221 offset:0\n\t"
      "v_mfma_f32_16x16x32_bf16 v[92:95], a[156:159], v[140:143], v[92:95]\n\t"
      "ds_read_b64_tr_b16 a[174:175], v221 offset:8\n\t"
      "ds_read_b64_tr_b16 a[178:179], v221 offset:64\n\t"
      "v_mfma_f32_16x16x32_bf16 v[96:99], a[160:163], v[140:143], v[96:99]\n\t"
      "ds_read_b64_tr_b16 a[182:183], v221 offset:72\n\t"
      "ds_read_b64_tr_b16 a[186:187], v221 offset:128\n\t"
      "v_mfma_f32_16x16x32_bf16 v[100:103], a[164:167], v[140:143], v[100:103]\n\t"
      "ds_read_b64_tr_b16 a[190:191], v221 offset:136\n\t"
      "ds_read_b128 v[156:159], v196 offset:12544\n\t"
      "s_waitcnt lgkmcnt(8)\n\t"
      "v_mfma_f32_16x16x32_bf16 v[104:107], a[144:147], v[152:155], v[104:107]\n\t"
      "s_mov_b32 s51, 0x3cc00\n\t"
      "s_add_u32 m0, s41, 0x1e000\n\t"
      "s_nop 0\n\t"
      "buffer_load_dwordx4 v211, s[16:19], s51 offen lds\n\t"
      "v_mfma_f32_16x16x32_bf16 v[108:111], a[148:151], v[152:155], v[108:111]\n\t"
      "s_add_u32 m0, s41, 0x1e400\n\t"
      "s_nop 0\n\t"
      "buffer_load_dwordx4 v212, s[16:19], s51 offen lds\n\t"
      "v_mfma_f32_16x16x32_bf16 v[112:115], a[152:155], v[152:155], v[112:115]\n\t"
      "s_add_u32 m0, s41, 0x1e800\n\t"
      "s_nop 0\n\t"
      "buffer_load_dwordx4 v213, s[16:19], s51 offen lds\n\t"
      "s_waitcnt vmcnt(15)\n\t"
      "v_add_u32_e32 v221, 0x24000, v214\n\t"
      "ds_read_b64_tr_b16 a[192:193], v221 offset:0\n\t"
      "v_mfma_f32_16x16x32_bf16 v[116:119], a[156:159], v[152:155], v[116:119]\n\t"
      "ds_read_b64_tr_b16 a[196:197], v221 offset:8\n\t"
      "ds_read_b64_tr_b16 a[200:201], v221 offset:64\n\t"
      "v_mfma_f32_16x16x32_bf16 v[120:123], a[160:163], v[152:155], v[120:123]\n\t"
      "ds_read_b64_tr_b16 a[204:205], v221 offset:72\n\t"
      "ds_read_b64_tr_b16 a[208:209], v221 offset:128\n\t"
      "v_mfma_f32_16x16x32_bf16 v[132:135], a[164:167], v[152:155], v[132:135]\n\t"
      "ds_read_b64_tr_b16 a[212:213], v221 offset:136\n\t"
      "ds_read_b128 v[148:151], v198 offset:512\n\t"
      "s_waitcnt lgkmcnt(8)\n\t"
      "v_mfma_f32_16x16x32_bf16 v[80:83], a[168:171], v[144:147], v[80:83]\n\t"
      "s_mov_b32 s51, 0x42000\n\t"
      "s_add_u32 m0, s41, 0x21000\n\t"
      "s_nop 0\n\t"
      "buffer_load_dwordx4 v211, s[16:19], s51 offen lds\n\t"
      "v_mfma_f32_16x16x32_bf16 v[84:87], a[172:175], v[144:147], v[84:87]\n\t"
      "s_add_u32 m0, s41, 0x21400\n\t"
      "s_nop 0\n\t"
      "buffer_load_dwordx4 v212, s[16:19], s51 offen lds\n\t"
      "v_mfma_f32_16x16x32_bf16 v[88:91], a[176:179], v[144:147], v[88:91]\n\t"
      "s_add_u32 m0, s41, 0x21800\n\t"
      "s_nop 0\n\t"
      "buffer_load_dwordx4 v213, s[16:19], s51 offen lds\n\t"
      "s_waitcnt vmcnt(15)\n\t"
      "v_add_u32_e32 v221, 0x12000, v214\n\t"
      "ds_read_b64_tr_b16 a[194:195], v221 offset:0\n\t"
      "v_mfma_f32_16x16x32_bf16 v[92:95], a[180:183], v[144:147], v[92:95]\n\t"
      "ds_read_b64_tr_b16 a[198:199], v221 offset:8\n\t"
      "ds_read_b64_tr_b16 a[202:203], v221 offset:64\n\t"
      "v_mfma_f32_16x16x32_bf16 v[96:99], a[184:187], v[144:147], v[96:99]\n\t"
      "ds_read_b64_tr_b16 a[206:207], v221 offset:72\n\t"
      "ds_read_b64_tr_b16 a[210:211], v221 offset:128\n\t"
      "v_mfma_f32_16x16x32_bf16 v[100:103], a[188:191], v[144:147], v[100:103]\n\t"
      "ds_read_b64_tr_b16 a[214:215], v221 offset:136\n\t"
      "ds_read_b128 v[160:163], v198 offset:12800\n\t"
      "s_waitcnt lgkmcnt(8)\n\t"
      "v_mfma_f32_16x16x32_bf16 v[104:107], a[168:171], v[156:159], v[104:107]\n\t"
      "s_mov_b32 s51, 0x42c00\n\t"
      "s_add_u32 m0, s41, 0x24000\n\t"
      "s_nop 0\n\t"
      "buffer_load_dwordx4 v211, s[16:19], s51 offen lds\n\t"
      "v_mfma_f32_16x16x32_bf16 v[108:111], a[172:175], v[156:159], v[108:111]\n\t"
      "s_add_u32 m0, s41, 0x24400\n\t"
      "s_nop 0\n\t"
      "buffer_load_dwordx4 v212, s[16:19], s51 offen lds\n\t"
      "v_mfma_f32_16x16x32_bf16 v[112:115], a[176:179], v[156:159], v[112:115]\n\t"
      "s_add_u32 m0, s41, 0x24800\n\t"
      "s_nop 0\n\t"
      "buffer_load_dwordx4 v213, s[16:19], s51 offen lds\n\t"
      "s_waitcnt vmcnt(15)\n\t"
      "v_add_u32_e32 v221, 0x15000, v214\n\t"
      "ds_read_b64_tr_b16 a[216:217], v221 offset:0\n\t"
      "v_mfma_f32_16x16x32_bf16 v[116:119], a[180:183], v[156:159], v[116:119]\n\t"
      "ds_read_b64_tr_b16 a[220:221], v221 offset:8\n\t"
      "ds_read_b64_tr_b16 a[224:225], v221 offset:64\n\t"
      "v_mfma_f32_16x16x32_bf16 v[120:123], a[184:187], v[156:159], v[120:123]\n\t"
      "ds_read_b64_tr_b16 a[228:229], v221 offset:72\n\t"
      "ds_read_b64_tr_b16 a[232:233], v221 offset:128\n\t"
      "v_mfma_f32_16x16x32_bf16 v[132:135], a[188:191], v[156:159], v[132:135]\n\t"
      "ds_read_b64_tr_b16 a[236:237], v221 offset:136\n\t"
      "ds_read_b128 v[140:143], v199 offset:512\n\t"
      "s_waitcnt lgkmcnt(8)\n\t"
      "v_mfma_f32_16x16x32_bf16 v[80:83], a[192:195], v[148:151], v[80:83]\n\t"
      "s_cmp_eq_u32 s94, 0\n\t"
      "s_cbranch_scc0 .Lc1_%=\n\t"
      "s_add_u32 m0, s41, 0x6000\n\t"
      "s_add_u32 s51, s40, 0x600000\n\t"
      "buffer_load_dwordx4 v208, s[20:23], s51 offen lds\n\t"
      "buffer_load_dwordx4 v208, s[20:23], s51 offen offset:1024 lds\n\t"
      "buffer_load_dwordx4 v208, s[20:23], s51 offen offset:2048 lds\n\t"
      ".Lc1_%=:\n\t"
      "v_mfma_f32_16x16x32_bf16 v[84:87], a[196:199], v[148:151], v[84:87]\n\t"
      "v_mfma_f32_16x16x32_bf16 v[88:91], a[200:203], v[148:151], v[88:91]\n\t"
      "s_waitcnt vmcnt(15)\n\t"
      "s_cmp_eq_u32 s94, 1\n\t"
      "s_cbranch_scc0 .Lwpw19_1_%=\n\t"
      "s_waitcnt vmcnt(12)\n\t"
      ".Lwpw19_1_%=:\n\t"
      "v_add_u32_e32 v221, 0x18000, v214\n\t"
      "ds_read_b64_tr_b16 a[218:219], v221 offset:0\n\t"
      "v_mfma_f32_16x16x32_bf16 v[92:95], a[204:207], v[148:151], v[92:95]\n\t"
      "ds_read_b64_tr_b16 a[222:223], v221 offset:8\n\t"
      "ds_read_b64_tr_b16 a[226:227], v221 offset:64\n\t"
      "v_mfma_f32_16x16x32_bf16 v[96:99], a[208:211], v[148:151], v[96:99]\n\t"
      "ds_read_b64_tr_b16 a[230:231], v221 offset:72\n\t"
      "ds_read_b64_tr_b16 a[234:235], v221 offset:128\n\t"
      "v_mfma_f32_16x16x32_bf16 v[100:103], a[212:215], v[148:151], v[100:103]\n\t"
      "ds_read_b64_tr_b16 a[238:239], v221 offset:136\n\t"
      "ds_read_b128 v[152:155], v199 offset:12800\n\t"
      "s_waitcnt lgkmcnt(14)\n\t"
      "v_mfma_f32_16x16x32_bf16 v[104:107], a[192:195], v[160:163], v[104:107]\n\t"
      "s_cmp_eq_u32 s94, 0\n\t"
      "s_cbranch_scc0 .Lc2_%=\n\t"
      "s_add_u32 m0, s41, 0x9000\n\t"
      "s_add_u32 s51, s40, 0x600000\n\t"
      "buffer_load_dwordx4 v209, s[20:23], s51 offen lds\n\t"
      "buffer_load_dwordx4 v209, s[20:23], s51 offen offset:1024 lds\n\t"
      "buffer_load_dwordx4 v209, s[20:23], s51 offen offset:2048 lds\n\t"
      ".Lc2_%=:\n\t"
      "v_mfma_f32_16x16x32_bf16 v[108:111], a[196:199], v[160:163], v[108:111]\n\t"
      "v_mfma_f32_16x16x32_bf16 v[112:115], a[200:203], v[160:163], v[112:115]\n\t"
      "s_waitcnt vmcnt(15)\n\t"
      "s_cmp_eq_u32 s94, 1\n\t"
      "s_cbranch_scc0 .Lwpw20_1_%=\n\t"
      "s_waitcnt vmcnt(9)\n\t"
      ".Lwpw20_1_%=:\n\t"
      "v_add_u32_e32 v221, 0x1b000, v214\n\t"
      "ds_read_b64_tr_b16 a[240:241], v221 offset:0\n\t"
      "v_mfma_f32_16x16x32_bf16 v[116:119], a[204:207], v[160:163], v[116:119]\n\t"
      "ds_read_b64_tr_b16 a[244:245], v221 offset:8\n\t"
      "ds_read_b64_tr_b16 a[248:249], v221 offset:64\n\t"
      "v_mfma_f32_16x16x32_bf16 v[120:123], a[208:211], v[160:163], v[120:123]\n\t"
      "ds_read_b64_tr_b16 a[252:253], v221 offset:72\n\t"
      "ds_read_b64_tr_b16 v[164:165], v221 offset:128\n\t"
      "v_mfma_f32_16x16x32_bf16 v[132:135], a[212:215], v[160:163], v[132:135]\n\t"
      "ds_read_b64_tr_b16 v[168:169], v221 offset:136\n\t"
      "ds_read_b128 v[144:147], v196 offset:512\n\t"
      "s_waitcnt lgkmcnt(8)\n\t"
      "v_mfma_f32_16x16x32_bf16 v[80:83], a[216:219], v[140:143], v[80:83]\n\t"
      "s_cmp_eq_u32 s94, 0\n\t"
      "s_cbranch_scc0 .Lc3_%=\n\t"
      "s_add_u32 m0, s41, 0xc000\n\t"
      "s_add_u32 s51, s40, 0xc00000\n\t"
      "buffer_load_dwordx4 v208, s[20:23], s51 offen lds\n\t"
      "buffer_load_dwordx4 v208, s[20:23], s51 offen offset:1024 lds\n\t"
      "buffer_load_dwordx4 v208, s[20:23], s51 offen offset:2048 lds\n\t"
      ".Lc3_%=:\n\t"
      "v_mfma_f32_16x16x32_bf16 v[84:87], a[220:223], v[140:143], v[84:87]\n\t"
      "s_cmp_eq_u32 s94, 1\n\t"
      "s_cbranch_scc0 .Lc4_%=\n\t"
      "s_add_u32 m0, s41, 0xc000\n\t"
      "s_add_u32 s51, s40, 0xc00000\n\t"
      "buffer_load_dwordx4 v208, s[20:23], s51 offen lds\n\t"
      "buffer_load_dwordx4 v208, s[20:23], s51 offen offset:1024 lds\n\t"
      "buffer_load_dwordx4 v208, s[20:23], s51 offen offset:2048 lds\n\t"
      ".Lc4_%=:\n\t"
      "v_mfma_f32_16x16x32_bf16 v[88:91], a[224:227], v[140:143], v[88:91]\n\t"
      "s_waitcnt vmcnt(15)\n\t"
      "s_cmp_eq_u32 s94, 1\n\t"
      "s_cbranch_scc0 .Lwpw21_1_%=\n\t"
      "s_waitcnt vmcnt(9)\n\t"
      ".Lwpw21_1_%=:\n\t"
      "v_add_u32_e32 v221, 0x1e000, v214\n\t"
      "ds_read_b64_tr_b16 a[242:243], v221 offset:0\n\t"
      "v_mfma_f32_16x16x32_bf16 v[92:95], a[228:231], v[140:143], v[92:95]\n\t"
      "ds_read_b64_tr_b16 a[246:247], v221 offset:8\n\t"
      "ds_read_b64_tr_b16 a[250:251], v221 offset:64\n\t"
      "v_mfma_f32_16x16x32_bf16 v[96:99], a[232:235], v[140:143], v[96:99]\n\t"
      "ds_read_b64_tr_b16 a[254:255], v221 offset:72\n\t"
      "ds_read_b64_tr_b16 v[166:167], v221 offset:128\n\t"
      "v_mfma_f32_16x16x32_bf16 v[100:103], a[236:239], v[140:143], v[100:103]\n\t"
      "ds_read_b64_tr_b16 v[170:171], v221 offset:136\n\t"
      "ds_read_b128 v[156:159], v196 offset:12800\n\t"
      "s_waitcnt lgkmcnt(14)\n\t"
      "v_mfma_f32_16x16x32_bf16 v[104:107], a[216:219], v[152:155], v[104:107]\n\t"
      "s_cmp_eq_u32 s94, 0\n\t"
      "s_cbranch_scc0 .Lc5_%=\n\t"
      "s_add_u32 m0, s41, 0xf000\n\t"
      "s_add_u32 s51, s40, 0xc00000\n\t"
      "buffer_load_dwordx4 v209, s[20:23], s51 offen lds\n\t"
      "buffer_load_dwordx4 v209, s[20:23], s51 offen offset:1024 lds\n\t"
      "buffer_load_dwordx4 v209, s[20:23], s51 offen offset:2048 lds\n\t"
      ".Lc5_%=:\n\t"
      "v_mfma_f32_16x16x32_bf16 v[108:111], a[220:223], v[152:155], v[108:111]\n\t"
      "s_cmp_eq_u32 s94, 1\n\t"
      "s_cbranch_scc0 .Lc6_%=\n\t"
      "s_add_u32 m0, s41, 0xf000\n\t"
      "s_add_u32 s51, s40, 0xc00000\n\t"
      "buffer_load_dwordx4 v209, s[20:23], s51 offen lds\n\t"
      "buffer_load_dwordx4 v209, s[20:23], s51 offen offset:1024 lds\n\t"
      "buffer_load_dwordx4 v209, s[20:23], s51 offen offset:2048 lds\n\t"
      ".Lc6_%=:\n\t"
      "v_mfma_f32_16x16x32_bf16 v[112:115], a[224:227], v[152:155], v[112:115]\n\t"
      "s_waitcnt vmcnt(15)\n\t"
      "s_cmp_eq_u32 s94, 1\n\t"
      "s_cbranch_scc0 .Lwpw22_1_%=\n\t"
      "s_waitcnt vmcnt(9)\n\t"
      ".Lwpw22_1_%=:\n\t"
      "v_add_u32_e32 v221, 0x21000, v214\n\t"
      "ds_read_b64_tr_b16 v[172:173], v221 offset:0\n\t"
      "v_mfma_f32_16x16x32_bf16 v[116:119], a[228:231], v[152:155], v[116:119]\n\t"
      "ds_read_b64_tr_b16 v[176:177], v221 offset:8\n\t"
      "ds_read_b64_tr_b16 v[180:181], v221 offset:64\n\t"
      "ds_read_b64_tr_b16 v[184:185], v221 offset:72\n\t"
      "v_mfma_f32_16x16x32_bf16 v[120:123], a[232:235], v[152:155], v[120:123]\n\t"
      "ds_read_b64_tr_b16 v[188:189], v221 offset:128\n\t"
      "ds_read_b64_tr_b16 v[192:193], v221 offset:136\n\t"
      "v_mfma_f32_16x16x32_bf16 v[132:135], a[236:239], v[152:155], v[132:135]\n\t"
      "ds_read_b128 v[148:151], v197 offset:512\n\t"
      "ds_read_b128 v[160:163], v197 offset:12800\n\t"
      "s_waitcnt lgkmcnt(9)\n\t"
      "v_mfma_f32_16x16x32_bf16 v[80:83], a[240:243], v[144:147], v[80:83]\n\t"
      "s_mov_b64 s[96:97], s[34:35]\n\t"
      "s_movk_i32 s98, 0x148\n\t"
      "s_mov_b32 s99, s23\n\t"
      "buffer_load_dwordx3 v[252:254], v207, s[96:99], 0 offen offset:40\n\t"
      "v_mfma_f32_16x16x32_bf16 v[84:87], a[244:247], v[144:147], v[84:87]\n\t"
      "buffer_load_dwordx2 v[240:241], v245, s[24:27], 0 offen sc1\n\t"
      "v_mfma_f32_16x16x32_bf16 v[88:91], a[248:251], v[144:147], v[88:91]\n\t"
      "s_waitcnt vmcnt(14)\n\t"
      "s_cmp_eq_u32 s94, 1\n\t"
      "s_cbranch_scc0 .Lwpw23_1_%=\n\t"
      "s_waitcnt vmcnt(8)\n\t"
      ".Lwpw23_1_%=:\n\t"
      "v_add_u32_e32 v221, 0x24000, v214\n\t"
      "ds_read_b64_tr_b16 v[174:175], v221 offset:0\n\t"
      "v_mfma_f32_16x16x32_bf16 v[92:95], a[252:255], v[144:147], v[92:95]\n\t"
      "ds_read_b64_tr_b16 v[178:179], v221 offset:8\n\t"
      "ds_read_b64_tr_b16 v[182:183], v221 offset:64\n\t"
      "v_mfma_f32_16x16x32_bf16 v[96:99], v[164:167], v[144:147], v[96:99]\n\t"
      "ds_read_b64_tr_b16 v[186:187], v221 offset:72\n\t"
      "ds_read_b64_tr_b16 v[190:191], v221 offset:128\n\t"
      "v_mfma_f32_16x16x32_bf16 v[100:103], v[168:171], v[144:147], v[100:103]\n\t"
      "ds_read_b64_tr_b16 v[194:195], v221 offset:136\n\t"
      ".Lcq_%=:\n\t"
      "v_add_u32_e32 v196, s46, v200\n\t"
      "v_add_u32_e32 v197, s46, v201\n\t"
      "v_add_u32_e32 v198, s46, v202\n\t"
      "v_add_u32_e32 v199, s46, v203\n\t"
      "s_cmp_eq_u32 s94, 0\n\t"
      "s_cbranch_scc0 .Lwpx2_0_%=\n\t"
      "s_waitcnt vmcnt(11)\n\t"
      ".Lwpx2_0_%=:\n\t"
      "s_waitcnt lgkmcnt(6)\n\t"
      "s_barrier\n\t"
      "v_mfma_f32_16x16x32_bf16 v[104:107], a[240:243], v[156:159], v[104:107]\n\t"
      "ds_read_b128 v[32:35], v196 offset:0\n\t"
      "v_mfma_f32_16x16x32_bf16 v[108:111], a[244:247], v[156:159], v[108:111]\n\t"
      "ds_read_b128 v[36:39], v197 offset:0\n\t"
      "v_mfma_f32_16x16x32_bf16 v[112:115], a[248:251], v[156:159], v[112:115]\n\t"
      "ds_read_b128 v[40:43], v198 offset:0\n\t"
      "v_mfma_f32_16x16x32_bf16 v[116:119], a[252:255], v[156:159], v[116:119]\n\t"
      "ds_read_b128 v[44:47], v199 offset:0\n\t"
      "v_mfma_f32_16x16x32_bf16 v[120:123], v[164:167], v[156:159], v[120:123]\n\t"
      "ds_read_b128 v[48:51], v197 offset:256\n\t"
      "v_mfma_f32_16x16x32_bf16 v[132:135], v[168:171], v[156:159], v[132:135]\n\t"
      "s_waitcnt lgkmcnt(5)\n\t"
      "v_mfma_f32_16x16x32_bf16 v[80:83], v[172:175], v[148:151], v[80:83]\n\t"
      "ds_read_b128 v[52:55], v198 offset:256\n\t"
      "v_mfma_f32_16x16x32_bf16 v[84:87], v[176:179], v[148:151], v[84:87]\n\t"
      "ds_read_b128 v[56:59], v199 offset:256\n\t"
      "v_mfma_f32_16x16x32_bf16 v[88:91], v[180:183], v[148:151], v[88:91]\n\t"
      "ds_read_b128 v[60:63], v196 offset:256\n\t"
      "v_mfma_f32_16x16x32_bf16 v[92:95], v[184:187], v[148:151], v[92:95]\n\t"
      "ds_read_b128 v[64:67], v198 offset:512\n\t"
      "v_mfma_f32_16x16x32_bf16 v[96:99], v[188:191], v[148:151], v[96:99]\n\t"
      "ds_read_b128 v[68:71], v199 offset:512\n\t"
      "v_mfma_f32_16x16x32_bf16 v[100:103], v[192:195], v[148:151], v[100:103]\n\t"
      "ds_read_b128 v[72:75], v196 offset:512\n\t"
      "v_mfma_f32_16x16x32_bf16 v[104:107], v[172:175], v[160:163], v[104:107]\n\t"
      "ds_read_b128 v[76:79], v197 offset:512\n\t"
      "v_mfma_f32_16x16x32_bf16 v[108:111], v[176:179], v[160:163], v[108:111]\n\t"
      "v_mfma_f32_16x16x32_bf16 v[112:115], v[180:183], v[160:163], v[112:115]\n\t"
      "v_mfma_f32_16x16x32_bf16 v[116:119], v[184:187], v[160:163], v[116:119]\n\t"
      "v_mfma_f32_16x16x32_bf16 v[120:123], v[188:191], v[160:163], v[120:123]\n\t"
      "v_mfma_f32_16x16x32_bf16 v[132:135], v[192:195], v[160:163], v[132:135]\n\t"
      "s_waitcnt lgkmcnt(8)\n\t"
      "v_mfma_f32_16x16x32_bf16 v[128:131], a[0:3], v[32:35], v[8:11]\n\t"
      "v_cvt_pk_bf16_f32 v80, v80, v81\n\t"
      "v_cvt_pk_bf16_f32 v81, v82, v83\n\t"
      "v_mfma_f32_16x16x32_bf16 v[128:131], a[24:27], v[36:39], v[128:131]\n\t"
      "v_cvt_pk_bf16_f32 v136, v120, v121\n\t"
      "v_cvt_pk_bf16_f32 v137, v122, v123\n\t"
      "v_mfma_f32_16x16x32_bf16 v[128:131], a[48:51], v[40:43], v[128:131]\n\t"
      "v_cvt_pk_bf16_f32 v138, v132, v133\n\t"
      "v_mfma_f32_16x16x32_bf16 v[128:131], a[72:75], v[44:47], v[128:131]\n\t"
      "v_cvt_pk_bf16_f32 v139, v134, v135\n\t"
      "s_waitcnt lgkmcnt(4)\n\t"
      "v_mfma_f32_16x16x32_bf16 v[128:131], a[96:99], v[48:51], v[128:131]\n\t"
      "ds_write_b128 v219, v[136:139] offset:12288\n\t"
      "v_mfma_f32_16x16x32_bf16 v[128:131], a[120:123], v[52:55], v[128:131]\n\t"
      "v_cvt_pk_bf16_f32 v82, v84, v85\n\t"
      "v_cvt_pk_bf16_f32 v83, v86, v87\n\t"
      "v_mfma_f32_16x16x32_bf16 v[128:131], a[144:147], v[56:59], v[128:131]\n\t"
      "v_cvt_pk_bf16_f32 v88, v88, v89\n\t"
      "v_cvt_pk_bf16_f32 v89, v90, v91\n\t"
      "v_mfma_f32_16x16x32_bf16 v[128:131], a[168:171], v[60:63], v[128:131]\n\t"
      "ds_write_b128 v217, v[80:83] offset:0\n\t"
      "v_cvt_pk_bf16_f32 v90, v92, v93\n\t"
      "s_waitcnt lgkmcnt(2)\n\t"
      "v_mfma_f32_16x16x32_bf16 v[128:131], a[192:195], v[64:67], v[128:131]\n\t"
      "v_cvt_pk_bf16_f32 v91, v94, v95\n\t"
      "v_cvt_pk_bf16_f32 v96, v96, v97\n\t"
      "v_mfma_f32_16x16x32_bf16 v[128:131], a[216:219], v[68:71], v[128:131]\n\t"
      "ds_write_b128 v218, v[88:91] offset:0\n\t"
      "v_cvt_pk_bf16_f32 v97, v98, v99\n\t"
      "v_mfma_f32_16x16x32_bf16 v[128:131], a[240:243], v[72:75], v[128:131]\n\t"
      "v_cvt_pk_bf16_f32 v98, v100, v101\n\t"
      "v_cvt_pk_bf16_f32 v99, v102, v103\n\t"
      "v_mfma_f32_16x16x32_bf16 v[128:131], v[172:175], v[76:79], v[128:131]\n\t"
      "v_cvt_pk_bf16_f32 v104, v104, v105\n\t"
      "v_cvt_pk_bf16_f32 v105, v106, v107\n\t"
      "v_mfma_f32_16x16x32_bf16 v[132:135], a[4:7], v[32:35], v[12:15]\n\t"
      "ds_write_b128 v219, v[96:99] offset:0\n\t"
      "v_cvt_pk_bf16_f32 v106, v108, v109\n\t"
      "v_mfma_f32_16x16x32_bf16 v[132:135], a[28:31], v[36:39], v[132:135]\n\t"
      "v_cvt_pk_bf16_f32 v107, v110, v111\n\t"
      "v_cvt_pk_bf16_f32 v112, v112, v113\n\t"
      "v_mfma_f32_16x16x32_bf16 v[132:135], a[52:55], v[40:43], v[132:135]\n\t"
      "v_cvt_pk_bf16_f32 v136, v128, v129\n\t"
      "v_mfma_f32_16x16x32_bf16 v[132:135], a[76:79], v[44:47], v[132:135]\n\t"
      "v_cvt_pk_bf16_f32 v137, v130, v131\n\t"
      "v_mfma_f32_16x16x32_bf16 v[132:135], a[100:103], v[48:51], v[132:135]\n\t"
      "ds_write_b128 v217, v[104:107] offset:12288\n\t"
      "v_cvt_pk_bf16_f32 v113, v114, v115\n\t"
      "s_add_u32 s48, s47, 0x6000\n\t"
      "v_mfma_f32_16x16x32_bf16 v[132:135], a[124:127], v[52:55], v[132:135]\n\t"
      "v_cvt_pk_bf16_f32 v114, v116, v117\n\t"
      "v_cvt_pk_bf16_f32 v115, v118, v119\n\t"
      "s_cmp_eq_u32 s48, s50\n\t"
      "s_cselect_b32 s48, s41, s48\n\t"
      "v_mfma_f32_16x16x32_bf16 v[132:135], a[148:151], v[56:59], v[132:135]\n\t"
      "ds_write_b128 v218, v[112:115] offset:12288\n\t"
      "v_mfma_f32_16x16x32_bf16 v[132:135], a[172:175], v[60:63], v[132:135]\n\t"
      "v_add_u32_e32 v217, s46, v204\n\t"
      "v_mfma_f32_16x16x32_bf16 v[132:135], a[196:199], v[64:67], v[132:135]\n\t"
      "v_add_u32_e32 v218, s46, v205\n\t"
      "v_mfma_f32_16x16x32_bf16 v[132:135], a[220:223], v[68:71], v[132:135]\n\t"
      "v_readlane_b32 s73, v248, 3\n\t"
      "v_add_u32_e32 v219, s46, v206\n\t"
      "v_mfma_f32_16x16x32_bf16 v[132:135], a[244:247], v[72:75], v[132:135]\n\t"
      "v_mfma_f32_16x16x32_bf16 v[132:135], v[176:179], v[76:79], v[132:135]\n\t"
      "s_waitcnt lgkmcnt(0)\n\t"
      "s_barrier\n\t"
      "v_mfma_f32_16x16x32_bf16 v[128:131], a[8:11], v[32:35], v[16:19]\n\t"
      "ds_read_b128 v[140:143], v220 offset:0\n\t"
      "v_mfma_f32_16x16x32_bf16 v[128:131], a[32:35], v[36:39], v[128:131]\n\t"
      "ds_read_b128 v[144:147], v220 offset:1024\n\t"
      "s_memrealtime s[80:81]\n\t"
      "v_mfma_f32_16x16x32_bf16 v[128:131], a[56:59], v[40:43], v[128:131]\n\t"
      "v_cvt_pk_bf16_f32 v138, v132, v133\n\t"
      "s_mov_b32 m0, s45\n\t"
      "v_mfma_f32_16x16x32_bf16 v[128:131], a[80:83], v[44:47], v[128:131]\n\t"
      "v_cvt_pk_bf16_f32 v139, v134, v135\n\t"
      "v_mfma_f32_16x16x32_bf16 v[128:131], a[104:107], v[48:51], v[128:131]\n\t"
      "ds_write_b128 v217, v[136:139] offset:0\n\t"
      "v_mfma_f32_16x16x32_bf16 v[128:131], a[128:131], v[52:55], v[128:131]\n\t"
      "s_waitcnt lgkmcnt(2)\n\t"
      "buffer_store_dwordx4 v[140:143], v208, s[28:31], s40 offen sc1\n\t"
      "v_mfma_f32_16x16x32_bf16 v[128:131], a[152:155], v[56:59], v[128:131]\n\t"
      "ds_read_b128 v[148:151], v220 offset:2048\n\t"
      "v_mfma_f32_16x16x32_bf16 v[128:131], a[176:179], v[60:63], v[128:131]\n\t"
      "ds_read_b128 v[152:155], v220 offset:12288\n\t"
      "v_mfma_f32_16x16x32_bf16 v[128:131], a[200:203], v[64:67], v[128:131]\n\t"
      "ds_read_b128 v[156:159], v220 offset:13312\n\t"
      "v_mfma_f32_16x16x32_bf16 v[128:131], a[224:227], v[68:71], v[128:131]\n\t"
      "buffer_load_dwordx4 v208, s[20:23], s73 offen lds\n\t"
      "v_mfma_f32_16x16x32_bf16 v[128:131], a[248:251], v[72:75], v[128:131]\n\t"
      "ds_read_b128 v[160:163], v220 offset:14336\n\t"
      "v_mfma_f32_16x16x32_bf16 v[128:131], v[180:183], v[76:79], v[128:131]\n\t"
      "v_mfma_f32_16x16x32_bf16 v[132:135], a[12:15], v[32:35], v[20:23]\n\t"
      "v_mfma_f32_16x16x32_bf16 v[132:135], a[36:39], v[36:39], v[132:135]\n\t"
      "s_waitcnt lgkmcnt(5)\n\t"
      "buffer_store_dwordx4 v[144:147], v208, s[28:31], s40 offen offset:1024 sc1\n\t"
      "v_mfma_f32_16x16x32_bf16 v[132:135], a[60:63], v[40:43], v[132:135]\n\t"
      "v_cvt_pk_bf16_f32 v136, v128, v129\n\t"
      "v_mfma_f32_16x16x32_bf16 v[132:135], a[84:87], v[44:47], v[132:135]\n\t"
      "v_cvt_pk_bf16_f32 v137, v130, v131\n\t"
      "v_mfma_f32_16x16x32_bf16 v[132:135], a[108:111], v[48:51], v[132:135]\n\t"
      "v_mov_b32_e32 v6, 0\n\t"
      "v_mfma_f32_16x16x32_bf16 v[132:135], a[132:135], v[52:55], v[132:135]\n\t"
      "buffer_load_dwordx4 v208, s[20:23], s73 offen offset:1024 lds\n\t"
      "v_mfma_f32_16x16x32_bf16 v[132:135], a[156:159], v[56:59], v[132:135]\n\t"
      "ds_write_b32 v238, v6\n\t"
      "v_mfma_f32_16x16x32_bf16 v[132:135], a[180:183], v[60:63], v[132:135]\n\t"
      "v_mfma_f32_16x16x32_bf16 v[132:135], a[204:207], v[64:67], v[132:135]\n\t"
      "v_mfma_f32_16x16x32_bf16 v[132:135], a[228:231], v[68:71], v[132:135]\n\t"
      "s_waitcnt lgkmcnt(4)\n\t"
      "buffer_store_dwordx4 v[148:151], v208, s[28:31], s40 offen offset:2048 sc1\n\t"
      "v_mfma_f32_16x16x32_bf16 v[132:135], a[252:255], v[72:75], v[132:135]\n\t"
      "v_mfma_f32_16x16x32_bf16 v[132:135], v[184:187], v[76:79], v[132:135]\n\t"
      "s_waitcnt vmcnt(13)\n\t"
      "s_barrier\n\t"
      "v_mfma_f32_16x16x32_bf16 v[128:131], a[16:19], v[32:35], v[24:27]\n\t"
      "ds_read_b128 v[80:83], v196 offset:12288\n\t"
      "v_mfma_f32_16x16x32_bf16 v[128:131], a[40:43], v[36:39], v[128:131]\n\t"
      "buffer_load_dwordx4 v208, s[20:23], s73 offen offset:2048 lds\n\t"
      "v_mfma_f32_16x16x32_bf16 v[128:131], a[64:67], v[40:43], v[128:131]\n\t"
      "ds_read_b128 v[84:87], v197 offset:12288\n\t"
      "v_cvt_pk_bf16_f32 v138, v132, v133\n\t"
      "s_add_u32 m0, s45, 0x3000\n\t"
      "v_mfma_f32_16x16x32_bf16 v[128:131], a[88:91], v[44:47], v[128:131]\n\t"
      "ds_read_b128 v[88:91], v198 offset:12288\n\t"
      "v_cvt_pk_bf16_f32 v139, v134, v135\n\t"
      "v_mfma_f32_16x16x32_bf16 v[128:131], a[112:115], v[48:51], v[128:131]\n\t"
      "ds_write_b128 v218, v[136:139] offset:0\n\t"
      "v_mfma_f32_16x16x32_bf16 v[128:131], a[136:139], v[52:55], v[128:131]\n\t"
      "s_waitcnt lgkmcnt(7)\n\t"
      "buffer_store_dwordx4 v[152:155], v209, s[28:31], s40 offen sc1\n\t"
      "s_waitcnt vmcnt(7)\n\t"
      "v_subrev_u32_e32 v242, s67, v240\n\t"
      "v_xor_b32_e32 v241, s63, v241\n\t"
      "v_mfma_f32_16x16x32_bf16 v[128:131], a[160:163], v[56:59], v[128:131]\n\t"
      "ds_read_b128 v[92:95], v199 offset:12288\n\t"
      "v_lshl_or_b32 v240, v242, 20, v241\n\t"
      "v_mfma_f32_16x16x32_bf16 v[128:131], a[184:187], v[60:63], v[128:131]\n\t"
      "ds_read_b128 v[96:99], v197 offset:12544\n\t"
      "v_mfma_f32_16x16x32_bf16 v[128:131], a[208:211], v[64:67], v[128:131]\n\t"
      "v_lshrrev_b32_e32 v242, 12, v242\n\t"
      "v_cmp_ne_u32_e32 vcc, 0, v240\n\t"
      "v_mfma_f32_16x16x32_bf16 v[128:131], a[232:235], v[68:71], v[128:131]\n\t"
      "buffer_load_dwordx4 v209, s[20:23], s73 offen lds\n\t"
      "v_mfma_f32_16x16x32_bf16 v[128:131], v[164:167], v[72:75], v[128:131]\n\t"
      "ds_read_b128 v[100:103], v198 offset:12544\n\t"
      "s_cbranch_vccnz .Lgs_%=\n\t"
      ".Lgr_%=:\n\t"
      "ds_write_b32 v246, v242\n\t"
      "v_mfma_f32_16x16x32_bf16 v[128:131], v[188:191], v[76:79], v[128:131]\n\t"
      "ds_read_b128 v[104:107], v199 offset:12544\n\t"
      "v_mfma_f32_16x16x32_bf16 v[132:135], a[20:23], v[32:35], v[28:31]\n\t"
      "ds_read_b128 v[108:111], v196 offset:12544\n\t"
      "v_mfma_f32_16x16x32_bf16 v[132:135], a[44:47], v[36:39], v[132:135]\n\t"
      "s_waitcnt lgkmcnt(12)\n\t"
      "buffer_store_dwordx4 v[156:159], v209, s[28:31], s40 offen offset:1024 sc1\n\t"
      "v_mfma_f32_16x16x32_bf16 v[132:135], a[68:71], v[40:43], v[132:135]\n\t"
      "ds_read_b128 v[112:115], v198 offset:12800\n\t"
      "v_cvt_pk_bf16_f32 v136, v128, v129\n\t"
      "v_add_u32_e32 v220, s46, v207\n\t"
      "v_mfma_f32_16x16x32_bf16 v[132:135], a[92:95], v[44:47], v[132:135]\n\t"
      "ds_read_b128 v[116:119], v199 offset:12800\n\t"
      "v_cvt_pk_bf16_f32 v137, v130, v131\n\t"
      "v_mfma_f32_16x16x32_bf16 v[132:135], a[116:119], v[48:51], v[132:135]\n\t"
      "s_sub_u32 s87, s80, s52\n\t"
      "v_mfma_f32_16x16x32_bf16 v[132:135], a[140:143], v[52:55], v[132:135]\n\t"
      "buffer_load_dwordx4 v209, s[20:23], s73 offen offset:1024 lds\n\t"
      "v_mfma_f32_16x16x32_bf16 v[132:135], a[164:167], v[56:59], v[132:135]\n\t"
      "ds_read_b128 v[120:123], v196 offset:12800\n\t"
      "v_mfma_f32_16x16x32_bf16 v[132:135], a[188:191], v[60:63], v[132:135]\n\t"
      "ds_read_b128 v[124:127], v197 offset:12800\n\t"
      "v_mfma_f32_16x16x32_bf16 v[132:135], a[212:215], v[64:67], v[132:135]\n\t"
      "s_add_u32 s44, s44, 1\n\t"
      "v_mfma_f32_16x16x32_bf16 v[132:135], a[236:239], v[68:71], v[132:135]\n\t"
      "s_waitcnt lgkmcnt(15)\n\t"
      "buffer_store_dwordx4 v[160:163], v209, s[28:31], s40 offen offset:2048 sc1\n\t"
      "v_mfma_f32_16x16x32_bf16 v[132:135], v[168:171], v[72:75], v[132:135]\n\t"
      "s_add_u32 s85, s85, 1\n\t"
      "s_add_u32 s82, s87, s84\n\t"
      "v_mfma_f32_16x16x32_bf16 v[132:135], v[192:195], v[76:79], v[132:135]\n\t"
      "s_waitcnt lgkmcnt(9)\n\t"
      "v_mfma_f32_16x16x32_bf16 v[128:131], a[0:3], v[80:83], v[8:11]\n\t"
      "v_mfma_f32_16x16x32_bf16 v[128:131], a[24:27], v[84:87], v[128:131]\n\t"
      "v_readlane_b32 s72, v248, s44\n\t"
      "v_mfma_f32_16x16x32_bf16 v[128:131], a[48:51], v[88:91], v[128:131]\n\t"
      "v_cvt_pk_bf16_f32 v138, v132, v133\n\t"
      "v_mfma_f32_16x16x32_bf16 v[128:131], a[72:75], v[92:95], v[128:131]\n\t"
      "v_cvt_pk_bf16_f32 v139, v134, v135\n\t"
      "s_waitcnt lgkmcnt(4)\n\t"
      "v_mfma_f32_16x16x32_bf16 v[128:131], a[96:99], v[96:99], v[128:131]\n\t"
      "ds_write_b128 v219, v[136:139] offset:0\n\t"
      "v_mfma_f32_16x16x32_bf16 v[128:131], a[120:123], v[100:103], v[128:131]\n\t"
      "v_mfma_f32_16x16x32_bf16 v[128:131], a[144:147], v[104:107], v[128:131]\n\t"
      "buffer_load_dwordx4 v209, s[20:23], s73 offen offset:2048 lds\n\t"
      "v_mfma_f32_16x16x32_bf16 v[128:131], a[168:171], v[108:111], v[128:131]\n\t"
      "v_add_u32_e32 v196, s47, v200\n\t"
      "s_waitcnt lgkmcnt(1)\n\t"
      "v_mfma_f32_16x16x32_bf16 v[128:131], a[192:195], v[112:115], v[128:131]\n\t"
      "v_add_u32_e32 v197, s47, v201\n\t"
      "v_mfma_f32_16x16x32_bf16 v[128:131], a[216:219], v[116:119], v[128:131]\n\t"
      "v_add_u32_e32 v198, s47, v202\n\t"
      "v_mfma_f32_16x16x32_bf16 v[128:131], a[240:243], v[120:123], v[128:131]\n\t"
      "v_add_u32_e32 v199, s47, v203\n\t"
      "v_mfma_f32_16x16x32_bf16 v[128:131], v[172:175], v[124:127], v[128:131]\n\t"
      "s_waitcnt vmcnt(17) lgkmcnt(0)\n\t"
      "s_barrier\n\t"
      "v_mfma_f32_16x16x32_bf16 v[132:135], a[4:7], v[80:83], v[12:15]\n\t"
      "ds_read_b128 v[140:143], v220 offset:0\n\t"
      "v_mfma_f32_16x16x32_bf16 v[132:135], a[28:31], v[84:87], v[132:135]\n\t"
      "ds_read_b128 v[240:243], v247 offset:0\n\t"
      "v_mfma_f32_16x16x32_bf16 v[132:135], a[52:55], v[88:91], v[132:135]\n\t"
      "v_cvt_pk_bf16_f32 v136, v128, v129\n\t"
      "v_mfma_f32_16x16x32_bf16 v[132:135], a[76:79], v[92:95], v[132:135]\n\t"
      "ds_read_b128 v[32:35], v196 offset:0\n\t"
      "v_cvt_pk_bf16_f32 v137, v130, v131\n\t"
      "v_mfma_f32_16x16x32_bf16 v[132:135], a[100:103], v[96:99], v[132:135]\n\t"
      "v_mfma_f32_16x16x32_bf16 v[132:135], a[124:127], v[100:103], v[132:135]\n\t"
      "v_mfma_f32_16x16x32_bf16 v[132:135], a[148:151], v[104:107], v[132:135]\n\t"
      "ds_read_b128 v[36:39], v197 offset:0\n\t"
      "v_mfma_f32_16x16x32_bf16 v[132:135], a[172:175], v[108:111], v[132:135]\n\t"
      "v_mfma_f32_16x16x32_bf16 v[132:135], a[196:199], v[112:115], v[132:135]\n\t"
      "s_waitcnt lgkmcnt(2)\n\t"
      "v_add3_u32 v240, v240, v241, v242\n\t"
      "v_mfma_f32_16x16x32_bf16 v[132:135], a[220:223], v[116:119], v[132:135]\n\t"
      "ds_read_b128 v[40:43], v198 offset:0\n\t"
      "v_mfma_f32_16x16x32_bf16 v[132:135], a[244:247], v[120:123], v[132:135]\n\t"
      "v_add_u32_e32 v240, v240, v243\n\t"
      "v_mfma_f32_16x16x32_bf16 v[132:135], v[176:179], v[124:127], v[132:135]\n\t"
      "buffer_store_dwordx4 v[140:143], v208, s[28:31], s72 offen sc1\n\t"
      "v_mfma_f32_16x16x32_bf16 v[128:131], a[8:11], v[80:83], v[16:19]\n\t"
      "ds_read_b128 v[44:47], v199 offset:0\n\t"
      "v_mfma_f32_16x16x32_bf16 v[128:131], a[32:35], v[84:87], v[128:131]\n\t"
      "v_add_u32_dpp v241, v240, v240 quad_perm:[1,0,3,2] row_mask:0xf bank_mask:0xf\n\t"
      "s_memrealtime s[80:81]\n\t"
      "v_mfma_f32_16x16x32_bf16 v[128:131], a[56:59], v[88:91], v[128:131]\n\t"
      "v_cvt_pk_bf16_f32 v138, v132, v133\n\t"
      "v_mfma_f32_16x16x32_bf16 v[128:131], a[80:83], v[92:95], v[128:131]\n\t"
      "v_cvt_pk_bf16_f32 v139, v134, v135\n\t"
      "v_mfma_f32_16x16x32_bf16 v[128:131], a[104:107], v[96:99], v[128:131]\n\t"
      "ds_write_b128 v217, v[136:139] offset:12288\n\t"
      "v_add_u32_dpp v242, v241, v241 quad_perm:[2,3,0,1] row_mask:0xf bank_mask:0xf\n\t"
      "v_mfma_f32_16x16x32_bf16 v[128:131], a[128:131], v[100:103], v[128:131]\n\t"
      "s_mov_b32 m0, s46\n\t"
      "v_and_b32_e32 v240, 0xff, v242\n\t"
      "v_mfma_f32_16x16x32_bf16 v[128:131], a[152:155], v[104:107], v[128:131]\n\t"
      "v_cmp_le_u32_e64 s[76:77], 19, v240\n\t"
      "v_cmp_ge_u32_e64 s[78:79], 13, v240\n\t"
      "v_mfma_f32_16x16x32_bf16 v[128:131], a[176:179], v[108:111], v[128:131]\n\t"
      "ds_read_b128 v[48:51], v197 offset:256\n\t"
      "v_readlane_b32 s70, v242, 0\n\t"
      "v_mfma_f32_16x16x32_bf16 v[128:131], a[200:203], v[112:115], v[128:131]\n\t"
      "v_cmp_lt_u32_e32 vcc, 32, v240\n\t"
      "s_mov_b32 s66, s76\n\t"
      "v_mfma_f32_16x16x32_bf16 v[128:131], a[224:227], v[116:119], v[128:131]\n\t"
      "ds_read_b128 v[52:55], v198 offset:256\n\t"
      "v_mov_b32_e32 v242, s62\n\t"
      "v_mfma_f32_16x16x32_bf16 v[128:131], a[248:251], v[120:123], v[128:131]\n\t"
      "s_and_b32 s65, s76, s77\n\t"
      "s_and_b32 s78, s78, s79\n\t"
      "v_mfma_f32_16x16x32_bf16 v[128:131], v[180:183], v[124:127], v[128:131]\n\t"
      "v_mov_b32_e32 v243, s63\n\t"
      "s_or_b32 s65, s65, s78\n\t"
      "v_mfma_f32_16x16x32_bf16 v[132:135], a[12:15], v[80:83], v[20:23]\n\t"
      "ds_read_b128 v[56:59], v199 offset:256\n\t"
      "s_cmp_lg_u64 vcc, 0\n\t"
      "s_cselect_b32 s65, 0, s65\n\t"
      "s_cselect_b32 s64, 0xfffc0000, 0\n\t"
      "s_cselect_b32 s49, 31, 17\n\t"
      "v_mfma_f32_16x16x32_bf16 v[132:135], a[36:39], v[84:87], v[132:135]\n\t"
      "v_and_b32_e32 v240, s66, v250\n\t"
      "v_and_b32_e32 v241, s65, v250\n\t"
      "v_mfma_f32_16x16x32_bf16 v[132:135], a[60:63], v[88:91], v[132:135]\n\t"
      "v_cvt_pk_bf16_f32 v136, v128, v129\n\t"
      "s_and_b32 s67, s64, 0x40000000\n\t"
      "v_mfma_f32_16x16x32_bf16 v[132:135], a[84:87], v[92:95], v[132:135]\n\t"
      "v_cvt_pk_bf16_f32 v137, v130, v131\n\t"
      "ds_read_b128 v[144:147], v220 offset:1024\n\t"
      "v_mfma_f32_16x16x32_bf16 v[132:135], a[108:111], v[96:99], v[132:135]\n\t"
      "v_cmp_ne_u32_e64 s[76:77], 0, v240\n\t"
      "v_cmp_ne_u32_e64 s[78:79], 0, v241\n\t"
      "v_mfma_f32_16x16x32_bf16 v[132:135], a[132:135], v[100:103], v[132:135]\n\t"
      "s_or_b32 s51, s67, s89\n\t"
      "buffer_store_dwordx2 v[242:243], v210, s[24:27], s51 offen sc1\n\t"
      "v_mfma_f32_16x16x32_bf16 v[132:135], a[156:159], v[104:107], v[132:135]\n\t"
      "ds_read_b128 v[60:63], v196 offset:256\n\t"
      "s_lshr_b32 s70, s70, 10\n\t"
      "v_mfma_f32_16x16x32_bf16 v[132:135], a[180:183], v[108:111], v[132:135]\n\t"
      "s_xor_b32 s75, s75, s76\n\t"
      "s_xor_b32 s74, s74, s76\n\t"
      "v_mfma_f32_16x16x32_bf16 v[132:135], a[204:207], v[112:115], v[132:135]\n\t"
      "s_and_b32 s76, s75, s78\n\t"
      "s_or_b32 s76, s76, s64\n\t"
      "v_cndmask_b32_e64 v249, v248, v251, s[76:77]\n\t"
      "v_mfma_f32_16x16x32_bf16 v[132:135], a[228:231], v[116:119], v[132:135]\n\t"
      "ds_read_b128 v[64:67], v198 offset:512\n\t"
      "s_add_u32 s70, s70, s68\n\t"
      "v_mfma_f32_16x16x32_bf16 v[132:135], a[252:255], v[120:123], v[132:135]\n\t"
      "s_and_b32 s76, s74, s78\n\t"
      "s_or_b32 s76, s76, s64\n\t"
      "v_cndmask_b32_e64 v248, v248, v251, s[76:77]\n\t"
      "v_mfma_f32_16x16x32_bf16 v[132:135], v[184:187], v[124:127], v[132:135]\n\t"
      "s_waitcnt lgkmcnt(2)\n\t"
      "buffer_store_dwordx4 v[144:147], v208, s[28:31], s72 offen offset:1024 sc1\n\t"
      "v_mfma_f32_16x16x32_bf16 v[128:131], a[16:19], v[80:83], v[24:27]\n\t"
      "ds_read_b128 v[68:71], v199 offset:512\n\t"
      "v_mfma_f32_16x16x32_bf16 v[128:131], a[40:43], v[84:87], v[128:131]\n\t"
      "s_lshr_b32 s74, s78, 3\n\t"
      "s_or_b32 s76, s74, s78\n\t"
      "v_mfma_f32_16x16x32_bf16 v[128:131], a[64:67], v[88:91], v[128:131]\n\t"
      "v_cvt_pk_bf16_f32 v138, v132, v133\n\t"
      "v_readlane_b32 s73, v248, s85\n\t"
      "v_mfma_f32_16x16x32_bf16 v[128:131], a[88:91], v[92:95], v[128:131]\n\t"
      "v_cvt_pk_bf16_f32 v139, v134, v135\n\t"
      "s_sub_u32 s87, s80, s52\n\t"
      "v_mfma_f32_16x16x32_bf16 v[128:131], a[112:115], v[96:99], v[128:131]\n\t"
      "ds_write_b128 v218, v[136:139] offset:12288\n\t"
      "s_sub_u32 s83, s82, s87\n\t"
      "v_mfma_f32_16x16x32_bf16 v[128:131], a[136:139], v[100:103], v[128:131]\n\t"
      "v_cndmask_b32_e64 v252, v252, v253, s[76:77]\n\t"
      "s_and_b32 s78, s74, s78\n\t"
      "v_mfma_f32_16x16x32_bf16 v[128:131], a[160:163], v[104:107], v[128:131]\n\t"
      "v_cndmask_b32_e64 v252, v252, v254, s[78:79]\n\t"
      "v_mfma_f32_16x16x32_bf16 v[128:131], a[184:187], v[108:111], v[128:131]\n\t"
      "ds_read_b128 v[148:151], v220 offset:2048\n\t"
      "v_mfma_f32_16x16x32_bf16 v[128:131], a[208:211], v[112:115], v[128:131]\n\t"
      "buffer_load_dwordx4 v208, s[20:23], s73 offen lds\n\t"
      "v_mfma_f32_16x16x32_bf16 v[128:131], a[232:235], v[116:119], v[128:131]\n\t"
      "ds_read_b128 v[72:75], v196 offset:512\n\t"
      "s_max_i32 s82, s82, s87\n\t"
      "v_mfma_f32_16x16x32_bf16 v[128:131], v[164:167], v[120:123], v[128:131]\n\t"
      "v_readlane_b32 s71, v252, s44\n\t"
      "v_mfma_f32_16x16x32_bf16 v[128:131], v[188:191], v[124:127], v[128:131]\n\t"
      "s_sub_u32 s84, s84, 4\n\t"
      "v_mfma_f32_16x16x32_bf16 v[132:135], a[20:23], v[80:83], v[28:31]\n\t"
      "ds_read_b128 v[76:79], v197 offset:512\n\t"
      "v_mfma_f32_16x16x32_bf16 v[132:135], a[44:47], v[84:87], v[132:135]\n\t"
      "buffer_load_dwordx4 v208, s[20:23], s73 offen offset:1024 lds\n\t"
      "v_mfma_f32_16x16x32_bf16 v[132:135], a[68:71], v[88:91], v[132:135]\n\t"
      "v_cvt_pk_bf16_f32 v136, v128, v129\n\t"
      "v_mfma_f32_16x16x32_bf16 v[132:135], a[92:95], v[92:95], v[132:135]\n\t"
      "v_cvt_pk_bf16_f32 v137, v130, v131\n\t"
      "s_lshr_b32 s60, s71, 16\n\t"
      "v_mfma_f32_16x16x32_bf16 v[132:135], a[116:119], v[96:99], v[132:135]\n\t"
      "s_and_b32 s86, s71, 0xff\n\t"
      "v_mfma_f32_16x16x32_bf16 v[132:135], a[140:143], v[100:103], v[132:135]\n\t"
      "s_bfe_u32 s71, s71, 0x80008\n\t"
      "v_mfma_f32_16x16x32_bf16 v[132:135], a[164:167], v[104:107], v[132:135]\n\t"
      "s_max_i32 s84, s84, s86\n\t"
      "s_mov_b32 s45, s46\n\t"
      "v_mfma_f32_16x16x32_bf16 v[132:135], a[188:191], v[108:111], v[132:135]\n\t"
      "s_add_u32 s70, s70, -1443\n\t"
      "s_mov_b32 s46, s47\n\t"
      "v_mfma_f32_16x16x32_bf16 v[132:135], a[212:215], v[112:115], v[132:135]\n\t"
      "s_add_u32 s82, s82, s84\n\t"
      "s_mov_b32 s47, s48\n\t"
      "v_mfma_f32_16x16x32_bf16 v[132:135], a[236:239], v[116:119], v[132:135]\n\t"
      "ds_write_b64 v219, v[136:137] offset:12288\n\t"
      "v_mfma_f32_16x16x32_bf16 v[132:135], v[168:171], v[120:123], v[132:135]\n\t"
      "v_add_u32_e32 v6, 1, v6\n\t"
      "v_mfma_f32_16x16x32_bf16 v[132:135], v[192:195], v[124:127], v[132:135]\n\t"
      "s_waitcnt lgkmcnt(3)\n\t"
      "buffer_store_dwordx4 v[148:151], v208, s[28:31], s72 offen offset:2048 sc1\n\t"
      ".p2align 6\n\t"
      ".Lloop_%=:\n\t"
      "v_mfma_f32_16x16x32_bf16 v[128:131], a[0:3], v[32:35], v[8:11]\n\t"
      "s_waitcnt vmcnt(17)\n\t"
      "v_mfma_f32_16x16x32_bf16 v[128:131], a[24:27], v[36:39], v[128:131]\n\t"
      "v_readlane_b32 s72, v249, s44\n\t"
      "v_mfma_f32_16x16x32_bf16 v[128:131], a[48:51], v[40:43], v[128:131]\n\t"
      "v_cvt_pk_bf16_f32 v138, v132, v133\n\t"
      "v_cvt_pk_bf16_f32 v139, v134, v135\n\t"
      "v_mfma_f32_16x16x32_bf16 v[128:131], a[72:75], v[44:47], v[128:131]\n\t"
      "ds_write_b32 v219, v138 offset:12296\n\t"
      "v_mfma_f32_16x16x32_bf16 v[128:131], a[96:99], v[48:51], v[128:131]\n\t"
      "ds_write_b32 v219, v139 offset:12300\n\t"
      "v_mfma_f32_16x16x32_bf16 v[128:131], a[120:123], v[52:55], v[128:131]\n\t"
      "ds_write_b32 v238, v6\n\t"
      "v_mfma_f32_16x16x32_bf16 v[128:131], a[144:147], v[56:59], v[128:131]\n\t"
      "buffer_load_dwordx4 v208, s[20:23], s73 offen offset:2048 lds\n\t"
      "v_mfma_f32_16x16x32_bf16 v[128:131], a[168:171], v[60:63], v[128:131]\n\t"
      "s_waitcnt lgkmcnt(4)\n\t"
      "v_add_u32_e32 v217, s46, v204\n\t"
      "v_mfma_f32_16x16x32_bf16 v[128:131], a[192:195], v[64:67], v[128:131]\n\t"
      "v_add_u32_e32 v218, s46, v205\n\t"
      "v_mfma_f32_16x16x32_bf16 v[128:131], a[216:219], v[68:71], v[128:131]\n\t"
      "v_add_u32_e32 v219, s46, v206\n\t"
      "v_mfma_f32_16x16x32_bf16 v[128:131], a[240:243], v[72:75], v[128:131]\n\t"
      "v_mfma_f32_16x16x32_bf16 v[128:131], v[172:175], v[76:79], v[128:131]\n\t"
      "s_cmp_gt_i32 s83, 1\n\t"
      "s_cbranch_scc1 .LslpA_%=\n\t"
      ".LretA_%=:\n\t"
      "v_mfma_f32_16x16x32_bf16 v[132:135], a[4:7], v[32:35], v[12:15]\n\t"
      "ds_read_b32 v7, v239\n\t"
      "v_mfma_f32_16x16x32_bf16 v[132:135], a[28:31], v[36:39], v[132:135]\n\t"
      "v_mfma_f32_16x16x32_bf16 v[132:135], a[52:55], v[40:43], v[132:135]\n\t"
      "v_cvt_pk_bf16_f32 v136, v128, v129\n\t"
      "v_cvt_pk_bf16_f32 v137, v130, v131\n\t"
      "v_mfma_f32_16x16x32_bf16 v[132:135], a[76:79], v[44:47], v[132:135]\n\t"
      "v_mfma_f32_16x16x32_bf16 v[132:135], a[100:103], v[48:51], v[132:135]\n\t"
      "s_waitcnt lgkmcnt(0)\n\t"
      "v_cmp_gt_u32_e32 vcc, v6, v7\n\t"
      "v_mfma_f32_16x16x32_bf16 v[132:135], a[124:127], v[52:55], v[132:135]\n\t"
      "s_cbranch_vccnz .LsbA_%=\n\t"
      ".LsrA_%=:\n\t"
      "v_mfma_f32_16x16x32_bf16 v[132:135], a[148:151], v[56:59], v[132:135]\n\t"
      "ds_read_b128 v[140:143], v220 offset:12288\n\t"
      "v_mfma_f32_16x16x32_bf16 v[132:135], a[172:175], v[60:63], v[132:135]\n\t"
      "ds_write_b32 v217, v136 offset:0\n\t"
      "v_mfma_f32_16x16x32_bf16 v[132:135], a[196:199], v[64:67], v[132:135]\n\t"
      "ds_write_b32 v217, v137 offset:4\n\t"
      "v_mfma_f32_16x16x32_bf16 v[132:135], a[220:223], v[68:71], v[132:135]\n\t"
      "ds_read_b128 v[80:83], v196 offset:12288\n\t"
      "v_mfma_f32_16x16x32_bf16 v[132:135], a[244:247], v[72:75], v[132:135]\n\t"
      "v_readlane_b32 s73, v249, s85\n\t"
      "v_mfma_f32_16x16x32_bf16 v[132:135], v[176:179], v[76:79], v[132:135]\n\t"
      "s_waitcnt lgkmcnt(3)\n\t"
      "buffer_store_dwordx4 v[140:143], v209, s[28:31], s72 offen sc1\n\t"
      "v_mfma_f32_16x16x32_bf16 v[128:131], a[8:11], v[32:35], v[16:19]\n\t"
      "ds_read_b128 v[84:87], v197 offset:12288\n\t"
      "v_mfma_f32_16x16x32_bf16 v[128:131], a[32:35], v[36:39], v[128:131]\n\t"
      "s_memrealtime s[80:81]\n\t"
      "v_mfma_f32_16x16x32_bf16 v[128:131], a[56:59], v[40:43], v[128:131]\n\t"
      "v_cvt_pk_bf16_f32 v138, v132, v133\n\t"
      "v_cvt_pk_bf16_f32 v139, v134, v135\n\t"
      "v_mfma_f32_16x16x32_bf16 v[128:131], a[80:83], v[44:47], v[128:131]\n\t"
      "ds_write_b32 v217, v138 offset:8\n\t"
      "v_mfma_f32_16x16x32_bf16 v[128:131], a[104:107], v[48:51], v[128:131]\n\t"
      "ds_write_b32 v217, v139 offset:12\n\t"
      "v_mfma_f32_16x16x32_bf16 v[128:131], a[128:131], v[52:55], v[128:131]\n\t"
      "ds_read_b128 v[88:91], v198 offset:12288\n\t"
      "v_mfma_f32_16x16x32_bf16 v[128:131], a[152:155], v[56:59], v[128:131]\n\t"
      "s_add_u32 m0, s45, 0x3000\n\t"
      "v_mfma_f32_16x16x32_bf16 v[128:131], a[176:179], v[60:63], v[128:131]\n\t"
      "ds_read_b128 v[92:95], v199 offset:12288\n\t"
      "v_mfma_f32_16x16x32_bf16 v[128:131], a[200:203], v[64:67], v[128:131]\n\t"
      "s_add_u32 s48, s47, 0x6000\n\t"
      "v_mfma_f32_16x16x32_bf16 v[128:131], a[224:227], v[68:71], v[128:131]\n\t"
      "ds_read_b128 v[96:99], v197 offset:12544\n\t"
      "v_mfma_f32_16x16x32_bf16 v[128:131], a[248:251], v[72:75], v[128:131]\n\t"
      "s_cmp_eq_u32 s48, s50\n\t"
      "s_cselect_b32 s48, s41, s48\n\t"
      "v_mfma_f32_16x16x32_bf16 v[128:131], v[180:183], v[76:79], v[128:131]\n\t"
      "buffer_load_dwordx4 v209, s[20:23], s73 offen lds\n\t"
      "v_mfma_f32_16x16x32_bf16 v[132:135], a[12:15], v[32:35], v[20:23]\n\t"
      "ds_read_b128 v[100:103], v198 offset:12544\n\t"
      "v_mfma_f32_16x16x32_bf16 v[132:135], a[36:39], v[36:39], v[132:135]\n\t"
      "v_mfma_f32_16x16x32_bf16 v[132:135], a[60:63], v[40:43], v[132:135]\n\t"
      "v_cvt_pk_bf16_f32 v136, v128, v129\n\t"
      "v_cvt_pk_bf16_f32 v137, v130, v131\n\t"
      "v_mfma_f32_16x16x32_bf16 v[132:135], a[84:87], v[44:47], v[132:135]\n\t"
      "ds_write_b32 v218, v136 offset:0\n\t"
      "v_mfma_f32_16x16x32_bf16 v[132:135], a[108:111], v[48:51], v[132:135]\n\t"
      "ds_write_b32 v218, v137 offset:4\n\t"
      "v_mfma_f32_16x16x32_bf16 v[132:135], a[132:135], v[52:55], v[132:135]\n\t"
      "ds_read_b128 v[144:147], v220 offset:13312\n\t"
      "v_mfma_f32_16x16x32_bf16 v[132:135], a[156:159], v[56:59], v[132:135]\n\t"
      "ds_read_b128 v[104:107], v199 offset:12544\n\t"
      "v_mfma_f32_16x16x32_bf16 v[132:135], a[180:183], v[60:63], v[132:135]\n\t"
      "s_sub_u32 s84, s84, 4\n\t"
      "v_mfma_f32_16x16x32_bf16 v[132:135], a[204:207], v[64:67], v[132:135]\n\t"
      "s_max_i32 s84, s84, s86\n\t"
      "v_mfma_f32_16x16x32_bf16 v[132:135], a[228:231], v[68:71], v[132:135]\n\t"
      "ds_read_b128 v[108:111], v196 offset:12544\n\t"
      "v_mfma_f32_16x16x32_bf16 v[132:135], a[252:255], v[72:75], v[132:135]\n\t"
      "v_mfma_f32_16x16x32_bf16 v[132:135], v[184:187], v[76:79], v[132:135]\n\t"
      "s_waitcnt lgkmcnt(2)\n\t"
      "buffer_store_dwordx4 v[144:147], v209, s[28:31], s72 offen offset:1024 sc1\n\t"
      "v_mfma_f32_16x16x32_bf16 v[128:131], a[16:19], v[32:35], v[24:27]\n\t"
      "ds_read_b128 v[112:115], v198 offset:12800\n\t"
      "v_mfma_f32_16x16x32_bf16 v[128:131], a[40:43], v[36:39], v[128:131]\n\t"
      "v_mfma_f32_16x16x32_bf16 v[128:131], a[64:67], v[40:43], v[128:131]\n\t"
      "v_cvt_pk_bf16_f32 v138, v132, v133\n\t"
      "v_cvt_pk_bf16_f32 v139, v134, v135\n\t"
      "v_mfma_f32_16x16x32_bf16 v[128:131], a[88:91], v[44:47], v[128:131]\n\t"
      "ds_write_b32 v218, v138 offset:8\n\t"
      "v_mfma_f32_16x16x32_bf16 v[128:131], a[112:115], v[48:51], v[128:131]\n\t"
      "ds_write_b32 v218, v139 offset:12\n\t"
      "v_mfma_f32_16x16x32_bf16 v[128:131], a[136:139], v[52:55], v[128:131]\n\t"
      "s_sub_u32 s87, s80, s52\n\t"
      "v_mfma_f32_16x16x32_bf16 v[128:131], a[160:163], v[56:59], v[128:131]\n\t"
      "s_sub_u32 s83, s82, s87\n\t"
      "ds_read_b128 v[116:119], v199 offset:12800\n\t"
      "v_mfma_f32_16x16x32_bf16 v[128:131], a[184:187], v[60:63], v[128:131]\n\t"
      "s_max_i32 s82, s82, s87\n\t"
      "v_mfma_f32_16x16x32_bf16 v[128:131], a[208:211], v[64:67], v[128:131]\n\t"
      "s_sub_u32 s61, s70, s82\n\t"
      "v_mfma_f32_16x16x32_bf16 v[128:131], a[232:235], v[68:71], v[128:131]\n\t"
      "s_mul_i32 s61, s61, s60\n\t"
      "ds_read_b128 v[120:123], v196 offset:12800\n\t"
      "v_mfma_f32_16x16x32_bf16 v[128:131], v[164:167], v[72:75], v[128:131]\n\t"
      "s_ashr_i32 s61, s61, 8\n\t"
      "v_mfma_f32_16x16x32_bf16 v[128:131], v[188:191], v[76:79], v[128:131]\n\t"
      "buffer_load_dwordx4 v209, s[20:23], s73 offen offset:1024 lds\n\t"
      "v_mfma_f32_16x16x32_bf16 v[132:135], a[20:23], v[32:35], v[28:31]\n\t"
      "s_add_u32 s61, s61, s71\n\t"
      "ds_read_b128 v[124:127], v197 offset:12800\n\t"
      "v_mfma_f32_16x16x32_bf16 v[132:135], a[44:47], v[36:39], v[132:135]\n\t"
      "s_max_i32 s61, s61, s84\n\t"
      "ds_read_b128 v[148:151], v220 offset:14336\n\t"
      "v_mfma_f32_16x16x32_bf16 v[132:135], a[68:71], v[40:43], v[132:135]\n\t"
      "s_add_u32 s82, s82, s61\n\t"
      "v_cvt_pk_bf16_f32 v136, v128, v129\n\t"
      "v_cvt_pk_bf16_f32 v137, v130, v131\n\t"
      "v_mfma_f32_16x16x32_bf16 v[132:135], a[92:95], v[44:47], v[132:135]\n\t"
      "ds_write_b32 v219, v136 offset:0\n\t"
      "v_mfma_f32_16x16x32_bf16 v[132:135], a[116:119], v[48:51], v[132:135]\n\t"
      "ds_write_b32 v219, v137 offset:4\n\t"
      "v_mfma_f32_16x16x32_bf16 v[132:135], a[140:143], v[52:55], v[132:135]\n\t"
      "s_add_u32 s70, s70, s71\n\t"
      "v_add_u32_e32 v220, s46, v207\n\t"
      "v_mfma_f32_16x16x32_bf16 v[132:135], a[164:167], v[56:59], v[132:135]\n\t"
      "v_add_u32_e32 v6, 1, v6\n\t"
      "v_mfma_f32_16x16x32_bf16 v[132:135], a[188:191], v[60:63], v[132:135]\n\t"
      "v_mfma_f32_16x16x32_bf16 v[132:135], a[212:215], v[64:67], v[132:135]\n\t"
      "s_add_u32 s44, s44, 1\n\t"
      "v_mfma_f32_16x16x32_bf16 v[132:135], a[236:239], v[68:71], v[132:135]\n\t"
      "s_add_u32 s85, s85, 1\n\t"
      "v_mfma_f32_16x16x32_bf16 v[132:135], v[168:171], v[72:75], v[132:135]\n\t"
      "v_mfma_f32_16x16x32_bf16 v[132:135], v[192:195], v[76:79], v[132:135]\n\t"
      "s_waitcnt lgkmcnt(2)\n\t"
      "buffer_store_dwordx4 v[148:151], v209, s[28:31], s72 offen offset:2048 sc1\n\t"
      "v_mfma_f32_16x16x32_bf16 v[128:131], a[0:3], v[80:83], v[8:11]\n\t"
      "s_waitcnt vmcnt(17)\n\t"
      "v_mfma_f32_16x16x32_bf16 v[128:131], a[24:27], v[84:87], v[128:131]\n\t"
      "v_readlane_b32 s72, v248, s44\n\t"
      "v_mfma_f32_16x16x32_bf16 v[128:131], a[48:51], v[88:91], v[128:131]\n\t"
      "v_cvt_pk_bf16_f32 v138, v132, v133\n\t"
      "v_cvt_pk_bf16_f32 v139, v134, v135\n\t"
      "v_mfma_f32_16x16x32_bf16 v[128:131], a[72:75], v[92:95], v[128:131]\n\t"
      "ds_write_b32 v219, v138 offset:8\n\t"
      "v_mfma_f32_16x16x32_bf16 v[128:131], a[96:99], v[96:99], v[128:131]\n\t"
      "ds_write_b32 v219, v139 offset:12\n\t"
      "v_mfma_f32_16x16x32_bf16 v[128:131], a[120:123], v[100:103], v[128:131]\n\t"
      "ds_write_b32 v238, v6\n\t"
      "v_mfma_f32_16x16x32_bf16 v[128:131], a[144:147], v[104:107], v[128:131]\n\t"
      "buffer_load_dwordx4 v209, s[20:23], s73 offen offset:2048 lds\n\t"
      "v_mfma_f32_16x16x32_bf16 v[128:131], a[168:171], v[108:111], v[128:131]\n\t"
      "v_add_u32_e32 v196, s47, v200\n\t"
      "v_mfma_f32_16x16x32_bf16 v[128:131], a[192:195], v[112:115], v[128:131]\n\t"
      "v_add_u32_e32 v197, s47, v201\n\t"
      "v_mfma_f32_16x16x32_bf16 v[128:131], a[216:219], v[116:119], v[128:131]\n\t"
      "v_add_u32_e32 v198, s47, v202\n\t"
      "v_mfma_f32_16x16x32_bf16 v[128:131], a[240:243], v[120:123], v[128:131]\n\t"
      "v_add_u32_e32 v199, s47, v203\n\t"
      "v_mfma_f32_16x16x32_bf16 v[128:131], v[172:175], v[124:127], v[128:131]\n\t"
      "s_cmp_gt_i32 s83, 1\n\t"
      "s_cbranch_scc1 .LslpB_%=\n\t"
      ".LretB_%=:\n\t"
      "v_mfma_f32_16x16x32_bf16 v[132:135], a[4:7], v[80:83], v[12:15]\n\t"
      "ds_read_b32 v7, v239\n\t"
      "v_mfma_f32_16x16x32_bf16 v[132:135], a[28:31], v[84:87], v[132:135]\n\t"
      "v_mfma_f32_16x16x32_bf16 v[132:135], a[52:55], v[88:91], v[132:135]\n\t"
      "v_cvt_pk_bf16_f32 v136, v128, v129\n\t"
      "v_cvt_pk_bf16_f32 v137, v130, v131\n\t"
      "v_mfma_f32_16x16x32_bf16 v[132:135], a[76:79], v[92:95], v[132:135]\n\t"
      "v_mfma_f32_16x16x32_bf16 v[132:135], a[100:103], v[96:99], v[132:135]\n\t"
      "s_waitcnt lgkmcnt(0)\n\t"
      "v_cmp_gt_u32_e32 vcc, v6, v7\n\t"
      "v_mfma_f32_16x16x32_bf16 v[132:135], a[124:127], v[100:103], v[132:135]\n\t"
      "s_cbranch_vccnz .LsbB_%=\n\t"
      ".LsrB_%=:\n\t"
      "v_mfma_f32_16x16x32_bf16 v[132:135], a[148:151], v[104:107], v[132:135]\n\t"
      "ds_read_b128 v[140:143], v220 offset:0\n\t"
      "v_mfma_f32_16x16x32_bf16 v[132:135], a[172:175], v[108:111], v[132:135]\n\t"
      "ds_write_b32 v217, v136 offset:12288\n\t"
      "v_mfma_f32_16x16x32_bf16 v[132:135], a[196:199], v[112:115], v[132:135]\n\t"
      "ds_write_b32 v217, v137 offset:12292\n\t"
      "v_mfma_f32_16x16x32_bf16 v[132:135], a[220:223], v[116:119], v[132:135]\n\t"
      "ds_read_b128 v[32:35], v196 offset:0\n\t"
      "v_mfma_f32_16x16x32_bf16 v[132:135], a[244:247], v[120:123], v[132:135]\n\t"
      "v_readlane_b32 s73, v248, s85\n\t"
      "v_mfma_f32_16x16x32_bf16 v[132:135], v[176:179], v[124:127], v[132:135]\n\t"
      "s_waitcnt lgkmcnt(3)\n\t"
      "buffer_store_dwordx4 v[140:143], v208, s[28:31], s72 offen sc1\n\t"
      "v_mfma_f32_16x16x32_bf16 v[128:131], a[8:11], v[80:83], v[16:19]\n\t"
      "ds_read_b128 v[36:39], v197 offset:0\n\t"
      "v_mfma_f32_16x16x32_bf16 v[128:131], a[32:35], v[84:87], v[128:131]\n\t"
      "s_memrealtime s[80:81]\n\t"
      "v_mfma_f32_16x16x32_bf16 v[128:131], a[56:59], v[88:91], v[128:131]\n\t"
      "v_cvt_pk_bf16_f32 v138, v132, v133\n\t"
      "v_cvt_pk_bf16_f32 v139, v134, v135\n\t"
      "v_mfma_f32_16x16x32_bf16 v[128:131], a[80:83], v[92:95], v[128:131]\n\t"
      "ds_write_b32 v217, v138 offset:12296\n\t"
      "v_mfma_f32_16x16x32_bf16 v[128:131], a[104:107], v[96:99], v[128:131]\n\t"
      "ds_write_b32 v217, v139 offset:12300\n\t"
      "v_mfma_f32_16x16x32_bf16 v[128:131], a[128:131], v[100:103], v[128:131]\n\t"
      "ds_read_b128 v[40:43], v198 offset:0\n\t"
      "v_mfma_f32_16x16x32_bf16 v[128:131], a[152:155], v[104:107], v[128:131]\n\t"
      "s_mov_b32 m0, s46\n\t"
      "v_mfma_f32_16x16x32_bf16 v[128:131], a[176:179], v[108:111], v[128:131]\n\t"
      "ds_read_b128 v[44:47], v199 offset:0\n\t"
      "v_mfma_f32_16x16x32_bf16 v[128:131], a[200:203], v[112:115], v[128:131]\n\t"
      "v_mfma_f32_16x16x32_bf16 v[128:131], a[224:227], v[116:119], v[128:131]\n\t"
      "ds_read_b128 v[48:51], v197 offset:256\n\t"
      "v_mfma_f32_16x16x32_bf16 v[128:131], a[248:251], v[120:123], v[128:131]\n\t"
      "v_mfma_f32_16x16x32_bf16 v[128:131], v[180:183], v[124:127], v[128:131]\n\t"
      "buffer_load_dwordx4 v208, s[20:23], s73 offen lds\n\t"
      "v_mfma_f32_16x16x32_bf16 v[132:135], a[12:15], v[80:83], v[20:23]\n\t"
      "ds_read_b128 v[52:55], v198 offset:256\n\t"
      "v_mfma_f32_16x16x32_bf16 v[132:135], a[36:39], v[84:87], v[132:135]\n\t"
      "v_readlane_b32 s71, v252, s44\n\t"
      "v_mfma_f32_16x16x32_bf16 v[132:135], a[60:63], v[88:91], v[132:135]\n\t"
      "v_cvt_pk_bf16_f32 v136, v128, v129\n\t"
      "v_cvt_pk_bf16_f32 v137, v130, v131\n\t"
      "v_mfma_f32_16x16x32_bf16 v[132:135], a[84:87], v[92:95], v[132:135]\n\t"
      "ds_write_b32 v218, v136 offset:12288\n\t"
      "v_mfma_f32_16x16x32_bf16 v[132:135], a[108:111], v[96:99], v[132:135]\n\t"
      "s_lshr_b32 s60, s71, 16\n\t"
      "ds_write_b32 v218, v137 offset:12292\n\t"
      "v_mfma_f32_16x16x32_bf16 v[132:135], a[132:135], v[100:103], v[132:135]\n\t"
      "s_and_b32 s86, s71, 0xff\n\t"
      "ds_read_b128 v[144:147], v220 offset:1024\n\t"
      "v_mfma_f32_16x16x32_bf16 v[132:135], a[156:159], v[104:107], v[132:135]\n\t"
      "s_bfe_u32 s71, s71, 0x80008\n\t"
      "ds_read_b128 v[56:59], v199 offset:256\n\t"
      "v_mfma_f32_16x16x32_bf16 v[132:135], a[180:183], v[108:111], v[132:135]\n\t"
      "s_sub_u32 s84, s84, 4\n\t"
      "v_mfma_f32_16x16x32_bf16 v[132:135], a[204:207], v[112:115], v[132:135]\n\t"
      "s_max_i32 s84, s84, s86\n\t"
      "v_mfma_f32_16x16x32_bf16 v[132:135], a[228:231], v[116:119], v[132:135]\n\t"
      "ds_read_b128 v[60:63], v196 offset:256\n\t"
      "v_mfma_f32_16x16x32_bf16 v[132:135], a[252:255], v[120:123], v[132:135]\n\t"
      "v_mfma_f32_16x16x32_bf16 v[132:135], v[184:187], v[124:127], v[132:135]\n\t"
      "s_waitcnt lgkmcnt(2)\n\t"
      "buffer_store_dwordx4 v[144:147], v208, s[28:31], s72 offen offset:1024 sc1\n\t"
      "v_mfma_f32_16x16x32_bf16 v[128:131], a[16:19], v[80:83], v[24:27]\n\t"
      "ds_read_b128 v[64:67], v198 offset:512\n\t"
      "v_mfma_f32_16x16x32_bf16 v[128:131], a[40:43], v[84:87], v[128:131]\n\t"
      "v_mfma_f32_16x16x32_bf16 v[128:131], a[64:67], v[88:91], v[128:131]\n\t"
      "v_cvt_pk_bf16_f32 v138, v132, v133\n\t"
      "v_cvt_pk_bf16_f32 v139, v134, v135\n\t"
      "v_mfma_f32_16x16x32_bf16 v[128:131], a[88:91], v[92:95], v[128:131]\n\t"
      "ds_write_b32 v218, v138 offset:12296\n\t"
      "v_mfma_f32_16x16x32_bf16 v[128:131], a[112:115], v[96:99], v[128:131]\n\t"
      "ds_write_b32 v218, v139 offset:12300\n\t"
      "v_mfma_f32_16x16x32_bf16 v[128:131], a[136:139], v[100:103], v[128:131]\n\t"
      "s_sub_u32 s87, s80, s52\n\t"
      "v_mfma_f32_16x16x32_bf16 v[128:131], a[160:163], v[104:107], v[128:131]\n\t"
      "s_sub_u32 s83, s82, s87\n\t"
      "ds_read_b128 v[68:71], v199 offset:512\n\t"
      "v_mfma_f32_16x16x32_bf16 v[128:131], a[184:187], v[108:111], v[128:131]\n\t"
      "s_max_i32 s82, s82, s87\n\t"
      "v_mfma_f32_16x16x32_bf16 v[128:131], a[208:211], v[112:115], v[128:131]\n\t"
      "s_sub_u32 s61, s70, s82\n\t"
      "v_mfma_f32_16x16x32_bf16 v[128:131], a[232:235], v[116:119], v[128:131]\n\t"
      "s_mul_i32 s61, s61, s60\n\t"
      "ds_read_b128 v[72:75], v196 offset:512\n\t"
      "v_mfma_f32_16x16x32_bf16 v[128:131], v[164:167], v[120:123], v[128:131]\n\t"
      "s_ashr_i32 s61, s61, 8\n\t"
      "v_mfma_f32_16x16x32_bf16 v[128:131], v[188:191], v[124:127], v[128:131]\n\t"
      "buffer_load_dwordx4 v208, s[20:23], s73 offen offset:1024 lds\n\t"
      "v_mfma_f32_16x16x32_bf16 v[132:135], a[20:23], v[80:83], v[28:31]\n\t"
      "s_add_u32 s61, s61, s71\n\t"
      "ds_read_b128 v[76:79], v197 offset:512\n\t"
      "v_mfma_f32_16x16x32_bf16 v[132:135], a[44:47], v[84:87], v[132:135]\n\t"
      "s_max_i32 s61, s61, s84\n\t"
      "ds_read_b128 v[148:151], v220 offset:2048\n\t"
      "v_mfma_f32_16x16x32_bf16 v[132:135], a[68:71], v[88:91], v[132:135]\n\t"
      "s_add_u32 s82, s82, s61\n\t"
      "v_cvt_pk_bf16_f32 v136, v128, v129\n\t"
      "v_cvt_pk_bf16_f32 v137, v130, v131\n\t"
      "v_mfma_f32_16x16x32_bf16 v[132:135], a[92:95], v[92:95], v[132:135]\n\t"
      "ds_write_b32 v219, v136 offset:12288\n\t"
      "v_mfma_f32_16x16x32_bf16 v[132:135], a[116:119], v[96:99], v[132:135]\n\t"
      "ds_write_b32 v219, v137 offset:12292\n\t"
      "v_mfma_f32_16x16x32_bf16 v[132:135], a[140:143], v[100:103], v[132:135]\n\t"
      "s_add_u32 s70, s70, s71\n\t"
      "v_mfma_f32_16x16x32_bf16 v[132:135], a[164:167], v[104:107], v[132:135]\n\t"
      "s_mov_b32 s45, s46\n\t"
      "v_add_u32_e32 v6, 1, v6\n\t"
      "v_mfma_f32_16x16x32_bf16 v[132:135], a[188:191], v[108:111], v[132:135]\n\t"
      "s_mov_b32 s46, s47\n\t"
      "v_mfma_f32_16x16x32_bf16 v[132:135], a[212:215], v[112:115], v[132:135]\n\t"
      "s_mov_b32 s47, s48\n\t"
      "v_mfma_f32_16x16x32_bf16 v[132:135], a[236:239], v[116:119], v[132:135]\n\t"
      "v_mfma_f32_16x16x32_bf16 v[132:135], v[168:171], v[120:123], v[132:135]\n\t"
      "s_cmp_lg_u32 s44, s49\n\t"
      "v_mfma_f32_16x16x32_bf16 v[132:135], v[192:195], v[124:127], v[132:135]\n\t"
      "s_waitcnt lgkmcnt(2)\n\t"
      "buffer_store_dwordx4 v[148:151], v208, s[28:31], s72 offen offset:2048 sc1\n\t"
      "s_cbranch_scc1 .Lloop_%=\n\t"
      "v_readlane_b32 s72, v249, s44\n\t"
      "buffer_load_dwordx2 v[240:241], v210, s[24:27], 0 offen sc1\n\t"
      "s_nop 5\n\t"
      "v_cvt_pk_bf16_f32 v138, v132, v133\n\t"
      "v_cvt_pk_bf16_f32 v139, v134, v135\n\t"
      "ds_write_b64 v219, v[138:139] offset:12296\n\t"
      "s_waitcnt lgkmcnt(0)\n\t"
      "s_barrier\n\t"
      "ds_read_b128 v[32:35], v220 offset:12288\n\t"
      "ds_read_b128 v[36:39], v220 offset:13312\n\t"
      "ds_read_b128 v[40:43], v220 offset:14336\n\t"
      "s_waitcnt lgkmcnt(2)\n\t"
      "buffer_store_dwordx4 v[32:35], v209, s[28:31], s72 offen sc1\n\t"
      "s_waitcnt lgkmcnt(1)\n\t"
      "buffer_store_dwordx4 v[36:39], v209, s[28:31], s72 offen offset:1024 sc1\n\t"
      "s_waitcnt lgkmcnt(0)\n\t"
      "buffer_store_dwordx4 v[40:43], v209, s[28:31], s72 offen offset:2048 sc1\n\t"
      "s_cmp_lg_u32 s41, s43\n\t"
      "s_cbranch_scc1 .Lw0ec_%=\n\t"
      "s_waitcnt vmcnt(3)\n\t"
      "v_cmpx_ne_u32_e32 vcc, v241, v243\n\t"
      "s_nop 4\n\t"
      "buffer_store_dwordx2 v[242:243], v210, s[24:27], s67 offen sc1\n\t"
      "s_mov_b64 exec, -1\n\t"
      ".Lw0ec_%=:\n\t"
      "s_branch .Lend_%=\n\t"
      ".LslpA_%=:\n\t"
      "s_min_i32 s83, s83, 0x190\n\t"
      ".Lsl2A_%=:\n\t"
      "s_sleep 1\n\t"
      "s_sub_u32 s83, s83, 3\n\t"
      "s_cmp_gt_i32 s83, 1\n\t"
      "s_cbranch_scc1 .Lsl2A_%=\n\t"
      "s_branch .LretA_%=\n\t"
      ".LslpB_%=:\n\t"
      "s_min_i32 s83, s83, 0x190\n\t"
      ".Lsl2B_%=:\n\t"
      "s_sleep 1\n\t"
      "s_sub_u32 s83, s83, 3\n\t"
      "s_cmp_gt_i32 s83, 1\n\t"
      "s_cbranch_scc1 .Lsl2B_%=\n\t"
      "s_branch .LretB_%=\n\t"
      ".Lgs_%=:\n\t"
      "s_sleep 8\n\t"
      "buffer_load_dwordx2 v[240:241], v245, s[24:27], 0 offen sc1\n\t"
      "s_memrealtime s[76:77]\n\t"
      "s_waitcnt vmcnt(0) lgkmcnt(0)\n\t"
      "v_subrev_u32_e32 v242, s67, v240\n\t"
      "v_xor_b32_e32 v241, s63, v241\n\t"
      "v_lshl_or_b32 v240, v242, 20, v241\n\t"
      "v_lshrrev_b32_e32 v242, 12, v242\n\t"
      "v_cmp_ne_u32_e32 vcc, 0, v240\n\t"
      "s_nop 0\n\t"
      "s_cbranch_vccz .Lgr_%=\n\t"
      "s_sub_u32 s76, s76, s52\n\t"
      "s_cmp_lt_u32 s76, 2000\n\t"
      "s_cbranch_scc1 .Lgs_%=\n\t"
      "v_mov_b32_e32 v242, 10\n\t"
      "s_branch .Lgr_%=\n\t"
      ".LsbA_%=:\n\t"
      "ds_read_b32 v7, v239\n\t"
      "s_waitcnt lgkmcnt(0)\n\t"
      "v_cmp_gt_u32_e32 vcc, v6, v7\n\t"
      "s_nop 0\n\t"
      "s_cbranch_vccnz .LsbA_%=\n\t"
      "s_branch .LsrA_%=\n\t"
      ".LsbB_%=:\n\t"
      "ds_read_b32 v7, v239\n\t"
      "s_waitcnt lgkmcnt(0)\n\t"
      "v_cmp_gt_u32_e32 vcc, v6, v7\n\t"
      "s_nop 0\n\t"
      "s_cbranch_vccnz .LsbB_%=\n\t"
      "s_branch .LsrB_%=\n\t"
      ".Lend_%=:\n\t"
      :
      : "{s[20:21]}"(xa), "{s[16:17]}"(wa), "{s[36:37]}"(ba), "{s[28:29]}"(oa), "{s[24:25]}"(sa), "{s[52:53]}"(rs),
        "{s[22:23]}"(0x0002000006c00000ull), "{s[26:27]}"(0x0002000000000808ull), "{s[30:31]}"(0x0002000006c00000ull),
        "{s[18:19]}"(0x0002000000048000ull), "{s[38:39]}"(0x0002000000000300ull),
        "{s40}"(t0), "{s41}"(p0), "{s43}"(lds0), "{s94}"(ty),
        "{v208}"(ga), "{v209}"(gb),
        "{v211}"(woff[0]), "{v212}"(woff[1]), "{v213}"(woff[2]), "{v215}"(boff),
        "{s95}"(tres), "{s92}"(pre1), "{s[60:61]}"(pres), "{s[62:63]}"(ep), "{s77}"(pfire), "{s89}"(blk ? 0x40000000u : 0u), "{s98}"(((tidx >> 3) * 4u + (tidx & 3u)) * 128u), "{v238}"(vfw), "{v239}"(vfr),
        "{v248}"(ta), "{v249}"(ta), "{v251}"(t1), "{v250}"(mk), "{s74}"(plm), "{s75}"(plb),
        "{s[34:35]}"(__builtin_amdgcn_kernarg_segment_ptr()),
        "{v244}"(vpub), "{v210}"(vbmp), "{v245}"(vgv), "{v246}"(vxw), "{v247}"(vxr),
        "{v200}"(cfa[0]), "{v201}"(cfa[1]), "{v202}"(cfa[2]), "{v203}"(cfa[3]),
        "{v204}"(csw[0]), "{v205}"(csw[1]), "{v206}"(csw[2]), "{v207}"(csr),
        "{v214}"(tbase)
      : "memory", "scc", "vcc",
      "s42", "s44", "s45", "s46", "s47", "s48", "s49", "s50", "s51", "s54", "s55", "s56", "s57", "s58", "s59", "s64", "s65", "s66", "s67", "s68",
      "s69", "s70", "s71", "s72", "s73", "s76", "s78", "s79", "s80", "s81", "s82", "s83", "s84", "s85", "s86", "s87", "s88", "s90", "s91", "s93",
      "s96", "s97", "s99",
      "v1", "v2", "v3", "v4", "v5", "v6", "v7", "v8", "v9", "v10", "v11", "v12", "v13", "v14", "v15", "v16", "v17", "v18", "v19", "v20", "v21", "v22",
      "v23", "v24", "v25", "v26", "v27", "v28", "v29", "v30", "v31", "v32", "v33", "v34", "v35", "v36", "v37", "v38", "v39", "v40", "v41", "v42",
      "v43", "v44", "v45", "v46", "v47", "v48", "v49", "v50", "v51", "v52", "v53", "v54", "v55", "v56", "v57", "v58", "v59", "v60", "v61", "v62",
      "v63", "v64", "v65", "v66", "v67", "v68", "v69", "v70", "v71", "v72", "v73", "v74", "v75", "v76", "v77", "v78", "v79", "v80", "v81", "v82",
      "v83", "v84", "v85", "v86", "v87", "v88", "v89", "v90", "v91", "v92", "v93", "v94", "v95", "v96", "v97", "v98", "v99", "v100", "v101", "v102",
      "v103", "v104", "v105", "v106", "v107", "v108", "v109", "v110", "v111", "v112", "v113", "v114", "v115", "v116", "v117", "v118", "v119", "v120",
      "v121", "v122", "v123", "v124", "v125", "v126", "v127", "v128", "v129", "v130", "v131", "v132", "v133", "v134", "v135", "v136", "v137", "v138",
      "v139", "v140", "v141", "v142", "v143", "v144", "v145", "v146", "v147", "v148", "v149", "v150", "v151", "v152", "v153", "v154", "v155", "v156",
      "v157", "v158", "v159", "v160", "v161", "v162", "v163", "v164", "v165", "v166", "v167", "v168", "v169", "v170", "v171", "v172", "v173", "v174",
      "v175", "v176", "v177", "v178", "v179", "v180", "v181", "v182", "v183", "v184", "v185", "v186", "v187", "v188", "v189", "v190", "v191", "v192",
      "v193", "v194", "v195", "v196", "v197", "v198", "v199", "v216", "v217", "v218", "v219", "v220", "v221", "v222", "v223", "v224", "v225", "v226",
      "v227", "v228", "v229", "v230", "v231", "v232", "v233", "v234", "v235", "v236", "v237", "v240", "v241", "v242", "v243", "v252", "v253", "v254",
      "v255",
      "a0", "a1", "a2", "a3", "a4", "a5", "a6", "a7", "a8", "a9", "a10", "a11", "a12", "a13", "a14", "a15", "a16", "a17", "a18", "a19", "a20", "a21",
      "a22", "a23", "a24", "a25", "a26", "a27", "a28", "a29", "a30", "a31", "a32", "a33", "a34", "a35", "a36", "a37", "a38", "a39", "a40", "a41",
      "a42", "a43", "a44", "a45", "a46", "a47", "a48", "a49", "a50", "a51", "a52", "a53", "a54", "a55", "a56", "a57", "a58", "a59", "a60", "a61",
      "a62", "a63", "a64", "a65", "a66", "a67", "a68", "a69", "a70", "a71", "a72", "a73", "a74", "a75", "a76", "a77", "a78", "a79", "a80", "a81",
      "a82", "a83", "a84", "a85", "a86", "a87", "a88", "a89", "a90", "a91", "a92", "a93", "a94", "a95", "a96", "a97", "a98", "a99", "a100", "a101",
      "a102", "a103", "a104", "a105", "a106", "a107", "a108", "a109", "a110", "a111", "a112", "a113", "a114", "a115", "a116", "a117", "a118", "a119",
      "a120", "a121", "a122", "a123", "a124", "a125", "a126", "a127", "a128", "a129", "a130", "a131", "a132", "a133", "a134", "a135", "a136", "a137",
      "a138", "a139", "a140", "a141", "a142", "a143", "a144", "a145", "a146", "a147", "a148", "a149", "a150", "a151", "a152", "a153", "a154", "a155",
      "a156", "a157", "a158", "a159", "a160", "a161", "a162", "a163", "a164", "a165", "a166", "a167", "a168", "a169", "a170", "a171", "a172", "a173",
      "a174", "a175", "a176", "a177", "a178", "a179", "a180", "a181", "a182", "a183", "a184", "a185", "a186", "a187", "a188", "a189", "a190", "a191",
      "a192", "a193", "a194", "a195", "a196", "a197", "a198", "a199", "a200", "a201", "a202", "a203", "a204", "a205", "a206", "a207", "a208", "a209",
      "a210", "a211", "a212", "a213", "a214", "a215", "a216", "a217", "a218", "a219", "a220", "a221", "a222", "a223", "a224", "a225", "a226", "a227",
      "a228", "a229", "a230", "a231", "a232", "a233", "a234", "a235", "a236", "a237", "a238", "a239", "a240", "a241", "a242", "a243", "a244", "a245",
      "a246", "a247", "a248", "a249", "a250", "a251", "a252", "a253", "a254", "a255");
}

extern "C" void kernel_launch(void* const* d_in, const int* in_sizes, int n_in,
                              void* d_out, int out_size, void* d_ws, size_t ws_size,
                              hipStream_t stream) {
  const uint16_t* x = (const uint16_t*)d_in[0];
  const uint16_t* w = (const uint16_t*)d_in[1];
  const uint16_t* b = (const uint16_t*)d_in[2];
  uint16_t* out = (uint16_t*)d_out;
  const PaceTab pt = {{
    0x5b5bu, 0x5b5bu, 0x4646u, 0u,
    0x5b5bu, 0x5b5bu, 0x4646u, 0u,
    0x514fu, 0x514au, 0x4646u, 0u,
    0x4f4fu, 0x4b4au, 0x4646u, 0u,
    0x4f4fu, 0x4a4au, 0x4646u, 0u,
    0x4f4fu, 0x4a4au, 0x4646u, 0u,
    0x4f4fu, 0x4b4au, 0x4746u, 0u,
    0x11514fu, 0x114e4au, 0x114946u, 0u,
    0x135350u, 0x13504cu, 0x134c48u, 0u,
    0x165450u, 0x16514du, 0x164e49u, 0u,
    0x1b5450u, 0x1b514du, 0x1b4e49u, 0u,
    0x225450u, 0x22514du, 0x224e49u, 0u,
    0x2f5450u, 0x2f514du, 0x2f4e49u, 0u,
    0x495450u, 0x49514du, 0x494e49u, 0u,
    0xab5450u, 0xab514du, 0xab4e49u, 0u,
    0x1004800u, 0x1004800u, 0x1004800u, 0u,
    0x1004800u, 0x1004800u, 0x1004800u, 0u,
    0x1004800u, 0x1004800u, 0x1004800u, 0u}};
  conv1x1_kernel<<<256, 256, 0, stream>>>(x, w, b, out, (unsigned long long*)d_ws, pt);
}
```

```cpp
#include <hip/hip_runtime.h>
#include <stdint.h>

#define LAS __attribute__((address_space(3)))

struct PaceTab { unsigned v[72]; };

__global__ __launch_bounds__(256, 1) void conv1x1_kernel(const uint16_t* __restrict__ x,
                                                         const uint16_t* __restrict__ w,
                                                         const uint16_t* __restrict__ bias,
                                                         uint16_t* __restrict__ out, unsigned long long* ws, PaceTab pt) {
  __shared__ __attribute__((aligned(1024))) unsigned char lds[161792];

  unsigned long long xa, wa, ba, oa, sa, rs;
  asm volatile(
      "s_load_dwordx2 s[16:17], %6, 0x8\n\t"
      "s_load_dwordx2 s[20:21], %6, 0x0\n\t"
      "s_load_dwordx2 s[36:37], %6, 0x10\n\t"
      "s_load_dwordx2 s[28:29], %6, 0x18\n\t"
      "s_load_dwordx2 s[24:25], %6, 0x20\n\t"
      "s_memrealtime s[52:53]\n\t"
      : "={s[20:21]}"(xa), "={s[16:17]}"(wa), "={s[36:37]}"(ba), "={s[28:29]}"(oa), "={s[24:25]}"(sa), "={s[52:53]}"(rs)
      : "s"(__builtin_amdgcn_kernarg_segment_ptr())
      : "memory");
  __builtin_amdgcn_sched_barrier(0);
  const unsigned tid  = threadIdx.x;
  unsigned lane = tid & 63;
  unsigned wv   = __builtin_amdgcn_readfirstlane(tid >> 6);
  const unsigned lds0 = (unsigned)(size_t)(LAS unsigned char*)lds;
  const unsigned blk  = blockIdx.x;
  const unsigned role = (blk >> 2) & 1u;
  unsigned pk = (blk & 3u) * 32u + (blk >> 3);
  const unsigned sst  = (role ^ (blk >> 4)) & 1u;
  const unsigned t0   = pk * 49152u + sst * 24576u;
  const unsigned p0   = lds0 + 3072u * wv;
  const unsigned lq = lane >> 4, ph = (lane >> 2) & 3, pl = lane & 3;
  const unsigned gs = 256u * lq + 64u * ((ph - lq) & 3) + 16u * (pl ^ ((4u - wv) & 3));
  const unsigned ga = gs + 3072u * (wv < 3u ? wv : 5u);
  const unsigned gb = gs + 3072u * (wv < 2u ? wv + 6u : wv + 1u);
  const unsigned pv = blk >> 6, ps = (blk & 63u) ^ (4u * (pv & 1u)), pgr = (ps >> 2) & 7u, pi = (ps & 3u) * 2u + (pv >> 1);
  const unsigned ppg = min(pgr * 8u, 52u) + (pi & (0x13777777u >> (4u * pgr)) & 7u);
  const unsigned pop = (ppg << 21) + ((ps & 3u) * 4u + pv) * 0x1c080u + 0x8000u;
  unsigned pfire = ((role ^ ppg) & 1u) << 13;
  const unsigned pfo = (blk >> 3) * 9216u;
  const unsigned pfv = tid * 16u;
  const unsigned ty = (blk >> 3) & 1u;
  const unsigned tidx = (blk >> 3) * 4u + wv, tsel = tidx >> 6;
  const unsigned toff = (tidx & 63u) < 54u ? (tidx & 63u) << 21 : 113246204u;
  unsigned tres, pre1;
  unsigned long long pres, ep;
  asm volatile(
      "s_add_u32 m0, s43, 0x27000\n\t"
      "s_add_u32 s51, s42, 0x1000\n\t"
      "s_waitcnt lgkmcnt(0)\n\t"
      "s_cmp_lg_u32 s41, s43\n\t"
      "s_cbranch_scc1 .Lw0pi_%=\n\t"
      "s_cmp_eq_u32 s78, 0\n\t"
      "s_cselect_b64 s[96:97], s[20:21], s[28:29]\n\t"
      "s_load_dword s92, s[96:97], s76 offset:0x0\n\t"
      "s_load_dwordx2 s[60:61], s[96:97], s76 offset:0x3300\n\t"
      ".Lw0pi_%=:\n\t"
      "buffer_load_dwordx4 v216, s[16:19], s42 offen lds\n\t"
      "buffer_load_dwordx4 v216, s[16:19], s51 offen lds\n\t"
      "s_add_u32 s51, s42, 0x2000\n\t"
      "buffer_load_dwordx4 v216, s[16:19], s51 offen lds\n\t"
      "s_mov_b32 m0, s41\n\t"
      "s_nop 0\n\t"
      "buffer_load_dwordx4 v208, s[20:23], s40 offen lds\n\t"
      "buffer_load_dwordx4 v208, s[20:23], s40 offen offset:1024 lds\n\t"
      "buffer_load_dwordx4 v208, s[20:23], s40 offen offset:2048 lds\n\t"
      "s_add_u32 m0, s41, 0x3000\n\t"
      "s_nop 0\n\t"
      "buffer_load_dwordx4 v209, s[20:23], s40 offen lds\n\t"
      "buffer_load_dwordx4 v209, s[20:23], s40 offen offset:1024 lds\n\t"
      "buffer_load_dwordx4 v209, s[20:23], s40 offen offset:2048 lds\n\t"
      "s_cmp_lg_u32 s41, s43\n\t"
      "s_cbranch_scc1 .Lw0pe_%=\n\t"
      "s_waitcnt lgkmcnt(2)\n\t"
      "s_nop 7\n\t"
      "s_getreg_b32 s51, hwreg(HW_REG_IB_STS, 8, 4)\n\t"
      "s_lshl_b32 s96, s51, 13\n\t"
      "s_xor_b32 s77, s77, s96\n\t"
      "s_and_b32 s77, s77, 0x2000\n\t"
      "s_cmp_lg_u32 s51, 0\n\t"
      "s_cselect_b32 s77, s77, 0x1000\n\t"
      ".Lw0pe_%=:\n\t"
      "s_load_dwordx2 s[62:63], s[24:25], 0x800\n\t"
      "s_cmp_eq_u32 s99, 0\n\t"
      "s_cselect_b64 s[96:97], s[20:21], s[28:29]\n\t"
      "s_load_dword s95, s[96:97], s98\n\t"
      : "={s95}"(tres), "={s92}"(pre1), "={s[60:61]}"(pres), "={s[62:63]}"(ep), "+{s77}"(pfire), "+v"(lane), "+s"(wv), "+s"(pk)
      : "{s[20:21]}"(xa), "{s[16:17]}"(wa), "{s[36:37]}"(ba), "{s[28:29]}"(oa), "{s[24:25]}"(sa), "{s[52:53]}"(rs),
        "{s[22:23]}"(0x0002000006c00000ull), "{s[26:27]}"(0x0002000000000808ull), "{s[30:31]}"(0x0002000006c00000ull),
        "{s[18:19]}"(0x0002000000048000ull), "{s[38:39]}"(0x0002000000000300ull),
        "{s40}"(t0), "{s41}"(p0), "{s43}"(lds0), "{s94}"(ty),
        "{v208}"(ga), "{v209}"(gb),
        "{s42}"(pfo), "{s98}"(toff), "{s99}"(tsel), "{s76}"(pop), "{s78}"(ps >> 5),
        "{v216}"(pfv)
      : "memory", "scc", "s51", "s96", "s97");
  __builtin_amdgcn_sched_barrier(0);
  const unsigned sp = lane < 18u ? lane : lane - 14u;
  const unsigned ob = (unsigned)(((sp < 12u ? 0x41ccd62ca418820ull >> (5u * sp) : 0x22a4c1eeull >> (5u * ((sp - 12u) & 7u)))) & 31u) * 6291456u + pk * 49152u;
  const unsigned pg = (ob + 24576u) >> 21;
  const unsigned ta = lane < 18u ? ob + 24576u * sst : 113246208u, t1 = lane < 32u ? ob + 24576u * (sst ^ 1u) : 113246208u;
  const unsigned mk = lane >= 4u && lane < 18u ? 1u << (pg < 52u ? (pg >> 3) * 4u : 28u) : 0u;
  const unsigned plm = (unsigned)__builtin_amdgcn_ballot_w64(((pg ^ role ^ sst) & 1u) != 0u), plb = wv < 2u ? plm : ~plm;
  const unsigned vgv = 0u + wv * 512u + lane * 8u;
  const unsigned vpub = vgv == 0u ? 0u + (pv * 64u + ps) * 8u : 0x40000000u, vbmp = vpub == 0u ? 2048u : 0x40000000u;
  const unsigned vxr = lds0 + 159744u + lane * 16u, vxw = vxr + wv * 4u;
  const unsigned vfw = lds0 + 160768u + lane * 4u + wv * 256u, vfr = lds0 + 160768u + lane * 4u + (lane & 3u) * 256u;
  const unsigned li = lane & 15, lg = lane >> 4, rl = li & 3, hi = li >> 2;
  const unsigned rb = 768u * li + 16u * (lg ^ ((4u - hi) & 3)) - 3072u * wv;
  unsigned cfa[4], csw[3];
#pragma unroll
  for (unsigned u = 0; u < 4; ++u) cfa[u] = rb + 64u * ((u - rl) & 3);
#pragma unroll
  for (unsigned j = 0; j < 3; ++j) {
    const unsigned e = 3u * wv + j;
    csw[j] = rb + 256u * (e >> 2) + 64u * (((e & 3) + (e >> 2) - rl) & 3);
  }
  const unsigned csr = 16u * lane;
  const unsigned tbase = p0 + (4u * lg + (li >> 2)) * 192u + 16u * (li & 3);
  unsigned woff[3];
#pragma unroll
  for (unsigned j = 0; j < 3; ++j) {
    const unsigned q = 64u * j + lane, rr = q / 12u;
    woff[j] = (8u * (rr >> 2) + (rr & 3)) * 768u + 192u * wv + 16u * (q % 12u) - 1024u * j;
  }
  const unsigned boff = 192u * wv + 16u * lg;
  asm volatile(
      "s_cmp_eq_u32 s94, 1\n\t"
      "s_cbranch_scc0 .Lc0_%=\n\t"
      "s_getpc_b64 s[96:97]\n\t"
      ".Lcp_%=:\n\t"
      "s_add_u32 s98, s98, .Lcq_%=-.Lcp_%=\n\t"
      "s_min_u32 s98, s98, .Lend_%=-.Lcp_%=-68\n\t"
      "s_load_dword s99, s[96:97], s98 offset:64\n\t"
      "s_load_dword s98, s[96:97], s98\n\t"
      "s_add_u32 m0, s41, 0x6000\n\t"
      "s_add_u32 s51, s40, 0x600000\n\t"
      "buffer_load_dwordx4 v208, s[20:23], s51 offen lds\n\t"
      "buffer_load_dwordx4 v208, s[20:23], s51 offen offset:1024 lds\n\t"
      "buffer_load_dwordx4 v208, s[20:23], s51 offen offset:2048 lds\n\t"
      "s_add_u32 m0, s41, 0x9000\n\t"
      "s_add_u32 s51, s40, 0x600000\n\t"
      "buffer_load_dwordx4 v209, s[20:23], s51 offen lds\n\t"
      "buffer_load_dwordx4 v209, s[20:23], s51 offen offset:1024 lds\n\t"
      "buffer_load_dwordx4 v209, s[20:23], s51 offen offset:2048 lds\n\t"
      ".Lc0_%=:\n\t"
      "buffer_load_dwordx4 v[226:229], v215, s[36:39], 0 offen offset:0\n\t"
      "buffer_load_dwordx4 v[230:233], v215, s[36:39], 0 offen offset:64\n\t"
      "buffer_load_dwordx4 v[234:237], v215, s[36:39], 0 offen offset:128\n\t"
      "s_add_u32 m0, s41, 0x1e000\n\t"
      "s_mov_b32 s51, 0x0\n\t"
      "buffer_load_dwordx4 v211, s[16:19], s51 offen lds\n\t"
      "buffer_load_dwordx4 v212, s[16:19], s51 offen offset:1024 lds\n\t"
      "buffer_load_dwordx4 v213, s[16:19], s51 offen offset:2048 lds\n\t"
      "s_add_u32 m0, s41, 0x21000\n\t"
      "s_mov_b32 s51, 0xc00\n\t"
      "buffer_load_dwordx4 v211, s[16:19], s51 offen lds\n\t"
      "buffer_load_dwordx4 v212, s[16:19], s51 offen offset:1024 lds\n\t"
      "buffer_load_dwordx4 v213, s[16:19], s51 offen offset:2048 lds\n\t"
      "s_add_u32 m0, s41, 0x24000\n\t"
      "s_mov_b32 s51, 0x6000\n\t"
      "buffer_load_dwordx4 v211, s[16:19], s51 offen lds\n\t"
      "buffer_load_dwordx4 v212, s[16:19], s51 offen offset:1024 lds\n\t"
      "buffer_load_dwordx4 v213, s[16:19], s51 offen offset:2048 lds\n\t"
      "s_add_u32 m0, s41, 0x12000\n\t"
      "s_mov_b32 s51, 0x6c00\n\t"
      "buffer_load_dwordx4 v211, s[16:19], s51 offen lds\n\t"
      "buffer_load_dwordx4 v212, s[16:19], s51 offen offset:1024 lds\n\t"
      "buffer_load_dwordx4 v213, s[16:19], s51 offen offset:2048 lds\n\t"
      "s_add_u32 m0, s41, 0x15000\n\t"
      "s_mov_b32 s51, 0xc000\n\t"
      "buffer_load_dwordx4 v211, s[16:19], s51 offen lds\n\t"
      "buffer_load_dwordx4 v212, s[16:19], s51 offen offset:1024 lds\n\t"
      "buffer_load_dwordx4 v213, s[16:19], s51 offen offset:2048 lds\n\t"
      "s_add_u32 m0, s41, 0x18000\n\t"
      "s_mov_b32 s51, 0xcc00\n\t"
      "buffer_load_dwordx4 v211, s[16:19], s51 offen lds\n\t"
      "buffer_load_dwordx4 v212, s[16:19], s51 offen offset:1024 lds\n\t"
      "buffer_load_dwordx4 v213, s[16:19], s51 offen offset:2048 lds\n\t"
      "s_add_u32 m0, s41, 0x1b000\n\t"
      "s_mov_b32 s51, 0x12000\n\t"
      "buffer_load_dwordx4 v211, s[16:19], s51 offen lds\n\t"
      "buffer_load_dwordx4 v212, s[16:19], s51 offen offset:1024 lds\n\t"
      "buffer_load_dwordx4 v213, s[16:19], s51 offen offset:2048 lds\n\t"
      "s_mov_b32 s44, 0\n\t"
      "s_mov_b32 s85, 3\n\t"
      "s_add_u32 s50, s41, 0x12000\n\t"
      "s_mov_b32 s45, s41\n\t"
      "s_add_u32 s46, s41, 0x6000\n\t"
      "s_add_u32 s47, s41, 0xc000\n\t"
      "s_mov_b32 s84, 96\n\t"
      "v_add_u32_e32 v196, s41, v200\n\t"
      "v_add_u32_e32 v197, s41, v201\n\t"
      "v_add_u32_e32 v198, s41, v202\n\t"
      "v_add_u32_e32 v199, s41, v203\n\t"
      "v_add_u32_e32 v217, s41, v204\n\t"
      "v_add_u32_e32 v218, s41, v205\n\t"
      "v_add_u32_e32 v219, s41, v206\n\t"
      "v_add_u32_e32 v220, s41, v207\n\t"
      "s_waitcnt vmcnt(18)\n\t"
      "s_waitcnt lgkmcnt(0)\n\t"
      "s_barrier\n\t"
      "s_memrealtime s[68:69]\n\t"
      "v_add_u32_e32 v221, 0x1e000, v214\n\t"
      "ds_read_b64_tr_b16 a[0:1], v221 offset:0\n\t"
      "ds_read_b64_tr_b16 a[4:5], v221 offset:8\n\t"
      "ds_read_b64_tr_b16 a[8:9], v221 offset:64\n\t"
      "ds_read_b64_tr_b16 a[12:13], v221 offset:72\n\t"
      "ds_read_b64_tr_b16 a[16:17], v221 offset:128\n\t"
      "ds_read_b64_tr_b16 a[20:21], v221 offset:136\n\t"
      "ds_read_b128 v[140:143], v196 offset:0\n\t"
      "v_lshlrev_b32_e32 v8, 16, v226\n\t"
      "v_and_b32_e32 v9, 0xffff0000, v226\n\t"
      "v_lshlrev_b32_e32 v10, 16, v227\n\t"
      "v_and_b32_e32 v11, 0xffff0000, v227\n\t"
      "v_lshlrev_b32_e32 v12, 16, v228\n\t"
      "v_and_b32_e32 v13, 0xffff0000, v228\n\t"
      "v_lshlrev_b32_e32 v14, 16, v229\n\t"
      "v_and_b32_e32 v15, 0xffff0000, v229\n\t"
      "v_lshlrev_b32_e32 v16, 16, v230\n\t"
      "v_and_b32_e32 v17, 0xffff0000, v230\n\t"
      "v_lshlrev_b32_e32 v18, 16, v231\n\t"
      "v_and_b32_e32 v19, 0xffff0000, v231\n\t"
      "v_lshlrev_b32_e32 v20, 16, v232\n\t"
      "v_and_b32_e32 v21, 0xffff0000, v232\n\t"
      "v_lshlrev_b32_e32 v22, 16, v233\n\t"
      "v_and_b32_e32 v23, 0xffff0000, v233\n\t"
      "v_lshlrev_b32_e32 v24, 16, v234\n\t"
      "v_and_b32_e32 v25, 0xffff0000, v234\n\t"
      "v_lshlrev_b32_e32 v26, 16, v235\n\t"
      "v_and_b32_e32 v27, 0xffff0000, v235\n\t"
      "v_lshlrev_b32_e32 v28, 16, v236\n\t"
      "v_and_b32_e32 v29, 0xffff0000, v236\n\t"
      "v_lshlrev_b32_e32 v30, 16, v237\n\t"
      "v_and_b32_e32 v31, 0xffff0000, v237\n\t"
      "s_add_u32 s62, s62, 0x7f4a7c15\n\t"
      "s_add_u32 s63, s63, 0x9e3779b9\n\t"
      "s_waitcnt vmcnt(15)\n\t"
      "v_add_u32_e32 v221, 0x21000, v214\n\t"
      "ds_read_b64_tr_b16 a[2:3], v221 offset:0\n\t"
      "ds_read_b64_tr_b16 a[6:7], v221 offset:8\n\t"
      "ds_read_b64_tr_b16 a[10:11], v221 offset:64\n\t"
      "ds_read_b64_tr_b16 a[14:15], v221 offset:72\n\t"
      "ds_read_b64_tr_b16 a[18:19], v221 offset:128\n\t"
      "ds_read_b64_tr_b16 a[22:23], v221 offset:136\n\t"
      "ds_read_b128 v[152:155], v196 offset:12288\n\t"
      "s_waitcnt lgkmcnt(8)\n\t"
      "s_add_u32 m0, s41, 0x1e000\n\t"
      "s_mov_b32 s51, 0x12c00\n\t"
      "buffer_load_dwordx4 v211, s[16:19], s51 offen lds\n\t"
      "buffer_load_dwordx4 v212, s[16:19], s51 offen offset:1024 lds\n\t"
      "buffer_load_dwordx4 v213, s[16:19], s51 offen offset:2048 lds\n\t"
      "s_lshl_b32 s67, s68, 18\n\t"
      "s_bfe_u32 s51, s62, 0xc0000\n\t"
      "s_and_b32 s67, s67, 0xfff00000\n\t"
      "s_or_b32 s67, s67, s51\n\t"
      "s_cmp_lg_u32 s41, s43\n\t"
      "s_cbranch_scc1 .Lw0pb_%=\n\t"
      "s_or_b32 s77, s77, s67\n\t"
      "v_mov_b32_e32 v241, s63\n\t"
      "v_mov_b32_e32 v240, s77\n\t"
      "buffer_store_dwordx2 v[240:241], v244, s[24:27], 0 offen sc1\n\t"
      ".Lw0pb_%=:\n\t"
      "s_waitcnt vmcnt(15)\n\t"
      "v_add_u32_e32 v221, 0x24000, v214\n\t"
      "ds_read_b64_tr_b16 a[24:25], v221 offset:0\n\t"
      "ds_read_b64_tr_b16 a[28:29], v221 offset:8\n\t"
      "ds_read_b64_tr_b16 a[32:33], v221 offset:64\n\t"
      "ds_read_b64_tr_b16 a[36:37], v221 offset:72\n\t"
      "ds_read_b64_tr_b16 a[40:41], v221 offset:128\n\t"
      "ds_read_b64_tr_b16 a[44:45], v221 offset:136\n\t"
      "ds_read_b128 v[144:147], v197 offset:0\n\t"
      "s_waitcnt lgkmcnt(8)\n\t"
      "v_mfma_f32_16x16x32_bf16 v[80:83], a[0:3], v[140:143], v[8:11]\n\t"
      "s_add_u32 m0, s41, 0x21000\n\t"
      "s_mov_b32 s51, 0x18000\n\t"
      "buffer_load_dwordx4 v211, s[16:19], s51 offen lds\n\t"
      "buffer_load_dwordx4 v212, s[16:19], s51 offen offset:1024 lds\n\t"
      "v_mfma_f32_16x16x32_bf16 v[84:87], a[4:7], v[140:143], v[12:15]\n\t"
      "buffer_load_dwordx4 v213, s[16:19], s51 offen offset:2048 lds\n\t"
      "v_mfma_f32_16x16x32_bf16 v[88:91], a[8:11], v[140:143], v[16:19]\n\t"
      "s_sub_u32 s67, s67, 0x20000000\n\t"
      "s_andn2_b32 s68, s68, 3\n\t"
      "s_sub_u32 s68, s68, s52\n\t"
      "s_waitcnt vmcnt(15)\n\t"
      "v_add_u32_e32 v221, 0x12000, v214\n\t"
      "ds_read_b64_tr_b16 a[26:27], v221 offset:0\n\t"
      "v_mfma_f32_16x16x32_bf16 v[92:95], a[12:15], v[140:143], v[20:23]\n\t"
      "ds_read_b64_tr_b16 a[30:31], v221 offset:8\n\t"
      "ds_read_b64_tr_b16 a[34:35], v221 offset:64\n\t"
      "v_mfma_f32_16x16x32_bf16 v[96:99], a[16:19], v[140:143], v[24:27]\n\t"
      "ds_read_b64_tr_b16 a[38:39], v221 offset:72\n\t"
      "ds_read_b64_tr_b16 a[42:43], v221 offset:128\n\t"
      "v_mfma_f32_16x16x32_bf16 v[100:103], a[20:23], v[140:143], v[28:31]\n\t"
      "ds_read_b64_tr_b16 a[46:47], v221 offset:136\n\t"
      "ds_read_b128 v[156:159], v197 offset:12288\n\t"
      "s_waitcnt lgkmcnt(8)\n\t"
      "v_mfma_f32_16x16x32_bf16 v[104:107], a[0:3], v[152:155], v[8:11]\n\t"
      "s_add_u32 m0, s41, 0x24000\n\t"
      "s_mov_b32 s51, 0x18c00\n\t"
      "buffer_load_dwordx4 v211, s[16:19], s51 offen lds\n\t"
      "v_mfma_f32_16x16x32_bf16 v[108:111], a[4:7], v[152:155], v[12:15]\n\t"
      "buffer_load_dwordx4 v212, s[16:19], s51 offen offset:1024 lds\n\t"
      "v_mfma_f32_16x16x32_bf16 v[112:115], a[8:11], v[152:155], v[16:19]\n\t"
      "buffer_load_dwordx4 v213, s[16:19], s51 offen offset:2048 lds\n\t"
      "s_waitcnt vmcnt(15)\n\t"
      "v_add_u32_e32 v221, 0x15000, v214\n\t"
      "ds_read_b64_tr_b16 a[48:49], v221 offset:0\n\t"
      "v_mfma_f32_16x16x32_bf16 v[116:119], a[12:15], v[152:155], v[20:23]\n\t"
      "ds_read_b64_tr_b16 a[52:53], v221 offset:8\n\t"
      "ds_read_b64_tr_b16 a[56:57], v221 offset:64\n\t"
      "v_mfma_f32_16x16x32_bf16 v[120:123], a[16:19], v[152:155], v[24:27]\n\t"
      "ds_read_b64_tr_b16 a[60:61], v221 offset:72\n\t"
      "ds_read_b64_tr_b16 a[64:65], v221 offset:128\n\t"
      "v_mfma_f32_16x16x32_bf16 v[132:135], a[20:23], v[152:155], v[28:31]\n\t"
      "ds_read_b64_tr_b16 a[68:69], v221 offset:136\n\t"
      "ds_read_b128 v[148:151], v198 offset:0\n\t"
      "s_waitcnt lgkmcnt(8)\n\t"
      "v_mfma_f32_16x16x32_bf16 v[80:83], a[24:27], v[144:147], v[80:83]\n\t"
      "s_add_u32 m0, s41, 0x12000\n\t"
      "s_mov_b32 s51, 0x1e000\n\t"
      "buffer_load_dwordx4 v211, s[16:19], s51 offen lds\n\t"
      "v_mfma_f32_16x16x32_bf16 v[84:87], a[28:31], v[144:147], v[84:87]\n\t"
      "buffer_load_dwordx4 v212, s[16:19], s51 offen offset:1024 lds\n\t"
      "v_mfma_f32_16x16x32_bf16 v[88:91], a[32:35], v[144:147], v[88:91]\n\t"
      "buffer_load_dwordx4 v213, s[16:19], s51 offen offset:2048 lds\n\t"
      "s_waitcnt vmcnt(15)\n\t"
      "v_add_u32_e32 v221, 0x18000, v214\n\t"
      "ds_read_b64_tr_b16 a[50:51], v221 offset:0\n\t"
      "v_mfma_f32_16x16x32_bf16 v[92:95], a[36:39], v[144:147], v[92:95]\n\t"
      "ds_read_b64_tr_b16 a[54:55], v221 offset:8\n\t"
      "ds_read_b64_tr_b16 a[58:59], v221 offset:64\n\t"
      "v_mfma_f32_16x16x32_bf16 v[96:99], a[40:43], v[144:147], v[96:99]\n\t"
      "ds_read_b64_tr_b16 a[62:63], v221 offset:72\n\t"
      "ds_read_b64_tr_b16 a[66:67], v221 offset:128\n\t"
      "v_mfma_f32_16x16x32_bf16 v[100:103], a[44:47], v[144:147], v[100:103]\n\t"
      "ds_read_b64_tr_b16 a[70:71], v221 offset:136\n\t"
      "ds_read_b128 v[160:163], v198 offset:12288\n\t"
      "s_waitcnt lgkmcnt(8)\n\t"
      "v_mfma_f32_16x16x32_bf16 v[104:107], a[24:27], v[156:159], v[104:107]\n\t"
      "s_add_u32 m0, s41, 0x15000\n\t"
      "s_mov_b32 s51, 0x1ec00\n\t"
      "buffer_load_dwordx4 v211, s[16:19], s51 offen lds\n\t"
      "v_mfma_f32_16x16x32_bf16 v[108:111], a[28:31], v[156:159], v[108:111]\n\t"
      "buffer_load_dwordx4 v212, s[16:19], s51 offen offset:1024 lds\n\t"
      "v_mfma_f32_16x16x32_bf16 v[112:115], a[32:35], v[156:159], v[112:115]\n\t"
      "buffer_load_dwordx4 v213, s[16:19], s51 offen offset:2048 lds\n\t"
      "s_waitcnt vmcnt(15)\n\t"
      "v_add_u32_e32 v221, 0x1b000, v214\n\t"
      "ds_read_b64_tr_b16 a[72:73], v221 offset:0\n\t"
      "v_mfma_f32_16x16x32_bf16 v[116:119], a[36:39], v[156:159], v[116:119]\n\t"
      "ds_read_b64_tr_b16 a[76:77], v221 offset:8\n\t"
      "ds_read_b64_tr_b16 a[80:81], v221 offset:64\n\t"
      "v_mfma_f32_16x16x32_bf16 v[120:123], a[40:43], v[156:159], v[120:123]\n\t"
      "ds_read_b64_tr_b16 a[84:85], v221 offset:72\n\t"
      "ds_read_b64_tr_b16 a[88:89], v221 offset:128\n\t"
      "v_mfma_f32_16x16x32_bf16 v[132:135], a[44:47], v[156:159], v[132:135]\n\t"
      "ds_read_b64_tr_b16 a[92:93], v221 offset:136\n\t"
      "ds_read_b128 v[140:143], v199 offset:0\n\t"
      "s_waitcnt lgkmcnt(8)\n\t"
      "v_mfma_f32_16x16x32_bf16 v[80:83], a[48:51], v[148:151], v[80:83]\n\t"
      "s_add_u32 m0, s41, 0x18000\n\t"
      "s_mov_b32 s51, 0x24000\n\t"
      "buffer_load_dwordx4 v211, s[16:19], s51 offen lds\n\t"
      "v_mfma_f32_16x16x32_bf16 v[84:87], a[52:55], v[148:151], v[84:87]\n\t"
      "buffer_load_dwordx4 v212, s[16:19], s51 offen offset:1024 lds\n\t"
      "v_mfma_f32_16x16x32_bf16 v[88:91], a[56:59], v[148:151], v[88:91]\n\t"
      "buffer_load_dwordx4 v213, s[16:19], s51 offen offset:2048 lds\n\t"
      "s_waitcnt vmcnt(15)\n\t"
      "v_add_u32_e32 v221, 0x1e000, v214\n\t"
      "ds_read_b64_tr_b16 a[74:75], v221 offset:0\n\t"
      "v_mfma_f32_16x16x32_bf16 v[92:95], a[60:63], v[148:151], v[92:95]\n\t"
      "ds_read_b64_tr_b16 a[78:79], v221 offset:8\n\t"
      "ds_read_b64_tr_b16 a[82:83], v221 offset:64\n\t"
      "v_mfma_f32_16x16x32_bf16 v[96:99], a[64:67], v[148:151], v[96:99]\n\t"
      "ds_read_b64_tr_b16 a[86:87], v221 offset:72\n\t"
      "ds_read_b64_tr_b16 a[90:91], v221 offset:128\n\t"
      "v_mfma_f32_16x16x32_bf16 v[100:103], a[68:71], v[148:151], v[100:103]\n\t"
      "ds_read_b64_tr_b16 a[94:95], v221 offset:136\n\t"
      "ds_read_b128 v[152:155], v199 offset:12288\n\t"
      "s_waitcnt lgkmcnt(8)\n\t"
      "v_mfma_f32_16x16x32_bf16 v[104:107], a[48:51], v[160:163], v[104:107]\n\t"
      "s_add_u32 m0, s41, 0x1b000\n\t"
      "s_mov_b32 s51, 0x24c00\n\t"
      "buffer_load_dwordx4 v211, s[16:19], s51 offen lds\n\t"
      "v_mfma_f32_16x16x32_bf16 v[108:111], a[52:55], v[160:163], v[108:111]\n\t"
      "buffer_load_dwordx4 v212, s[16:19], s51 offen offset:1024 lds\n\t"
      "v_mfma_f32_16x16x32_bf16 v[112:115], a[56:59], v[160:163], v[112:115]\n\t"
      "buffer_load_dwordx4 v213, s[16:19], s51 offen offset:2048 lds\n\t"
      "s_waitcnt vmcnt(15)\n\t"
      "v_add_u32_e32 v221, 0x21000, v214\n\t"
      "ds_read_b64_tr_b16 a[96:97], v221 offset:0\n\t"
      "v_mfma_f32_16x16x32_bf16 v[116:119], a[60:63], v[160:163], v[116:119]\n\t"
      "ds_read_b64_tr_b16 a[100:101], v221 offset:8\n\t"
      "ds_read_b64_tr_b16 a[104:105], v221 offset:64\n\t"
      "v_mfma_f32_16x16x32_bf16 v[120:123], a[64:67], v[160:163], v[120:123]\n\t"
      "ds_read_b64_tr_b16 a[108:109], v221 offset:72\n\t"
      "ds_read_b64_tr_b16 a[112:113], v221 offset:128\n\t"
      "v_mfma_f32_16x16x32_bf16 v[132:135], a[68:71], v[160:163], v[132:135]\n\t"
      "ds_read_b64_tr_b16 a[116:117], v221 offset:136\n\t"
      "ds_read_b128 v[144:147], v197 offset:256\n\t"
      "s_waitcnt lgkmcnt(8)\n\t"
      "v_mfma_f32_16x16x32_bf16 v[80:83], a[72:75], v[140:143], v[80:83]\n\t"
      "s_add_u32 m0, s41, 0x1e000\n\t"
      "s_mov_b32 s51, 0x2a000\n\t"
      "buffer_load_dwordx4 v211, s[16:19], s51 offen lds\n\t"
      "v_mfma_f32_16x16x32_bf16 v[84:87], a[76:79], v[140:143], v[84:87]\n\t"
      "buffer_load_dwordx4 v212, s[16:19], s51 offen offset:1024 lds\n\t"
      "v_mfma_f32_16x16x32_bf16 v[88:91], a[80:83], v[140:143], v[88:91]\n\t"
      "buffer_load_dwordx4 v213, s[16:19], s51 offen offset:2048 lds\n\t"
      "s_waitcnt vmcnt(15)\n\t"
      "v_add_u32_e32 v221, 0x24000, v214\n\t"
      "ds_read_b64_tr_b16 a[98:99], v221 offset:0\n\t"
      "v_mfma_f32_16x16x32_bf16 v[92:95], a[84:87], v[140:143], v[92:95]\n\t"
      "ds_read_b64_tr_b16 a[102:103], v221 offset:8\n\t"
      "ds_read_b64_tr_b16 a[106:107], v221 offset:64\n\t"
      "v_mfma_f32_16x16x32_bf16 v[96:99], a[88:91], v[140:143], v[96:99]\n\t"
      "ds_read_b64_tr_b16 a[110:111], v221 offset:72\n\t"
      "ds_read_b64_tr_b16 a[114:115], v221 offset:128\n\t"
      "v_mfma_f32_16x16x32_bf16 v[100:103], a[92:95], v[140:143], v[100:103]\n\t"
      "ds_read_b64_tr_b16 a[118:119], v221 offset:136\n\t"
      "ds_read_b128 v[156:159], v197 offset:12544\n\t"
      "s_waitcnt lgkmcnt(8)\n\t"
      "v_mfma_f32_16x16x32_bf16 v[104:107], a[72:75], v[152:155], v[104:107]\n\t"
      "s_add_u32 m0, s41, 0x21000\n\t"
      "s_mov_b32 s51, 0x2ac00\n\t"
      "buffer_load_dwordx4 v211, s[16:19], s51 offen lds\n\t"
      "v_mfma_f32_16x16x32_bf16 v[108:111], a[76:79], v[152:155], v[108:111]\n\t"
      "buffer_load_dwordx4 v212, s[16:19], s51 offen offset:1024 lds\n\t"
      "v_mfma_f32_16x16x32_bf16 v[112:115], a[80:83], v[152:155], v[112:115]\n\t"
      "buffer_load_dwordx4 v213, s[16:19], s51 offen offset:2048 lds\n\t"
      "s_waitcnt vmcnt(15)\n\t"
      "v_add_u32_e32 v221, 0x12000, v214\n\t"
      "ds_read_b64_tr_b16 a[120:121], v221 offset:0\n\t"
      "v_mfma_f32_16x16x32_bf16 v[116:119], a[84:87], v[152:155], v[116:119]\n\t"
      "ds_read_b64_tr_b16 a[124:125], v221 offset:8\n\t"
      "ds_read_b64_tr_b16 a[128:129], v221 offset:64\n\t"
      "v_mfma_f32_16x16x32_bf16 v[120:123], a[88:91], v[152:155], v[120:123]\n\t"
      "ds_read_b64_tr_b16 a[132:133], v221 offset:72\n\t"
      "ds_read_b64_tr_b16 a[136:137], v221 offset:128\n\t"
      "v_mfma_f32_16x16x32_bf16 v[132:135], a[92:95], v[152:155], v[132:135]\n\t"
      "ds_read_b64_tr_b16 a[140:141], v221 offset:136\n\t"
      "ds_read_b128 v[148:151], v198 offset:256\n\t"
      "s_waitcnt lgkmcnt(8)\n\t"
      "v_mfma_f32_16x16x32_bf16 v[80:83], a[96:99], v[144:147], v[80:83]\n\t"
      "s_add_u32 m0, s41, 0x24000\n\t"
      "s_mov_b32 s51, 0x30000\n\t"
      "buffer_load_dwordx4 v211, s[16:19], s51 offen lds\n\t"
      "v_mfma_f32_16x16x32_bf16 v[84:87], a[100:103], v[144:147], v[84:87]\n\t"
      "buffer_load_dwordx4 v212, s[16:19], s51 offen offset:1024 lds\n\t"
      "v_mfma_f32_16x16x32_bf16 v[88:91], a[104:107], v[144:147], v[88:91]\n\t"
      "buffer_load_dwordx4 v213, s[16:19], s51 offen offset:2048 lds\n\t"
      "s_waitcnt vmcnt(15)\n\t"
      "v_add_u32_e32 v221, 0x15000, v214\n\t"
      "ds_read_b64_tr_b16 a[122:123], v221 offset:0\n\t"
      "v_mfma_f32_16x16x32_bf16 v[92:95], a[108:111], v[144:147], v[92:95]\n\t"
      "ds_read_b64_tr_b16 a[126:127], v221 offset:8\n\t"
      "ds_read_b64_tr_b16 a[130:131], v221 offset:64\n\t"
      "v_mfma_f32_16x16x32_bf16 v[96:99], a[112:115], v[144:147], v[96:99]\n\t"
      "ds_read_b64_tr_b16 a[134:135], v221 offset:72\n\t"
      "ds_read_b64_tr_b16 a[138:139], v221 offset:128\n\t"
      "v_mfma_f32_16x16x32_bf16 v[100:103], a[116:119], v[144:147], v[100:103]\n\t"
      "ds_read_b64_tr_b16 a[142:143], v221 offset:136\n\t"
      "ds_read_b128 v[160:163], v198 offset:12544\n\t"
      "s_waitcnt lgkmcnt(8)\n\t"
      "v_mfma_f32_16x16x32_bf16 v[104:107], a[96:99], v[156:159], v[104:107]\n\t"
      "s_add_u32 m0, s41, 0x12000\n\t"
      "s_mov_b32 s51, 0x30c00\n\t"
      "buffer_load_dwordx4 v211, s[16:19], s51 offen lds\n\t"
      "v_mfma_f32_16x16x32_bf16 v[108:111], a[100:103], v[156:159], v[108:111]\n\t"
      "buffer_load_dwordx4 v212, s[16:19], s51 offen offset:1024 lds\n\t"
      "v_mfma_f32_16x16x32_bf16 v[112:115], a[104:107], v[156:159], v[112:115]\n\t"
      "buffer_load_dwordx4 v213, s[16:19], s51 offen offset:2048 lds\n\t"
      "s_waitcnt vmcnt(15)\n\t"
      "v_add_u32_e32 v221, 0x18000, v214\n\t"
      "ds_read_b64_tr_b16 a[144:145], v221 offset:0\n\t"
      "v_mfma_f32_16x16x32_bf16 v[116:119], a[108:111], v[156:159], v[116:119]\n\t"
      "ds_read_b64_tr_b16 a[148:149], v221 offset:8\n\t"
      "ds_read_b64_tr_b16 a[152:153], v221 offset:64\n\t"
      "v_mfma_f32_16x16x32_bf16 v[120:123], a[112:115], v[156:159], v[120:123]\n\t"
      "ds_read_b64_tr_b16 a[156:157], v221 offset:72\n\t"
      "ds_read_b64_tr_b16 a[160:161], v221 offset:128\n\t"
      "v_mfma_f32_16x16x32_bf16 v[132:135], a[116:119], v[156:159], v[132:135]\n\t"
      "ds_read_b64_tr_b16 a[164:165], v221 offset:136\n\t"
      "ds_read_b128 v[140:143], v199 offset:256\n\t"
      "s_waitcnt lgkmcnt(8)\n\t"
      "v_mfma_f32_16x16x32_bf16 v[80:83], a[120:123], v[148:151], v[80:83]\n\t"
      "s_add_u32 m0, s41, 0x15000\n\t"
      "s_mov_b32 s51, 0x36000\n\t"
      "buffer_load_dwordx4 v211, s[16:19], s51 offen lds\n\t"
      "v_mfma_f32_16x16x32_bf16 v[84:87], a[124:127], v[148:151], v[84:87]\n\t"
      "buffer_load_dwordx4 v212, s[16:19], s51 offen offset:1024 lds\n\t"
      "v_mfma_f32_16x16x32_bf16 v[88:91], a[128:131], v[148:151], v[88:91]\n\t"
      "buffer_load_dwordx4 v213, s[16:19], s51 offen offset:2048 lds\n\t"
      "s_waitcnt vmcnt(15)\n\t"
      "v_add_u32_e32 v221, 0x1b000, v214\n\t"
      "ds_read_b64_tr_b16 a[146:147], v221 offset:0\n\t"
      "v_mfma_f32_16x16x32_bf16 v[92:95], a[132:135], v[148:151], v[92:95]\n\t"
      "ds_read_b64_tr_b16 a[150:151], v221 offset:8\n\t"
      "ds_read_b64_tr_b16 a[154:155], v221 offset:64\n\t"
      "v_mfma_f32_16x16x32_bf16 v[96:99], a[136:139], v[148:151], v[96:99]\n\t"
      "ds_read_b64_tr_b16 a[158:159], v221 offset:72\n\t"
      "ds_read_b64_tr_b16 a[162:163], v221 offset:128\n\t"
      "v_mfma_f32_16x16x32_bf16 v[100:103], a[140:143], v[148:151], v[100:103]\n\t"
      "ds_read_b64_tr_b16 a[166:167], v221 offset:136\n\t"
      "ds_read_b128 v[152:155], v199 offset:12544\n\t"
      "s_waitcnt lgkmcnt(8)\n\t"
      "v_mfma_f32_16x16x32_bf16 v[104:107], a[120:123], v[160:163], v[104:107]\n\t"
      "s_add_u32 m0, s41, 0x18000\n\t"
      "s_mov_b32 s51, 0x36c00\n\t"
      "buffer_load_dwordx4 v211, s[16:19], s51 offen lds\n\t"
      "v_mfma_f32_16x16x32_bf16 v[108:111], a[124:127], v[160:163], v[108:111]\n\t"
      "buffer_load_dwordx4 v212, s[16:19], s51 offen offset:1024 lds\n\t"
      "v_mfma_f32_16x16x32_bf16 v[112:115], a[128:131], v[160:163], v[112:115]\n\t"
      "buffer_load_dwordx4 v213, s[16:19], s51 offen offset:2048 lds\n\t"
      "s_waitcnt vmcnt(15)\n\t"
      "v_add_u32_e32 v221, 0x1e000, v214\n\t"
      "ds_read_b64_tr_b16 a[168:169], v221 offset:0\n\t"
      "v_mfma_f32_16x16x32_bf16 v[116:119], a[132:135], v[160:163], v[116:119]\n\t"
      "ds_read_b64_tr_b16 a[172:173], v221 offset:8\n\t"
      "ds_read_b64_tr_b16 a[176:177], v221 offset:64\n\t"
      "v_mfma_f32_16x16x32_bf16 v[120:123], a[136:139], v[160:163], v[120:123]\n\t"
      "ds_read_b64_tr_b16 a[180:181], v221 offset:72\n\t"
      "ds_read_b64_tr_b16 a[184:185], v221 offset:128\n\t"
      "v_mfma_f32_16x16x32_bf16 v[132:135], a[140:143], v[160:163], v[132:135]\n\t"
      "ds_read_b64_tr_b16 a[188:189], v221 offset:136\n\t"
      "ds_read_b128 v[144:147], v196 offset:256\n\t"
      "s_waitcnt lgkmcnt(8)\n\t"
      "v_mfma_f32_16x16x32_bf16 v[80:83], a[144:147], v[140:143], v[80:83]\n\t"
      "s_add_u32 m0, s41, 0x1b000\n\t"
      "s_mov_b32 s51, 0x3c000\n\t"
      "buffer_load_dwordx4 v211, s[16:19], s51 offen lds\n\t"
      "v_mfma_f32_16x16x32_bf16 v[84:87], a[148:151], v[140:143], v[84:87]\n\t"
      "buffer_load_dwordx4 v212, s[16:19], s51 offen offset:1024 lds\n\t"
      "v_mfma_f32_16x16x32_bf16 v[88:91], a[152:155], v[140:143], v[88:91]\n\t"
      "buffer_load_dwordx4 v213, s[16:19], s51 offen offset:2048 lds\n\t"
      "s_waitcnt vmcnt(15)\n\t"
      "v_add_u32_e32 v221, 0x21000, v214\n\t"
      "ds_read_b64_tr_b16 a[170:171], v221 offset:0\n\t"
      "v_mfma_f32_16x16x32_bf16 v[92:95], a[156:159], v[140:143], v[92:95]\n\t"
      "ds_read_b64_tr_b16 a[174:175], v221 offset:8\n\t"
      "ds_read_b64_tr_b16 a[178:179], v221 offset:64\n\t"
      "v_mfma_f32_16x16x32_bf16 v[96:99], a[160:163], v[140:143], v[96:99]\n\t"
      "ds_read_b64_tr_b16 a[182:183], v221 offset:72\n\t"
      "ds_read_b64_tr_b16 a[186:187], v221 offset:128\n\t"
      "v_mfma_f32_16x16x32_bf16 v[100:103], a[164:167], v[140:143], v[100:103]\n\t"
      "ds_read_b64_tr_b16 a[190:191], v221 offset:136\n\t"
      "ds_read_b128 v[156:159], v196 offset:12544\n\t"
      "s_waitcnt lgkmcnt(8)\n\t"
      "v_mfma_f32_16x16x32_bf16 v[104:107], a[144:147], v[152:155], v[104:107]\n\t"
      "s_add_u32 m0, s41, 0x1e000\n\t"
      "s_mov_b32 s51, 0x3cc00\n\t"
      "buffer_load_dwordx4 v211, s[16:19], s51 offen lds\n\t"
      "v_mfma_f32_16x16x32_bf16 v[108:111], a[148:151], v[152:155], v[108:111]\n\t"
      "buffer_load_dwordx4 v212, s[16:19], s51 offen offset:1024 lds\n\t"
      "v_mfma_f32_16x16x32_bf16 v[112:115], a[152:155], v[152:155], v[112:115]\n\t"
      "buffer_load_dwordx4 v213, s[16:19], s51 offen offset:2048 lds\n\t"
      "s_waitcnt vmcnt(15)\n\t"
      "v_add_u32_e32 v221, 0x24000, v214\n\t"
      "ds_read_b64_tr_b16 a[192:193], v221 offset:0\n\t"
      "v_mfma_f32_16x16x32_bf16 v[116:119], a[156:159], v[152:155], v[116:119]\n\t"
      "ds_read_b64_tr_b16 a[196:197], v221 offset:8\n\t"
      "ds_read_b64_tr_b16 a[200:201], v221 offset:64\n\t"
      "v_mfma_f32_16x16x32_bf16 v[120:123], a[160:163], v[152:155], v[120:123]\n\t"
      "ds_read_b64_tr_b16 a[204:205], v221 offset:72\n\t"
      "ds_read_b64_tr_b16 a[208:209], v221 offset:128\n\t"
      "v_mfma_f32_16x16x32_bf16 v[132:135], a[164:167], v[152:155], v[132:135]\n\t"
      "ds_read_b64_tr_b16 a[212:213], v221 offset:136\n\t"
      "ds_read_b128 v[148:151], v198 offset:512\n\t"
      "s_waitcnt lgkmcnt(8)\n\t"
      "v_mfma_f32_16x16x32_bf16 v[80:83], a[168:171], v[144:147], v[80:83]\n\t"
      "s_add_u32 m0, s41, 0x21000\n\t"
      "s_mov_b32 s51, 0x42000\n\t"
      "buffer_load_dwordx4 v211, s[16:19], s51 offen lds\n\t"
      "v_mfma_f32_16x16x32_bf16 v[84:87], a[172:175], v[144:147], v[84:87]\n\t"
      "buffer_load_dwordx4 v212, s[16:19], s51 offen offset:1024 lds\n\t"
      "v_mfma_f32_16x16x32_bf16 v[88:91], a[176:179], v[144:147], v[88:91]\n\t"
      "buffer_load_dwordx4 v213, s[16:19], s51 offen offset:2048 lds\n\t"
      "s_waitcnt vmcnt(15)\n\t"
      "v_add_u32_e32 v221, 0x12000, v214\n\t"
      "ds_read_b64_tr_b16 a[194:195], v221 offset:0\n\t"
      "v_mfma_f32_16x16x32_bf16 v[92:95], a[180:183], v[144:147], v[92:95]\n\t"
      "ds_read_b64_tr_b16 a[198:199], v221 offset:8\n\t"
      "ds_read_b64_tr_b16 a[202:203], v221 offset:64\n\t"
      "v_mfma_f32_16x16x32_bf16 v[96:99], a[184:187], v[144:147], v[96:99]\n\t"
      "ds_read_b64_tr_b16 a[206:207], v221 offset:72\n\t"
      "ds_read_b64_tr_b16 a[210:211], v221 offset:128\n\t"
      "v_mfma_f32_16x16x32_bf16 v[100:103], a[188:191], v[144:147], v[100:103]\n\t"
      "ds_read_b64_tr_b16 a[214:215], v221 offset:136\n\t"
      "ds_read_b128 v[160:163], v198 offset:12800\n\t"
      "s_waitcnt lgkmcnt(8)\n\t"
      "v_mfma_f32_16x16x32_bf16 v[104:107], a[168:171], v[156:159], v[104:107]\n\t"
      "s_add_u32 m0, s41, 0x24000\n\t"
      "s_mov_b32 s51, 0x42c00\n\t"
      "buffer_load_dwordx4 v211, s[16:19], s51 offen lds\n\t"
      "v_mfma_f32_16x16x32_bf16 v[108:111], a[172:175], v[156:159], v[108:111]\n\t"
      "buffer_load_dwordx4 v212, s[16:19], s51 offen offset:1024 lds\n\t"
      "v_mfma_f32_16x16x32_bf16 v[112:115], a[176:179], v[156:159], v[112:115]\n\t"
      "buffer_load_dwordx4 v213, s[16:19], s51 offen offset:2048 lds\n\t"
      "s_waitcnt vmcnt(15)\n\t"
      "v_add_u32_e32 v221, 0x15000, v214\n\t"
      "ds_read_b64_tr_b16 a[216:217], v221 offset:0\n\t"
      "v_mfma_f32_16x16x32_bf16 v[116:119], a[180:183], v[156:159], v[116:119]\n\t"
      "ds_read_b64_tr_b16 a[220:221], v221 offset:8\n\t"
      "ds_read_b64_tr_b16 a[224:225], v221 offset:64\n\t"
      "v_mfma_f32_16x16x32_bf16 v[120:123], a[184:187], v[156:159], v[120:123]\n\t"
      "ds_read_b64_tr_b16 a[228:229], v221 offset:72\n\t"
      "ds_read_b64_tr_b16 a[232:233], v221 offset:128\n\t"
      "v_mfma_f32_16x16x32_bf16 v[132:135], a[188:191], v[156:159], v[132:135]\n\t"
      "ds_read_b64_tr_b16 a[236:237], v221 offset:136\n\t"
      "ds_read_b128 v[140:143], v199 offset:512\n\t"
      "s_waitcnt lgkmcnt(8)\n\t"
      "v_mfma_f32_16x16x32_bf16 v[80:83], a[192:195], v[148:151], v[80:83]\n\t"
      "s_cmp_eq_u32 s94, 0\n\t"
      "s_cbranch_scc0 .Lc1_%=\n\t"
      "s_add_u32 m0, s41, 0x6000\n\t"
      "s_add_u32 s51, s40, 0x600000\n\t"
      "buffer_load_dwordx4 v208, s[20:23], s51 offen lds\n\t"
      "buffer_load_dwordx4 v208, s[20:23], s51 offen offset:1024 lds\n\t"
      "buffer_load_dwordx4 v208, s[20:23], s51 offen offset:2048 lds\n\t"
      ".Lc1_%=:\n\t"
      "v_mfma_f32_16x16x32_bf16 v[84:87], a[196:199], v[148:151], v[84:87]\n\t"
      "v_mfma_f32_16x16x32_bf16 v[88:91], a[200:203], v[148:151], v[88:91]\n\t"
      "s_waitcnt vmcnt(15)\n\t"
      "s_cmp_eq_u32 s94, 1\n\t"
      "s_cbranch_scc0 .Lwpw19_1_%=\n\t"
      "s_waitcnt vmcnt(12)\n\t"
      ".Lwpw19_1_%=:\n\t"
      "v_add_u32_e32 v221, 0x18000, v214\n\t"
      "ds_read_b64_tr_b16 a[218:219], v221 offset:0\n\t"
      "v_mfma_f32_16x16x32_bf16 v[92:95], a[204:207], v[148:151], v[92:95]\n\t"
      "ds_read_b64_tr_b16 a[222:223], v221 offset:8\n\t"
      "ds_read_b64_tr_b16 a[226:227], v221 offset:64\n\t"
      "v_mfma_f32_16x16x32_bf16 v[96:99], a[208:211], v[148:151], v[96:99]\n\t"
      "ds_read_b64_tr_b16 a[230:231], v221 offset:72\n\t"
      "ds_read_b64_tr_b16 a[234:235], v221 offset:128\n\t"
      "v_mfma_f32_16x16x32_bf16 v[100:103], a[212:215], v[148:151], v[100:103]\n\t"
      "ds_read_b64_tr_b16 a[238:239], v221 offset:136\n\t"
      "ds_read_b128 v[152:155], v199 offset:12800\n\t"
      "s_waitcnt lgkmcnt(14)\n\t"
      "v_mfma_f32_16x16x32_bf16 v[104:107], a[192:195], v[160:163], v[104:107]\n\t"
      "s_cmp_eq_u32 s94, 0\n\t"
      "s_cbranch_scc0 .Lc2_%=\n\t"
      "s_add_u32 m0, s41, 0x9000\n\t"
      "s_add_u32 s51, s40, 0x600000\n\t"
      "buffer_load_dwordx4 v209, s[20:23], s51 offen lds\n\t"
      "buffer_load_dwordx4 v209, s[20:23], s51 offen offset:1024 lds\n\t"
      "buffer_load_dwordx4 v209, s[20:23], s51 offen offset:2048 lds\n\t"
      ".Lc2_%=:\n\t"
      "v_mfma_f32_16x16x32_bf16 v[108:111], a[196:199], v[160:163], v[108:111]\n\t"
      "v_mfma_f32_16x16x32_bf16 v[112:115], a[200:203], v[160:163], v[112:115]\n\t"
      "s_waitcnt vmcnt(15)\n\t"
      "s_cmp_eq_u32 s94, 1\n\t"
      "s_cbranch_scc0 .Lwpw20_1_%=\n\t"
      "s_waitcnt vmcnt(9)\n\t"
      ".Lwpw20_1_%=:\n\t"
      "v_add_u32_e32 v221, 0x1b000, v214\n\t"
      "ds_read_b64_tr_b16 a[240:241], v221 offset:0\n\t"
      "v_mfma_f32_16x16x32_bf16 v[116:119], a[204:207], v[160:163], v[116:119]\n\t"
      "ds_read_b64_tr_b16 a[244:245], v221 offset:8\n\t"
      "ds_read_b64_tr_b16 a[248:249], v221 offset:64\n\t"
      "v_mfma_f32_16x16x32_bf16 v[120:123], a[208:211], v[160:163], v[120:123]\n\t"
      "ds_read_b64_tr_b16 a[252:253], v221 offset:72\n\t"
      "ds_read_b64_tr_b16 v[164:165], v221 offset:128\n\t"
      "v_mfma_f32_16x16x32_bf16 v[132:135], a[212:215], v[160:163], v[132:135]\n\t"
      "ds_read_b64_tr_b16 v[168:169], v221 offset:136\n\t"
      "ds_read_b128 v[144:147], v196 offset:512\n\t"
      "s_waitcnt lgkmcnt(8)\n\t"
      "v_mfma_f32_16x16x32_bf16 v[80:83], a[216:219], v[140:143], v[80:83]\n\t"
      "s_cmp_eq_u32 s94, 0\n\t"
      "s_cbranch_scc0 .Lc3_%=\n\t"
      "s_add_u32 m0, s41, 0xc000\n\t"
      "s_add_u32 s51, s40, 0xc00000\n\t"
      "buffer_load_dwordx4 v208, s[20:23], s51 offen lds\n\t"
      "buffer_load_dwordx4 v208, s[20:23], s51 offen offset:1024 lds\n\t"
      "buffer_load_dwordx4 v208, s[20:23], s51 offen offset:2048 lds\n\t"
      ".Lc3_%=:\n\t"
      "v_mfma_f32_16x16x32_bf16 v[84:87], a[220:223], v[140:143], v[84:87]\n\t"
      "s_cmp_eq_u32 s94, 1\n\t"
      "s_cbranch_scc0 .Lc4_%=\n\t"
      "s_add_u32 m0, s41, 0xc000\n\t"
      "s_add_u32 s51, s40, 0xc00000\n\t"
      "buffer_load_dwordx4 v208, s[20:23], s51 offen lds\n\t"
      "buffer_load_dwordx4 v208, s[20:23], s51 offen offset:1024 lds\n\t"
      "buffer_load_dwordx4 v208, s[20:23], s51 offen offset:2048 lds\n\t"
      ".Lc4_%=:\n\t"
      "v_mfma_f32_16x16x32_bf16 v[88:91], a[224:227], v[140:143], v[88:91]\n\t"
      "s_waitcnt vmcnt(15)\n\t"
      "s_cmp_eq_u32 s94, 1\n\t"
      "s_cbranch_scc0 .Lwpw21_1_%=\n\t"
      "s_waitcnt vmcnt(9)\n\t"
      ".Lwpw21_1_%=:\n\t"
      "v_add_u32_e32 v221, 0x1e000, v214\n\t"
      "ds_read_b64_tr_b16 a[242:243], v221 offset:0\n\t"
      "v_mfma_f32_16x16x32_bf16 v[92:95], a[228:231], v[140:143], v[92:95]\n\t"
      "ds_read_b64_tr_b16 a[246:247], v221 offset:8\n\t"
      "ds_read_b64_tr_b16 a[250:251], v221 offset:64\n\t"
      "v_mfma_f32_16x16x32_bf16 v[96:99], a[232:235], v[140:143], v[96:99]\n\t"
      "ds_read_b64_tr_b16 a[254:255], v221 offset:72\n\t"
      "ds_read_b64_tr_b16 v[166:167], v221 offset:128\n\t"
      "v_mfma_f32_16x16x32_bf16 v[100:103], a[236:239], v[140:143], v[100:103]\n\t"
      "ds_read_b64_tr_b16 v[170:171], v221 offset:136\n\t"
      "ds_read_b128 v[156:159], v196 offset:12800\n\t"
      "s_waitcnt lgkmcnt(14)\n\t"
      "v_mfma_f32_16x16x32_bf16 v[104:107], a[216:219], v[152:155], v[104:107]\n\t"
      "s_cmp_eq_u32 s94, 0\n\t"
      "s_cbranch_scc0 .Lc5_%=\n\t"
      "s_add_u32 m0, s41, 0xf000\n\t"
      "s_add_u32 s51, s40, 0xc00000\n\t"
      "buffer_load_dwordx4 v209, s[20:23], s51 offen lds\n\t"
      "buffer_load_dwordx4 v209, s[20:23], s51 offen offset:1024 lds\n\t"
      "buffer_load_dwordx4 v209, s[20:23], s51 offen offset:2048 lds\n\t"
      ".Lc5_%=:\n\t"
      "v_mfma_f32_16x16x32_bf16 v[108:111], a[220:223], v[152:155], v[108:111]\n\t"
      "s_cmp_eq_u32 s94, 1\n\t"
      "s_cbranch_scc0 .Lc6_%=\n\t"
      "s_add_u32 m0, s41, 0xf000\n\t"
      "s_add_u32 s51, s40, 0xc00000\n\t"
      "buffer_load_dwordx4 v209, s[20:23], s51 offen lds\n\t"
      "buffer_load_dwordx4 v209, s[20:23], s51 offen offset:1024 lds\n\t"
      "buffer_load_dwordx4 v209, s[20:23], s51 offen offset:2048 lds\n\t"
      ".Lc6_%=:\n\t"
      "v_mfma_f32_16x16x32_bf16 v[112:115], a[224:227], v[152:155], v[112:115]\n\t"
      "s_waitcnt vmcnt(15)\n\t"
      "s_cmp_eq_u32 s94, 1\n\t"
      "s_cbranch_scc0 .Lwpw22_1_%=\n\t"
      "s_waitcnt vmcnt(9)\n\t"
      ".Lwpw22_1_%=:\n\t"
      "v_add_u32_e32 v221, 0x21000, v214\n\t"
      "ds_read_b64_tr_b16 v[172:173], v221 offset:0\n\t"
      "v_mfma_f32_16x16x32_bf16 v[116:119], a[228:231], v[152:155], v[116:119]\n\t"
      "ds_read_b64_tr_b16 v[176:177], v221 offset:8\n\t"
      "ds_read_b64_tr_b16 v[180:181], v221 offset:64\n\t"
      "ds_read_b64_tr_b16 v[184:185], v221 offset:72\n\t"
      "v_mfma_f32_16x16x32_bf16 v[120:123], a[232:235], v[152:155], v[120:123]\n\t"
      "ds_read_b64_tr_b16 v[188:189], v221 offset:128\n\t"
      "ds_read_b64_tr_b16 v[192:193], v221 offset:136\n\t"
      "v_mfma_f32_16x16x32_bf16 v[132:135], a[236:239], v[152:155], v[132:135]\n\t"
      "ds_read_b128 v[148:151], v197 offset:512\n\t"
      "ds_read_b128 v[160:163], v197 offset:12800\n\t"
      "s_waitcnt lgkmcnt(9)\n\t"
      "v_mfma_f32_16x16x32_bf16 v[80:83], a[240:243], v[144:147], v[80:83]\n\t"
      "s_mov_b64 s[96:97], s[34:35]\n\t"
      "s_movk_i32 s98, 0x148\n\t"
      "s_mov_b32 s99, s23\n\t"
      "buffer_load_dwordx3 v[252:254], v207, s[96:99], 0 offen offset:40\n\t"
      "v_mfma_f32_16x16x32_bf16 v[84:87], a[244:247], v[144:147], v[84:87]\n\t"
      "buffer_load_dwordx2 v[240:241], v245, s[24:27], 0 offen sc1\n\t"
      "v_mfma_f32_16x16x32_bf16 v[88:91], a[248:251], v[144:147], v[88:91]\n\t"
      "s_waitcnt vmcnt(14)\n\t"
      "s_cmp_eq_u32 s94, 1\n\t"
      "s_cbranch_scc0 .Lwpw23_1_%=\n\t"
      "s_waitcnt vmcnt(8)\n\t"
      ".Lwpw23_1_%=:\n\t"
      "v_add_u32_e32 v221, 0x24000, v214\n\t"
      "ds_read_b64_tr_b16 v[174:175], v221 offset:0\n\t"
      "v_mfma_f32_16x16x32_bf16 v[92:95], a[252:255], v[144:147], v[92:95]\n\t"
      "ds_read_b64_tr_b16 v[178:179], v221 offset:8\n\t"
      "ds_read_b64_tr_b16 v[182:183], v221 offset:64\n\t"
      "v_mfma_f32_16x16x32_bf16 v[96:99], v[164:167], v[144:147], v[96:99]\n\t"
      "ds_read_b64_tr_b16 v[186:187], v221 offset:72\n\t"
      "ds_read_b64_tr_b16 v[190:191], v221 offset:128\n\t"
      "v_mfma_f32_16x16x32_bf16 v[100:103], v[168:171], v[144:147], v[100:103]\n\t"
      "ds_read_b64_tr_b16 v[194:195], v221 offset:136\n\t"
      ".Lcq_%=:\n\t"
      "v_add_u32_e32 v196, s46, v200\n\t"
      "v_add_u32_e32 v197, s46, v201\n\t"
      "v_add_u32_e32 v198, s46, v202\n\t"
      "v_add_u32_e32 v199, s46, v203\n\t"
      "s_cmp_eq_u32 s94, 0\n\t"
      "s_cbranch_scc0 .Lwpx2_0_%=\n\t"
      "s_waitcnt vmcnt(11)\n\t"
      ".Lwpx2_0_%=:\n\t"
      "s_waitcnt lgkmcnt(6)\n\t"
      "s_barrier\n\t"
      "v_mfma_f32_16x16x32_bf16 v[104:107], a[240:243], v[156:159], v[104:107]\n\t"
      "ds_read_b128 v[32:35], v196 offset:0\n\t"
      "v_mfma_f32_16x16x32_bf16 v[108:111], a[244:247], v[156:159], v[108:111]\n\t"
      "ds_read_b128 v[36:39], v197 offset:0\n\t"
      "v_mfma_f32_16x16x32_bf16 v[112:115], a[248:251], v[156:159], v[112:115]\n\t"
      "ds_read_b128 v[40:43], v198 offset:0\n\t"
      "v_mfma_f32_16x16x32_bf16 v[116:119], a[252:255], v[156:159], v[116:119]\n\t"
      "ds_read_b128 v[44:47], v199 offset:0\n\t"
      "v_mfma_f32_16x16x32_bf16 v[120:123], v[164:167], v[156:159], v[120:123]\n\t"
      "ds_read_b128 v[48:51], v197 offset:256\n\t"
      "v_mfma_f32_16x16x32_bf16 v[132:135], v[168:171], v[156:159], v[132:135]\n\t"
      "s_waitcnt lgkmcnt(5)\n\t"
      "v_mfma_f32_16x16x32_bf16 v[80:83], v[172:175], v[148:151], v[80:83]\n\t"
      "ds_read_b128 v[52:55], v198 offset:256\n\t"
      "v_mfma_f32_16x16x32_bf16 v[84:87], v[176:179], v[148:151], v[84:87]\n\t"
      "ds_read_b128 v[56:59], v199 offset:256\n\t"
      "v_mfma_f32_16x16x32_bf16 v[88:91], v[180:183], v[148:151], v[88:91]\n\t"
      "ds_read_b128 v[60:63], v196 offset:256\n\t"
      "v_mfma_f32_16x16x32_bf16 v[92:95], v[184:187], v[148:151], v[92:95]\n\t"
      "ds_read_b128 v[64:67], v198 offset:512\n\t"
      "v_mfma_f32_16x16x32_bf16 v[96:99], v[188:191], v[148:151], v[96:99]\n\t"
      "ds_read_b128 v[68:71], v199 offset:512\n\t"
      "v_mfma_f32_16x16x32_bf16 v[100:103], v[192:195], v[148:151], v[100:103]\n\t"
      "ds_read_b128 v[72:75], v196 offset:512\n\t"
      "v_mfma_f32_16x16x32_bf16 v[104:107], v[172:175], v[160:163], v[104:107]\n\t"
      "ds_read_b128 v[76:79], v197 offset:512\n\t"
      "v_mfma_f32_16x16x32_bf16 v[108:111], v[176:179], v[160:163], v[108:111]\n\t"
      "v_mfma_f32_16x16x32_bf16 v[112:115], v[180:183], v[160:163], v[112:115]\n\t"
      "v_mfma_f32_16x16x32_bf16 v[116:119], v[184:187], v[160:163], v[116:119]\n\t"
      "v_mfma_f32_16x16x32_bf16 v[120:123], v[188:191], v[160:163], v[120:123]\n\t"
      "v_mfma_f32_16x16x32_bf16 v[132:135], v[192:195], v[160:163], v[132:135]\n\t"
      "s_waitcnt lgkmcnt(8)\n\t"
      "v_mfma_f32_16x16x32_bf16 v[128:131], a[0:3], v[32:35], v[8:11]\n\t"
      "v_cvt_pk_bf16_f32 v80, v80, v81\n\t"
      "v_cvt_pk_bf16_f32 v81, v82, v83\n\t"
      "v_mfma_f32_16x16x32_bf16 v[128:131], a[24:27], v[36:39], v[128:131]\n\t"
      "v_cvt_pk_bf16_f32 v136, v120, v121\n\t"
      "v_cvt_pk_bf16_f32 v137, v122, v123\n\t"
      "v_mfma_f32_16x16x32_bf16 v[128:131], a[48:51], v[40:43], v[128:131]\n\t"
      "v_cvt_pk_bf16_f32 v138, v132, v133\n\t"
      "v_mfma_f32_16x16x32_bf16 v[128:131], a[72:75], v[44:47], v[128:131]\n\t"
      "v_cvt_pk_bf16_f32 v139, v134, v135\n\t"
      "s_waitcnt lgkmcnt(4)\n\t"
      "v_mfma_f32_16x16x32_bf16 v[128:131], a[96:99], v[48:51], v[128:131]\n\t"
      "ds_write_b128 v219, v[136:139] offset:12288\n\t"
      "v_mfma_f32_16x16x32_bf16 v[128:131], a[120:123], v[52:55], v[128:131]\n\t"
      "v_cvt_pk_bf16_f32 v82, v84, v85\n\t"
      "v_cvt_pk_bf16_f32 v83, v86, v87\n\t"
      "v_mfma_f32_16x16x32_bf16 v[128:131], a[144:147], v[56:59], v[128:131]\n\t"
      "v_cvt_pk_bf16_f32 v88, v88, v89\n\t"
      "v_cvt_pk_bf16_f32 v89, v90, v91\n\t"
      "v_mfma_f32_16x16x32_bf16 v[128:131], a[168:171], v[60:63], v[128:131]\n\t"
      "ds_write_b128 v217, v[80:83] offset:0\n\t"
      "v_cvt_pk_bf16_f32 v90, v92, v93\n\t"
      "s_waitcnt lgkmcnt(2)\n\t"
      "v_mfma_f32_16x16x32_bf16 v[128:131], a[192:195], v[64:67], v[128:131]\n\t"
      "v_cvt_pk_bf16_f32 v91, v94, v95\n\t"
      "v_cvt_pk_bf16_f32 v96, v96, v97\n\t"
      "v_mfma_f32_16x16x32_bf16 v[128:131], a[216:219], v[68:71], v[128:131]\n\t"
      "ds_write_b128 v218, v[88:91] offset:0\n\t"
      "v_cvt_pk_bf16_f32 v97, v98, v99\n\t"
      "v_mfma_f32_16x16x32_bf16 v[128:131], a[240:243], v[72:75], v[128:131]\n\t"
      "v_cvt_pk_bf16_f32 v98, v100, v101\n\t"
      "v_cvt_pk_bf16_f32 v99, v102, v103\n\t"
      "v_mfma_f32_16x16x32_bf16 v[128:131], v[172:175], v[76:79], v[128:131]\n\t"
      "v_cvt_pk_bf16_f32 v104, v104, v105\n\t"
      "v_cvt_pk_bf16_f32 v105, v106, v107\n\t"
      "v_mfma_f32_16x16x32_bf16 v[132:135], a[4:7], v[32:35], v[12:15]\n\t"
      "ds_write_b128 v219, v[96:99] offset:0\n\t"
      "v_cvt_pk_bf16_f32 v106, v108, v109\n\t"
      "v_mfma_f32_16x16x32_bf16 v[132:135], a[28:31], v[36:39], v[132:135]\n\t"
      "v_cvt_pk_bf16_f32 v107, v110, v111\n\t"
      "v_cvt_pk_bf16_f32 v112, v112, v113\n\t"
      "v_mfma_f32_16x16x32_bf16 v[132:135], a[52:55], v[40:43], v[132:135]\n\t"
      "v_cvt_pk_bf16_f32 v136, v128, v129\n\t"
      "v_mfma_f32_16x16x32_bf16 v[132:135], a[76:79], v[44:47], v[132:135]\n\t"
      "v_cvt_pk_bf16_f32 v137, v130, v131\n\t"
      "v_mfma_f32_16x16x32_bf16 v[132:135], a[100:103], v[48:51], v[132:135]\n\t"
      "ds_write_b128 v217, v[104:107] offset:12288\n\t"
      "v_cvt_pk_bf16_f32 v113, v114, v115\n\t"
      "s_add_u32 s48, s47, 0x6000\n\t"
      "v_mfma_f32_16x16x32_bf16 v[132:135], a[124:127], v[52:55], v[132:135]\n\t"
      "v_cvt_pk_bf16_f32 v114, v116, v117\n\t"
      "v_cvt_pk_bf16_f32 v115, v118, v119\n\t"
      "s_cmp_eq_u32 s48, s50\n\t"
      "s_cselect_b32 s48, s41, s48\n\t"
      "v_mfma_f32_16x16x32_bf16 v[132:135], a[148:151], v[56:59], v[132:135]\n\t"
      "ds_write_b128 v218, v[112:115] offset:12288\n\t"
      "v_mfma_f32_16x16x32_bf16 v[132:135], a[172:175], v[60:63], v[132:135]\n\t"
      "v_add_u32_e32 v217, s46, v204\n\t"
      "v_mfma_f32_16x16x32_bf16 v[132:135], a[196:199], v[64:67], v[132:135]\n\t"
      "v_add_u32_e32 v218, s46, v205\n\t"
      "v_mfma_f32_16x16x32_bf16 v[132:135], a[220:223], v[68:71], v[132:135]\n\t"
      "v_readlane_b32 s73, v248, 3\n\t"
      "v_add_u32_e32 v219, s46, v206\n\t"
      "v_mfma_f32_16x16x32_bf16 v[132:135], a[244:247], v[72:75], v[132:135]\n\t"
      "v_mfma_f32_16x16x32_bf16 v[132:135], v[176:179], v[76:79], v[132:135]\n\t"
      "s_waitcnt lgkmcnt(0)\n\t"
      "s_barrier\n\t"
      "v_mfma_f32_16x16x32_bf16 v[128:131], a[8:11], v[32:35], v[16:19]\n\t"
      "ds_read_b128 v[140:143], v220 offset:0\n\t"
      "v_mfma_f32_16x16x32_bf16 v[128:131], a[32:35], v[36:39], v[128:131]\n\t"
      "ds_read_b128 v[144:147], v220 offset:1024\n\t"
      "s_memrealtime s[80:81]\n\t"
      "v_mfma_f32_16x16x32_bf16 v[128:131], a[56:59], v[40:43], v[128:131]\n\t"
      "v_cvt_pk_bf16_f32 v138, v132, v133\n\t"
      "s_mov_b32 m0, s45\n\t"
      "v_mfma_f32_16x16x32_bf16 v[128:131], a[80:83], v[44:47], v[128:131]\n\t"
      "v_cvt_pk_bf16_f32 v139, v134, v135\n\t"
      "v_mfma_f32_16x16x32_bf16 v[128:131], a[104:107], v[48:51], v[128:131]\n\t"
      "ds_write_b128 v217, v[136:139] offset:0\n\t"
      "v_mfma_f32_16x16x32_bf16 v[128:131], a[128:131], v[52:55], v[128:131]\n\t"
      "s_waitcnt lgkmcnt(2)\n\t"
      "buffer_store_dwordx4 v[140:143], v208, s[28:31], s40 offen sc1\n\t"
      "v_mfma_f32_16x16x32_bf16 v[128:131], a[152:155], v[56:59], v[128:131]\n\t"
      "ds_read_b128 v[148:151], v220 offset:2048\n\t"
      "v_mfma_f32_16x16x32_bf16 v[128:131], a[176:179], v[60:63], v[128:131]\n\t"
      "ds_read_b128 v[152:155], v220 offset:12288\n\t"
      "v_mfma_f32_16x16x32_bf16 v[128:131], a[200:203], v[64:67], v[128:131]\n\t"
      "ds_read_b128 v[156:159], v220 offset:13312\n\t"
      "v_mfma_f32_16x16x32_bf16 v[128:131], a[224:227], v[68:71], v[128:131]\n\t"
      "buffer_load_dwordx4 v208, s[20:23], s73 offen lds\n\t"
      "v_mfma_f32_16x16x32_bf16 v[128:131], a[248:251], v[72:75], v[128:131]\n\t"
      "ds_read_b128 v[160:163], v220 offset:14336\n\t"
      "v_mfma_f32_16x16x32_bf16 v[128:131], v[180:183], v[76:79], v[128:131]\n\t"
      "v_mfma_f32_16x16x32_bf16 v[132:135], a[12:15], v[32:35], v[20:23]\n\t"
      "v_mfma_f32_16x16x32_bf16 v[132:135], a[36:39], v[36:39], v[132:135]\n\t"
      "s_waitcnt lgkmcnt(5)\n\t"
      "buffer_store_dwordx4 v[144:147], v208, s[28:31], s40 offen offset:1024 sc1\n\t"
      "v_mfma_f32_16x16x32_bf16 v[132:135], a[60:63], v[40:43], v[132:135]\n\t"
      "v_cvt_pk_bf16_f32 v136, v128, v129\n\t"
      "v_mfma_f32_16x16x32_bf16 v[132:135], a[84:87], v[44:47], v[132:135]\n\t"
      "v_cvt_pk_bf16_f32 v137, v130, v131\n\t"
      "v_mfma_f32_16x16x32_bf16 v[132:135], a[108:111], v[48:51], v[132:135]\n\t"
      "v_mov_b32_e32 v6, 0\n\t"
      "v_mfma_f32_16x16x32_bf16 v[132:135], a[132:135], v[52:55], v[132:135]\n\t"
      "buffer_load_dwordx4 v208, s[20:23], s73 offen offset:1024 lds\n\t"
      "v_mfma_f32_16x16x32_bf16 v[132:135], a[156:159], v[56:59], v[132:135]\n\t"
      "ds_write_b32 v238, v6\n\t"
      "v_mfma_f32_16x16x32_bf16 v[132:135], a[180:183], v[60:63], v[132:135]\n\t"
      "v_mfma_f32_16x16x32_bf16 v[132:135], a[204:207], v[64:67], v[132:135]\n\t"
      "v_mfma_f32_16x16x32_bf16 v[132:135], a[228:231], v[68:71], v[132:135]\n\t"
      "s_waitcnt lgkmcnt(4)\n\t"
      "buffer_store_dwordx4 v[148:151], v208, s[28:31], s40 offen offset:2048 sc1\n\t"
      "v_mfma_f32_16x16x32_bf16 v[132:135], a[252:255], v[72:75], v[132:135]\n\t"
      "v_mfma_f32_16x16x32_bf16 v[132:135], v[184:187], v[76:79], v[132:135]\n\t"
      "s_waitcnt vmcnt(13)\n\t"
      "s_barrier\n\t"
      "v_mfma_f32_16x16x32_bf16 v[128:131], a[16:19], v[32:35], v[24:27]\n\t"
      "ds_read_b128 v[80:83], v196 offset:12288\n\t"
      "v_mfma_f32_16x16x32_bf16 v[128:131], a[40:43], v[36:39], v[128:131]\n\t"
      "buffer_load_dwordx4 v208, s[20:23], s73 offen offset:2048 lds\n\t"
      "v_mfma_f32_16x16x32_bf16 v[128:131], a[64:67], v[40:43], v[128:131]\n\t"
      "ds_read_b128 v[84:87], v197 offset:12288\n\t"
      "v_cvt_pk_bf16_f32 v138, v132, v133\n\t"
      "s_add_u32 m0, s45, 0x3000\n\t"
      "v_mfma_f32_16x16x32_bf16 v[128:131], a[88:91], v[44:47], v[128:131]\n\t"
      "ds_read_b128 v[88:91], v198 offset:12288\n\t"
      "v_cvt_pk_bf16_f32 v139, v134, v135\n\t"
      "v_mfma_f32_16x16x32_bf16 v[128:131], a[112:115], v[48:51], v[128:131]\n\t"
      "ds_write_b128 v218, v[136:139] offset:0\n\t"
      "v_mfma_f32_16x16x32_bf16 v[128:131], a[136:139], v[52:55], v[128:131]\n\t"
      "s_waitcnt lgkmcnt(7)\n\t"
      "buffer_store_dwordx4 v[152:155], v209, s[28:31], s40 offen sc1\n\t"
      "s_waitcnt vmcnt(7)\n\t"
      "v_subrev_u32_e32 v242, s67, v240\n\t"
      "v_xor_b32_e32 v241, s63, v241\n\t"
      "v_mfma_f32_16x16x32_bf16 v[128:131], a[160:163], v[56:59], v[128:131]\n\t"
      "ds_read_b128 v[92:95], v199 offset:12288\n\t"
      "v_lshl_or_b32 v240, v242, 20, v241\n\t"
      "v_mfma_f32_16x16x32_bf16 v[128:131], a[184:187], v[60:63], v[128:131]\n\t"
      "ds_read_b128 v[96:99], v197 offset:12544\n\t"
      "v_mfma_f32_16x16x32_bf16 v[128:131], a[208:211], v[64:67], v[128:131]\n\t"
      "v_lshrrev_b32_e32 v242, 12, v242\n\t"
      "v_cmp_ne_u32_e32 vcc, 0, v240\n\t"
      "v_mfma_f32_16x16x32_bf16 v[128:131], a[232:235], v[68:71], v[128:131]\n\t"
      "buffer_load_dwordx4 v209, s[20:23], s73 offen lds\n\t"
      "v_mfma_f32_16x16x32_bf16 v[128:131], v[164:167], v[72:75], v[128:131]\n\t"
      "ds_read_b128 v[100:103], v198 offset:12544\n\t"
      "s_cbranch_vccnz .Lgs_%=\n\t"
      ".Lgr_%=:\n\t"
      "ds_write_b32 v246, v242\n\t"
      "v_mfma_f32_16x16x32_bf16 v[128:131], v[188:191], v[76:79], v[128:131]\n\t"
      "ds_read_b128 v[104:107], v199 offset:12544\n\t"
      "v_mfma_f32_16x16x32_bf16 v[132:135], a[20:23], v[32:35], v[28:31]\n\t"
      "ds_read_b128 v[108:111], v196 offset:12544\n\t"
      "v_mfma_f32_16x16x32_bf16 v[132:135], a[44:47], v[36:39], v[132:135]\n\t"
      "s_waitcnt lgkmcnt(12)\n\t"
      "buffer_store_dwordx4 v[156:159], v209, s[28:31], s40 offen offset:1024 sc1\n\t"
      "v_mfma_f32_16x16x32_bf16 v[132:135], a[68:71], v[40:43], v[132:135]\n\t"
      "ds_read_b128 v[112:115], v198 offset:12800\n\t"
      "v_cvt_pk_bf16_f32 v136, v128, v129\n\t"
      "v_add_u32_e32 v220, s46, v207\n\t"
      "v_mfma_f32_16x16x32_bf16 v[132:135], a[92:95], v[44:47], v[132:135]\n\t"
      "ds_read_b128 v[116:119], v199 offset:12800\n\t"
      "v_cvt_pk_bf16_f32 v137, v130, v131\n\t"
      "v_mfma_f32_16x16x32_bf16 v[132:135], a[116:119], v[48:51], v[132:135]\n\t"
      "s_sub_u32 s87, s80, s52\n\t"
      "v_mfma_f32_16x16x32_bf16 v[132:135], a[140:143], v[52:55], v[132:135]\n\t"
      "buffer_load_dwordx4 v209, s[20:23], s73 offen offset:1024 lds\n\t"
      "v_mfma_f32_16x16x32_bf16 v[132:135], a[164:167], v[56:59], v[132:135]\n\t"
      "ds_read_b128 v[120:123], v196 offset:12800\n\t"
      "v_mfma_f32_16x16x32_bf16 v[132:135], a[188:191], v[60:63], v[132:135]\n\t"
      "ds_read_b128 v[124:127], v197 offset:12800\n\t"
      "v_mfma_f32_16x16x32_bf16 v[132:135], a[212:215], v[64:67], v[132:135]\n\t"
      "s_add_u32 s44, s44, 1\n\t"
      "v_mfma_f32_16x16x32_bf16 v[132:135], a[236:239], v[68:71], v[132:135]\n\t"
      "s_waitcnt lgkmcnt(15)\n\t"
      "buffer_store_dwordx4 v[160:163], v209, s[28:31], s40 offen offset:2048 sc1\n\t"
      "v_mfma_f32_16x16x32_bf16 v[132:135], v[168:171], v[72:75], v[132:135]\n\t"
      "s_add_u32 s85, s85, 1\n\t"
      "s_add_u32 s82, s87, s84\n\t"
      "v_mfma_f32_16x16x32_bf16 v[132:135], v[192:195], v[76:79], v[132:135]\n\t"
      "s_waitcnt lgkmcnt(9)\n\t"
      "v_mfma_f32_16x16x32_bf16 v[128:131], a[0:3], v[80:83], v[8:11]\n\t"
      "v_mfma_f32_16x16x32_bf16 v[128:131], a[24:27], v[84:87], v[128:131]\n\t"
      "v_readlane_b32 s72, v248, s44\n\t"
      "v_mfma_f32_16x16x32_bf16 v[128:131], a[48:51], v[88:91], v[128:131]\n\t"
      "v_cvt_pk_bf16_f32 v138, v132, v133\n\t"
      "v_mfma_f32_16x16x32_bf16 v[128:131], a[72:75], v[92:95], v[128:131]\n\t"
      "v_cvt_pk_bf16_f32 v139, v134, v135\n\t"
      "s_waitcnt lgkmcnt(4)\n\t"
      "v_mfma_f32_16x16x32_bf16 v[128:131], a[96:99], v[96:99], v[128:131]\n\t"
      "ds_write_b128 v219, v[136:139] offset:0\n\t"
      "v_mfma_f32_16x16x32_bf16 v[128:131], a[120:123], v[100:103], v[128:131]\n\t"
      "v_mfma_f32_16x16x32_bf16 v[128:131], a[144:147], v[104:107], v[128:131]\n\t"
      "buffer_load_dwordx4 v209, s[20:23], s73 offen offset:2048 lds\n\t"
      "v_mfma_f32_16x16x32_bf16 v[128:131], a[168:171], v[108:111], v[128:131]\n\t"
      "v_add_u32_e32 v196, s47, v200\n\t"
      "s_waitcnt lgkmcnt(1)\n\t"
      "v_mfma_f32_16x16x32_bf16 v[128:131], a[192:195], v[112:115], v[128:131]\n\t"
      "v_add_u32_e32 v197, s47, v201\n\t"
      "v_mfma_f32_16x16x32_bf16 v[128:131], a[216:219], v[116:119], v[128:131]\n\t"
      "v_add_u32_e32 v198, s47, v202\n\t"
      "v_mfma_f32_16x16x32_bf16 v[128:131], a[240:243], v[120:123], v[128:131]\n\t"
      "v_add_u32_e32 v199, s47, v203\n\t"
      "v_mfma_f32_16x16x32_bf16 v[128:131], v[172:175], v[124:127], v[128:131]\n\t"
      "s_waitcnt vmcnt(17) lgkmcnt(0)\n\t"
      "s_barrier\n\t"
      "v_mfma_f32_16x16x32_bf16 v[132:135], a[4:7], v[80:83], v[12:15]\n\t"
      "ds_read_b128 v[140:143], v220 offset:0\n\t"
      "v_mfma_f32_16x16x32_bf16 v[132:135], a[28:31], v[84:87], v[132:135]\n\t"
      "ds_read_b128 v[240:243], v247 offset:0\n\t"
      "v_mfma_f32_16x16x32_bf16 v[132:135], a[52:55], v[88:91], v[132:135]\n\t"
      "v_cvt_pk_bf16_f32 v136, v128, v129\n\t"
      "v_mfma_f32_16x16x32_bf16 v[132:135], a[76:79], v[92:95], v[132:135]\n\t"
      "ds_read_b128 v[32:35], v196 offset:0\n\t"
      "v_cvt_pk_bf16_f32 v137, v130, v131\n\t"
      "v_mfma_f32_16x16x32_bf16 v[132:135], a[100:103], v[96:99], v[132:135]\n\t"
      "v_mfma_f32_16x16x32_bf16 v[132:135], a[124:127], v[100:103], v[132:135]\n\t"
      "v_mfma_f32_16x16x32_bf16 v[132:135], a[148:151], v[104:107], v[132:135]\n\t"
      "ds_read_b128 v[36:39], v197 offset:0\n\t"
      "v_mfma_f32_16x16x32_bf16 v[132:135], a[172:175], v[108:111], v[132:135]\n\t"
      "v_mfma_f32_16x16x32_bf16 v[132:135], a[196:199], v[112:115], v[132:135]\n\t"
      "s_waitcnt lgkmcnt(2)\n\t"
      "v_add3_u32 v240, v240, v241, v242\n\t"
      "v_mfma_f32_16x16x32_bf16 v[132:135], a[220:223], v[116:119], v[132:135]\n\t"
      "ds_read_b128 v[40:43], v198 offset:0\n\t"
      "v_mfma_f32_16x16x32_bf16 v[132:135], a[244:247], v[120:123], v[132:135]\n\t"
      "v_add_u32_e32 v240, v240, v243\n\t"
      "v_mfma_f32_16x16x32_bf16 v[132:135], v[176:179], v[124:127], v[132:135]\n\t"
      "buffer_store_dwordx4 v[140:143], v208, s[28:31], s72 offen sc1\n\t"
      "v_mfma_f32_16x16x32_bf16 v[128:131], a[8:11], v[80:83], v[16:19]\n\t"
      "ds_read_b128 v[44:47], v199 offset:0\n\t"
      "v_mfma_f32_16x16x32_bf16 v[128:131], a[32:35], v[84:87], v[128:131]\n\t"
      "v_add_u32_dpp v241, v240, v240 quad_perm:[1,0,3,2] row_mask:0xf bank_mask:0xf\n\t"
      "s_memrealtime s[80:81]\n\t"
      "v_mfma_f32_16x16x32_bf16 v[128:131], a[56:59], v[88:91], v[128:131]\n\t"
      "v_cvt_pk_bf16_f32 v138, v132, v133\n\t"
      "v_mfma_f32_16x16x32_bf16 v[128:131], a[80:83], v[92:95], v[128:131]\n\t"
      "v_cvt_pk_bf16_f32 v139, v134, v135\n\t"
      "v_mfma_f32_16x16x32_bf16 v[128:131], a[104:107], v[96:99], v[128:131]\n\t"
      "ds_write_b128 v217, v[136:139] offset:12288\n\t"
      "v_add_u32_dpp v242, v241, v241 quad_perm:[2,3,0,1] row_mask:0xf bank_mask:0xf\n\t"
      "v_mfma_f32_16x16x32_bf16 v[128:131], a[128:131], v[100:103], v[128:131]\n\t"
      "s_mov_b32 m0, s46\n\t"
      "v_and_b32_e32 v240, 0xff, v242\n\t"
      "v_mfma_f32_16x16x32_bf16 v[128:131], a[152:155], v[104:107], v[128:131]\n\t"
      "v_cmp_le_u32_e64 s[76:77], 19, v240\n\t"
      "v_cmp_ge_u32_e64 s[78:79], 13, v240\n\t"
      "v_mfma_f32_16x16x32_bf16 v[128:131], a[176:179], v[108:111], v[128:131]\n\t"
      "ds_read_b128 v[48:51], v197 offset:256\n\t"
      "v_readlane_b32 s70, v242, 0\n\t"
      "v_mfma_f32_16x16x32_bf16 v[128:131], a[200:203], v[112:115], v[128:131]\n\t"
      "v_cmp_lt_u32_e32 vcc, 32, v240\n\t"
      "s_mov_b32 s66, s76\n\t"
      "v_mfma_f32_16x16x32_bf16 v[128:131], a[224:227], v[116:119], v[128:131]\n\t"
      "ds_read_b128 v[52:55], v198 offset:256\n\t"
      "v_mov_b32_e32 v242, s62\n\t"
      "v_mfma_f32_16x16x32_bf16 v[128:131], a[248:251], v[120:123], v[128:131]\n\t"
      "s_and_b32 s65, s76, s77\n\t"
      "s_and_b32 s78, s78, s79\n\t"
      "v_mfma_f32_16x16x32_bf16 v[128:131], v[180:183], v[124:127], v[128:131]\n\t"
      "v_mov_b32_e32 v243, s63\n\t"
      "s_or_b32 s65, s65, s78\n\t"
      "v_mfma_f32_16x16x32_bf16 v[132:135], a[12:15], v[80:83], v[20:23]\n\t"
      "ds_read_b128 v[56:59], v199 offset:256\n\t"
      "s_cmp_lg_u64 vcc, 0\n\t"
      "s_cselect_b32 s65, 0, s65\n\t"
      "s_cselect_b32 s64, 0xfffc0000, 0\n\t"
      "s_cselect_b32 s49, 31, 17\n\t"
      "v_mfma_f32_16x16x32_bf16 v[132:135], a[36:39], v[84:87], v[132:135]\n\t"
      "v_and_b32_e32 v240, s66, v250\n\t"
      "v_and_b32_e32 v241, s65, v250\n\t"
      "v_mfma_f32_16x16x32_bf16 v[132:135], a[60:63], v[88:91], v[132:135]\n\t"
      "v_cvt_pk_bf16_f32 v136, v128, v129\n\t"
      "s_andn2_b32 s67, 0x40000000, s64\n\t"
      "v_mfma_f32_16x16x32_bf16 v[132:135], a[84:87], v[92:95], v[132:135]\n\t"
      "v_cvt_pk_bf16_f32 v137, v130, v131\n\t"
      "ds_read_b128 v[144:147], v220 offset:1024\n\t"
      "v_mfma_f32_16x16x32_bf16 v[132:135], a[108:111], v[96:99], v[132:135]\n\t"
      "v_cmp_ne_u32_e64 s[76:77], 0, v240\n\t"
      "v_cmp_ne_u32_e64 s[78:79], 0, v241\n\t"
      "v_mfma_f32_16x16x32_bf16 v[132:135], a[132:135], v[100:103], v[132:135]\n\t"
      "buffer_store_dwordx2 v[242:243], v210, s[24:27], 0 offen sc1\n\t"
      "v_mfma_f32_16x16x32_bf16 v[132:135], a[156:159], v[104:107], v[132:135]\n\t"
      "ds_read_b128 v[60:63], v196 offset:256\n\t"
      "s_lshr_b32 s70, s70, 10\n\t"
      "v_mfma_f32_16x16x32_bf16 v[132:135], a[180:183], v[108:111], v[132:135]\n\t"
      "s_xor_b32 s75, s75, s76\n\t"
      "s_xor_b32 s74, s74, s76\n\t"
      "v_mfma_f32_16x16x32_bf16 v[132:135], a[204:207], v[112:115], v[132:135]\n\t"
      "s_and_b32 s76, s75, s78\n\t"
      "s_or_b32 s76, s76, s64\n\t"
      "v_cndmask_b32_e64 v249, v248, v251, s[76:77]\n\t"
      "v_mfma_f32_16x16x32_bf16 v[132:135], a[228:231], v[116:119], v[132:135]\n\t"
      "ds_read_b128 v[64:67], v198 offset:512\n\t"
      "s_add_u32 s70, s70, s68\n\t"
      "v_mfma_f32_16x16x32_bf16 v[132:135], a[252:255], v[120:123], v[132:135]\n\t"
      "s_and_b32 s76, s74, s78\n\t"
      "s_or_b32 s76, s76, s64\n\t"
      "v_cndmask_b32_e64 v248, v248, v251, s[76:77]\n\t"
      "v_mfma_f32_16x16x32_bf16 v[132:135], v[184:187], v[124:127], v[132:135]\n\t"
      "s_waitcnt lgkmcnt(2)\n\t"
      "buffer_store_dwordx4 v[144:147], v208, s[28:31], s72 offen offset:1024 sc1\n\t"
      "v_mfma_f32_16x16x32_bf16 v[128:131], a[16:19], v[80:83], v[24:27]\n\t"
      "ds_read_b128 v[68:71], v199 offset:512\n\t"
      "v_mfma_f32_16x16x32_bf16 v[128:131], a[40:43], v[84:87], v[128:131]\n\t"
      "s_lshr_b32 s74, s78, 3\n\t"
      "s_or_b32 s76, s74, s78\n\t"
      "v_mfma_f32_16x16x32_bf16 v[128:131], a[64:67], v[88:91], v[128:131]\n\t"
      "v_cvt_pk_bf16_f32 v138, v132, v133\n\t"
      "v_readlane_b32 s73, v248, s85\n\t"
      "v_mfma_f32_16x16x32_bf16 v[128:131], a[88:91], v[92:95], v[128:131]\n\t"
      "v_cvt_pk_bf16_f32 v139, v134, v135\n\t"
      "s_sub_u32 s87, s80, s52\n\t"
      "v_mfma_f32_16x16x32_bf16 v[128:131], a[112:115], v[96:99], v[128:131]\n\t"
      "ds_write_b128 v218, v[136:139] offset:12288\n\t"
      "s_sub_u32 s83, s82, s87\n\t"
      "v_mfma_f32_16x16x32_bf16 v[128:131], a[136:139], v[100:103], v[128:131]\n\t"
      "v_cndmask_b32_e64 v252, v252, v253, s[76:77]\n\t"
      "s_and_b32 s78, s74, s78\n\t"
      "v_mfma_f32_16x16x32_bf16 v[128:131], a[160:163], v[104:107], v[128:131]\n\t"
      "v_cndmask_b32_e64 v252, v252, v254, s[78:79]\n\t"
      "v_mfma_f32_16x16x32_bf16 v[128:131], a[184:187], v[108:111], v[128:131]\n\t"
      "ds_read_b128 v[148:151], v220 offset:2048\n\t"
      "v_mfma_f32_16x16x32_bf16 v[128:131], a[208:211], v[112:115], v[128:131]\n\t"
      "buffer_load_dwordx4 v208, s[20:23], s73 offen lds\n\t"
      "v_mfma_f32_16x16x32_bf16 v[128:131], a[232:235], v[116:119], v[128:131]\n\t"
      "ds_read_b128 v[72:75], v196 offset:512\n\t"
      "s_max_i32 s82, s82, s87\n\t"
      "v_mfma_f32_16x16x32_bf16 v[128:131], v[164:167], v[120:123], v[128:131]\n\t"
      "v_readlane_b32 s71, v252, s44\n\t"
      "v_mfma_f32_16x16x32_bf16 v[128:131], v[188:191], v[124:127], v[128:131]\n\t"
      "v_subrev_u32_e32 v240, 0x9e3779b9, v243\n\t"
      "s_sub_u32 s84, s84, 4\n\t"
      "v_mfma_f32_16x16x32_bf16 v[132:135], a[20:23], v[80:83], v[28:31]\n\t"
      "ds_read_b128 v[76:79], v197 offset:512\n\t"
      "v_mfma_f32_16x16x32_bf16 v[132:135], a[44:47], v[84:87], v[132:135]\n\t"
      "buffer_load_dwordx4 v208, s[20:23], s73 offen offset:1024 lds\n\t"
      "v_mfma_f32_16x16x32_bf16 v[132:135], a[68:71], v[88:91], v[132:135]\n\t"
      "v_cvt_pk_bf16_f32 v136, v128, v129\n\t"
      "v_mfma_f32_16x16x32_bf16 v[132:135], a[92:95], v[92:95], v[132:135]\n\t"
      "v_cvt_pk_bf16_f32 v137, v130, v131\n\t"
      "s_lshr_b32 s60, s71, 16\n\t"
      "v_mfma_f32_16x16x32_bf16 v[132:135], a[116:119], v[96:99], v[132:135]\n\t"
      "s_and_b32 s86, s71, 0xff\n\t"
      "v_mfma_f32_16x16x32_bf16 v[132:135], a[140:143], v[100:103], v[132:135]\n\t"
      "buffer_store_dword v240, v244, s[24:27], s67 offen offset:4 sc1\n\t"
      "s_bfe_u32 s71, s71, 0x80008\n\t"
      "v_mfma_f32_16x16x32_bf16 v[132:135], a[164:167], v[104:107], v[132:135]\n\t"
      "s_max_i32 s84, s84, s86\n\t"
      "s_mov_b32 s45, s46\n\t"
      "v_mfma_f32_16x16x32_bf16 v[132:135], a[188:191], v[108:111], v[132:135]\n\t"
      "s_add_u32 s70, s70, -1443\n\t"
      "s_mov_b32 s46, s47\n\t"
      "v_mfma_f32_16x16x32_bf16 v[132:135], a[212:215], v[112:115], v[132:135]\n\t"
      "s_add_u32 s82, s82, s84\n\t"
      "s_mov_b32 s47, s48\n\t"
      "v_mfma_f32_16x16x32_bf16 v[132:135], a[236:239], v[116:119], v[132:135]\n\t"
      "ds_write_b64 v219, v[136:137] offset:12288\n\t"
      "v_mfma_f32_16x16x32_bf16 v[132:135], v[168:171], v[120:123], v[132:135]\n\t"
      "v_add_u32_e32 v6, 1, v6\n\t"
      "v_mfma_f32_16x16x32_bf16 v[132:135], v[192:195], v[124:127], v[132:135]\n\t"
      "s_waitcnt lgkmcnt(3)\n\t"
      "buffer_store_dwordx4 v[148:151], v208, s[28:31], s72 offen offset:2048 sc1\n\t"
      ".p2align 6\n\t"
      ".Lloop_%=:\n\t"
      "v_mfma_f32_16x16x32_bf16 v[128:131], a[0:3], v[32:35], v[8:11]\n\t"
      "s_waitcnt vmcnt(17)\n\t"
      "v_mfma_f32_16x16x32_bf16 v[128:131], a[24:27], v[36:39], v[128:131]\n\t"
      "v_readlane_b32 s72, v249, s44\n\t"
      "v_mfma_f32_16x16x32_bf16 v[128:131], a[48:51], v[40:43], v[128:131]\n\t"
      "v_cvt_pk_bf16_f32 v138, v132, v133\n\t"
      "v_cvt_pk_bf16_f32 v139, v134, v135\n\t"
      "v_mfma_f32_16x16x32_bf16 v[128:131], a[72:75], v[44:47], v[128:131]\n\t"
      "ds_write_b32 v219, v138 offset:12296\n\t"
      "v_mfma_f32_16x16x32_bf16 v[128:131], a[96:99], v[48:51], v[128:131]\n\t"
      "ds_write_b32 v219, v139 offset:12300\n\t"
      "v_mfma_f32_16x16x32_bf16 v[128:131], a[120:123], v[52:55], v[128:131]\n\t"
      "ds_write_b32 v238, v6\n\t"
      "v_mfma_f32_16x16x32_bf16 v[128:131], a[144:147], v[56:59], v[128:131]\n\t"
      "buffer_load_dwordx4 v208, s[20:23], s73 offen offset:2048 lds\n\t"
      "v_mfma_f32_16x16x32_bf16 v[128:131], a[168:171], v[60:63], v[128:131]\n\t"
      "s_waitcnt lgkmcnt(4)\n\t"
      "v_add_u32_e32 v217, s46, v204\n\t"
      "v_mfma_f32_16x16x32_bf16 v[128:131], a[192:195], v[64:67], v[128:131]\n\t"
      "v_add_u32_e32 v218, s46, v205\n\t"
      "v_mfma_f32_16x16x32_bf16 v[128:131], a[216:219], v[68:71], v[128:131]\n\t"
      "v_add_u32_e32 v219, s46, v206\n\t"
      "v_mfma_f32_16x16x32_bf16 v[128:131], a[240:243], v[72:75], v[128:131]\n\t"
      "v_mfma_f32_16x16x32_bf16 v[128:131], v[172:175], v[76:79], v[128:131]\n\t"
      "s_cmp_gt_i32 s83, 1\n\t"
      "s_cbranch_scc1 .LslpA_%=\n\t"
      ".LretA_%=:\n\t"
      "v_mfma_f32_16x16x32_bf16 v[132:135], a[4:7], v[32:35], v[12:15]\n\t"
      "ds_read_b32 v7, v239\n\t"
      "v_mfma_f32_16x16x32_bf16 v[132:135], a[28:31], v[36:39], v[132:135]\n\t"
      "v_mfma_f32_16x16x32_bf16 v[132:135], a[52:55], v[40:43], v[132:135]\n\t"
      "v_cvt_pk_bf16_f32 v136, v128, v129\n\t"
      "v_cvt_pk_bf16_f32 v137, v130, v131\n\t"
      "v_mfma_f32_16x16x32_bf16 v[132:135], a[76:79], v[44:47], v[132:135]\n\t"
      "v_mfma_f32_16x16x32_bf16 v[132:135], a[100:103], v[48:51], v[132:135]\n\t"
      "s_waitcnt lgkmcnt(0)\n\t"
      "v_cmp_gt_u32_e32 vcc, v6, v7\n\t"
      "v_mfma_f32_16x16x32_bf16 v[132:135], a[124:127], v[52:55], v[132:135]\n\t"
      "s_cbranch_vccnz .LsbA_%=\n\t"
      ".LsrA_%=:\n\t"
      "v_mfma_f32_16x16x32_bf16 v[132:135], a[148:151], v[56:59], v[132:135]\n\t"
      "ds_read_b128 v[140:143], v220 offset:12288\n\t"
      "v_mfma_f32_16x16x32_bf16 v[132:135], a[172:175], v[60:63], v[132:135]\n\t"
      "ds_write_b32 v217, v136 offset:0\n\t"
      "v_mfma_f32_16x16x32_bf16 v[132:135], a[196:199], v[64:67], v[132:135]\n\t"
      "ds_write_b32 v217, v137 offset:4\n\t"
      "v_mfma_f32_16x16x32_bf16 v[132:135], a[220:223], v[68:71], v[132:135]\n\t"
      "ds_read_b128 v[80:83], v196 offset:12288\n\t"
      "v_mfma_f32_16x16x32_bf16 v[132:135], a[244:247], v[72:75], v[132:135]\n\t"
      "v_readlane_b32 s73, v249, s85\n\t"
      "v_mfma_f32_16x16x32_bf16 v[132:135], v[176:179], v[76:79], v[132:135]\n\t"
      "s_waitcnt lgkmcnt(3)\n\t"
      "buffer_store_dwordx4 v[140:143], v209, s[28:31], s72 offen sc1\n\t"
      "v_mfma_f32_16x16x32_bf16 v[128:131], a[8:11], v[32:35], v[16:19]\n\t"
      "ds_read_b128 v[84:87], v197 offset:12288\n\t"
      "v_mfma_f32_16x16x32_bf16 v[128:131], a[32:35], v[36:39], v[128:131]\n\t"
      "s_memrealtime s[80:81]\n\t"
      "v_mfma_f32_16x16x32_bf16 v[128:131], a[56:59], v[40:43], v[128:131]\n\t"
      "v_cvt_pk_bf16_f32 v138, v132, v133\n\t"
      "v_cvt_pk_bf16_f32 v139, v134, v135\n\t"
      "v_mfma_f32_16x16x32_bf16 v[128:131], a[80:83], v[44:47], v[128:131]\n\t"
      "ds_write_b32 v217, v138 offset:8\n\t"
      "v_mfma_f32_16x16x32_bf16 v[128:131], a[104:107], v[48:51], v[128:131]\n\t"
      "ds_write_b32 v217, v139 offset:12\n\t"
      "v_mfma_f32_16x16x32_bf16 v[128:131], a[128:131], v[52:55], v[128:131]\n\t"
      "ds_read_b128 v[88:91], v198 offset:12288\n\t"
      "v_mfma_f32_16x16x32_bf16 v[128:131], a[152:155], v[56:59], v[128:131]\n\t"
      "s_add_u32 m0, s45, 0x3000\n\t"
      "v_mfma_f32_16x16x32_bf16 v[128:131], a[176:179], v[60:63], v[128:131]\n\t"
      "ds_read_b128 v[92:95], v199 offset:12288\n\t"
      "v_mfma_f32_16x16x32_bf16 v[128:131], a[200:203], v[64:67], v[128:131]\n\t"
      "s_add_u32 s48, s47, 0x6000\n\t"
      "v_mfma_f32_16x16x32_bf16 v[128:131], a[224:227], v[68:71], v[128:131]\n\t"
      "ds_read_b128 v[96:99], v197 offset:12544\n\t"
      "v_mfma_f32_16x16x32_bf16 v[128:131], a[248:251], v[72:75], v[128:131]\n\t"
      "s_cmp_eq_u32 s48, s50\n\t"
      "s_cselect_b32 s48, s41, s48\n\t"
      "v_mfma_f32_16x16x32_bf16 v[128:131], v[180:183], v[76:79], v[128:131]\n\t"
      "buffer_load_dwordx4 v209, s[20:23], s73 offen lds\n\t"
      "v_mfma_f32_16x16x32_bf16 v[132:135], a[12:15], v[32:35], v[20:23]\n\t"
      "ds_read_b128 v[100:103], v198 offset:12544\n\t"
      "v_mfma_f32_16x16x32_bf16 v[132:135], a[36:39], v[36:39], v[132:135]\n\t"
      "v_mfma_f32_16x16x32_bf16 v[132:135], a[60:63], v[40:43], v[132:135]\n\t"
      "v_cvt_pk_bf16_f32 v136, v128, v129\n\t"
      "v_cvt_pk_bf16_f32 v137, v130, v131\n\t"
      "v_mfma_f32_16x16x32_bf16 v[132:135], a[84:87], v[44:47], v[132:135]\n\t"
      "ds_write_b32 v218, v136 offset:0\n\t"
      "v_mfma_f32_16x16x32_bf16 v[132:135], a[108:111], v[48:51], v[132:135]\n\t"
      "ds_write_b32 v218, v137 offset:4\n\t"
      "v_mfma_f32_16x16x32_bf16 v[132:135], a[132:135], v[52:55], v[132:135]\n\t"
      "ds_read_b128 v[144:147], v220 offset:13312\n\t"
      "v_mfma_f32_16x16x32_bf16 v[132:135], a[156:159], v[56:59], v[132:135]\n\t"
      "ds_read_b128 v[104:107], v199 offset:12544\n\t"
      "v_mfma_f32_16x16x32_bf16 v[132:135], a[180:183], v[60:63], v[132:135]\n\t"
      "s_sub_u32 s84, s84, 4\n\t"
      "v_mfma_f32_16x16x32_bf16 v[132:135], a[204:207], v[64:67], v[132:135]\n\t"
      "s_max_i32 s84, s84, s86\n\t"
      "v_mfma_f32_16x16x32_bf16 v[132:135], a[228:231], v[68:71], v[132:135]\n\t"
      "ds_read_b128 v[108:111], v196 offset:12544\n\t"
      "v_mfma_f32_16x16x32_bf16 v[132:135], a[252:255], v[72:75], v[132:135]\n\t"
      "v_mfma_f32_16x16x32_bf16 v[132:135], v[184:187], v[76:79], v[132:135]\n\t"
      "s_waitcnt lgkmcnt(2)\n\t"
      "buffer_store_dwordx4 v[144:147], v209, s[28:31], s72 offen offset:1024 sc1\n\t"
      "v_mfma_f32_16x16x32_bf16 v[128:131], a[16:19], v[32:35], v[24:27]\n\t"
      "ds_read_b128 v[112:115], v198 offset:12800\n\t"
      "v_mfma_f32_16x16x32_bf16 v[128:131], a[40:43], v[36:39], v[128:131]\n\t"
      "v_mfma_f32_16x16x32_bf16 v[128:131], a[64:67], v[40:43], v[128:131]\n\t"
      "v_cvt_pk_bf16_f32 v138, v132, v133\n\t"
      "v_cvt_pk_bf16_f32 v139, v134, v135\n\t"
      "v_mfma_f32_16x16x32_bf16 v[128:131], a[88:91], v[44:47], v[128:131]\n\t"
      "ds_write_b32 v218, v138 offset:8\n\t"
      "v_mfma_f32_16x16x32_bf16 v[128:131], a[112:115], v[48:51], v[128:131]\n\t"
      "ds_write_b32 v218, v139 offset:12\n\t"
      "v_mfma_f32_16x16x32_bf16 v[128:131], a[136:139], v[52:55], v[128:131]\n\t"
      "s_sub_u32 s87, s80, s52\n\t"
      "v_mfma_f32_16x16x32_bf16 v[128:131], a[160:163], v[56:59], v[128:131]\n\t"
      "s_sub_u32 s83, s82, s87\n\t"
      "ds_read_b128 v[116:119], v199 offset:12800\n\t"
      "v_mfma_f32_16x16x32_bf16 v[128:131], a[184:187], v[60:63], v[128:131]\n\t"
      "s_max_i32 s82, s82, s87\n\t"
      "v_mfma_f32_16x16x32_bf16 v[128:131], a[208:211], v[64:67], v[128:131]\n\t"
      "s_sub_u32 s61, s70, s82\n\t"
      "v_mfma_f32_16x16x32_bf16 v[128:131], a[232:235], v[68:71], v[128:131]\n\t"
      "s_mul_i32 s61, s61, s60\n\t"
      "ds_read_b128 v[120:123], v196 offset:12800\n\t"
      "v_mfma_f32_16x16x32_bf16 v[128:131], v[164:167], v[72:75], v[128:131]\n\t"
      "s_ashr_i32 s61, s61, 8\n\t"
      "v_mfma_f32_16x16x32_bf16 v[128:131], v[188:191], v[76:79], v[128:131]\n\t"
      "buffer_load_dwordx4 v209, s[20:23], s73 offen offset:1024 lds\n\t"
      "v_mfma_f32_16x16x32_bf16 v[132:135], a[20:23], v[32:35], v[28:31]\n\t"
      "s_add_u32 s61, s61, s71\n\t"
      "ds_read_b128 v[124:127], v197 offset:12800\n\t"
      "v_mfma_f32_16x16x32_bf16 v[132:135], a[44:47], v[36:39], v[132:135]\n\t"
      "s_max_i32 s61, s61, s84\n\t"
      "ds_read_b128 v[148:151], v220 offset:14336\n\t"
      "v_mfma_f32_16x16x32_bf16 v[132:135], a[68:71], v[40:43], v[132:135]\n\t"
      "s_add_u32 s82, s82, s61\n\t"
      "v_cvt_pk_bf16_f32 v136, v128, v129\n\t"
      "v_cvt_pk_bf16_f32 v137, v130, v131\n\t"
      "v_mfma_f32_16x16x32_bf16 v[132:135], a[92:95], v[44:47], v[132:135]\n\t"
      "ds_write_b32 v219, v136 offset:0\n\t"
      "v_mfma_f32_16x16x32_bf16 v[132:135], a[116:119], v[48:51], v[132:135]\n\t"
      "ds_write_b32 v219, v137 offset:4\n\t"
      "v_mfma_f32_16x16x32_bf16 v[132:135], a[140:143], v[52:55], v[132:135]\n\t"
      "s_add_u32 s70, s70, s71\n\t"
      "v_add_u32_e32 v220, s46, v207\n\t"
      "v_mfma_f32_16x16x32_bf16 v[132:135], a[164:167], v[56:59], v[132:135]\n\t"
      "v_add_u32_e32 v6, 1, v6\n\t"
      "v_mfma_f32_16x16x32_bf16 v[132:135], a[188:191], v[60:63], v[132:135]\n\t"
      "v_mfma_f32_16x16x32_bf16 v[132:135], a[212:215], v[64:67], v[132:135]\n\t"
      "s_add_u32 s44, s44, 1\n\t"
      "v_mfma_f32_16x16x32_bf16 v[132:135], a[236:239], v[68:71], v[132:135]\n\t"
      "s_add_u32 s85, s85, 1\n\t"
      "v_mfma_f32_16x16x32_bf16 v[132:135], v[168:171], v[72:75], v[132:135]\n\t"
      "v_mfma_f32_16x16x32_bf16 v[132:135], v[192:195], v[76:79], v[132:135]\n\t"
      "s_waitcnt lgkmcnt(2)\n\t"
      "buffer_store_dwordx4 v[148:151], v209, s[28:31], s72 offen offset:2048 sc1\n\t"
      "v_mfma_f32_16x16x32_bf16 v[128:131], a[0:3], v[80:83], v[8:11]\n\t"
      "s_waitcnt vmcnt(17)\n\t"
      "v_mfma_f32_16x16x32_bf16 v[128:131], a[24:27], v[84:87], v[128:131]\n\t"
      "v_readlane_b32 s72, v248, s44\n\t"
      "v_mfma_f32_16x16x32_bf16 v[128:131], a[48:51], v[88:91], v[128:131]\n\t"
      "v_cvt_pk_bf16_f32 v138, v132, v133\n\t"
      "v_cvt_pk_bf16_f32 v139, v134, v135\n\t"
      "v_mfma_f32_16x16x32_bf16 v[128:131], a[72:75], v[92:95], v[128:131]\n\t"
      "ds_write_b32 v219, v138 offset:8\n\t"
      "v_mfma_f32_16x16x32_bf16 v[128:131], a[96:99], v[96:99], v[128:131]\n\t"
      "ds_write_b32 v219, v139 offset:12\n\t"
      "v_mfma_f32_16x16x32_bf16 v[128:131], a[120:123], v[100:103], v[128:131]\n\t"
      "ds_write_b32 v238, v6\n\t"
      "v_mfma_f32_16x16x32_bf16 v[128:131], a[144:147], v[104:107], v[128:131]\n\t"
      "buffer_load_dwordx4 v209, s[20:23], s73 offen offset:2048 lds\n\t"
      "v_mfma_f32_16x16x32_bf16 v[128:131], a[168:171], v[108:111], v[128:131]\n\t"
      "v_add_u32_e32 v196, s47, v200\n\t"
      "v_mfma_f32_16x16x32_bf16 v[128:131], a[192:195], v[112:115], v[128:131]\n\t"
      "v_add_u32_e32 v197, s47, v201\n\t"
      "v_mfma_f32_16x16x32_bf16 v[128:131], a[216:219], v[116:119], v[128:131]\n\t"
      "v_add_u32_e32 v198, s47, v202\n\t"
      "v_mfma_f32_16x16x32_bf16 v[128:131], a[240:243], v[120:123], v[128:131]\n\t"
      "v_add_u32_e32 v199, s47, v203\n\t"
      "v_mfma_f32_16x16x32_bf16 v[128:131], v[172:175], v[124:127], v[128:131]\n\t"
      "s_cmp_gt_i32 s83, 1\n\t"
      "s_cbranch_scc1 .LslpB_%=\n\t"
      ".LretB_%=:\n\t"
      "v_mfma_f32_16x16x32_bf16 v[132:135], a[4:7], v[80:83], v[12:15]\n\t"
      "ds_read_b32 v7, v239\n\t"
      "v_mfma_f32_16x16x32_bf16 v[132:135], a[28:31], v[84:87], v[132:135]\n\t"
      "v_mfma_f32_16x16x32_bf16 v[132:135], a[52:55], v[88:91], v[132:135]\n\t"
      "v_cvt_pk_bf16_f32 v136, v128, v129\n\t"
      "v_cvt_pk_bf16_f32 v137, v130, v131\n\t"
      "v_mfma_f32_16x16x32_bf16 v[132:135], a[76:79], v[92:95], v[132:135]\n\t"
      "v_mfma_f32_16x16x32_bf16 v[132:135], a[100:103], v[96:99], v[132:135]\n\t"
      "s_waitcnt lgkmcnt(0)\n\t"
      "v_cmp_gt_u32_e32 vcc, v6, v7\n\t"
      "v_mfma_f32_16x16x32_bf16 v[132:135], a[124:127], v[100:103], v[132:135]\n\t"
      "s_cbranch_vccnz .LsbB_%=\n\t"
      ".LsrB_%=:\n\t"
      "v_mfma_f32_16x16x32_bf16 v[132:135], a[148:151], v[104:107], v[132:135]\n\t"
      "ds_read_b128 v[140:143], v220 offset:0\n\t"
      "v_mfma_f32_16x16x32_bf16 v[132:135], a[172:175], v[108:111], v[132:135]\n\t"
      "ds_write_b32 v217, v136 offset:12288\n\t"
      "v_mfma_f32_16x16x32_bf16 v[132:135], a[196:199], v[112:115], v[132:135]\n\t"
      "ds_write_b32 v217, v137 offset:12292\n\t"
      "v_mfma_f32_16x16x32_bf16 v[132:135], a[220:223], v[116:119], v[132:135]\n\t"
      "ds_read_b128 v[32:35], v196 offset:0\n\t"
      "v_mfma_f32_16x16x32_bf16 v[132:135], a[244:247], v[120:123], v[132:135]\n\t"
      "v_readlane_b32 s73, v248, s85\n\t"
      "v_mfma_f32_16x16x32_bf16 v[132:135], v[176:179], v[124:127], v[132:135]\n\t"
      "s_waitcnt lgkmcnt(3)\n\t"
      "buffer_store_dwordx4 v[140:143], v208, s[28:31], s72 offen sc1\n\t"
      "v_mfma_f32_16x16x32_bf16 v[128:131], a[8:11], v[80:83], v[16:19]\n\t"
      "ds_read_b128 v[36:39], v197 offset:0\n\t"
      "v_mfma_f32_16x16x32_bf16 v[128:131], a[32:35], v[84:87], v[128:131]\n\t"
      "s_memrealtime s[80:81]\n\t"
      "v_mfma_f32_16x16x32_bf16 v[128:131], a[56:59], v[88:91], v[128:131]\n\t"
      "v_cvt_pk_bf16_f32 v138, v132, v133\n\t"
      "v_cvt_pk_bf16_f32 v139, v134, v135\n\t"
      "v_mfma_f32_16x16x32_bf16 v[128:131], a[80:83], v[92:95], v[128:131]\n\t"
      "ds_write_b32 v217, v138 offset:12296\n\t"
      "v_mfma_f32_16x16x32_bf16 v[128:131], a[104:107], v[96:99], v[128:131]\n\t"
      "ds_write_b32 v217, v139 offset:12300\n\t"
      "v_mfma_f32_16x16x32_bf16 v[128:131], a[128:131], v[100:103], v[128:131]\n\t"
      "ds_read_b128 v[40:43], v198 offset:0\n\t"
      "v_mfma_f32_16x16x32_bf16 v[128:131], a[152:155], v[104:107], v[128:131]\n\t"
      "s_mov_b32 m0, s46\n\t"
      "v_mfma_f32_16x16x32_bf16 v[128:131], a[176:179], v[108:111], v[128:131]\n\t"
      "ds_read_b128 v[44:47], v199 offset:0\n\t"
      "v_mfma_f32_16x16x32_bf16 v[128:131], a[200:203], v[112:115], v[128:131]\n\t"
      "v_mfma_f32_16x16x32_bf16 v[128:131], a[224:227], v[116:119], v[128:131]\n\t"
      "ds_read_b128 v[48:51], v197 offset:256\n\t"
      "v_mfma_f32_16x16x32_bf16 v[128:131], a[248:251], v[120:123], v[128:131]\n\t"
      "v_mfma_f32_16x16x32_bf16 v[128:131], v[180:183], v[124:127], v[128:131]\n\t"
      "buffer_load_dwordx4 v208, s[20:23], s73 offen lds\n\t"
      "v_mfma_f32_16x16x32_bf16 v[132:135], a[12:15], v[80:83], v[20:23]\n\t"
      "ds_read_b128 v[52:55], v198 offset:256\n\t"
      "v_mfma_f32_16x16x32_bf16 v[132:135], a[36:39], v[84:87], v[132:135]\n\t"
      "v_readlane_b32 s71, v252, s44\n\t"
      "v_mfma_f32_16x16x32_bf16 v[132:135], a[60:63], v[88:91], v[132:135]\n\t"
      "v_cvt_pk_bf16_f32 v136, v128, v129\n\t"
      "v_cvt_pk_bf16_f32 v137, v130, v131\n\t"
      "v_mfma_f32_16x16x32_bf16 v[132:135], a[84:87], v[92:95], v[132:135]\n\t"
      "ds_write_b32 v218, v136 offset:12288\n\t"
      "v_mfma_f32_16x16x32_bf16 v[132:135], a[108:111], v[96:99], v[132:135]\n\t"
      "s_lshr_b32 s60, s71, 16\n\t"
      "ds_write_b32 v218, v137 offset:12292\n\t"
      "v_mfma_f32_16x16x32_bf16 v[132:135], a[132:135], v[100:103], v[132:135]\n\t"
      "s_and_b32 s86, s71, 0xff\n\t"
      "ds_read_b128 v[144:147], v220 offset:1024\n\t"
      "v_mfma_f32_16x16x32_bf16 v[132:135], a[156:159], v[104:107], v[132:135]\n\t"
      "s_bfe_u32 s71, s71, 0x80008\n\t"
      "ds_read_b128 v[56:59], v199 offset:256\n\t"
      "v_mfma_f32_16x16x32_bf16 v[132:135], a[180:183], v[108:111], v[132:135]\n\t"
      "s_sub_u32 s84, s84, 4\n\t"
      "v_mfma_f32_16x16x32_bf16 v[132:135], a[204:207], v[112:115], v[132:135]\n\t"
      "s_max_i32 s84, s84, s86\n\t"
      "v_mfma_f32_16x16x32_bf16 v[132:135], a[228:231], v[116:119], v[132:135]\n\t"
      "ds_read_b128 v[60:63], v196 offset:256\n\t"
      "v_mfma_f32_16x16x32_bf16 v[132:135], a[252:255], v[120:123], v[132:135]\n\t"
      "v_mfma_f32_16x16x32_bf16 v[132:135], v[184:187], v[124:127], v[132:135]\n\t"
      "s_waitcnt lgkmcnt(2)\n\t"
      "buffer_store_dwordx4 v[144:147], v208, s[28:31], s72 offen offset:1024 sc1\n\t"
      "v_mfma_f32_16x16x32_bf16 v[128:131], a[16:19], v[80:83], v[24:27]\n\t"
      "ds_read_b128 v[64:67], v198 offset:512\n\t"
      "v_mfma_f32_16x16x32_bf16 v[128:131], a[40:43], v[84:87], v[128:131]\n\t"
      "v_mfma_f32_16x16x32_bf16 v[128:131], a[64:67], v[88:91], v[128:131]\n\t"
      "v_cvt_pk_bf16_f32 v138, v132, v133\n\t"
      "v_cvt_pk_bf16_f32 v139, v134, v135\n\t"
      "v_mfma_f32_16x16x32_bf16 v[128:131], a[88:91], v[92:95], v[128:131]\n\t"
      "ds_write_b32 v218, v138 offset:12296\n\t"
      "v_mfma_f32_16x16x32_bf16 v[128:131], a[112:115], v[96:99], v[128:131]\n\t"
      "ds_write_b32 v218, v139 offset:12300\n\t"
      "v_mfma_f32_16x16x32_bf16 v[128:131], a[136:139], v[100:103], v[128:131]\n\t"
      "s_sub_u32 s87, s80, s52\n\t"
      "v_mfma_f32_16x16x32_bf16 v[128:131], a[160:163], v[104:107], v[128:131]\n\t"
      "s_sub_u32 s83, s82, s87\n\t"
      "ds_read_b128 v[68:71], v199 offset:512\n\t"
      "v_mfma_f32_16x16x32_bf16 v[128:131], a[184:187], v[108:111], v[128:131]\n\t"
      "s_max_i32 s82, s82, s87\n\t"
      "v_mfma_f32_16x16x32_bf16 v[128:131], a[208:211], v[112:115], v[128:131]\n\t"
      "s_sub_u32 s61, s70, s82\n\t"
      "v_mfma_f32_16x16x32_bf16 v[128:131], a[232:235], v[116:119], v[128:131]\n\t"
      "s_mul_i32 s61, s61, s60\n\t"
      "ds_read_b128 v[72:75], v196 offset:512\n\t"
      "v_mfma_f32_16x16x32_bf16 v[128:131], v[164:167], v[120:123], v[128:131]\n\t"
      "s_ashr_i32 s61, s61, 8\n\t"
      "v_mfma_f32_16x16x32_bf16 v[128:131], v[188:191], v[124:127], v[128:131]\n\t"
      "buffer_load_dwordx4 v208, s[20:23], s73 offen offset:1024 lds\n\t"
      "v_mfma_f32_16x16x32_bf16 v[132:135], a[20:23], v[80:83], v[28:31]\n\t"
      "s_add_u32 s61, s61, s71\n\t"
      "ds_read_b128 v[76:79], v197 offset:512\n\t"
      "v_mfma_f32_16x16x32_bf16 v[132:135], a[44:47], v[84:87], v[132:135]\n\t"
      "s_max_i32 s61, s61, s84\n\t"
      "ds_read_b128 v[148:151], v220 offset:2048\n\t"
      "v_mfma_f32_16x16x32_bf16 v[132:135], a[68:71], v[88:91], v[132:135]\n\t"
      "s_add_u32 s82, s82, s61\n\t"
      "v_cvt_pk_bf16_f32 v136, v128, v129\n\t"
      "v_cvt_pk_bf16_f32 v137, v130, v131\n\t"
      "v_mfma_f32_16x16x32_bf16 v[132:135], a[92:95], v[92:95], v[132:135]\n\t"
      "ds_write_b32 v219, v136 offset:12288\n\t"
      "v_mfma_f32_16x16x32_bf16 v[132:135], a[116:119], v[96:99], v[132:135]\n\t"
      "ds_write_b32 v219, v137 offset:12292\n\t"
      "v_mfma_f32_16x16x32_bf16 v[132:135], a[140:143], v[100:103], v[132:135]\n\t"
      "s_add_u32 s70, s70, s71\n\t"
      "v_mfma_f32_16x16x32_bf16 v[132:135], a[164:167], v[104:107], v[132:135]\n\t"
      "s_mov_b32 s45, s46\n\t"
      "v_add_u32_e32 v6, 1, v6\n\t"
      "v_mfma_f32_16x16x32_bf16 v[132:135], a[188:191], v[108:111], v[132:135]\n\t"
      "s_mov_b32 s46, s47\n\t"
      "v_mfma_f32_16x16x32_bf16 v[132:135], a[212:215], v[112:115], v[132:135]\n\t"
      "s_mov_b32 s47, s48\n\t"
      "v_mfma_f32_16x16x32_bf16 v[132:135], a[236:239], v[116:119], v[132:135]\n\t"
      "v_mfma_f32_16x16x32_bf16 v[132:135], v[168:171], v[120:123], v[132:135]\n\t"
      "s_cmp_lg_u32 s44, s49\n\t"
      "v_mfma_f32_16x16x32_bf16 v[132:135], v[192:195], v[124:127], v[132:135]\n\t"
      "s_waitcnt lgkmcnt(2)\n\t"
      "buffer_store_dwordx4 v[148:151], v208, s[28:31], s72 offen offset:2048 sc1\n\t"
      "s_cbranch_scc1 .Lloop_%=\n\t"
      "v_readlane_b32 s72, v249, s44\n\t"
      "s_nop 6\n\t"
      "v_cvt_pk_bf16_f32 v138, v132, v133\n\t"
      "v_cvt_pk_bf16_f32 v139, v134, v135\n\t"
      "ds_write_b64 v219, v[138:139] offset:12296\n\t"
      "s_waitcnt lgkmcnt(0)\n\t"
      "s_barrier\n\t"
      "ds_read_b128 v[32:35], v220 offset:12288\n\t"
      "ds_read_b128 v[36:39], v220 offset:13312\n\t"
      "ds_read_b128 v[40:43], v220 offset:14336\n\t"
      "s_waitcnt lgkmcnt(2)\n\t"
      "buffer_store_dwordx4 v[32:35], v209, s[28:31], s72 offen sc1\n\t"
      "s_waitcnt lgkmcnt(1)\n\t"
      "buffer_store_dwordx4 v[36:39], v209, s[28:31], s72 offen offset:1024 sc1\n\t"
      "s_waitcnt lgkmcnt(0)\n\t"
      "buffer_store_dwordx4 v[40:43], v209, s[28:31], s72 offen offset:2048 sc1\n\t"
      "s_endpgm\n\t"
      ".LslpA_%=:\n\t"
      "s_min_i32 s83, s83, 0x190\n\t"
      ".Lsl2A_%=:\n\t"
      "s_sleep 1\n\t"
      "s_sub_u32 s83, s83, 3\n\t"
      "s_cmp_gt_i32 s83, 1\n\t"
      "s_cbranch_scc1 .Lsl2A_%=\n\t"
      "s_branch .LretA_%=\n\t"
      ".LslpB_%=:\n\t"
      "s_min_i32 s83, s83, 0x190\n\t"
      ".Lsl2B_%=:\n\t"
      "s_sleep 1\n\t"
      "s_sub_u32 s83, s83, 3\n\t"
      "s_cmp_gt_i32 s83, 1\n\t"
      "s_cbranch_scc1 .Lsl2B_%=\n\t"
      "s_branch .LretB_%=\n\t"
      ".Lgs_%=:\n\t"
      "s_sleep 8\n\t"
      "buffer_load_dwordx2 v[240:241], v245, s[24:27], 0 offen sc1\n\t"
      "s_memrealtime s[76:77]\n\t"
      "s_waitcnt vmcnt(0) lgkmcnt(0)\n\t"
      "v_subrev_u32_e32 v242, s67, v240\n\t"
      "v_xor_b32_e32 v241, s63, v241\n\t"
      "v_lshl_or_b32 v240, v242, 20, v241\n\t"
      "v_lshrrev_b32_e32 v242, 12, v242\n\t"
      "v_cmp_ne_u32_e32 vcc, 0, v240\n\t"
      "s_nop 0\n\t"
      "s_cbranch_vccz .Lgr_%=\n\t"
      "s_sub_u32 s76, s76, s52\n\t"
      "s_cmp_lt_u32 s76, 2000\n\t"
      "s_cbranch_scc1 .Lgs_%=\n\t"
      "v_mov_b32_e32 v242, 10\n\t"
      "s_branch .Lgr_%=\n\t"
      ".LsbA_%=:\n\t"
      "ds_read_b32 v7, v239\n\t"
      "s_waitcnt lgkmcnt(0)\n\t"
      "v_cmp_gt_u32_e32 vcc, v6, v7\n\t"
      "s_nop 0\n\t"
      "s_cbranch_vccnz .LsbA_%=\n\t"
      "s_branch .LsrA_%=\n\t"
      ".LsbB_%=:\n\t"
      "ds_read_b32 v7, v239\n\t"
      "s_waitcnt lgkmcnt(0)\n\t"
      "v_cmp_gt_u32_e32 vcc, v6, v7\n\t"
      "s_nop 0\n\t"
      "s_cbranch_vccnz .LsbB_%=\n\t"
      "s_branch .LsrB_%=\n\t"
      ".Lend_%=:\n\t"
      :
      : "{s[20:21]}"(xa), "{s[16:17]}"(wa), "{s[36:37]}"(ba), "{s[28:29]}"(oa), "{s[24:25]}"(sa), "{s[52:53]}"(rs),
        "{s[22:23]}"(0x0002000006c00000ull), "{s[26:27]}"(0x0002000000000808ull), "{s[30:31]}"(0x0002000006c00000ull),
        "{s[18:19]}"(0x0002000000048000ull), "{s[38:39]}"(0x0002000000000300ull),
        "{s40}"(t0), "{s41}"(p0), "{s43}"(lds0), "{s94}"(ty),
        "{v208}"(ga), "{v209}"(gb),
        "{v211}"(woff[0]), "{v212}"(woff[1]), "{v213}"(woff[2]), "{v215}"(boff),
        "{s95}"(tres), "{s92}"(pre1), "{s[60:61]}"(pres), "{s[62:63]}"(ep), "{s77}"(pfire), "{s98}"(((tidx >> 3) * 4u + (tidx & 3u)) * 128u), "{v238}"(vfw), "{v239}"(vfr),
        "{v248}"(ta), "{v249}"(ta), "{v251}"(t1), "{v250}"(mk), "{s74}"(plm), "{s75}"(plb),
        "{s[34:35]}"(__builtin_amdgcn_kernarg_segment_ptr()),
        "{v244}"(vpub), "{v210}"(vbmp), "{v245}"(vgv), "{v246}"(vxw), "{v247}"(vxr),
        "{v200}"(cfa[0]), "{v201}"(cfa[1]), "{v202}"(cfa[2]), "{v203}"(cfa[3]),
        "{v204}"(csw[0]), "{v205}"(csw[1]), "{v206}"(csw[2]), "{v207}"(csr),
        "{v214}"(tbase)
      : "memory", "scc", "vcc",
      "s42", "s44", "s45", "s46", "s47", "s48", "s49", "s50", "s51", "s54", "s55", "s56", "s57", "s58", "s59", "s64", "s65", "s66", "s67", "s68",
      "s69", "s70", "s71", "s72", "s73", "s76", "s78", "s79", "s80", "s81", "s82", "s83", "s84", "s85", "s86", "s87", "s88", "s89", "s90", "s91",
      "s93", "s96", "s97", "s99",
      "v1", "v2", "v3", "v4", "v5", "v6", "v7", "v8", "v9", "v10", "v11", "v12", "v13", "v14", "v15", "v16", "v17", "v18", "v19", "v20", "v21", "v22",
      "v23", "v24", "v25", "v26", "v27", "v28", "v29", "v30", "v31", "v32", "v33", "v34", "v35", "v36", "v37", "v38", "v39", "v40", "v41", "v42",
      "v43", "v44", "v45", "v46", "v47", "v48", "v49", "v50", "v51", "v52", "v53", "v54", "v55", "v56", "v57", "v58", "v59", "v60", "v61", "v62",
      "v63", "v64", "v65", "v66", "v67", "v68", "v69", "v70", "v71", "v72", "v73", "v74", "v75", "v76", "v77", "v78", "v79", "v80", "v81", "v82",
      "v83", "v84", "v85", "v86", "v87", "v88", "v89", "v90", "v91", "v92", "v93", "v94", "v95", "v96", "v97", "v98", "v99", "v100", "v101", "v102",
      "v103", "v104", "v105", "v106", "v107", "v108", "v109", "v110", "v111", "v112", "v113", "v114", "v115", "v116", "v117", "v118", "v119", "v120",
      "v121", "v122", "v123", "v124", "v125", "v126", "v127", "v128", "v129", "v130", "v131", "v132", "v133", "v134", "v135", "v136", "v137", "v138",
      "v139", "v140", "v141", "v142", "v143", "v144", "v145", "v146", "v147", "v148", "v149", "v150", "v151", "v152", "v153", "v154", "v155", "v156",
      "v157", "v158", "v159", "v160", "v161", "v162", "v163", "v164", "v165", "v166", "v167", "v168", "v169", "v170", "v171", "v172", "v173", "v174",
      "v175", "v176", "v177", "v178", "v179", "v180", "v181", "v182", "v183", "v184", "v185", "v186", "v187", "v188", "v189", "v190", "v191", "v192",
      "v193", "v194", "v195", "v196", "v197", "v198", "v199", "v216", "v217", "v218", "v219", "v220", "v221", "v222", "v223", "v224", "v225", "v226",
      "v227", "v228", "v229", "v230", "v231", "v232", "v233", "v234", "v235", "v236", "v237", "v240", "v241", "v242", "v243", "v252", "v253", "v254",
      "v255",
      "a0", "a1", "a2", "a3", "a4", "a5", "a6", "a7", "a8", "a9", "a10", "a11", "a12", "a13", "a14", "a15", "a16", "a17", "a18", "a19", "a20", "a21",
      "a22", "a23", "a24", "a25", "a26", "a27", "a28", "a29", "a30", "a31", "a32", "a33", "a34", "a35", "a36", "a37", "a38", "a39", "a40", "a41",
      "a42", "a43", "a44", "a45", "a46", "a47", "a48", "a49", "a50", "a51", "a52", "a53", "a54", "a55", "a56", "a57", "a58", "a59", "a60", "a61",
      "a62", "a63", "a64", "a65", "a66", "a67", "a68", "a69", "a70", "a71", "a72", "a73", "a74", "a75", "a76", "a77", "a78", "a79", "a80", "a81",
      "a82", "a83", "a84", "a85", "a86", "a87", "a88", "a89", "a90", "a91", "a92", "a93", "a94", "a95", "a96", "a97", "a98", "a99", "a100", "a101",
      "a102", "a103", "a104", "a105", "a106", "a107", "a108", "a109", "a110", "a111", "a112", "a113", "a114", "a115", "a116", "a117", "a118", "a119",
      "a120", "a121", "a122", "a123", "a124", "a125", "a126", "a127", "a128", "a129", "a130", "a131", "a132", "a133", "a134", "a135", "a136", "a137",
      "a138", "a139", "a140", "a141", "a142", "a143", "a144", "a145", "a146", "a147", "a148", "a149", "a150", "a151", "a152", "a153", "a154", "a155",
      "a156", "a157", "a158", "a159", "a160", "a161", "a162", "a163", "a164", "a165", "a166", "a167", "a168", "a169", "a170", "a171", "a172", "a173",
      "a174", "a175", "a176", "a177", "a178", "a179", "a180", "a181", "a182", "a183", "a184", "a185", "a186", "a187", "a188", "a189", "a190", "a191",
      "a192", "a193", "a194", "a195", "a196", "a197", "a198", "a199", "a200", "a201", "a202", "a203", "a204", "a205", "a206", "a207", "a208", "a209",
      "a210", "a211", "a212", "a213", "a214", "a215", "a216", "a217", "a218", "a219", "a220", "a221", "a222", "a223", "a224", "a225", "a226", "a227",
      "a228", "a229", "a230", "a231", "a232", "a233", "a234", "a235", "a236", "a237", "a238", "a239", "a240", "a241", "a242", "a243", "a244", "a245",
      "a246", "a247", "a248", "a249", "a250", "a251", "a252", "a253", "a254", "a255");
}

extern "C" void kernel_launch(void* const* d_in, const int* in_sizes, int n_in,
                              void* d_out, int out_size, void* d_ws, size_t ws_size,
                              hipStream_t stream) {
  const uint16_t* x = (const uint16_t*)d_in[0];
  const uint16_t* w = (const uint16_t*)d_in[1];
  const uint16_t* b = (const uint16_t*)d_in[2];
  uint16_t* out = (uint16_t*)d_out;
  const PaceTab pt = {{
    0x5b5bu, 0x5b5bu, 0x4646u, 0u,
    0x5b5bu, 0x5b5bu, 0x4646u, 0u,
    0x514fu, 0x514au, 0x4646u, 0u,
    0x4f4fu, 0x4b4au, 0x4646u, 0u,
    0x4f4fu, 0x4a4au, 0x4646u, 0u,
    0x4f4fu, 0x4a4au, 0x4646u, 0u,
    0x4f4fu, 0x4b4au, 0x4744u, 0u,
    0x11514fu, 0x114e48u, 0x114944u, 0u,
    0x13534eu, 0x135049u, 0x134c44u, 0u,
    0x16544eu, 0x16514au, 0x164e46u, 0u,
    0x1b544eu, 0x1b514au, 0x1b4e46u, 0u,
    0x22544eu, 0x22514au, 0x224e46u, 0u,
    0x2f544eu, 0x2f514au, 0x2f4e46u, 0u,
    0x49544eu, 0x49514au, 0x494e46u, 0u,
    0xab544eu, 0xab514au, 0xab4e46u, 0u,
    0x1004400u, 0x1004400u, 0x1004400u, 0u,
    0x1004400u, 0x1004400u, 0x1004400u, 0u,
    0x1004400u, 0x1004400u, 0x1004400u, 0u}};
  conv1x1_kernel<<<256, 256, 0, stream>>>(x, w, b, out, (unsigned long long*)d_ws, pt);
}
```

```cpp
#include <hip/hip_runtime.h>
#include <stdint.h>

#define LAS __attribute__((address_space(3)))

struct PaceTab { unsigned v[72]; };

__global__ __launch_bounds__(256, 1) void conv1x1_kernel(const uint16_t* __restrict__ x,
                                                         const uint16_t* __restrict__ w,
                                                         const uint16_t* __restrict__ bias,
                                                         uint16_t* __restrict__ out, unsigned long long* ws, PaceTab pt) {
  __shared__ __attribute__((aligned(1024))) unsigned char lds[161792];

  unsigned long long xa, wa, ba, oa, sa, rs;
  asm volatile(
      "s_load_dwordx2 s[16:17], %6, 0x8\n\t"
      "s_load_dwordx2 s[20:21], %6, 0x0\n\t"
      "s_load_dwordx2 s[36:37], %6, 0x10\n\t"
      "s_load_dwordx2 s[28:29], %6, 0x18\n\t"
      "s_load_dwordx2 s[24:25], %6, 0x20\n\t"
      "s_memrealtime s[52:53]\n\t"
      : "={s[20:21]}"(xa), "={s[16:17]}"(wa), "={s[36:37]}"(ba), "={s[28:29]}"(oa), "={s[24:25]}"(sa), "={s[52:53]}"(rs)
      : "s"(__builtin_amdgcn_kernarg_segment_ptr())
      : "memory");
  __builtin_amdgcn_sched_barrier(0);
  const unsigned tid  = threadIdx.x;
  unsigned lane = tid & 63;
  unsigned wv   = __builtin_amdgcn_readfirstlane(tid >> 6);
  const unsigned lds0 = (unsigned)(size_t)(LAS unsigned char*)lds;
  const unsigned blk  = blockIdx.x;
  const unsigned role = (blk >> 2) & 1u;
  unsigned pk = (blk & 3u) * 32u + (blk >> 3);
  const unsigned sst  = (role ^ (blk >> 4)) & 1u;
  const unsigned t0   = pk * 49152u + sst * 24576u;
  const unsigned p0   = lds0 + 3072u * wv;
  const unsigned lq = lane >> 4, ph = (lane >> 2) & 3, pl = lane & 3;
  const unsigned gs = 256u * lq + 64u * ((ph - lq) & 3) + 16u * (pl ^ ((4u - wv) & 3));
  const unsigned ga = gs + 3072u * (wv < 3u ? wv : 5u);
  const unsigned gb = gs + 3072u * (wv < 2u ? wv + 6u : wv + 1u);
  const unsigned pv = blk >> 6, ps = (blk & 63u) ^ (4u * (pv & 1u)), pgr = (ps >> 2) & 7u, pi = (ps & 3u) * 2u + (pv >> 1);
  const unsigned ppg = min(pgr * 8u, 52u) + (pi & (0x13777777u >> (4u * pgr)) & 7u);
  const unsigned pop = (ppg << 21) + ((ps & 3u) * 4u + pv) * 0x1c080u + 0x8000u;
  unsigned pfire = ((role ^ ppg) & 1u) << 13;
  const unsigned pfo = (blk >> 3) * 9216u;
  const unsigned pfv = tid * 16u;
  const unsigned ty = (blk >> 3) & 1u;
  const unsigned tidx = (blk >> 3) * 4u + wv, tsel = tidx >> 6;
  const unsigned toff = (tidx & 63u) < 54u ? (tidx & 63u) << 21 : 113246204u;
  unsigned tres, pre1;
  unsigned long long pres, ep;
  asm volatile(
      "s_add_u32 m0, s43, 0x27000\n\t"
      "s_add_u32 s51, s42, 0x1000\n\t"
      "s_waitcnt lgkmcnt(0)\n\t"
      "buffer_load_dwordx4 v216, s[16:19], s42 offen lds\n\t"
      "buffer_load_dwordx4 v216, s[16:19], s51 offen lds\n\t"
      "s_add_u32 s51, s42, 0x2000\n\t"
      "buffer_load_dwordx4 v216, s[16:19], s51 offen lds\n\t"
      "s_cmp_lg_u32 s41, s43\n\t"
      "s_cbranch_scc1 .Lw0pi_%=\n\t"
      "s_cmp_eq_u32 s78, 0\n\t"
      "s_cselect_b64 s[96:97], s[20:21], s[28:29]\n\t"
      "s_load_dword s92, s[96:97], s76 offset:0x0\n\t"
      "s_load_dwordx2 s[60:61], s[96:97], s76 offset:0x3300\n\t"
      ".Lw0pi_%=:\n\t"
      "s_mov_b32 m0, s41\n\t"
      "s_nop 0\n\t"
      "buffer_load_dwordx4 v208, s[20:23], s40 offen lds\n\t"
      "buffer_load_dwordx4 v208, s[20:23], s40 offen offset:1024 lds\n\t"
      "buffer_load_dwordx4 v208, s[20:23], s40 offen offset:2048 lds\n\t"
      "s_add_u32 m0, s41, 0x3000\n\t"
      "s_nop 0\n\t"
      "buffer_load_dwordx4 v209, s[20:23], s40 offen lds\n\t"
      "buffer_load_dwordx4 v209, s[20:23], s40 offen offset:1024 lds\n\t"
      "buffer_load_dwordx4 v209, s[20:23], s40 offen offset:2048 lds\n\t"
      "s_cmp_lg_u32 s41, s43\n\t"
      "s_cbranch_scc1 .Lw0pe_%=\n\t"
      "s_waitcnt lgkmcnt(2)\n\t"
      "s_nop 7\n\t"
      "s_getreg_b32 s51, hwreg(HW_REG_IB_STS, 8, 4)\n\t"
      "s_lshl_b32 s96, s51, 13\n\t"
      "s_xor_b32 s77, s77, s96\n\t"
      "s_and_b32 s77, s77, 0x2000\n\t"
      "s_cmp_lg_u32 s51, 0\n\t"
      "s_cselect_b32 s77, s77, 0x1000\n\t"
      ".Lw0pe_%=:\n\t"
      "s_load_dwordx2 s[62:63], s[24:25], 0x800\n\t"
      "s_cmp_eq_u32 s99, 0\n\t"
      "s_cselect_b64 s[96:97], s[20:21], s[28:29]\n\t"
      "s_load_dword s95, s[96:97], s98\n\t"
      : "={s95}"(tres), "={s92}"(pre1), "={s[60:61]}"(pres), "={s[62:63]}"(ep), "+{s77}"(pfire), "+v"(lane), "+s"(wv), "+s"(pk)
      : "{s[20:21]}"(xa), "{s[16:17]}"(wa), "{s[36:37]}"(ba), "{s[28:29]}"(oa), "{s[24:25]}"(sa), "{s[52:53]}"(rs),
        "{s[22:23]}"(0x0002000006c00000ull), "{s[26:27]}"(0x0002000000000808ull), "{s[30:31]}"(0x0002000006c00000ull),
        "{s[18:19]}"(0x0002000000048000ull), "{s[38:39]}"(0x0002000000000300ull),
        "{s40}"(t0), "{s41}"(p0), "{s43}"(lds0), "{s94}"(ty),
        "{v208}"(ga), "{v209}"(gb),
        "{s42}"(pfo), "{s98}"(toff), "{s99}"(tsel), "{s76}"(pop), "{s78}"(ps >> 5),
        "{v216}"(pfv)
      : "memory", "scc", "s51", "s96", "s97");
  __builtin_amdgcn_sched_barrier(0);
  const unsigned sp = lane < 18u ? lane : lane - 14u;
  const unsigned ob = (unsigned)(((sp < 12u ? 0x41ccd62ca418820ull >> (5u * sp) : 0x22a4c1eeull >> (5u * ((sp - 12u) & 7u)))) & 31u) * 6291456u + pk * 49152u;
  const unsigned pg = (ob + 24576u) >> 21;
  const unsigned ta = lane < 18u ? ob + 24576u * sst : 113246208u, t1 = lane < 32u ? ob + 24576u * (sst ^ 1u) : 113246208u;
  const unsigned mk = lane >= 4u && lane < 18u ? 1u << (pg < 52u ? (pg >> 3) * 4u : 28u) : 0u;
  const unsigned plm = (unsigned)__builtin_amdgcn_ballot_w64(((pg ^ role ^ sst) & 1u) != 0u), plb = wv < 2u ? plm : ~plm;
  const unsigned vgv = 0u + wv * 512u + lane * 8u;
  const unsigned vpub = vgv == 0u ? 0u + (pv * 64u + ps) * 8u : 0x40000000u, vbmp = vpub == 0u ? 2048u : 0x40000000u;
  const unsigned vxr = lds0 + 159744u + lane * 16u, vxw = vxr + wv * 4u;
  const unsigned vfw = lds0 + 160768u + lane * 4u + wv * 256u, vfr = lds0 + 160768u + lane * 4u + (lane & 3u) * 256u;
  const unsigned li = lane & 15, lg = lane >> 4, rl = li & 3, hi = li >> 2;
  const unsigned rb = 768u * li + 16u * (lg ^ ((4u - hi) & 3)) - 3072u * wv;
  unsigned cfa[4], csw[3];
#pragma unroll
  for (unsigned u = 0; u < 4; ++u) cfa[u] = rb + 64u * ((u - rl) & 3);
#pragma unroll
  for (unsigned j = 0; j < 3; ++j) {
    const unsigned e = 3u * wv + j;
    csw[j] = rb + 256u * (e >> 2) + 64u * (((e & 3) + (e >> 2) - rl) & 3);
  }
  const unsigned csr = 16u * lane;
  const unsigned tbase = p0 + (4u * lg + (li >> 2)) * 192u + 16u * (li & 3);
  unsigned woff[3];
#pragma unroll
  for (unsigned j = 0; j < 3; ++j) {
    const unsigned q = 64u * j + lane, rr = q / 12u;
    woff[j] = (8u * (rr >> 2) + (rr & 3)) * 768u + 192u * wv + 16u * (q % 12u) - 1024u * j;
  }
  const unsigned boff = 192u * wv + 16u * lg;
  asm volatile(
      "s_cmp_eq_u32 s94, 1\n\t"
      "s_cbranch_scc0 .Lc0_%=\n\t"
      "s_getpc_b64 s[96:97]\n\t"
      ".Lcp_%=:\n\t"
      "s_add_u32 s98, s98, .Lcq_%=-.Lcp_%=\n\t"
      "s_min_u32 s98, s98, .Lend_%=-.Lcp_%=-68\n\t"
      "s_load_dword s99, s[96:97], s98 offset:64\n\t"
      "s_load_dword s98, s[96:97], s98\n\t"
      "s_add_u32 m0, s41, 0x6000\n\t"
      "s_add_u32 s51, s40, 0x600000\n\t"
      "buffer_load_dwordx4 v208, s[20:23], s51 offen lds\n\t"
      "buffer_load_dwordx4 v208, s[20:23], s51 offen offset:1024 lds\n\t"
      "buffer_load_dwordx4 v208, s[20:23], s51 offen offset:2048 lds\n\t"
      "s_add_u32 m0, s41, 0x9000\n\t"
      "s_add_u32 s51, s40, 0x600000\n\t"
      "buffer_load_dwordx4 v209, s[20:23], s51 offen lds\n\t"
      "buffer_load_dwordx4 v209, s[20:23], s51 offen offset:1024 lds\n\t"
      "buffer_load_dwordx4 v209, s[20:23], s51 offen offset:2048 lds\n\t"
      ".Lc0_%=:\n\t"
      "buffer_load_dwordx4 v[226:229], v215, s[36:39], 0 offen offset:0\n\t"
      "buffer_load_dwordx4 v[230:233], v215, s[36:39], 0 offen offset:64\n\t"
      "buffer_load_dwordx4 v[234:237], v215, s[36:39], 0 offen offset:128\n\t"
      "s_add_u32 m0, s41, 0x1e000\n\t"
      "s_mov_b32 s51, 0x0\n\t"
      "buffer_load_dwordx4 v211, s[16:19], s51 offen lds\n\t"
      "buffer_load_dwordx4 v212, s[16:19], s51 offen offset:1024 lds\n\t"
      "buffer_load_dwordx4 v213, s[16:19], s51 offen offset:2048 lds\n\t"
      "s_add_u32 m0, s41, 0x21000\n\t"
      "s_mov_b32 s51, 0xc00\n\t"
      "buffer_load_dwordx4 v211, s[16:19], s51 offen lds\n\t"
      "buffer_load_dwordx4 v212, s[16:19], s51 offen offset:1024 lds\n\t"
      "buffer_load_dwordx4 v213, s[16:19], s51 offen offset:2048 lds\n\t"
      "s_add_u32 m0, s41, 0x24000\n\t"
      "s_mov_b32 s51, 0x6000\n\t"
      "buffer_load_dwordx4 v211, s[16:19], s51 offen lds\n\t"
      "buffer_load_dwordx4 v212, s[16:19], s51 offen offset:1024 lds\n\t"
      "buffer_load_dwordx4 v213, s[16:19], s51 offen offset:2048 lds\n\t"
      "s_add_u32 m0, s41, 0x12000\n\t"
      "s_mov_b32 s51, 0x6c00\n\t"
      "buffer_load_dwordx4 v211, s[16:19], s51 offen lds\n\t"
      "buffer_load_dwordx4 v212, s[16:19], s51 offen offset:1024 lds\n\t"
      "buffer_load_dwordx4 v213, s[16:19], s51 offen offset:2048 lds\n\t"
      "s_add_u32 m0, s41, 0x15000\n\t"
      "s_mov_b32 s51, 0xc000\n\t"
      "buffer_load_dwordx4 v211, s[16:19], s51 offen lds\n\t"
      "buffer_load_dwordx4 v212, s[16:19], s51 offen offset:1024 lds\n\t"
      "buffer_load_dwordx4 v213, s[16:19], s51 offen offset:2048 lds\n\t"
      "s_add_u32 m0, s41, 0x18000\n\t"
      "s_mov_b32 s51, 0xcc00\n\t"
      "buffer_load_dwordx4 v211, s[16:19], s51 offen lds\n\t"
      "buffer_load_dwordx4 v212, s[16:19], s51 offen offset:1024 lds\n\t"
      "buffer_load_dwordx4 v213, s[16:19], s51 offen offset:2048 lds\n\t"
      "s_add_u32 m0, s41, 0x1b000\n\t"
      "s_mov_b32 s51, 0x12000\n\t"
      "buffer_load_dwordx4 v211, s[16:19], s51 offen lds\n\t"
      "buffer_load_dwordx4 v212, s[16:19], s51 offen offset:1024 lds\n\t"
      "buffer_load_dwordx4 v213, s[16:19], s51 offen offset:2048 lds\n\t"
      "s_mov_b32 s44, 0\n\t"
      "s_mov_b32 s85, 3\n\t"
      "s_add_u32 s50, s41, 0x12000\n\t"
      "s_mov_b32 s45, s41\n\t"
      "s_add_u32 s46, s41, 0x6000\n\t"
      "s_add_u32 s47, s41, 0xc000\n\t"
      "s_mov_b32 s84, 96\n\t"
      "v_add_u32_e32 v196, s41, v200\n\t"
      "v_add_u32_e32 v197, s41, v201\n\t"
      "v_add_u32_e32 v198, s41, v202\n\t"
      "v_add_u32_e32 v199, s41, v203\n\t"
      "v_add_u32_e32 v217, s41, v204\n\t"
      "v_add_u32_e32 v218, s41, v205\n\t"
      "v_add_u32_e32 v219, s41, v206\n\t"
      "v_add_u32_e32 v220, s41, v207\n\t"
      "s_waitcnt vmcnt(18)\n\t"
      "s_waitcnt lgkmcnt(0)\n\t"
      "s_barrier\n\t"
      "s_memrealtime s[68:69]\n\t"
      "v_add_u32_e32 v221, 0x1e000, v214\n\t"
      "ds_read_b64_tr_b16 a[0:1], v221 offset:0\n\t"
      "ds_read_b64_tr_b16 a[4:5], v221 offset:8\n\t"
      "ds_read_b64_tr_b16 a[8:9], v221 offset:64\n\t"
      "ds_read_b64_tr_b16 a[12:13], v221 offset:72\n\t"
      "ds_read_b64_tr_b16 a[16:17], v221 offset:128\n\t"
      "ds_read_b64_tr_b16 a[20:21], v221 offset:136\n\t"
      "ds_read_b128 v[140:143], v196 offset:0\n\t"
      "v_lshlrev_b32_e32 v8, 16, v226\n\t"
      "v_and_b32_e32 v9, 0xffff0000, v226\n\t"
      "v_lshlrev_b32_e32 v10, 16, v227\n\t"
      "v_and_b32_e32 v11, 0xffff0000, v227\n\t"
      "v_lshlrev_b32_e32 v12, 16, v228\n\t"
      "v_and_b32_e32 v13, 0xffff0000, v228\n\t"
      "v_lshlrev_b32_e32 v14, 16, v229\n\t"
      "v_and_b32_e32 v15, 0xffff0000, v229\n\t"
      "v_lshlrev_b32_e32 v16, 16, v230\n\t"
      "v_and_b32_e32 v17, 0xffff0000, v230\n\t"
      "v_lshlrev_b32_e32 v18, 16, v231\n\t"
      "v_and_b32_e32 v19, 0xffff0000, v231\n\t"
      "v_lshlrev_b32_e32 v20, 16, v232\n\t"
      "v_and_b32_e32 v21, 0xffff0000, v232\n\t"
      "v_lshlrev_b32_e32 v22, 16, v233\n\t"
      "v_and_b32_e32 v23, 0xffff0000, v233\n\t"
      "v_lshlrev_b32_e32 v24, 16, v234\n\t"
      "v_and_b32_e32 v25, 0xffff0000, v234\n\t"
      "v_lshlrev_b32_e32 v26, 16, v235\n\t"
      "v_and_b32_e32 v27, 0xffff0000, v235\n\t"
      "v_lshlrev_b32_e32 v28, 16, v236\n\t"
      "v_and_b32_e32 v29, 0xffff0000, v236\n\t"
      "v_lshlrev_b32_e32 v30, 16, v237\n\t"
      "v_and_b32_e32 v31, 0xffff0000, v237\n\t"
      "s_add_u32 s62, s62, 0x7f4a7c15\n\t"
      "s_add_u32 s63, s63, 0x9e3779b9\n\t"
      "s_waitcnt vmcnt(15)\n\t"
      "v_add_u32_e32 v221, 0x21000, v214\n\t"
      "ds_read_b64_tr_b16 a[2:3], v221 offset:0\n\t"
      "ds_read_b64_tr_b16 a[6:7], v221 offset:8\n\t"
      "ds_read_b64_tr_b16 a[10:11], v221 offset:64\n\t"
      "ds_read_b64_tr_b16 a[14:15], v221 offset:72\n\t"
      "ds_read_b64_tr_b16 a[18:19], v221 offset:128\n\t"
      "ds_read_b64_tr_b16 a[22:23], v221 offset:136\n\t"
      "ds_read_b128 v[152:155], v196 offset:12288\n\t"
      "s_waitcnt lgkmcnt(8)\n\t"
      "s_add_u32 m0, s41, 0x1e000\n\t"
      "s_mov_b32 s51, 0x12c00\n\t"
      "buffer_load_dwordx4 v211, s[16:19], s51 offen lds\n\t"
      "buffer_load_dwordx4 v212, s[16:19], s51 offen offset:1024 lds\n\t"
      "buffer_load_dwordx4 v213, s[16:19], s51 offen offset:2048 lds\n\t"
      "s_lshl_b32 s67, s68, 18\n\t"
      "s_bfe_u32 s51, s62, 0xc0000\n\t"
      "s_and_b32 s67, s67, 0xfff00000\n\t"
      "s_or_b32 s67, s67, s51\n\t"
      "s_cmp_lg_u32 s41, s43\n\t"
      "s_cbranch_scc1 .Lw0pb_%=\n\t"
      "s_or_b32 s77, s77, s67\n\t"
      "v_mov_b32_e32 v241, s63\n\t"
      "v_mov_b32_e32 v240, s77\n\t"
      "buffer_store_dwordx2 v[240:241], v244, s[24:27], 0 offen sc1\n\t"
      ".Lw0pb_%=:\n\t"
      "s_waitcnt vmcnt(15)\n\t"
      "v_add_u32_e32 v221, 0x24000, v214\n\t"
      "ds_read_b64_tr_b16 a[24:25], v221 offset:0\n\t"
      "ds_read_b64_tr_b16 a[28:29], v221 offset:8\n\t"
      "ds_read_b64_tr_b16 a[32:33], v221 offset:64\n\t"
      "ds_read_b64_tr_b16 a[36:37], v221 offset:72\n\t"
      "ds_read_b64_tr_b16 a[40:41], v221 offset:128\n\t"
      "ds_read_b64_tr_b16 a[44:45], v221 offset:136\n\t"
      "ds_read_b128 v[144:147], v197 offset:0\n\t"
      "s_waitcnt lgkmcnt(8)\n\t"
      "v_mfma_f32_16x16x32_bf16 v[80:83], a[0:3], v[140:143], v[8:11]\n\t"
      "s_add_u32 m0, s41, 0x21000\n\t"
      "s_mov_b32 s51, 0x18000\n\t"
      "buffer_load_dwordx4 v211, s[16:19], s51 offen lds\n\t"
      "buffer_load_dwordx4 v212, s[16:19], s51 offen offset:1024 lds\n\t"
      "v_mfma_f32_16x16x32_bf16 v[84:87], a[4:7], v[140:143], v[12:15]\n\t"
      "buffer_load_dwordx4 v213, s[16:19], s51 offen offset:2048 lds\n\t"
      "v_mfma_f32_16x16x32_bf16 v[88:91], a[8:11], v[140:143], v[16:19]\n\t"
      "s_sub_u32 s67, s67, 0x20000000\n\t"
      "s_andn2_b32 s68, s68, 3\n\t"
      "s_sub_u32 s68, s68, s52\n\t"
      "s_waitcnt vmcnt(15)\n\t"
      "v_add_u32_e32 v221, 0x12000, v214\n\t"
      "ds_read_b64_tr_b16 a[26:27], v221 offset:0\n\t"
      "v_mfma_f32_16x16x32_bf16 v[92:95], a[12:15], v[140:143], v[20:23]\n\t"
      "ds_read_b64_tr_b16 a[30:31], v221 offset:8\n\t"
      "ds_read_b64_tr_b16 a[34:35], v221 offset:64\n\t"
      "v_mfma_f32_16x16x32_bf16 v[96:99], a[16:19], v[140:143], v[24:27]\n\t"
      "ds_read_b64_tr_b16 a[38:39], v221 offset:72\n\t"
      "ds_read_b64_tr_b16 a[42:43], v221 offset:128\n\t"
      "v_mfma_f32_16x16x32_bf16 v[100:103], a[20:23], v[140:143], v[28:31]\n\t"
      "ds_read_b64_tr_b16 a[46:47], v221 offset:136\n\t"
      "ds_read_b128 v[156:159], v197 offset:12288\n\t"
      "s_waitcnt lgkmcnt(8)\n\t"
      "v_mfma_f32_16x16x32_bf16 v[104:107], a[0:3], v[152:155], v[8:11]\n\t"
      "s_add_u32 m0, s41, 0x24000\n\t"
      "s_mov_b32 s51, 0x18c00\n\t"
      "buffer_load_dwordx4 v211, s[16:19], s51 offen lds\n\t"
      "v_mfma_f32_16x16x32_bf16 v[108:111], a[4:7], v[152:155], v[12:15]\n\t"
      "buffer_load_dwordx4 v212, s[16:19], s51 offen offset:1024 lds\n\t"
      "v_mfma_f32_16x16x32_bf16 v[112:115], a[8:11], v[152:155], v[16:19]\n\t"
      "buffer_load_dwordx4 v213, s[16:19], s51 offen offset:2048 lds\n\t"
      "s_waitcnt vmcnt(15)\n\t"
      "v_add_u32_e32 v221, 0x15000, v214\n\t"
      "ds_read_b64_tr_b16 a[48:49], v221 offset:0\n\t"
      "v_mfma_f32_16x16x32_bf16 v[116:119], a[12:15], v[152:155], v[20:23]\n\t"
      "ds_read_b64_tr_b16 a[52:53], v221 offset:8\n\t"
      "ds_read_b64_tr_b16 a[56:57], v221 offset:64\n\t"
      "v_mfma_f32_16x16x32_bf16 v[120:123], a[16:19], v[152:155], v[24:27]\n\t"
      "ds_read_b64_tr_b16 a[60:61], v221 offset:72\n\t"
      "ds_read_b64_tr_b16 a[64:65], v221 offset:128\n\t"
      "v_mfma_f32_16x16x32_bf16 v[132:135], a[20:23], v[152:155], v[28:31]\n\t"
      "ds_read_b64_tr_b16 a[68:69], v221 offset:136\n\t"
      "ds_read_b128 v[148:151], v198 offset:0\n\t"
      "s_waitcnt lgkmcnt(8)\n\t"
      "v_mfma_f32_16x16x32_bf16 v[80:83], a[24:27], v[144:147], v[80:83]\n\t"
      "s_add_u32 m0, s41, 0x12000\n\t"
      "s_mov_b32 s51, 0x1e000\n\t"
      "buffer_load_dwordx4 v211, s[16:19], s51 offen lds\n\t"
      "v_mfma_f32_16x16x32_bf16 v[84:87], a[28:31], v[144:147], v[84:87]\n\t"
      "buffer_load_dwordx4 v212, s[16:19], s51 offen offset:1024 lds\n\t"
      "v_mfma_f32_16x16x32_bf16 v[88:91], a[32:35], v[144:147], v[88:91]\n\t"
      "buffer_load_dwordx4 v213, s[16:19], s51 offen offset:2048 lds\n\t"
      "s_waitcnt vmcnt(15)\n\t"
      "v_add_u32_e32 v221, 0x18000, v214\n\t"
      "ds_read_b64_tr_b16 a[50:51], v221 offset:0\n\t"
      "v_mfma_f32_16x16x32_bf16 v[92:95], a[36:39], v[144:147], v[92:95]\n\t"
      "ds_read_b64_tr_b16 a[54:55], v221 offset:8\n\t"
      "ds_read_b64_tr_b16 a[58:59], v221 offset:64\n\t"
      "v_mfma_f32_16x16x32_bf16 v[96:99], a[40:43], v[144:147], v[96:99]\n\t"
      "ds_read_b64_tr_b16 a[62:63], v221 offset:72\n\t"
      "ds_read_b64_tr_b16 a[66:67], v221 offset:128\n\t"
      "v_mfma_f32_16x16x32_bf16 v[100:103], a[44:47], v[144:147], v[100:103]\n\t"
      "ds_read_b64_tr_b16 a[70:71], v221 offset:136\n\t"
      "ds_read_b128 v[160:163], v198 offset:12288\n\t"
      "s_waitcnt lgkmcnt(8)\n\t"
      "v_mfma_f32_16x16x32_bf16 v[104:107], a[24:27], v[156:159], v[104:107]\n\t"
      "s_add_u32 m0, s41, 0x15000\n\t"
      "s_mov_b32 s51, 0x1ec00\n\t"
      "buffer_load_dwordx4 v211, s[16:19], s51 offen lds\n\t"
      "v_mfma_f32_16x16x32_bf16 v[108:111], a[28:31], v[156:159], v[108:111]\n\t"
      "buffer_load_dwordx4 v212, s[16:19], s51 offen offset:1024 lds\n\t"
      "v_mfma_f32_16x16x32_bf16 v[112:115], a[32:35], v[156:159], v[112:115]\n\t"
      "buffer_load_dwordx4 v213, s[16:19], s51 offen offset:2048 lds\n\t"
      "s_waitcnt vmcnt(15)\n\t"
      "v_add_u32_e32 v221, 0x1b000, v214\n\t"
      "ds_read_b64_tr_b16 a[72:73], v221 offset:0\n\t"
      "v_mfma_f32_16x16x32_bf16 v[116:119], a[36:39], v[156:159], v[116:119]\n\t"
      "ds_read_b64_tr_b16 a[76:77], v221 offset:8\n\t"
      "ds_read_b64_tr_b16 a[80:81], v221 offset:64\n\t"
      "v_mfma_f32_16x16x32_bf16 v[120:123], a[40:43], v[156:159], v[120:123]\n\t"
      "ds_read_b64_tr_b16 a[84:85], v221 offset:72\n\t"
      "ds_read_b64_tr_b16 a[88:89], v221 offset:128\n\t"
      "v_mfma_f32_16x16x32_bf16 v[132:135], a[44:47], v[156:159], v[132:135]\n\t"
      "ds_read_b64_tr_b16 a[92:93], v221 offset:136\n\t"
      "ds_read_b128 v[140:143], v199 offset:0\n\t"
      "s_waitcnt lgkmcnt(8)\n\t"
      "v_mfma_f32_16x16x32_bf16 v[80:83], a[48:51], v[148:151], v[80:83]\n\t"
      "s_add_u32 m0, s41, 0x18000\n\t"
      "s_mov_b32 s51, 0x24000\n\t"
      "buffer_load_dwordx4 v211, s[16:19], s51 offen lds\n\t"
      "v_mfma_f32_16x16x32_bf16 v[84:87], a[52:55], v[148:151], v[84:87]\n\t"
      "buffer_load_dwordx4 v212, s[16:19], s51 offen offset:1024 lds\n\t"
      "v_mfma_f32_16x16x32_bf16 v[88:91], a[56:59], v[148:151], v[88:91]\n\t"
      "buffer_load_dwordx4 v213, s[16:19], s51 offen offset:2048 lds\n\t"
      "s_waitcnt vmcnt(15)\n\t"
      "v_add_u32_e32 v221, 0x1e000, v214\n\t"
      "ds_read_b64_tr_b16 a[74:75], v221 offset:0\n\t"
      "v_mfma_f32_16x16x32_bf16 v[92:95], a[60:63], v[148:151], v[92:95]\n\t"
      "ds_read_b64_tr_b16 a[78:79], v221 offset:8\n\t"
      "ds_read_b64_tr_b16 a[82:83], v221 offset:64\n\t"
      "v_mfma_f32_16x16x32_bf16 v[96:99], a[64:67], v[148:151], v[96:99]\n\t"
      "ds_read_b64_tr_b16 a[86:87], v221 offset:72\n\t"
      "ds_read_b64_tr_b16 a[90:91], v221 offset:128\n\t"
      "v_mfma_f32_16x16x32_bf16 v[100:103], a[68:71], v[148:151], v[100:103]\n\t"
      "ds_read_b64_tr_b16 a[94:95], v221 offset:136\n\t"
      "ds_read_b128 v[152:155], v199 offset:12288\n\t"
      "s_waitcnt lgkmcnt(8)\n\t"
      "v_mfma_f32_16x16x32_bf16 v[104:107], a[48:51], v[160:163], v[104:107]\n\t"
      "s_add_u32 m0, s41, 0x1b000\n\t"
      "s_mov_b32 s51, 0x24c00\n\t"
      "buffer_load_dwordx4 v211, s[16:19], s51 offen lds\n\t"
      "v_mfma_f32_16x16x32_bf16 v[108:111], a[52:55], v[160:163], v[108:111]\n\t"
      "buffer_load_dwordx4 v212, s[16:19], s51 offen offset:1024 lds\n\t"
      "v_mfma_f32_16x16x32_bf16 v[112:115], a[56:59], v[160:163], v[112:115]\n\t"
      "buffer_load_dwordx4 v213, s[16:19], s51 offen offset:2048 lds\n\t"
      "s_waitcnt vmcnt(15)\n\t"
      "v_add_u32_e32 v221, 0x21000, v214\n\t"
      "ds_read_b64_tr_b16 a[96:97], v221 offset:0\n\t"
      "v_mfma_f32_16x16x32_bf16 v[116:119], a[60:63], v[160:163], v[116:119]\n\t"
      "ds_read_b64_tr_b16 a[100:101], v221 offset:8\n\t"
      "ds_read_b64_tr_b16 a[104:105], v221 offset:64\n\t"
      "v_mfma_f32_16x16x32_bf16 v[120:123], a[64:67], v[160:163], v[120:123]\n\t"
      "ds_read_b64_tr_b16 a[108:109], v221 offset:72\n\t"
      "ds_read_b64_tr_b16 a[112:113], v221 offset:128\n\t"
      "v_mfma_f32_16x16x32_bf16 v[132:135], a[68:71], v[160:163], v[132:135]\n\t"
      "ds_read_b64_tr_b16 a[116:117], v221 offset:136\n\t"
      "ds_read_b128 v[144:147], v197 offset:256\n\t"
      "s_waitcnt lgkmcnt(8)\n\t"
      "v_mfma_f32_16x16x32_bf16 v[80:83], a[72:75], v[140:143], v[80:83]\n\t"
      "s_add_u32 m0, s41, 0x1e000\n\t"
      "s_mov_b32 s51, 0x2a000\n\t"
      "buffer_load_dwordx4 v211, s[16:19], s51 offen lds\n\t"
      "v_mfma_f32_16x16x32_bf16 v[84:87], a[76:79], v[140:143], v[84:87]\n\t"
      "buffer_load_dwordx4 v212, s[16:19], s51 offen offset:1024 lds\n\t"
      "v_mfma_f32_16x16x32_bf16 v[88:91], a[80:83], v[140:143], v[88:91]\n\t"
      "buffer_load_dwordx4 v213, s[16:19], s51 offen offset:2048 lds\n\t"
      "s_waitcnt vmcnt(15)\n\t"
      "v_add_u32_e32 v221, 0x24000, v214\n\t"
      "ds_read_b64_tr_b16 a[98:99], v221 offset:0\n\t"
      "v_mfma_f32_16x16x32_bf16 v[92:95], a[84:87], v[140:143], v[92:95]\n\t"
      "ds_read_b64_tr_b16 a[102:103], v221 offset:8\n\t"
      "ds_read_b64_tr_b16 a[106:107], v221 offset:64\n\t"
      "v_mfma_f32_16x16x32_bf16 v[96:99], a[88:91], v[140:143], v[96:99]\n\t"
      "ds_read_b64_tr_b16 a[110:111], v221 offset:72\n\t"
      "ds_read_b64_tr_b16 a[114:115], v221 offset:128\n\t"
      "v_mfma_f32_16x16x32_bf16 v[100:103], a[92:95], v[140:143], v[100:103]\n\t"
      "ds_read_b64_tr_b16 a[118:119], v221 offset:136\n\t"
      "ds_read_b128 v[156:159], v197 offset:12544\n\t"
      "s_waitcnt lgkmcnt(8)\n\t"
      "v_mfma_f32_16x16x32_bf16 v[104:107], a[72:75], v[152:155], v[104:107]\n\t"
      "s_add_u32 m0, s41, 0x21000\n\t"
      "s_mov_b32 s51, 0x2ac00\n\t"
      "buffer_load_dwordx4 v211, s[16:19], s51 offen lds\n\t"
      "v_mfma_f32_16x16x32_bf16 v[108:111], a[76:79], v[152:155], v[108:111]\n\t"
      "buffer_load_dwordx4 v212, s[16:19], s51 offen offset:1024 lds\n\t"
      "v_mfma_f32_16x16x32_bf16 v[112:115], a[80:83], v[152:155], v[112:115]\n\t"
      "buffer_load_dwordx4 v213, s[16:19], s51 offen offset:2048 lds\n\t"
      "s_waitcnt vmcnt(15)\n\t"
      "v_add_u32_e32 v221, 0x12000, v214\n\t"
      "ds_read_b64_tr_b16 a[120:121], v221 offset:0\n\t"
      "v_mfma_f32_16x16x32_bf16 v[116:119], a[84:87], v[152:155], v[116:119]\n\t"
      "ds_read_b64_tr_b16 a[124:125], v221 offset:8\n\t"
      "ds_read_b64_tr_b16 a[128:129], v221 offset:64\n\t"
      "v_mfma_f32_16x16x32_bf16 v[120:123], a[88:91], v[152:155], v[120:123]\n\t"
      "ds_read_b64_tr_b16 a[132:133], v221 offset:72\n\t"
      "ds_read_b64_tr_b16 a[136:137], v221 offset:128\n\t"
      "v_mfma_f32_16x16x32_bf16 v[132:135], a[92:95], v[152:155], v[132:135]\n\t"
      "ds_read_b64_tr_b16 a[140:141], v221 offset:136\n\t"
      "ds_read_b128 v[148:151], v198 offset:256\n\t"
      "s_waitcnt lgkmcnt(8)\n\t"
      "v_mfma_f32_16x16x32_bf16 v[80:83], a[96:99], v[144:147], v[80:83]\n\t"
      "s_add_u32 m0, s41, 0x24000\n\t"
      "s_mov_b32 s51, 0x30000\n\t"
      "buffer_load_dwordx4 v211, s[16:19], s51 offen lds\n\t"
      "v_mfma_f32_16x16x32_bf16 v[84:87], a[100:103], v[144:147], v[84:87]\n\t"
      "buffer_load_dwordx4 v212, s[16:19], s51 offen offset:1024 lds\n\t"
      "v_mfma_f32_16x16x32_bf16 v[88:91], a[104:107], v[144:147], v[88:91]\n\t"
      "buffer_load_dwordx4 v213, s[16:19], s51 offen offset:2048 lds\n\t"
      "s_waitcnt vmcnt(15)\n\t"
      "v_add_u32_e32 v221, 0x15000, v214\n\t"
      "ds_read_b64_tr_b16 a[122:123], v221 offset:0\n\t"
      "v_mfma_f32_16x16x32_bf16 v[92:95], a[108:111], v[144:147], v[92:95]\n\t"
      "ds_read_b64_tr_b16 a[126:127], v221 offset:8\n\t"
      "ds_read_b64_tr_b16 a[130:131], v221 offset:64\n\t"
      "v_mfma_f32_16x16x32_bf16 v[96:99], a[112:115], v[144:147], v[96:99]\n\t"
      "ds_read_b64_tr_b16 a[134:135], v221 offset:72\n\t"
      "ds_read_b64_tr_b16 a[138:139], v221 offset:128\n\t"
      "v_mfma_f32_16x16x32_bf16 v[100:103], a[116:119], v[144:147], v[100:103]\n\t"
      "ds_read_b64_tr_b16 a[142:143], v221 offset:136\n\t"
      "ds_read_b128 v[160:163], v198 offset:12544\n\t"
      "s_waitcnt lgkmcnt(8)\n\t"
      "v_mfma_f32_16x16x32_bf16 v[104:107], a[96:99], v[156:159], v[104:107]\n\t"
      "s_add_u32 m0, s41, 0x12000\n\t"
      "s_mov_b32 s51, 0x30c00\n\t"
      "buffer_load_dwordx4 v211, s[16:19], s51 offen lds\n\t"
      "v_mfma_f32_16x16x32_bf16 v[108:111], a[100:103], v[156:159], v[108:111]\n\t"
      "buffer_load_dwordx4 v212, s[16:19], s51 offen offset:1024 lds\n\t"
      "v_mfma_f32_16x16x32_bf16 v[112:115], a[104:107], v[156:159], v[112:115]\n\t"
      "buffer_load_dwordx4 v213, s[16:19], s51 offen offset:2048 lds\n\t"
      "s_waitcnt vmcnt(15)\n\t"
      "v_add_u32_e32 v221, 0x18000, v214\n\t"
      "ds_read_b64_tr_b16 a[144:145], v221 offset:0\n\t"
      "v_mfma_f32_16x16x32_bf16 v[116:119], a[108:111], v[156:159], v[116:119]\n\t"
      "ds_read_b64_tr_b16 a[148:149], v221 offset:8\n\t"
      "ds_read_b64_tr_b16 a[152:153], v221 offset:64\n\t"
      "v_mfma_f32_16x16x32_bf16 v[120:123], a[112:115], v[156:159], v[120:123]\n\t"
      "ds_read_b64_tr_b16 a[156:157], v221 offset:72\n\t"
      "ds_read_b64_tr_b16 a[160:161], v221 offset:128\n\t"
      "v_mfma_f32_16x16x32_bf16 v[132:135], a[116:119], v[156:159], v[132:135]\n\t"
      "ds_read_b64_tr_b16 a[164:165], v221 offset:136\n\t"
      "ds_read_b128 v[140:143], v199 offset:256\n\t"
      "s_waitcnt lgkmcnt(8)\n\t"
      "v_mfma_f32_16x16x32_bf16 v[80:83], a[120:123], v[148:151], v[80:83]\n\t"
      "s_add_u32 m0, s41, 0x15000\n\t"
      "s_mov_b32 s51, 0x36000\n\t"
      "buffer_load_dwordx4 v211, s[16:19], s51 offen lds\n\t"
      "v_mfma_f32_16x16x32_bf16 v[84:87], a[124:127], v[148:151], v[84:87]\n\t"
      "buffer_load_dwordx4 v212, s[16:19], s51 offen offset:1024 lds\n\t"
      "v_mfma_f32_16x16x32_bf16 v[88:91], a[128:131], v[148:151], v[88:91]\n\t"
      "buffer_load_dwordx4 v213, s[16:19], s51 offen offset:2048 lds\n\t"
      "s_waitcnt vmcnt(15)\n\t"
      "v_add_u32_e32 v221, 0x1b000, v214\n\t"
      "ds_read_b64_tr_b16 a[146:147], v221 offset:0\n\t"
      "v_mfma_f32_16x16x32_bf16 v[92:95], a[132:135], v[148:151], v[92:95]\n\t"
      "ds_read_b64_tr_b16 a[150:151], v221 offset:8\n\t"
      "ds_read_b64_tr_b16 a[154:155], v221 offset:64\n\t"
      "v_mfma_f32_16x16x32_bf16 v[96:99], a[136:139], v[148:151], v[96:99]\n\t"
      "ds_read_b64_tr_b16 a[158:159], v221 offset:72\n\t"
      "ds_read_b64_tr_b16 a[162:163], v221 offset:128\n\t"
      "v_mfma_f32_16x16x32_bf16 v[100:103], a[140:143], v[148:151], v[100:103]\n\t"
      "ds_read_b64_tr_b16 a[166:167], v221 offset:136\n\t"
      "ds_read_b128 v[152:155], v199 offset:12544\n\t"
      "s_waitcnt lgkmcnt(8)\n\t"
      "v_mfma_f32_16x16x32_bf16 v[104:107], a[120:123], v[160:163], v[104:107]\n\t"
      "s_add_u32 m0, s41, 0x18000\n\t"
      "s_mov_b32 s51, 0x36c00\n\t"
      "buffer_load_dwordx4 v211, s[16:19], s51 offen lds\n\t"
      "v_mfma_f32_16x16x32_bf16 v[108:111], a[124:127], v[160:163], v[108:111]\n\t"
      "buffer_load_dwordx4 v212, s[16:19], s51 offen offset:1024 lds\n\t"
      "v_mfma_f32_16x16x32_bf16 v[112:115], a[128:131], v[160:163], v[112:115]\n\t"
      "buffer_load_dwordx4 v213, s[16:19], s51 offen offset:2048 lds\n\t"
      "s_waitcnt vmcnt(15)\n\t"
      "v_add_u32_e32 v221, 0x1e000, v214\n\t"
      "ds_read_b64_tr_b16 a[168:169], v221 offset:0\n\t"
      "v_mfma_f32_16x16x32_bf16 v[116:119], a[132:135], v[160:163], v[116:119]\n\t"
      "ds_read_b64_tr_b16 a[172:173], v221 offset:8\n\t"
      "ds_read_b64_tr_b16 a[176:177], v221 offset:64\n\t"
      "v_mfma_f32_16x16x32_bf16 v[120:123], a[136:139], v[160:163], v[120:123]\n\t"
      "ds_read_b64_tr_b16 a[180:181], v221 offset:72\n\t"
      "ds_read_b64_tr_b16 a[184:185], v221 offset:128\n\t"
      "v_mfma_f32_16x16x32_bf16 v[132:135], a[140:143], v[160:163], v[132:135]\n\t"
      "ds_read_b64_tr_b16 a[188:189], v221 offset:136\n\t"
      "ds_read_b128 v[144:147], v196 offset:256\n\t"
      "s_waitcnt lgkmcnt(8)\n\t"
      "v_mfma_f32_16x16x32_bf16 v[80:83], a[144:147], v[140:143], v[80:83]\n\t"
      "s_add_u32 m0, s41, 0x1b000\n\t"
      "s_mov_b32 s51, 0x3c000\n\t"
      "buffer_load_dwordx4 v211, s[16:19], s51 offen lds\n\t"
      "v_mfma_f32_16x16x32_bf16 v[84:87], a[148:151], v[140:143], v[84:87]\n\t"
      "buffer_load_dwordx4 v212, s[16:19], s51 offen offset:1024 lds\n\t"
      "v_mfma_f32_16x16x32_bf16 v[88:91], a[152:155], v[140:143], v[88:91]\n\t"
      "buffer_load_dwordx4 v213, s[16:19], s51 offen offset:2048 lds\n\t"
      "s_waitcnt vmcnt(15)\n\t"
      "v_add_u32_e32 v221, 0x21000, v214\n\t"
      "ds_read_b64_tr_b16 a[170:171], v221 offset:0\n\t"
      "v_mfma_f32_16x16x32_bf16 v[92:95], a[156:159], v[140:143], v[92:95]\n\t"
      "ds_read_b64_tr_b16 a[174:175], v221 offset:8\n\t"
      "ds_read_b64_tr_b16 a[178:179], v221 offset:64\n\t"
      "v_mfma_f32_16x16x32_bf16 v[96:99], a[160:163], v[140:143], v[96:99]\n\t"
      "ds_read_b64_tr_b16 a[182:183], v221 offset:72\n\t"
      "ds_read_b64_tr_b16 a[186:187], v221 offset:128\n\t"
      "v_mfma_f32_16x16x32_bf16 v[100:103], a[164:167], v[140:143], v[100:103]\n\t"
      "ds_read_b64_tr_b16 a[190:191], v221 offset:136\n\t"
      "ds_read_b128 v[156:159], v196 offset:12544\n\t"
      "s_waitcnt lgkmcnt(8)\n\t"
      "v_mfma_f32_16x16x32_bf16 v[104:107], a[144:147], v[152:155], v[104:107]\n\t"
      "s_add_u32 m0, s41, 0x1e000\n\t"
      "s_mov_b32 s51, 0x3cc00\n\t"
      "buffer_load_dwordx4 v211, s[16:19], s51 offen lds\n\t"
      "v_mfma_f32_16x16x32_bf16 v[108:111], a[148:151], v[152:155], v[108:111]\n\t"
      "buffer_load_dwordx4 v212, s[16:19], s51 offen offset:1024 lds\n\t"
      "v_mfma_f32_16x16x32_bf16 v[112:115], a[152:155], v[152:155], v[112:115]\n\t"
      "buffer_load_dwordx4 v213, s[16:19], s51 offen offset:2048 lds\n\t"
      "s_waitcnt vmcnt(15)\n\t"
      "v_add_u32_e32 v221, 0x24000, v214\n\t"
      "ds_read_b64_tr_b16 a[192:193], v221 offset:0\n\t"
      "v_mfma_f32_16x16x32_bf16 v[116:119], a[156:159], v[152:155], v[116:119]\n\t"
      "ds_read_b64_tr_b16 a[196:197], v221 offset:8\n\t"
      "ds_read_b64_tr_b16 a[200:201], v221 offset:64\n\t"
      "v_mfma_f32_16x16x32_bf16 v[120:123], a[160:163], v[152:155], v[120:123]\n\t"
      "ds_read_b64_tr_b16 a[204:205], v221 offset:72\n\t"
      "ds_read_b64_tr_b16 a[208:209], v221 offset:128\n\t"
      "v_mfma_f32_16x16x32_bf16 v[132:135], a[164:167], v[152:155], v[132:135]\n\t"
      "ds_read_b64_tr_b16 a[212:213], v221 offset:136\n\t"
      "ds_read_b128 v[148:151], v198 offset:512\n\t"
      "s_waitcnt lgkmcnt(8)\n\t"
      "v_mfma_f32_16x16x32_bf16 v[80:83], a[168:171], v[144:147], v[80:83]\n\t"
      "s_add_u32 m0, s41, 0x21000\n\t"
      "s_mov_b32 s51, 0x42000\n\t"
      "buffer_load_dwordx4 v211, s[16:19], s51 offen lds\n\t"
      "v_mfma_f32_16x16x32_bf16 v[84:87], a[172:175], v[144:147], v[84:87]\n\t"
      "buffer_load_dwordx4 v212, s[16:19], s51 offen offset:1024 lds\n\t"
      "v_mfma_f32_16x16x32_bf16 v[88:91], a[176:179], v[144:147], v[88:91]\n\t"
      "buffer_load_dwordx4 v213, s[16:19], s51 offen offset:2048 lds\n\t"
      "s_waitcnt vmcnt(15)\n\t"
      "v_add_u32_e32 v221, 0x12000, v214\n\t"
      "ds_read_b64_tr_b16 a[194:195], v221 offset:0\n\t"
      "v_mfma_f32_16x16x32_bf16 v[92:95], a[180:183], v[144:147], v[92:95]\n\t"
      "ds_read_b64_tr_b16 a[198:199], v221 offset:8\n\t"
      "ds_read_b64_tr_b16 a[202:203], v221 offset:64\n\t"
      "v_mfma_f32_16x16x32_bf16 v[96:99], a[184:187], v[144:147], v[96:99]\n\t"
      "ds_read_b64_tr_b16 a[206:207], v221 offset:72\n\t"
      "ds_read_b64_tr_b16 a[210:211], v221 offset:128\n\t"
      "v_mfma_f32_16x16x32_bf16 v[100:103], a[188:191], v[144:147], v[100:103]\n\t"
      "ds_read_b64_tr_b16 a[214:215], v221 offset:136\n\t"
      "ds_read_b128 v[160:163], v198 offset:12800\n\t"
      "s_waitcnt lgkmcnt(8)\n\t"
      "v_mfma_f32_16x16x32_bf16 v[104:107], a[168:171], v[156:159], v[104:107]\n\t"
      "s_add_u32 m0, s41, 0x24000\n\t"
      "s_mov_b32 s51, 0x42c00\n\t"
      "buffer_load_dwordx4 v211, s[16:19], s51 offen lds\n\t"
      "v_mfma_f32_16x16x32_bf16 v[108:111], a[172:175], v[156:159], v[108:111]\n\t"
      "buffer_load_dwordx4 v212, s[16:19], s51 offen offset:1024 lds\n\t"
      "v_mfma_f32_16x16x32_bf16 v[112:115], a[176:179], v[156:159], v[112:115]\n\t"
      "buffer_load_dwordx4 v213, s[16:19], s51 offen offset:2048 lds\n\t"
      "s_waitcnt vmcnt(15)\n\t"
      "v_add_u32_e32 v221, 0x15000, v214\n\t"
      "ds_read_b64_tr_b16 a[216:217], v221 offset:0\n\t"
      "v_mfma_f32_16x16x32_bf16 v[116:119], a[180:183], v[156:159], v[116:119]\n\t"
      "ds_read_b64_tr_b16 a[220:221], v221 offset:8\n\t"
      "ds_read_b64_tr_b16 a[224:225], v221 offset:64\n\t"
      "v_mfma_f32_16x16x32_bf16 v[120:123], a[184:187], v[156:159], v[120:123]\n\t"
      "ds_read_b64_tr_b16 a[228:229], v221 offset:72\n\t"
      "ds_read_b64_tr_b16 a[232:233], v221 offset:128\n\t"
      "v_mfma_f32_16x16x32_bf16 v[132:135], a[188:191], v[156:159], v[132:135]\n\t"
      "ds_read_b64_tr_b16 a[236:237], v221 offset:136\n\t"
      "ds_read_b128 v[140:143], v199 offset:512\n\t"
      "s_waitcnt lgkmcnt(8)\n\t"
      "v_mfma_f32_16x16x32_bf16 v[80:83], a[192:195], v[148:151], v[80:83]\n\t"
      "s_cmp_eq_u32 s94, 0\n\t"
      "s_cbranch_scc0 .Lc1_%=\n\t"
      "s_add_u32 m0, s41, 0x6000\n\t"
      "s_add_u32 s51, s40, 0x600000\n\t"
      "buffer_load_dwordx4 v208, s[20:23], s51 offen lds\n\t"
      "buffer_load_dwordx4 v208, s[20:23], s51 offen offset:1024 lds\n\t"
      "buffer_load_dwordx4 v208, s[20:23], s51 offen offset:2048 lds\n\t"
      ".Lc1_%=:\n\t"
      "v_mfma_f32_16x16x32_bf16 v[84:87], a[196:199], v[148:151], v[84:87]\n\t"
      "v_mfma_f32_16x16x32_bf16 v[88:91], a[200:203], v[148:151], v[88:91]\n\t"
      "s_waitcnt vmcnt(15)\n\t"
      "s_cmp_eq_u32 s94, 1\n\t"
      "s_cbranch_scc0 .Lwpw19_1_%=\n\t"
      "s_waitcnt vmcnt(12)\n\t"
      ".Lwpw19_1_%=:\n\t"
      "v_add_u32_e32 v221, 0x18000, v214\n\t"
      "ds_read_b64_tr_b16 a[218:219], v221 offset:0\n\t"
      "v_mfma_f32_16x16x32_bf16 v[92:95], a[204:207], v[148:151], v[92:95]\n\t"
      "ds_read_b64_tr_b16 a[222:223], v221 offset:8\n\t"
      "ds_read_b64_tr_b16 a[226:227], v221 offset:64\n\t"
      "v_mfma_f32_16x16x32_bf16 v[96:99], a[208:211], v[148:151], v[96:99]\n\t"
      "ds_read_b64_tr_b16 a[230:231], v221 offset:72\n\t"
      "ds_read_b64_tr_b16 a[234:235], v221 offset:128\n\t"
      "v_mfma_f32_16x16x32_bf16 v[100:103], a[212:215], v[148:151], v[100:103]\n\t"
      "ds_read_b64_tr_b16 a[238:239], v221 offset:136\n\t"
      "ds_read_b128 v[152:155], v199 offset:12800\n\t"
      "s_waitcnt lgkmcnt(14)\n\t"
      "v_mfma_f32_16x16x32_bf16 v[104:107], a[192:195], v[160:163], v[104:107]\n\t"
      "s_cmp_eq_u32 s94, 0\n\t"
      "s_cbranch_scc0 .Lc2_%=\n\t"
      "s_add_u32 m0, s41, 0x9000\n\t"
      "s_add_u32 s51, s40, 0x600000\n\t"
      "buffer_load_dwordx4 v209, s[20:23], s51 offen lds\n\t"
      "buffer_load_dwordx4 v209, s[20:23], s51 offen offset:1024 lds\n\t"
      "buffer_load_dwordx4 v209, s[20:23], s51 offen offset:2048 lds\n\t"
      ".Lc2_%=:\n\t"
      "v_mfma_f32_16x16x32_bf16 v[108:111], a[196:199], v[160:163], v[108:111]\n\t"
      "v_mfma_f32_16x16x32_bf16 v[112:115], a[200:203], v[160:163], v[112:115]\n\t"
      "s_waitcnt vmcnt(15)\n\t"
      "s_cmp_eq_u32 s94, 1\n\t"
      "s_cbranch_scc0 .Lwpw20_1_%=\n\t"
      "s_waitcnt vmcnt(9)\n\t"
      ".Lwpw20_1_%=:\n\t"
      "v_add_u32_e32 v221, 0x1b000, v214\n\t"
      "ds_read_b64_tr_b16 a[240:241], v221 offset:0\n\t"
      "v_mfma_f32_16x16x32_bf16 v[116:119], a[204:207], v[160:163], v[116:119]\n\t"
      "ds_read_b64_tr_b16 a[244:245], v221 offset:8\n\t"
      "ds_read_b64_tr_b16 a[248:249], v221 offset:64\n\t"
      "v_mfma_f32_16x16x32_bf16 v[120:123], a[208:211], v[160:163], v[120:123]\n\t"
      "ds_read_b64_tr_b16 a[252:253], v221 offset:72\n\t"
      "ds_read_b64_tr_b16 v[164:165], v221 offset:128\n\t"
      "v_mfma_f32_16x16x32_bf16 v[132:135], a[212:215], v[160:163], v[132:135]\n\t"
      "ds_read_b64_tr_b16 v[168:169], v221 offset:136\n\t"
      "ds_read_b128 v[144:147], v196 offset:512\n\t"
      "s_waitcnt lgkmcnt(8)\n\t"
      "v_mfma_f32_16x16x32_bf16 v[80:83], a[216:219], v[140:143], v[80:83]\n\t"
      "s_cmp_eq_u32 s94, 0\n\t"
      "s_cbranch_scc0 .Lc3_%=\n\t"
      "s_add_u32 m0, s41, 0xc000\n\t"
      "s_add_u32 s51, s40, 0xc00000\n\t"
      "buffer_load_dwordx4 v208, s[20:23], s51 offen lds\n\t"
      "buffer_load_dwordx4 v208, s[20:23], s51 offen offset:1024 lds\n\t"
      "buffer_load_dwordx4 v208, s[20:23], s51 offen offset:2048 lds\n\t"
      ".Lc3_%=:\n\t"
      "v_mfma_f32_16x16x32_bf16 v[84:87], a[220:223], v[140:143], v[84:87]\n\t"
      "s_cmp_eq_u32 s94, 1\n\t"
      "s_cbranch_scc0 .Lc4_%=\n\t"
      "s_add_u32 m0, s41, 0xc000\n\t"
      "s_add_u32 s51, s40, 0xc00000\n\t"
      "buffer_load_dwordx4 v208, s[20:23], s51 offen lds\n\t"
      "buffer_load_dwordx4 v208, s[20:23], s51 offen offset:1024 lds\n\t"
      "buffer_load_dwordx4 v208, s[20:23], s51 offen offset:2048 lds\n\t"
      ".Lc4_%=:\n\t"
      "v_mfma_f32_16x16x32_bf16 v[88:91], a[224:227], v[140:143], v[88:91]\n\t"
      "s_waitcnt vmcnt(15)\n\t"
      "s_cmp_eq_u32 s94, 1\n\t"
      "s_cbranch_scc0 .Lwpw21_1_%=\n\t"
      "s_waitcnt vmcnt(9)\n\t"
      ".Lwpw21_1_%=:\n\t"
      "v_add_u32_e32 v221, 0x1e000, v214\n\t"
      "ds_read_b64_tr_b16 a[242:243], v221 offset:0\n\t"
      "v_mfma_f32_16x16x32_bf16 v[92:95], a[228:231], v[140:143], v[92:95]\n\t"
      "ds_read_b64_tr_b16 a[246:247], v221 offset:8\n\t"
      "ds_read_b64_tr_b16 a[250:251], v221 offset:64\n\t"
      "v_mfma_f32_16x16x32_bf16 v[96:99], a[232:235], v[140:143], v[96:99]\n\t"
      "ds_read_b64_tr_b16 a[254:255], v221 offset:72\n\t"
      "ds_read_b64_tr_b16 v[166:167], v221 offset:128\n\t"
      "v_mfma_f32_16x16x32_bf16 v[100:103], a[236:239], v[140:143], v[100:103]\n\t"
      "ds_read_b64_tr_b16 v[170:171], v221 offset:136\n\t"
      "ds_read_b128 v[156:159], v196 offset:12800\n\t"
      "s_waitcnt lgkmcnt(14)\n\t"
      "v_mfma_f32_16x16x32_bf16 v[104:107], a[216:219], v[152:155], v[104:107]\n\t"
      "s_cmp_eq_u32 s94, 0\n\t"
      "s_cbranch_scc0 .Lc5_%=\n\t"
      "s_add_u32 m0, s41, 0xf000\n\t"
      "s_add_u32 s51, s40, 0xc00000\n\t"
      "buffer_load_dwordx4 v209, s[20:23], s51 offen lds\n\t"
      "buffer_load_dwordx4 v209, s[20:23], s51 offen offset:1024 lds\n\t"
      "buffer_load_dwordx4 v209, s[20:23], s51 offen offset:2048 lds\n\t"
      ".Lc5_%=:\n\t"
      "v_mfma_f32_16x16x32_bf16 v[108:111], a[220:223], v[152:155], v[108:111]\n\t"
      "s_cmp_eq_u32 s94, 1\n\t"
      "s_cbranch_scc0 .Lc6_%=\n\t"
      "s_add_u32 m0, s41, 0xf000\n\t"
      "s_add_u32 s51, s40, 0xc00000\n\t"
      "buffer_load_dwordx4 v209, s[20:23], s51 offen lds\n\t"
      "buffer_load_dwordx4 v209, s[20:23], s51 offen offset:1024 lds\n\t"
      "buffer_load_dwordx4 v209, s[20:23], s51 offen offset:2048 lds\n\t"
      ".Lc6_%=:\n\t"
      "v_mfma_f32_16x16x32_bf16 v[112:115], a[224:227], v[152:155], v[112:115]\n\t"
      "s_waitcnt vmcnt(15)\n\t"
      "s_cmp_eq_u32 s94, 1\n\t"
      "s_cbranch_scc0 .Lwpw22_1_%=\n\t"
      "s_waitcnt vmcnt(9)\n\t"
      ".Lwpw22_1_%=:\n\t"
      "v_add_u32_e32 v221, 0x21000, v214\n\t"
      "ds_read_b64_tr_b16 v[172:173], v221 offset:0\n\t"
      "v_mfma_f32_16x16x32_bf16 v[116:119], a[228:231], v[152:155], v[116:119]\n\t"
      "ds_read_b64_tr_b16 v[176:177], v221 offset:8\n\t"
      "ds_read_b64_tr_b16 v[180:181], v221 offset:64\n\t"
      "ds_read_b64_tr_b16 v[184:185], v221 offset:72\n\t"
      "v_mfma_f32_16x16x32_bf16 v[120:123], a[232:235], v[152:155], v[120:123]\n\t"
      "ds_read_b64_tr_b16 v[188:189], v221 offset:128\n\t"
      "ds_read_b64_tr_b16 v[192:193], v221 offset:136\n\t"
      "v_mfma_f32_16x16x32_bf16 v[132:135], a[236:239], v[152:155], v[132:135]\n\t"
      "ds_read_b128 v[148:151], v197 offset:512\n\t"
      "ds_read_b128 v[160:163], v197 offset:12800\n\t"
      "s_waitcnt lgkmcnt(9)\n\t"
      "v_mfma_f32_16x16x32_bf16 v[80:83], a[240:243], v[144:147], v[80:83]\n\t"
      "s_mov_b64 s[96:97], s[34:35]\n\t"
      "s_movk_i32 s98, 0x148\n\t"
      "s_mov_b32 s99, s23\n\t"
      "buffer_load_dwordx3 v[252:254], v207, s[96:99], 0 offen offset:40\n\t"
      "v_mfma_f32_16x16x32_bf16 v[84:87], a[244:247], v[144:147], v[84:87]\n\t"
      "buffer_load_dwordx2 v[240:241], v245, s[24:27], 0 offen sc1\n\t"
      "v_mfma_f32_16x16x32_bf16 v[88:91], a[248:251], v[144:147], v[88:91]\n\t"
      "s_waitcnt vmcnt(14)\n\t"
      "s_cmp_eq_u32 s94, 1\n\t"
      "s_cbranch_scc0 .Lwpw23_1_%=\n\t"
      "s_waitcnt vmcnt(8)\n\t"
      ".Lwpw23_1_%=:\n\t"
      "v_add_u32_e32 v221, 0x24000, v214\n\t"
      "ds_read_b64_tr_b16 v[174:175], v221 offset:0\n\t"
      "v_mfma_f32_16x16x32_bf16 v[92:95], a[252:255], v[144:147], v[92:95]\n\t"
      "ds_read_b64_tr_b16 v[178:179], v221 offset:8\n\t"
      "ds_read_b64_tr_b16 v[182:183], v221 offset:64\n\t"
      "v_mfma_f32_16x16x32_bf16 v[96:99], v[164:167], v[144:147], v[96:99]\n\t"
      "ds_read_b64_tr_b16 v[186:187], v221 offset:72\n\t"
      "ds_read_b64_tr_b16 v[190:191], v221 offset:128\n\t"
      "v_mfma_f32_16x16x32_bf16 v[100:103], v[168:171], v[144:147], v[100:103]\n\t"
      "ds_read_b64_tr_b16 v[194:195], v221 offset:136\n\t"
      ".Lcq_%=:\n\t"
      "v_add_u32_e32 v196, s46, v200\n\t"
      "v_add_u32_e32 v197, s46, v201\n\t"
      "v_add_u32_e32 v198, s46, v202\n\t"
      "v_add_u32_e32 v199, s46, v203\n\t"
      "s_cmp_eq_u32 s94, 0\n\t"
      "s_cbranch_scc0 .Lwpx2_0_%=\n\t"
      "s_waitcnt vmcnt(11)\n\t"
      ".Lwpx2_0_%=:\n\t"
      "s_waitcnt lgkmcnt(6)\n\t"
      "s_barrier\n\t"
      "v_mfma_f32_16x16x32_bf16 v[104:107], a[240:243], v[156:159], v[104:107]\n\t"
      "ds_read_b128 v[32:35], v196 offset:0\n\t"
      "v_mfma_f32_16x16x32_bf16 v[108:111], a[244:247], v[156:159], v[108:111]\n\t"
      "ds_read_b128 v[36:39], v197 offset:0\n\t"
      "v_mfma_f32_16x16x32_bf16 v[112:115], a[248:251], v[156:159], v[112:115]\n\t"
      "ds_read_b128 v[40:43], v198 offset:0\n\t"
      "v_mfma_f32_16x16x32_bf16 v[116:119], a[252:255], v[156:159], v[116:119]\n\t"
      "ds_read_b128 v[44:47], v199 offset:0\n\t"
      "v_mfma_f32_16x16x32_bf16 v[120:123], v[164:167], v[156:159], v[120:123]\n\t"
      "ds_read_b128 v[48:51], v197 offset:256\n\t"
      "v_mfma_f32_16x16x32_bf16 v[132:135], v[168:171], v[156:159], v[132:135]\n\t"
      "s_waitcnt lgkmcnt(5)\n\t"
      "v_mfma_f32_16x16x32_bf16 v[80:83], v[172:175], v[148:151], v[80:83]\n\t"
      "ds_read_b128 v[52:55], v198 offset:256\n\t"
      "v_mfma_f32_16x16x32_bf16 v[84:87], v[176:179], v[148:151], v[84:87]\n\t"
      "ds_read_b128 v[56:59], v199 offset:256\n\t"
      "v_mfma_f32_16x16x32_bf16 v[88:91], v[180:183], v[148:151], v[88:91]\n\t"
      "ds_read_b128 v[60:63], v196 offset:256\n\t"
      "v_mfma_f32_16x16x32_bf16 v[92:95], v[184:187], v[148:151], v[92:95]\n\t"
      "ds_read_b128 v[64:67], v198 offset:512\n\t"
      "v_mfma_f32_16x16x32_bf16 v[96:99], v[188:191], v[148:151], v[96:99]\n\t"
      "ds_read_b128 v[68:71], v199 offset:512\n\t"
      "v_mfma_f32_16x16x32_bf16 v[100:103], v[192:195], v[148:151], v[100:103]\n\t"
      "ds_read_b128 v[72:75], v196 offset:512\n\t"
      "v_mfma_f32_16x16x32_bf16 v[104:107], v[172:175], v[160:163], v[104:107]\n\t"
      "ds_read_b128 v[76:79], v197 offset:512\n\t"
      "v_mfma_f32_16x16x32_bf16 v[108:111], v[176:179], v[160:163], v[108:111]\n\t"
      "v_mfma_f32_16x16x32_bf16 v[112:115], v[180:183], v[160:163], v[112:115]\n\t"
      "v_mfma_f32_16x16x32_bf16 v[116:119], v[184:187], v[160:163], v[116:119]\n\t"
      "v_mfma_f32_16x16x32_bf16 v[120:123], v[188:191], v[160:163], v[120:123]\n\t"
      "v_mfma_f32_16x16x32_bf16 v[132:135], v[192:195], v[160:163], v[132:135]\n\t"
      "s_waitcnt lgkmcnt(8)\n\t"
      "v_mfma_f32_16x16x32_bf16 v[128:131], a[0:3], v[32:35], v[8:11]\n\t"
      "v_cvt_pk_bf16_f32 v80, v80, v81\n\t"
      "v_cvt_pk_bf16_f32 v81, v82, v83\n\t"
      "v_mfma_f32_16x16x32_bf16 v[128:131], a[24:27], v[36:39], v[128:131]\n\t"
      "v_cvt_pk_bf16_f32 v136, v120, v121\n\t"
      "v_cvt_pk_bf16_f32 v137, v122, v123\n\t"
      "v_mfma_f32_16x16x32_bf16 v[128:131], a[48:51], v[40:43], v[128:131]\n\t"
      "v_cvt_pk_bf16_f32 v138, v132, v133\n\t"
      "v_mfma_f32_16x16x32_bf16 v[128:131], a[72:75], v[44:47], v[128:131]\n\t"
      "v_cvt_pk_bf16_f32 v139, v134, v135\n\t"
      "s_waitcnt lgkmcnt(4)\n\t"
      "v_mfma_f32_16x16x32_bf16 v[128:131], a[96:99], v[48:51], v[128:131]\n\t"
      "ds_write_b128 v219, v[136:139] offset:12288\n\t"
      "v_mfma_f32_16x16x32_bf16 v[128:131], a[120:123], v[52:55], v[128:131]\n\t"
      "v_cvt_pk_bf16_f32 v82, v84, v85\n\t"
      "v_cvt_pk_bf16_f32 v83, v86, v87\n\t"
      "v_mfma_f32_16x16x32_bf16 v[128:131], a[144:147], v[56:59], v[128:131]\n\t"
      "v_cvt_pk_bf16_f32 v88, v88, v89\n\t"
      "v_cvt_pk_bf16_f32 v89, v90, v91\n\t"
      "v_mfma_f32_16x16x32_bf16 v[128:131], a[168:171], v[60:63], v[128:131]\n\t"
      "ds_write_b128 v217, v[80:83] offset:0\n\t"
      "v_cvt_pk_bf16_f32 v90, v92, v93\n\t"
      "s_waitcnt lgkmcnt(2)\n\t"
      "v_mfma_f32_16x16x32_bf16 v[128:131], a[192:195], v[64:67], v[128:131]\n\t"
      "v_cvt_pk_bf16_f32 v91, v94, v95\n\t"
      "v_cvt_pk_bf16_f32 v96, v96, v97\n\t"
      "v_mfma_f32_16x16x32_bf16 v[128:131], a[216:219], v[68:71], v[128:131]\n\t"
      "ds_write_b128 v218, v[88:91] offset:0\n\t"
      "v_cvt_pk_bf16_f32 v97, v98, v99\n\t"
      "v_mfma_f32_16x16x32_bf16 v[128:131], a[240:243], v[72:75], v[128:131]\n\t"
      "v_cvt_pk_bf16_f32 v98, v100, v101\n\t"
      "v_cvt_pk_bf16_f32 v99, v102, v103\n\t"
      "v_mfma_f32_16x16x32_bf16 v[128:131], v[172:175], v[76:79], v[128:131]\n\t"
      "v_cvt_pk_bf16_f32 v104, v104, v105\n\t"
      "v_cvt_pk_bf16_f32 v105, v106, v107\n\t"
      "v_mfma_f32_16x16x32_bf16 v[132:135], a[4:7], v[32:35], v[12:15]\n\t"
      "ds_write_b128 v219, v[96:99] offset:0\n\t"
      "v_cvt_pk_bf16_f32 v106, v108, v109\n\t"
      "v_mfma_f32_16x16x32_bf16 v[132:135], a[28:31], v[36:39], v[132:135]\n\t"
      "v_cvt_pk_bf16_f32 v107, v110, v111\n\t"
      "v_cvt_pk_bf16_f32 v112, v112, v113\n\t"
      "v_mfma_f32_16x16x32_bf16 v[132:135], a[52:55], v[40:43], v[132:135]\n\t"
      "v_cvt_pk_bf16_f32 v136, v128, v129\n\t"
      "v_mfma_f32_16x16x32_bf16 v[132:135], a[76:79], v[44:47], v[132:135]\n\t"
      "v_cvt_pk_bf16_f32 v137, v130, v131\n\t"
      "v_mfma_f32_16x16x32_bf16 v[132:135], a[100:103], v[48:51], v[132:135]\n\t"
      "ds_write_b128 v217, v[104:107] offset:12288\n\t"
      "v_cvt_pk_bf16_f32 v113, v114, v115\n\t"
      "s_add_u32 s48, s47, 0x6000\n\t"
      "v_mfma_f32_16x16x32_bf16 v[132:135], a[124:127], v[52:55], v[132:135]\n\t"
      "v_cvt_pk_bf16_f32 v114, v116, v117\n\t"
      "v_cvt_pk_bf16_f32 v115, v118, v119\n\t"
      "s_cmp_eq_u32 s48, s50\n\t"
      "s_cselect_b32 s48, s41, s48\n\t"
      "v_mfma_f32_16x16x32_bf16 v[132:135], a[148:151], v[56:59], v[132:135]\n\t"
      "ds_write_b128 v218, v[112:115] offset:12288\n\t"
      "v_mfma_f32_16x16x32_bf16 v[132:135], a[172:175], v[60:63], v[132:135]\n\t"
      "v_add_u32_e32 v217, s46, v204\n\t"
      "v_mfma_f32_16x16x32_bf16 v[132:135], a[196:199], v[64:67], v[132:135]\n\t"
      "v_add_u32_e32 v218, s46, v205\n\t"
      "v_mfma_f32_16x16x32_bf16 v[132:135], a[220:223], v[68:71], v[132:135]\n\t"
      "v_readlane_b32 s73, v248, 3\n\t"
      "v_add_u32_e32 v219, s46, v206\n\t"
      "v_mfma_f32_16x16x32_bf16 v[132:135], a[244:247], v[72:75], v[132:135]\n\t"
      "v_mfma_f32_16x16x32_bf16 v[132:135], v[176:179], v[76:79], v[132:135]\n\t"
      "s_waitcnt lgkmcnt(0)\n\t"
      "s_barrier\n\t"
      "v_mfma_f32_16x16x32_bf16 v[128:131], a[8:11], v[32:35], v[16:19]\n\t"
      "ds_read_b128 v[140:143], v220 offset:0\n\t"
      "v_mfma_f32_16x16x32_bf16 v[128:131], a[32:35], v[36:39], v[128:131]\n\t"
      "ds_read_b128 v[144:147], v220 offset:1024\n\t"
      "s_memrealtime s[80:81]\n\t"
      "v_mfma_f32_16x16x32_bf16 v[128:131], a[56:59], v[40:43], v[128:131]\n\t"
      "v_cvt_pk_bf16_f32 v138, v132, v133\n\t"
      "s_mov_b32 m0, s45\n\t"
      "v_mfma_f32_16x16x32_bf16 v[128:131], a[80:83], v[44:47], v[128:131]\n\t"
      "v_cvt_pk_bf16_f32 v139, v134, v135\n\t"
      "v_mfma_f32_16x16x32_bf16 v[128:131], a[104:107], v[48:51], v[128:131]\n\t"
      "ds_write_b128 v217, v[136:139] offset:0\n\t"
      "v_mfma_f32_16x16x32_bf16 v[128:131], a[128:131], v[52:55], v[128:131]\n\t"
      "s_waitcnt lgkmcnt(2)\n\t"
      "buffer_store_dwordx4 v[140:143], v208, s[28:31], s40 offen sc1\n\t"
      "v_mfma_f32_16x16x32_bf16 v[128:131], a[152:155], v[56:59], v[128:131]\n\t"
      "ds_read_b128 v[148:151], v220 offset:2048\n\t"
      "v_mfma_f32_16x16x32_bf16 v[128:131], a[176:179], v[60:63], v[128:131]\n\t"
      "ds_read_b128 v[152:155], v220 offset:12288\n\t"
      "v_mfma_f32_16x16x32_bf16 v[128:131], a[200:203], v[64:67], v[128:131]\n\t"
      "ds_read_b128 v[156:159], v220 offset:13312\n\t"
      "v_mfma_f32_16x16x32_bf16 v[128:131], a[224:227], v[68:71], v[128:131]\n\t"
      "s_waitcnt lgkmcnt(4)\n\t"
      "buffer_store_dwordx4 v[144:147], v208, s[28:31], s40 offen offset:1024 sc1\n\t"
      "v_mfma_f32_16x16x32_bf16 v[128:131], a[248:251], v[72:75], v[128:131]\n\t"
      "ds_read_b128 v[160:163], v220 offset:14336\n\t"
      "v_mfma_f32_16x16x32_bf16 v[128:131], v[180:183], v[76:79], v[128:131]\n\t"
      "v_mfma_f32_16x16x32_bf16 v[132:135], a[12:15], v[32:35], v[20:23]\n\t"
      "v_mfma_f32_16x16x32_bf16 v[132:135], a[36:39], v[36:39], v[132:135]\n\t"
      "s_waitcnt lgkmcnt(3)\n\t"
      "buffer_store_dwordx4 v[148:151], v208, s[28:31], s40 offen offset:2048 sc1\n\t"
      "v_mfma_f32_16x16x32_bf16 v[132:135], a[60:63], v[40:43], v[132:135]\n\t"
      "v_cvt_pk_bf16_f32 v136, v128, v129\n\t"
      "v_mfma_f32_16x16x32_bf16 v[132:135], a[84:87], v[44:47], v[132:135]\n\t"
      "v_cvt_pk_bf16_f32 v137, v130, v131\n\t"
      "v_mfma_f32_16x16x32_bf16 v[132:135], a[108:111], v[48:51], v[132:135]\n\t"
      "v_mov_b32_e32 v6, 0\n\t"
      "v_mfma_f32_16x16x32_bf16 v[132:135], a[132:135], v[52:55], v[132:135]\n\t"
      "buffer_load_dwordx4 v208, s[20:23], s73 offen lds\n\t"
      "v_mfma_f32_16x16x32_bf16 v[132:135], a[156:159], v[56:59], v[132:135]\n\t"
      "ds_write_b32 v238, v6\n\t"
      "v_mfma_f32_16x16x32_bf16 v[132:135], a[180:183], v[60:63], v[132:135]\n\t"
      "v_mfma_f32_16x16x32_bf16 v[132:135], a[204:207], v[64:67], v[132:135]\n\t"
      "v_mfma_f32_16x16x32_bf16 v[132:135], a[228:231], v[68:71], v[132:135]\n\t"
      "buffer_load_dwordx4 v208, s[20:23], s73 offen offset:1024 lds\n\t"
      "v_mfma_f32_16x16x32_bf16 v[132:135], a[252:255], v[72:75], v[132:135]\n\t"
      "v_mfma_f32_16x16x32_bf16 v[132:135], v[184:187], v[76:79], v[132:135]\n\t"
      "s_waitcnt vmcnt(13)\n\t"
      "s_barrier\n\t"
      "v_mfma_f32_16x16x32_bf16 v[128:131], a[16:19], v[32:35], v[24:27]\n\t"
      "ds_read_b128 v[80:83], v196 offset:12288\n\t"
      "v_mfma_f32_16x16x32_bf16 v[128:131], a[40:43], v[36:39], v[128:131]\n\t"
      "buffer_load_dwordx4 v208, s[20:23], s73 offen offset:2048 lds\n\t"
      "v_mfma_f32_16x16x32_bf16 v[128:131], a[64:67], v[40:43], v[128:131]\n\t"
      "ds_read_b128 v[84:87], v197 offset:12288\n\t"
      "v_cvt_pk_bf16_f32 v138, v132, v133\n\t"
      "s_add_u32 m0, s45, 0x3000\n\t"
      "v_mfma_f32_16x16x32_bf16 v[128:131], a[88:91], v[44:47], v[128:131]\n\t"
      "ds_read_b128 v[88:91], v198 offset:12288\n\t"
      "v_cvt_pk_bf16_f32 v139, v134, v135\n\t"
      "v_mfma_f32_16x16x32_bf16 v[128:131], a[112:115], v[48:51], v[128:131]\n\t"
      "ds_write_b128 v218, v[136:139] offset:0\n\t"
      "v_mfma_f32_16x16x32_bf16 v[128:131], a[136:139], v[52:55], v[128:131]\n\t"
      "s_waitcnt lgkmcnt(7)\n\t"
      "buffer_store_dwordx4 v[152:155], v209, s[28:31], s40 offen sc1\n\t"
      "s_waitcnt vmcnt(7)\n\t"
      "v_subrev_u32_e32 v242, s67, v240\n\t"
      "v_xor_b32_e32 v241, s63, v241\n\t"
      "v_mfma_f32_16x16x32_bf16 v[128:131], a[160:163], v[56:59], v[128:131]\n\t"
      "ds_read_b128 v[92:95], v199 offset:12288\n\t"
      "v_lshl_or_b32 v240, v242, 20, v241\n\t"
      "v_mfma_f32_16x16x32_bf16 v[128:131], a[184:187], v[60:63], v[128:131]\n\t"
      "ds_read_b128 v[96:99], v197 offset:12544\n\t"
      "v_mfma_f32_16x16x32_bf16 v[128:131], a[208:211], v[64:67], v[128:131]\n\t"
      "v_lshrrev_b32_e32 v242, 12, v242\n\t"
      "v_cmp_ne_u32_e32 vcc, 0, v240\n\t"
      "v_mfma_f32_16x16x32_bf16 v[128:131], a[232:235], v[68:71], v[128:131]\n\t"
      "s_waitcnt lgkmcnt(8)\n\t"
      "buffer_store_dwordx4 v[156:159], v209, s[28:31], s40 offen offset:1024 sc1\n\t"
      "v_mfma_f32_16x16x32_bf16 v[128:131], v[164:167], v[72:75], v[128:131]\n\t"
      "ds_read_b128 v[100:103], v198 offset:12544\n\t"
      "s_cbranch_vccnz .Lgs_%=\n\t"
      ".Lgr_%=:\n\t"
      "ds_write_b32 v246, v242\n\t"
      "v_mfma_f32_16x16x32_bf16 v[128:131], v[188:191], v[76:79], v[128:131]\n\t"
      "ds_read_b128 v[104:107], v199 offset:12544\n\t"
      "v_mfma_f32_16x16x32_bf16 v[132:135], a[20:23], v[32:35], v[28:31]\n\t"
      "ds_read_b128 v[108:111], v196 offset:12544\n\t"
      "v_mfma_f32_16x16x32_bf16 v[132:135], a[44:47], v[36:39], v[132:135]\n\t"
      "s_waitcnt lgkmcnt(11)\n\t"
      "buffer_store_dwordx4 v[160:163], v209, s[28:31], s40 offen offset:2048 sc1\n\t"
      "v_mfma_f32_16x16x32_bf16 v[132:135], a[68:71], v[40:43], v[132:135]\n\t"
      "ds_read_b128 v[112:115], v198 offset:12800\n\t"
      "v_cvt_pk_bf16_f32 v136, v128, v129\n\t"
      "v_add_u32_e32 v220, s46, v207\n\t"
      "v_mfma_f32_16x16x32_bf16 v[132:135], a[92:95], v[44:47], v[132:135]\n\t"
      "ds_read_b128 v[116:119], v199 offset:12800\n\t"
      "v_cvt_pk_bf16_f32 v137, v130, v131\n\t"
      "v_mfma_f32_16x16x32_bf16 v[132:135], a[116:119], v[48:51], v[132:135]\n\t"
      "s_sub_u32 s87, s80, s52\n\t"
      "v_mfma_f32_16x16x32_bf16 v[132:135], a[140:143], v[52:55], v[132:135]\n\t"
      "buffer_load_dwordx4 v209, s[20:23], s73 offen lds\n\t"
      "v_mfma_f32_16x16x32_bf16 v[132:135], a[164:167], v[56:59], v[132:135]\n\t"
      "ds_read_b128 v[120:123], v196 offset:12800\n\t"
      "v_mfma_f32_16x16x32_bf16 v[132:135], a[188:191], v[60:63], v[132:135]\n\t"
      "ds_read_b128 v[124:127], v197 offset:12800\n\t"
      "v_mfma_f32_16x16x32_bf16 v[132:135], a[212:215], v[64:67], v[132:135]\n\t"
      "s_add_u32 s44, s44, 1\n\t"
      "v_mfma_f32_16x16x32_bf16 v[132:135], a[236:239], v[68:71], v[132:135]\n\t"
      "buffer_load_dwordx4 v209, s[20:23], s73 offen offset:1024 lds\n\t"
      "v_mfma_f32_16x16x32_bf16 v[132:135], v[168:171], v[72:75], v[132:135]\n\t"
      "s_add_u32 s85, s85, 1\n\t"
      "s_add_u32 s82, s87, s84\n\t"
      "v_mfma_f32_16x16x32_bf16 v[132:135], v[192:195], v[76:79], v[132:135]\n\t"
      "s_waitcnt lgkmcnt(9)\n\t"
      "v_mfma_f32_16x16x32_bf16 v[128:131], a[0:3], v[80:83], v[8:11]\n\t"
      "v_mfma_f32_16x16x32_bf16 v[128:131], a[24:27], v[84:87], v[128:131]\n\t"
      "v_readlane_b32 s72, v248, s44\n\t"
      "v_mfma_f32_16x16x32_bf16 v[128:131], a[48:51], v[88:91], v[128:131]\n\t"
      "v_cvt_pk_bf16_f32 v138, v132, v133\n\t"
      "v_mfma_f32_16x16x32_bf16 v[128:131], a[72:75], v[92:95], v[128:131]\n\t"
      "v_cvt_pk_bf16_f32 v139, v134, v135\n\t"
      "s_waitcnt lgkmcnt(4)\n\t"
      "v_mfma_f32_16x16x32_bf16 v[128:131], a[96:99], v[96:99], v[128:131]\n\t"
      "ds_write_b128 v219, v[136:139] offset:0\n\t"
      "v_mfma_f32_16x16x32_bf16 v[128:131], a[120:123], v[100:103], v[128:131]\n\t"
      "v_mfma_f32_16x16x32_bf16 v[128:131], a[144:147], v[104:107], v[128:131]\n\t"
      "buffer_load_dwordx4 v209, s[20:23], s73 offen offset:2048 lds\n\t"
      "v_mfma_f32_16x16x32_bf16 v[128:131], a[168:171], v[108:111], v[128:131]\n\t"
      "v_add_u32_e32 v196, s47, v200\n\t"
      "s_waitcnt lgkmcnt(1)\n\t"
      "v_mfma_f32_16x16x32_bf16 v[128:131], a[192:195], v[112:115], v[128:131]\n\t"
      "v_add_u32_e32 v197, s47, v201\n\t"
      "v_mfma_f32_16x16x32_bf16 v[128:131], a[216:219], v[116:119], v[128:131]\n\t"
      "v_add_u32_e32 v198, s47, v202\n\t"
      "v_mfma_f32_16x16x32_bf16 v[128:131], a[240:243], v[120:123], v[128:131]\n\t"
      "v_add_u32_e32 v199, s47, v203\n\t"
      "v_mfma_f32_16x16x32_bf16 v[128:131], v[172:175], v[124:127], v[128:131]\n\t"
      "s_waitcnt vmcnt(17) lgkmcnt(0)\n\t"
      "s_barrier\n\t"
      "v_mfma_f32_16x16x32_bf16 v[132:135], a[4:7], v[80:83], v[12:15]\n\t"
      "ds_read_b128 v[140:143], v220 offset:0\n\t"
      "v_mfma_f32_16x16x32_bf16 v[132:135], a[28:31], v[84:87], v[132:135]\n\t"
      "ds_read_b128 v[240:243], v247 offset:0\n\t"
      "v_mfma_f32_16x16x32_bf16 v[132:135], a[52:55], v[88:91], v[132:135]\n\t"
      "v_cvt_pk_bf16_f32 v136, v128, v129\n\t"
      "v_mfma_f32_16x16x32_bf16 v[132:135], a[76:79], v[92:95], v[132:135]\n\t"
      "ds_read_b128 v[32:35], v196 offset:0\n\t"
      "v_cvt_pk_bf16_f32 v137, v130, v131\n\t"
      "v_mfma_f32_16x16x32_bf16 v[132:135], a[100:103], v[96:99], v[132:135]\n\t"
      "v_mfma_f32_16x16x32_bf16 v[132:135], a[124:127], v[100:103], v[132:135]\n\t"
      "v_mfma_f32_16x16x32_bf16 v[132:135], a[148:151], v[104:107], v[132:135]\n\t"
      "ds_read_b128 v[36:39], v197 offset:0\n\t"
      "v_mfma_f32_16x16x32_bf16 v[132:135], a[172:175], v[108:111], v[132:135]\n\t"
      "v_mfma_f32_16x16x32_bf16 v[132:135], a[196:199], v[112:115], v[132:135]\n\t"
      "s_waitcnt lgkmcnt(2)\n\t"
      "v_add3_u32 v240, v240, v241, v242\n\t"
      "v_mfma_f32_16x16x32_bf16 v[132:135], a[220:223], v[116:119], v[132:135]\n\t"
      "ds_read_b128 v[40:43], v198 offset:0\n\t"
      "v_mfma_f32_16x16x32_bf16 v[132:135], a[244:247], v[120:123], v[132:135]\n\t"
      "v_add_u32_e32 v240, v240, v243\n\t"
      "v_mfma_f32_16x16x32_bf16 v[132:135], v[176:179], v[124:127], v[132:135]\n\t"
      "buffer_store_dwordx4 v[140:143], v208, s[28:31], s72 offen sc1\n\t"
      "v_mfma_f32_16x16x32_bf16 v[128:131], a[8:11], v[80:83], v[16:19]\n\t"
      "ds_read_b128 v[44:47], v199 offset:0\n\t"
      "v_mfma_f32_16x16x32_bf16 v[128:131], a[32:35], v[84:87], v[128:131]\n\t"
      "v_add_u32_dpp v241, v240, v240 quad_perm:[1,0,3,2] row_mask:0xf bank_mask:0xf\n\t"
      "s_memrealtime s[80:81]\n\t"
      "v_mfma_f32_16x16x32_bf16 v[128:131], a[56:59], v[88:91], v[128:131]\n\t"
      "v_cvt_pk_bf16_f32 v138, v132, v133\n\t"
      "v_mfma_f32_16x16x32_bf16 v[128:131], a[80:83], v[92:95], v[128:131]\n\t"
      "v_cvt_pk_bf16_f32 v139, v134, v135\n\t"
      "v_mfma_f32_16x16x32_bf16 v[128:131], a[104:107], v[96:99], v[128:131]\n\t"
      "ds_write_b128 v217, v[136:139] offset:12288\n\t"
      "v_add_u32_dpp v242, v241, v241 quad_perm:[2,3,0,1] row_mask:0xf bank_mask:0xf\n\t"
      "v_mfma_f32_16x16x32_bf16 v[128:131], a[128:131], v[100:103], v[128:131]\n\t"
      "s_mov_b32 m0, s46\n\t"
      "v_and_b32_e32 v240, 0xff, v242\n\t"
      "v_mfma_f32_16x16x32_bf16 v[128:131], a[152:155], v[104:107], v[128:131]\n\t"
      "v_cmp_le_u32_e64 s[76:77], 19, v240\n\t"
      "v_cmp_ge_u32_e64 s[78:79], 13, v240\n\t"
      "v_mfma_f32_16x16x32_bf16 v[128:131], a[176:179], v[108:111], v[128:131]\n\t"
      "ds_read_b128 v[48:51], v197 offset:256\n\t"
      "v_readlane_b32 s70, v242, 0\n\t"
      "v_mfma_f32_16x16x32_bf16 v[128:131], a[200:203], v[112:115], v[128:131]\n\t"
      "v_cmp_lt_u32_e32 vcc, 32, v240\n\t"
      "s_mov_b32 s66, s76\n\t"
      "v_mfma_f32_16x16x32_bf16 v[128:131], a[224:227], v[116:119], v[128:131]\n\t"
      "ds_read_b128 v[52:55], v198 offset:256\n\t"
      "v_mov_b32_e32 v242, s62\n\t"
      "v_mfma_f32_16x16x32_bf16 v[128:131], a[248:251], v[120:123], v[128:131]\n\t"
      "s_and_b32 s65, s76, s77\n\t"
      "s_and_b32 s78, s78, s79\n\t"
      "v_mfma_f32_16x16x32_bf16 v[128:131], v[180:183], v[124:127], v[128:131]\n\t"
      "v_mov_b32_e32 v243, s63\n\t"
      "s_or_b32 s65, s65, s78\n\t"
      "v_mfma_f32_16x16x32_bf16 v[132:135], a[12:15], v[80:83], v[20:23]\n\t"
      "ds_read_b128 v[56:59], v199 offset:256\n\t"
      "s_cmp_lg_u64 vcc, 0\n\t"
      "s_cselect_b32 s65, 0, s65\n\t"
      "s_cselect_b32 s64, 0xfffc0000, 0\n\t"
      "s_cselect_b32 s49, 31, 17\n\t"
      "v_mfma_f32_16x16x32_bf16 v[132:135], a[36:39], v[84:87], v[132:135]\n\t"
      "v_and_b32_e32 v240, s66, v250\n\t"
      "v_and_b32_e32 v241, s65, v250\n\t"
      "v_mfma_f32_16x16x32_bf16 v[132:135], a[60:63], v[88:91], v[132:135]\n\t"
      "v_cvt_pk_bf16_f32 v136, v128, v129\n\t"
      "s_andn2_b32 s67, 0x40000000, s64\n\t"
      "v_mfma_f32_16x16x32_bf16 v[132:135], a[84:87], v[92:95], v[132:135]\n\t"
      "v_cvt_pk_bf16_f32 v137, v130, v131\n\t"
      "ds_read_b128 v[144:147], v220 offset:1024\n\t"
      "v_mfma_f32_16x16x32_bf16 v[132:135], a[108:111], v[96:99], v[132:135]\n\t"
      "v_cmp_ne_u32_e64 s[76:77], 0, v240\n\t"
      "v_cmp_ne_u32_e64 s[78:79], 0, v241\n\t"
      "v_mfma_f32_16x16x32_bf16 v[132:135], a[132:135], v[100:103], v[132:135]\n\t"
      "buffer_store_dwordx2 v[242:243], v210, s[24:27], 0 offen sc1\n\t"
      "v_mfma_f32_16x16x32_bf16 v[132:135], a[156:159], v[104:107], v[132:135]\n\t"
      "ds_read_b128 v[60:63], v196 offset:256\n\t"
      "s_lshr_b32 s70, s70, 10\n\t"
      "v_mfma_f32_16x16x32_bf16 v[132:135], a[180:183], v[108:111], v[132:135]\n\t"
      "s_xor_b32 s75, s75, s76\n\t"
      "s_xor_b32 s74, s74, s76\n\t"
      "v_mfma_f32_16x16x32_bf16 v[132:135], a[204:207], v[112:115], v[132:135]\n\t"
      "s_and_b32 s76, s75, s78\n\t"
      "s_or_b32 s76, s76, s64\n\t"
      "v_cndmask_b32_e64 v249, v248, v251, s[76:77]\n\t"
      "v_mfma_f32_16x16x32_bf16 v[132:135], a[228:231], v[116:119], v[132:135]\n\t"
      "ds_read_b128 v[64:67], v198 offset:512\n\t"
      "s_add_u32 s70, s70, s68\n\t"
      "v_mfma_f32_16x16x32_bf16 v[132:135], a[252:255], v[120:123], v[132:135]\n\t"
      "s_and_b32 s76, s74, s78\n\t"
      "s_or_b32 s76, s76, s64\n\t"
      "v_cndmask_b32_e64 v248, v248, v251, s[76:77]\n\t"
      "v_mfma_f32_16x16x32_bf16 v[132:135], v[184:187], v[124:127], v[132:135]\n\t"
      "s_waitcnt lgkmcnt(2)\n\t"
      "buffer_store_dwordx4 v[144:147], v208, s[28:31], s72 offen offset:1024 sc1\n\t"
      "v_mfma_f32_16x16x32_bf16 v[128:131], a[16:19], v[80:83], v[24:27]\n\t"
      "ds_read_b128 v[68:71], v199 offset:512\n\t"
      "v_mfma_f32_16x16x32_bf16 v[128:131], a[40:43], v[84:87], v[128:131]\n\t"
      "s_lshr_b32 s74, s78, 3\n\t"
      "s_or_b32 s76, s74, s78\n\t"
      "v_mfma_f32_16x16x32_bf16 v[128:131], a[64:67], v[88:91], v[128:131]\n\t"
      "v_cvt_pk_bf16_f32 v138, v132, v133\n\t"
      "v_readlane_b32 s73, v248, s85\n\t"
      "v_mfma_f32_16x16x32_bf16 v[128:131], a[88:91], v[92:95], v[128:131]\n\t"
      "v_cvt_pk_bf16_f32 v139, v134, v135\n\t"
      "s_sub_u32 s87, s80, s52\n\t"
      "v_mfma_f32_16x16x32_bf16 v[128:131], a[112:115], v[96:99], v[128:131]\n\t"
      "ds_write_b128 v218, v[136:139] offset:12288\n\t"
      "s_sub_u32 s83, s82, s87\n\t"
      "v_mfma_f32_16x16x32_bf16 v[128:131], a[136:139], v[100:103], v[128:131]\n\t"
      "v_cndmask_b32_e64 v252, v252, v253, s[76:77]\n\t"
      "s_and_b32 s78, s74, s78\n\t"
      "v_mfma_f32_16x16x32_bf16 v[128:131], a[160:163], v[104:107], v[128:131]\n\t"
      "v_cndmask_b32_e64 v252, v252, v254, s[78:79]\n\t"
      "v_mfma_f32_16x16x32_bf16 v[128:131], a[184:187], v[108:111], v[128:131]\n\t"
      "ds_read_b128 v[148:151], v220 offset:2048\n\t"
      "s_add_u32 s70, s70, -1573\n\t"
      "v_mfma_f32_16x16x32_bf16 v[128:131], a[208:211], v[112:115], v[128:131]\n\t"
      "buffer_load_dwordx4 v208, s[20:23], s73 offen lds\n\t"
      "v_mfma_f32_16x16x32_bf16 v[128:131], a[232:235], v[116:119], v[128:131]\n\t"
      "ds_read_b128 v[72:75], v196 offset:512\n\t"
      "v_readlane_b32 s71, v252, s44\n\t"
      "s_max_i32 s82, s82, s87\n\t"
      "v_mfma_f32_16x16x32_bf16 v[128:131], v[164:167], v[120:123], v[128:131]\n\t"
      "s_sub_u32 s61, s70, s82\n\t"
      "v_mfma_f32_16x16x32_bf16 v[128:131], v[188:191], v[124:127], v[128:131]\n\t"
      "v_subrev_u32_e32 v240, 0x9e3779b9, v243\n\t"
      "s_sub_u32 s84, s84, 19\n\t"
      "v_mfma_f32_16x16x32_bf16 v[132:135], a[20:23], v[80:83], v[28:31]\n\t"
      "ds_read_b128 v[76:79], v197 offset:512\n\t"
      "s_lshr_b32 s60, s71, 16\n\t"
      "v_mfma_f32_16x16x32_bf16 v[132:135], a[44:47], v[84:87], v[132:135]\n\t"
      "buffer_load_dwordx4 v208, s[20:23], s73 offen offset:1024 lds\n\t"
      "s_and_b32 s86, s71, 0xff\n\t"
      "v_mfma_f32_16x16x32_bf16 v[132:135], a[68:71], v[88:91], v[132:135]\n\t"
      "v_cvt_pk_bf16_f32 v136, v128, v129\n\t"
      "s_bfe_u32 s71, s71, 0x80008\n\t"
      "v_mfma_f32_16x16x32_bf16 v[132:135], a[92:95], v[92:95], v[132:135]\n\t"
      "v_cvt_pk_bf16_f32 v137, v130, v131\n\t"
      "s_max_i32 s84, s84, s86\n\t"
      "v_mfma_f32_16x16x32_bf16 v[132:135], a[116:119], v[96:99], v[132:135]\n\t"
      "s_mul_i32 s61, s61, s60\n\t"
      "s_ashr_i32 s61, s61, 8\n\t"
      "v_mfma_f32_16x16x32_bf16 v[132:135], a[140:143], v[100:103], v[132:135]\n\t"
      "buffer_store_dword v240, v244, s[24:27], s67 offen offset:4 sc1\n\t"
      "v_mfma_f32_16x16x32_bf16 v[132:135], a[164:167], v[104:107], v[132:135]\n\t"
      "s_add_u32 s61, s61, s71\n\t"
      "s_mov_b32 s45, s46\n\t"
      "v_mfma_f32_16x16x32_bf16 v[132:135], a[188:191], v[108:111], v[132:135]\n\t"
      "s_max_i32 s61, s61, s84\n\t"
      "s_mov_b32 s46, s47\n\t"
      "v_mfma_f32_16x16x32_bf16 v[132:135], a[212:215], v[112:115], v[132:135]\n\t"
      "s_add_u32 s70, s70, s71\n\t"
      "s_mov_b32 s47, s48\n\t"
      "v_mfma_f32_16x16x32_bf16 v[132:135], a[236:239], v[116:119], v[132:135]\n\t"
      "s_add_u32 s82, s82, s61\n\t"
      "ds_write_b64 v219, v[136:137] offset:12288\n\t"
      "v_mfma_f32_16x16x32_bf16 v[132:135], v[168:171], v[120:123], v[132:135]\n\t"
      "v_add_u32_e32 v6, 1, v6\n\t"
      "v_mfma_f32_16x16x32_bf16 v[132:135], v[192:195], v[124:127], v[132:135]\n\t"
      "s_waitcnt lgkmcnt(3)\n\t"
      "buffer_store_dwordx4 v[148:151], v208, s[28:31], s72 offen offset:2048 sc1\n\t"
      ".p2align 6\n\t"
      ".Lloop_%=:\n\t"
      "v_mfma_f32_16x16x32_bf16 v[128:131], a[0:3], v[32:35], v[8:11]\n\t"
      "s_waitcnt vmcnt(17)\n\t"
      "v_mfma_f32_16x16x32_bf16 v[128:131], a[24:27], v[36:39], v[128:131]\n\t"
      "v_readlane_b32 s72, v249, s44\n\t"
      "v_mfma_f32_16x16x32_bf16 v[128:131], a[48:51], v[40:43], v[128:131]\n\t"
      "v_cvt_pk_bf16_f32 v138, v132, v133\n\t"
      "v_cvt_pk_bf16_f32 v139, v134, v135\n\t"
      "v_mfma_f32_16x16x32_bf16 v[128:131], a[72:75], v[44:47], v[128:131]\n\t"
      "ds_write_b32 v219, v138 offset:12296\n\t"
      "v_mfma_f32_16x16x32_bf16 v[128:131], a[96:99], v[48:51], v[128:131]\n\t"
      "ds_write_b32 v219, v139 offset:12300\n\t"
      "v_mfma_f32_16x16x32_bf16 v[128:131], a[120:123], v[52:55], v[128:131]\n\t"
      "ds_write_b32 v238, v6\n\t"
      "v_mfma_f32_16x16x32_bf16 v[128:131], a[144:147], v[56:59], v[128:131]\n\t"
      "buffer_load_dwordx4 v208, s[20:23], s73 offen offset:2048 lds\n\t"
      "v_mfma_f32_16x16x32_bf16 v[128:131], a[168:171], v[60:63], v[128:131]\n\t"
      "s_waitcnt lgkmcnt(4)\n\t"
      "v_add_u32_e32 v217, s46, v204\n\t"
      "v_mfma_f32_16x16x32_bf16 v[128:131], a[192:195], v[64:67], v[128:131]\n\t"
      "v_add_u32_e32 v218, s46, v205\n\t"
      "v_mfma_f32_16x16x32_bf16 v[128:131], a[216:219], v[68:71], v[128:131]\n\t"
      "v_add_u32_e32 v219, s46, v206\n\t"
      "v_mfma_f32_16x16x32_bf16 v[128:131], a[240:243], v[72:75], v[128:131]\n\t"
      "v_mfma_f32_16x16x32_bf16 v[128:131], v[172:175], v[76:79], v[128:131]\n\t"
      "s_cmp_gt_i32 s83, 1\n\t"
      "s_cbranch_scc1 .LslpA_%=\n\t"
      ".LretA_%=:\n\t"
      "v_mfma_f32_16x16x32_bf16 v[132:135], a[4:7], v[32:35], v[12:15]\n\t"
      "ds_read_b32 v7, v239\n\t"
      "v_mfma_f32_16x16x32_bf16 v[132:135], a[28:31], v[36:39], v[132:135]\n\t"
      "v_mfma_f32_16x16x32_bf16 v[132:135], a[52:55], v[40:43], v[132:135]\n\t"
      "v_cvt_pk_bf16_f32 v136, v128, v129\n\t"
      "v_cvt_pk_bf16_f32 v137, v130, v131\n\t"
      "v_mfma_f32_16x16x32_bf16 v[132:135], a[76:79], v[44:47], v[132:135]\n\t"
      "v_mfma_f32_16x16x32_bf16 v[132:135], a[100:103], v[48:51], v[132:135]\n\t"
      "s_waitcnt lgkmcnt(0)\n\t"
      "v_cmp_gt_u32_e32 vcc, v6, v7\n\t"
      "v_mfma_f32_16x16x32_bf16 v[132:135], a[124:127], v[52:55], v[132:135]\n\t"
      "s_cbranch_vccnz .LsbA_%=\n\t"
      ".LsrA_%=:\n\t"
      "v_mfma_f32_16x16x32_bf16 v[132:135], a[148:151], v[56:59], v[132:135]\n\t"
      "ds_read_b128 v[140:143], v220 offset:12288\n\t"
      "v_mfma_f32_16x16x32_bf16 v[132:135], a[172:175], v[60:63], v[132:135]\n\t"
      "ds_write_b32 v217, v136 offset:0\n\t"
      "v_mfma_f32_16x16x32_bf16 v[132:135], a[196:199], v[64:67], v[132:135]\n\t"
      "ds_write_b32 v217, v137 offset:4\n\t"
      "v_mfma_f32_16x16x32_bf16 v[132:135], a[220:223], v[68:71], v[132:135]\n\t"
      "ds_read_b128 v[80:83], v196 offset:12288\n\t"
      "v_mfma_f32_16x16x32_bf16 v[132:135], a[244:247], v[72:75], v[132:135]\n\t"
      "v_readlane_b32 s73, v249, s85\n\t"
      "v_mfma_f32_16x16x32_bf16 v[132:135], v[176:179], v[76:79], v[132:135]\n\t"
      "s_waitcnt lgkmcnt(3)\n\t"
      "buffer_store_dwordx4 v[140:143], v209, s[28:31], s72 offen sc1\n\t"
      "v_mfma_f32_16x16x32_bf16 v[128:131], a[8:11], v[32:35], v[16:19]\n\t"
      "ds_read_b128 v[84:87], v197 offset:12288\n\t"
      "v_mfma_f32_16x16x32_bf16 v[128:131], a[32:35], v[36:39], v[128:131]\n\t"
      "s_memrealtime s[80:81]\n\t"
      "v_mfma_f32_16x16x32_bf16 v[128:131], a[56:59], v[40:43], v[128:131]\n\t"
      "v_cvt_pk_bf16_f32 v138, v132, v133\n\t"
      "v_cvt_pk_bf16_f32 v139, v134, v135\n\t"
      "v_mfma_f32_16x16x32_bf16 v[128:131], a[80:83], v[44:47], v[128:131]\n\t"
      "ds_write_b32 v217, v138 offset:8\n\t"
      "v_mfma_f32_16x16x32_bf16 v[128:131], a[104:107], v[48:51], v[128:131]\n\t"
      "ds_write_b32 v217, v139 offset:12\n\t"
      "v_mfma_f32_16x16x32_bf16 v[128:131], a[128:131], v[52:55], v[128:131]\n\t"
      "ds_read_b128 v[88:91], v198 offset:12288\n\t"
      "v_mfma_f32_16x16x32_bf16 v[128:131], a[152:155], v[56:59], v[128:131]\n\t"
      "s_add_u32 m0, s45, 0x3000\n\t"
      "v_mfma_f32_16x16x32_bf16 v[128:131], a[176:179], v[60:63], v[128:131]\n\t"
      "ds_read_b128 v[92:95], v199 offset:12288\n\t"
      "v_mfma_f32_16x16x32_bf16 v[128:131], a[200:203], v[64:67], v[128:131]\n\t"
      "s_add_u32 s48, s47, 0x6000\n\t"
      "v_mfma_f32_16x16x32_bf16 v[128:131], a[224:227], v[68:71], v[128:131]\n\t"
      "ds_read_b128 v[96:99], v197 offset:12544\n\t"
      "v_mfma_f32_16x16x32_bf16 v[128:131], a[248:251], v[72:75], v[128:131]\n\t"
      "s_cmp_eq_u32 s48, s50\n\t"
      "s_cselect_b32 s48, s41, s48\n\t"
      "v_mfma_f32_16x16x32_bf16 v[128:131], v[180:183], v[76:79], v[128:131]\n\t"
      "buffer_load_dwordx4 v209, s[20:23], s73 offen lds\n\t"
      "v_mfma_f32_16x16x32_bf16 v[132:135], a[12:15], v[32:35], v[20:23]\n\t"
      "ds_read_b128 v[100:103], v198 offset:12544\n\t"
      "v_mfma_f32_16x16x32_bf16 v[132:135], a[36:39], v[36:39], v[132:135]\n\t"
      "v_mfma_f32_16x16x32_bf16 v[132:135], a[60:63], v[40:43], v[132:135]\n\t"
      "v_cvt_pk_bf16_f32 v136, v128, v129\n\t"
      "v_cvt_pk_bf16_f32 v137, v130, v131\n\t"
      "v_mfma_f32_16x16x32_bf16 v[132:135], a[84:87], v[44:47], v[132:135]\n\t"
      "ds_write_b32 v218, v136 offset:0\n\t"
      "v_mfma_f32_16x16x32_bf16 v[132:135], a[108:111], v[48:51], v[132:135]\n\t"
      "ds_write_b32 v218, v137 offset:4\n\t"
      "v_mfma_f32_16x16x32_bf16 v[132:135], a[132:135], v[52:55], v[132:135]\n\t"
      "ds_read_b128 v[144:147], v220 offset:13312\n\t"
      "v_mfma_f32_16x16x32_bf16 v[132:135], a[156:159], v[56:59], v[132:135]\n\t"
      "ds_read_b128 v[104:107], v199 offset:12544\n\t"
      "v_mfma_f32_16x16x32_bf16 v[132:135], a[180:183], v[60:63], v[132:135]\n\t"
      "s_sub_u32 s84, s84, 19\n\t"
      "v_mfma_f32_16x16x32_bf16 v[132:135], a[204:207], v[64:67], v[132:135]\n\t"
      "s_max_i32 s84, s84, s86\n\t"
      "v_mfma_f32_16x16x32_bf16 v[132:135], a[228:231], v[68:71], v[132:135]\n\t"
      "ds_read_b128 v[108:111], v196 offset:12544\n\t"
      "v_mfma_f32_16x16x32_bf16 v[132:135], a[252:255], v[72:75], v[132:135]\n\t"
      "v_mfma_f32_16x16x32_bf16 v[132:135], v[184:187], v[76:79], v[132:135]\n\t"
      "s_waitcnt lgkmcnt(2)\n\t"
      "buffer_store_dwordx4 v[144:147], v209, s[28:31], s72 offen offset:1024 sc1\n\t"
      "v_mfma_f32_16x16x32_bf16 v[128:131], a[16:19], v[32:35], v[24:27]\n\t"
      "ds_read_b128 v[112:115], v198 offset:12800\n\t"
      "v_mfma_f32_16x16x32_bf16 v[128:131], a[40:43], v[36:39], v[128:131]\n\t"
      "v_mfma_f32_16x16x32_bf16 v[128:131], a[64:67], v[40:43], v[128:131]\n\t"
      "v_cvt_pk_bf16_f32 v138, v132, v133\n\t"
      "v_cvt_pk_bf16_f32 v139, v134, v135\n\t"
      "v_mfma_f32_16x16x32_bf16 v[128:131], a[88:91], v[44:47], v[128:131]\n\t"
      "ds_write_b32 v218, v138 offset:8\n\t"
      "v_mfma_f32_16x16x32_bf16 v[128:131], a[112:115], v[48:51], v[128:131]\n\t"
      "ds_write_b32 v218, v139 offset:12\n\t"
      "v_mfma_f32_16x16x32_bf16 v[128:131], a[136:139], v[52:55], v[128:131]\n\t"
      "s_sub_u32 s87, s80, s52\n\t"
      "v_mfma_f32_16x16x32_bf16 v[128:131], a[160:163], v[56:59], v[128:131]\n\t"
      "s_sub_u32 s83, s82, s87\n\t"
      "ds_read_b128 v[116:119], v199 offset:12800\n\t"
      "v_mfma_f32_16x16x32_bf16 v[128:131], a[184:187], v[60:63], v[128:131]\n\t"
      "s_max_i32 s82, s82, s87\n\t"
      "v_mfma_f32_16x16x32_bf16 v[128:131], a[208:211], v[64:67], v[128:131]\n\t"
      "s_sub_u32 s61, s70, s82\n\t"
      "v_mfma_f32_16x16x32_bf16 v[128:131], a[232:235], v[68:71], v[128:131]\n\t"
      "s_mul_i32 s61, s61, s60\n\t"
      "ds_read_b128 v[120:123], v196 offset:12800\n\t"
      "v_mfma_f32_16x16x32_bf16 v[128:131], v[164:167], v[72:75], v[128:131]\n\t"
      "s_ashr_i32 s61, s61, 8\n\t"
      "v_mfma_f32_16x16x32_bf16 v[128:131], v[188:191], v[76:79], v[128:131]\n\t"
      "buffer_load_dwordx4 v209, s[20:23], s73 offen offset:1024 lds\n\t"
      "v_mfma_f32_16x16x32_bf16 v[132:135], a[20:23], v[32:35], v[28:31]\n\t"
      "s_add_u32 s61, s61, s71\n\t"
      "ds_read_b128 v[124:127], v197 offset:12800\n\t"
      "v_mfma_f32_16x16x32_bf16 v[132:135], a[44:47], v[36:39], v[132:135]\n\t"
      "s_max_i32 s61, s61, s84\n\t"
      "ds_read_b128 v[148:151], v220 offset:14336\n\t"
      "v_mfma_f32_16x16x32_bf16 v[132:135], a[68:71], v[40:43], v[132:135]\n\t"
      "s_add_u32 s82, s82, s61\n\t"
      "v_cvt_pk_bf16_f32 v136, v128, v129\n\t"
      "v_cvt_pk_bf16_f32 v137, v130, v131\n\t"
      "v_mfma_f32_16x16x32_bf16 v[132:135], a[92:95], v[44:47], v[132:135]\n\t"
      "ds_write_b32 v219, v136 offset:0\n\t"
      "v_mfma_f32_16x16x32_bf16 v[132:135], a[116:119], v[48:51], v[132:135]\n\t"
      "ds_write_b32 v219, v137 offset:4\n\t"
      "v_mfma_f32_16x16x32_bf16 v[132:135], a[140:143], v[52:55], v[132:135]\n\t"
      "s_add_u32 s70, s70, s71\n\t"
      "v_add_u32_e32 v220, s46, v207\n\t"
      "v_mfma_f32_16x16x32_bf16 v[132:135], a[164:167], v[56:59], v[132:135]\n\t"
      "v_add_u32_e32 v6, 1, v6\n\t"
      "v_mfma_f32_16x16x32_bf16 v[132:135], a[188:191], v[60:63], v[132:135]\n\t"
      "v_mfma_f32_16x16x32_bf16 v[132:135], a[212:215], v[64:67], v[132:135]\n\t"
      "s_add_u32 s44, s44, 1\n\t"
      "v_mfma_f32_16x16x32_bf16 v[132:135], a[236:239], v[68:71], v[132:135]\n\t"
      "s_add_u32 s85, s85, 1\n\t"
      "v_mfma_f32_16x16x32_bf16 v[132:135], v[168:171], v[72:75], v[132:135]\n\t"
      "v_mfma_f32_16x16x32_bf16 v[132:135], v[192:195], v[76:79], v[132:135]\n\t"
      "s_waitcnt lgkmcnt(2)\n\t"
      "buffer_store_dwordx4 v[148:151], v209, s[28:31], s72 offen offset:2048 sc1\n\t"
      "v_mfma_f32_16x16x32_bf16 v[128:131], a[0:3], v[80:83], v[8:11]\n\t"
      "s_waitcnt vmcnt(17)\n\t"
      "v_mfma_f32_16x16x32_bf16 v[128:131], a[24:27], v[84:87], v[128:131]\n\t"
      "v_readlane_b32 s72, v248, s44\n\t"
      "v_mfma_f32_16x16x32_bf16 v[128:131], a[48:51], v[88:91], v[128:131]\n\t"
      "v_cvt_pk_bf16_f32 v138, v132, v133\n\t"
      "v_cvt_pk_bf16_f32 v139, v134, v135\n\t"
      "v_mfma_f32_16x16x32_bf16 v[128:131], a[72:75], v[92:95], v[128:131]\n\t"
      "ds_write_b32 v219, v138 offset:8\n\t"
      "v_mfma_f32_16x16x32_bf16 v[128:131], a[96:99], v[96:99], v[128:131]\n\t"
      "ds_write_b32 v219, v139 offset:12\n\t"
      "v_mfma_f32_16x16x32_bf16 v[128:131], a[120:123], v[100:103], v[128:131]\n\t"
      "ds_write_b32 v238, v6\n\t"
      "v_mfma_f32_16x16x32_bf16 v[128:131], a[144:147], v[104:107], v[128:131]\n\t"
      "buffer_load_dwordx4 v209, s[20:23], s73 offen offset:2048 lds\n\t"
      "v_mfma_f32_16x16x32_bf16 v[128:131], a[168:171], v[108:111], v[128:131]\n\t"
      "v_add_u32_e32 v196, s47, v200\n\t"
      "v_mfma_f32_16x16x32_bf16 v[128:131], a[192:195], v[112:115], v[128:131]\n\t"
      "v_add_u32_e32 v197, s47, v201\n\t"
      "v_mfma_f32_16x16x32_bf16 v[128:131], a[216:219], v[116:119], v[128:131]\n\t"
      "v_add_u32_e32 v198, s47, v202\n\t"
      "v_mfma_f32_16x16x32_bf16 v[128:131], a[240:243], v[120:123], v[128:131]\n\t"
      "v_add_u32_e32 v199, s47, v203\n\t"
      "v_mfma_f32_16x16x32_bf16 v[128:131], v[172:175], v[124:127], v[128:131]\n\t"
      "s_cmp_gt_i32 s83, 1\n\t"
      "s_cbranch_scc1 .LslpB_%=\n\t"
      ".LretB_%=:\n\t"
      "v_mfma_f32_16x16x32_bf16 v[132:135], a[4:7], v[80:83], v[12:15]\n\t"
      "ds_read_b32 v7, v239\n\t"
      "v_mfma_f32_16x16x32_bf16 v[132:135], a[28:31], v[84:87], v[132:135]\n\t"
      "v_mfma_f32_16x16x32_bf16 v[132:135], a[52:55], v[88:91], v[132:135]\n\t"
      "v_cvt_pk_bf16_f32 v136, v128, v129\n\t"
      "v_cvt_pk_bf16_f32 v137, v130, v131\n\t"
      "v_mfma_f32_16x16x32_bf16 v[132:135], a[76:79], v[92:95], v[132:135]\n\t"
      "v_mfma_f32_16x16x32_bf16 v[132:135], a[100:103], v[96:99], v[132:135]\n\t"
      "s_waitcnt lgkmcnt(0)\n\t"
      "v_cmp_gt_u32_e32 vcc, v6, v7\n\t"
      "v_mfma_f32_16x16x32_bf16 v[132:135], a[124:127], v[100:103], v[132:135]\n\t"
      "s_cbranch_vccnz .LsbB_%=\n\t"
      ".LsrB_%=:\n\t"
      "v_mfma_f32_16x16x32_bf16 v[132:135], a[148:151], v[104:107], v[132:135]\n\t"
      "ds_read_b128 v[140:143], v220 offset:0\n\t"
      "v_mfma_f32_16x16x32_bf16 v[132:135], a[172:175], v[108:111], v[132:135]\n\t"
      "ds_write_b32 v217, v136 offset:12288\n\t"
      "v_mfma_f32_16x16x32_bf16 v[132:135], a[196:199], v[112:115], v[132:135]\n\t"
      "ds_write_b32 v217, v137 offset:12292\n\t"
      "v_mfma_f32_16x16x32_bf16 v[132:135], a[220:223], v[116:119], v[132:135]\n\t"
      "ds_read_b128 v[32:35], v196 offset:0\n\t"
      "v_mfma_f32_16x16x32_bf16 v[132:135], a[244:247], v[120:123], v[132:135]\n\t"
      "v_readlane_b32 s73, v248, s85\n\t"
      "v_mfma_f32_16x16x32_bf16 v[132:135], v[176:179], v[124:127], v[132:135]\n\t"
      "s_waitcnt lgkmcnt(3)\n\t"
      "buffer_store_dwordx4 v[140:143], v208, s[28:31], s72 offen sc1\n\t"
      "v_mfma_f32_16x16x32_bf16 v[128:131], a[8:11], v[80:83], v[16:19]\n\t"
      "ds_read_b128 v[36:39], v197 offset:0\n\t"
      "v_mfma_f32_16x16x32_bf16 v[128:131], a[32:35], v[84:87], v[128:131]\n\t"
      "s_memrealtime s[80:81]\n\t"
      "v_mfma_f32_16x16x32_bf16 v[128:131], a[56:59], v[88:91], v[128:131]\n\t"
      "v_cvt_pk_bf16_f32 v138, v132, v133\n\t"
      "v_cvt_pk_bf16_f32 v139, v134, v135\n\t"
      "v_mfma_f32_16x16x32_bf16 v[128:131], a[80:83], v[92:95], v[128:131]\n\t"
      "ds_write_b32 v217, v138 offset:12296\n\t"
      "v_mfma_f32_16x16x32_bf16 v[128:131], a[104:107], v[96:99], v[128:131]\n\t"
      "ds_write_b32 v217, v139 offset:12300\n\t"
      "v_mfma_f32_16x16x32_bf16 v[128:131], a[128:131], v[100:103], v[128:131]\n\t"
      "ds_read_b128 v[40:43], v198 offset:0\n\t"
      "v_mfma_f32_16x16x32_bf16 v[128:131], a[152:155], v[104:107], v[128:131]\n\t"
      "s_mov_b32 m0, s46\n\t"
      "v_mfma_f32_16x16x32_bf16 v[128:131], a[176:179], v[108:111], v[128:131]\n\t"
      "ds_read_b128 v[44:47], v199 offset:0\n\t"
      "v_mfma_f32_16x16x32_bf16 v[128:131], a[200:203], v[112:115], v[128:131]\n\t"
      "v_mfma_f32_16x16x32_bf16 v[128:131], a[224:227], v[116:119], v[128:131]\n\t"
      "ds_read_b128 v[48:51], v197 offset:256\n\t"
      "v_mfma_f32_16x16x32_bf16 v[128:131], a[248:251], v[120:123], v[128:131]\n\t"
      "v_mfma_f32_16x16x32_bf16 v[128:131], v[180:183], v[124:127], v[128:131]\n\t"
      "buffer_load_dwordx4 v208, s[20:23], s73 offen lds\n\t"
      "v_mfma_f32_16x16x32_bf16 v[132:135], a[12:15], v[80:83], v[20:23]\n\t"
      "ds_read_b128 v[52:55], v198 offset:256\n\t"
      "v_mfma_f32_16x16x32_bf16 v[132:135], a[36:39], v[84:87], v[132:135]\n\t"
      "v_readlane_b32 s71, v252, s44\n\t"
      "v_mfma_f32_16x16x32_bf16 v[132:135], a[60:63], v[88:91], v[132:135]\n\t"
      "v_cvt_pk_bf16_f32 v136, v128, v129\n\t"
      "v_cvt_pk_bf16_f32 v137, v130, v131\n\t"
      "v_mfma_f32_16x16x32_bf16 v[132:135], a[84:87], v[92:95], v[132:135]\n\t"
      "ds_write_b32 v218, v136 offset:12288\n\t"
      "v_mfma_f32_16x16x32_bf16 v[132:135], a[108:111], v[96:99], v[132:135]\n\t"
      "s_lshr_b32 s60, s71, 16\n\t"
      "ds_write_b32 v218, v137 offset:12292\n\t"
      "v_mfma_f32_16x16x32_bf16 v[132:135], a[132:135], v[100:103], v[132:135]\n\t"
      "s_and_b32 s86, s71, 0xff\n\t"
      "ds_read_b128 v[144:147], v220 offset:1024\n\t"
      "v_mfma_f32_16x16x32_bf16 v[132:135], a[156:159], v[104:107], v[132:135]\n\t"
      "s_bfe_u32 s71, s71, 0x80008\n\t"
      "ds_read_b128 v[56:59], v199 offset:256\n\t"
      "v_mfma_f32_16x16x32_bf16 v[132:135], a[180:183], v[108:111], v[132:135]\n\t"
      "s_sub_u32 s84, s84, 19\n\t"
      "v_mfma_f32_16x16x32_bf16 v[132:135], a[204:207], v[112:115], v[132:135]\n\t"
      "s_max_i32 s84, s84, s86\n\t"
      "v_mfma_f32_16x16x32_bf16 v[132:135], a[228:231], v[116:119], v[132:135]\n\t"
      "ds_read_b128 v[60:63], v196 offset:256\n\t"
      "v_mfma_f32_16x16x32_bf16 v[132:135], a[252:255], v[120:123], v[132:135]\n\t"
      "v_mfma_f32_16x16x32_bf16 v[132:135], v[184:187], v[124:127], v[132:135]\n\t"
      "s_waitcnt lgkmcnt(2)\n\t"
      "buffer_store_dwordx4 v[144:147], v208, s[28:31], s72 offen offset:1024 sc1\n\t"
      "v_mfma_f32_16x16x32_bf16 v[128:131], a[16:19], v[80:83], v[24:27]\n\t"
      "ds_read_b128 v[64:67], v198 offset:512\n\t"
      "v_mfma_f32_16x16x32_bf16 v[128:131], a[40:43], v[84:87], v[128:131]\n\t"
      "v_mfma_f32_16x16x32_bf16 v[128:131], a[64:67], v[88:91], v[128:131]\n\t"
      "v_cvt_pk_bf16_f32 v138, v132, v133\n\t"
      "v_cvt_pk_bf16_f32 v139, v134, v135\n\t"
      "v_mfma_f32_16x16x32_bf16 v[128:131], a[88:91], v[92:95], v[128:131]\n\t"
      "ds_write_b32 v218, v138 offset:12296\n\t"
      "v_mfma_f32_16x16x32_bf16 v[128:131], a[112:115], v[96:99], v[128:131]\n\t"
      "ds_write_b32 v218, v139 offset:12300\n\t"
      "v_mfma_f32_16x16x32_bf16 v[128:131], a[136:139], v[100:103], v[128:131]\n\t"
      "s_sub_u32 s87, s80, s52\n\t"
      "v_mfma_f32_16x16x32_bf16 v[128:131], a[160:163], v[104:107], v[128:131]\n\t"
      "s_sub_u32 s83, s82, s87\n\t"
      "ds_read_b128 v[68:71], v199 offset:512\n\t"
      "v_mfma_f32_16x16x32_bf16 v[128:131], a[184:187], v[108:111], v[128:131]\n\t"
      "s_max_i32 s82, s82, s87\n\t"
      "v_mfma_f32_16x16x32_bf16 v[128:131], a[208:211], v[112:115], v[128:131]\n\t"
      "s_sub_u32 s61, s70, s82\n\t"
      "v_mfma_f32_16x16x32_bf16 v[128:131], a[232:235], v[116:119], v[128:131]\n\t"
      "s_mul_i32 s61, s61, s60\n\t"
      "ds_read_b128 v[72:75], v196 offset:512\n\t"
      "v_mfma_f32_16x16x32_bf16 v[128:131], v[164:167], v[120:123], v[128:131]\n\t"
      "s_ashr_i32 s61, s61, 8\n\t"
      "v_mfma_f32_16x16x32_bf16 v[128:131], v[188:191], v[124:127], v[128:131]\n\t"
      "buffer_load_dwordx4 v208, s[20:23], s73 offen offset:1024 lds\n\t"
      "v_mfma_f32_16x16x32_bf16 v[132:135], a[20:23], v[80:83], v[28:31]\n\t"
      "s_add_u32 s61, s61, s71\n\t"
      "ds_read_b128 v[76:79], v197 offset:512\n\t"
      "v_mfma_f32_16x16x32_bf16 v[132:135], a[44:47], v[84:87], v[132:135]\n\t"
      "s_max_i32 s61, s61, s84\n\t"
      "ds_read_b128 v[148:151], v220 offset:2048\n\t"
      "v_mfma_f32_16x16x32_bf16 v[132:135], a[68:71], v[88:91], v[132:135]\n\t"
      "s_add_u32 s82, s82, s61\n\t"
      "v_cvt_pk_bf16_f32 v136, v128, v129\n\t"
      "v_cvt_pk_bf16_f32 v137, v130, v131\n\t"
      "v_mfma_f32_16x16x32_bf16 v[132:135], a[92:95], v[92:95], v[132:135]\n\t"
      "ds_write_b32 v219, v136 offset:12288\n\t"
      "v_mfma_f32_16x16x32_bf16 v[132:135], a[116:119], v[96:99], v[132:135]\n\t"
      "ds_write_b32 v219, v137 offset:12292\n\t"
      "v_mfma_f32_16x16x32_bf16 v[132:135], a[140:143], v[100:103], v[132:135]\n\t"
      "s_add_u32 s70, s70, s71\n\t"
      "v_mfma_f32_16x16x32_bf16 v[132:135], a[164:167], v[104:107], v[132:135]\n\t"
      "s_mov_b32 s45, s46\n\t"
      "v_add_u32_e32 v6, 1, v6\n\t"
      "v_mfma_f32_16x16x32_bf16 v[132:135], a[188:191], v[108:111], v[132:135]\n\t"
      "s_mov_b32 s46, s47\n\t"
      "v_mfma_f32_16x16x32_bf16 v[132:135], a[212:215], v[112:115], v[132:135]\n\t"
      "s_mov_b32 s47, s48\n\t"
      "v_mfma_f32_16x16x32_bf16 v[132:135], a[236:239], v[116:119], v[132:135]\n\t"
      "v_mfma_f32_16x16x32_bf16 v[132:135], v[168:171], v[120:123], v[132:135]\n\t"
      "s_cmp_lg_u32 s44, s49\n\t"
      "v_mfma_f32_16x16x32_bf16 v[132:135], v[192:195], v[124:127], v[132:135]\n\t"
      "s_waitcnt lgkmcnt(2)\n\t"
      "buffer_store_dwordx4 v[148:151], v208, s[28:31], s72 offen offset:2048 sc1\n\t"
      "s_cbranch_scc1 .Lloop_%=\n\t"
      "v_readlane_b32 s72, v249, s44\n\t"
      "s_nop 6\n\t"
      "v_cvt_pk_bf16_f32 v138, v132, v133\n\t"
      "v_cvt_pk_bf16_f32 v139, v134, v135\n\t"
      "ds_write_b64 v219, v[138:139] offset:12296\n\t"
      "s_waitcnt lgkmcnt(0)\n\t"
      "s_barrier\n\t"
      "ds_read_b128 v[32:35], v220 offset:12288\n\t"
      "ds_read_b128 v[36:39], v220 offset:13312\n\t"
      "ds_read_b128 v[40:43], v220 offset:14336\n\t"
      "s_waitcnt lgkmcnt(2)\n\t"
      "buffer_store_dwordx4 v[32:35], v209, s[28:31], s72 offen sc1\n\t"
      "s_waitcnt lgkmcnt(1)\n\t"
      "buffer_store_dwordx4 v[36:39], v209, s[28:31], s72 offen offset:1024 sc1\n\t"
      "s_waitcnt lgkmcnt(0)\n\t"
      "buffer_store_dwordx4 v[40:43], v209, s[28:31], s72 offen offset:2048 sc1\n\t"
      "s_endpgm\n\t"
      ".LslpA_%=:\n\t"
      "s_min_i32 s83, s83, 0x190\n\t"
      ".Lsl2A_%=:\n\t"
      "s_sleep 1\n\t"
      "s_sub_u32 s83, s83, 3\n\t"
      "s_cmp_gt_i32 s83, 1\n\t"
      "s_cbranch_scc1 .Lsl2A_%=\n\t"
      "s_branch .LretA_%=\n\t"
      ".LslpB_%=:\n\t"
      "s_min_i32 s83, s83, 0x190\n\t"
      ".Lsl2B_%=:\n\t"
      "s_sleep 1\n\t"
      "s_sub_u32 s83, s83, 3\n\t"
      "s_cmp_gt_i32 s83, 1\n\t"
      "s_cbranch_scc1 .Lsl2B_%=\n\t"
      "s_branch .LretB_%=\n\t"
      ".Lgs_%=:\n\t"
      "s_sleep 8\n\t"
      "buffer_load_dwordx2 v[240:241], v245, s[24:27], 0 offen sc1\n\t"
      "s_memrealtime s[76:77]\n\t"
      "s_waitcnt vmcnt(0) lgkmcnt(0)\n\t"
      "v_subrev_u32_e32 v242, s67, v240\n\t"
      "v_xor_b32_e32 v241, s63, v241\n\t"
      "v_lshl_or_b32 v240, v242, 20, v241\n\t"
      "v_lshrrev_b32_e32 v242, 12, v242\n\t"
      "v_cmp_ne_u32_e32 vcc, 0, v240\n\t"
      "s_nop 0\n\t"
      "s_cbranch_vccz .Lgr_%=\n\t"
      "s_sub_u32 s76, s76, s52\n\t"
      "s_cmp_lt_u32 s76, 2000\n\t"
      "s_cbranch_scc1 .Lgs_%=\n\t"
      "v_mov_b32_e32 v242, 10\n\t"
      "s_branch .Lgr_%=\n\t"
      ".LsbA_%=:\n\t"
      "ds_read_b32 v7, v239\n\t"
      "s_waitcnt lgkmcnt(0)\n\t"
      "v_cmp_gt_u32_e32 vcc, v6, v7\n\t"
      "s_nop 0\n\t"
      "s_cbranch_vccnz .LsbA_%=\n\t"
      "s_branch .LsrA_%=\n\t"
      ".LsbB_%=:\n\t"
      "ds_read_b32 v7, v239\n\t"
      "s_waitcnt lgkmcnt(0)\n\t"
      "v_cmp_gt_u32_e32 vcc, v6, v7\n\t"
      "s_nop 0\n\t"
      "s_cbranch_vccnz .LsbB_%=\n\t"
      "s_branch .LsrB_%=\n\t"
      ".Lend_%=:\n\t"
      :
      : "{s[20:21]}"(xa), "{s[16:17]}"(wa), "{s[36:37]}"(ba), "{s[28:29]}"(oa), "{s[24:25]}"(sa), "{s[52:53]}"(rs),
        "{s[22:23]}"(0x0002000006c00000ull), "{s[26:27]}"(0x0002000000000808ull), "{s[30:31]}"(0x0002000006c00000ull),
        "{s[18:19]}"(0x0002000000048000ull), "{s[38:39]}"(0x0002000000000300ull),
        "{s40}"(t0), "{s41}"(p0), "{s43}"(lds0), "{s94}"(ty),
        "{v208}"(ga), "{v209}"(gb),
        "{v211}"(woff[0]), "{v212}"(woff[1]), "{v213}"(woff[2]), "{v215}"(boff),
        "{s95}"(tres), "{s92}"(pre1), "{s[60:61]}"(pres), "{s[62:63]}"(ep), "{s77}"(pfire), "{s98}"(((tidx >> 3) * 4u + (tidx & 3u)) * 128u), "{v238}"(vfw), "{v239}"(vfr),
        "{v248}"(ta), "{v249}"(ta), "{v251}"(t1), "{v250}"(mk), "{s74}"(plm), "{s75}"(plb),
        "{s[34:35]}"(__builtin_amdgcn_kernarg_segment_ptr()),
        "{v244}"(vpub), "{v210}"(vbmp), "{v245}"(vgv), "{v246}"(vxw), "{v247}"(vxr),
        "{v200}"(cfa[0]), "{v201}"(cfa[1]), "{v202}"(cfa[2]), "{v203}"(cfa[3]),
        "{v204}"(csw[0]), "{v205}"(csw[1]), "{v206}"(csw[2]), "{v207}"(csr),
        "{v214}"(tbase)
      : "memory", "scc", "vcc",
      "s42", "s44", "s45", "s46", "s47", "s48", "s49", "s50", "s51", "s54", "s55", "s56", "s57", "s58", "s59", "s64", "s65", "s66", "s67", "s68",
      "s69", "s70", "s71", "s72", "s73", "s76", "s78", "s79", "s80", "s81", "s82", "s83", "s84", "s85", "s86", "s87", "s88", "s89", "s90", "s91",
      "s93", "s96", "s97", "s99",
      "v1", "v2", "v3", "v4", "v5", "v6", "v7", "v8", "v9", "v10", "v11", "v12", "v13", "v14", "v15", "v16", "v17", "v18", "v19", "v20", "v21", "v22",
      "v23", "v24", "v25", "v26", "v27", "v28", "v29", "v30", "v31", "v32", "v33", "v34", "v35", "v36", "v37", "v38", "v39", "v40", "v41", "v42",
      "v43", "v44", "v45", "v46", "v47", "v48", "v49", "v50", "v51", "v52", "v53", "v54", "v55", "v56", "v57", "v58", "v59", "v60", "v61", "v62",
      "v63", "v64", "v65", "v66", "v67", "v68", "v69", "v70", "v71", "v72", "v73", "v74", "v75", "v76", "v77", "v78", "v79", "v80", "v81", "v82",
      "v83", "v84", "v85", "v86", "v87", "v88", "v89", "v90", "v91", "v92", "v93", "v94", "v95", "v96", "v97", "v98", "v99", "v100", "v101", "v102",
      "v103", "v104", "v105", "v106", "v107", "v108", "v109", "v110", "v111", "v112", "v113", "v114", "v115", "v116", "v117", "v118", "v119", "v120",
      "v121", "v122", "v123", "v124", "v125", "v126", "v127", "v128", "v129", "v130", "v131", "v132", "v133", "v134", "v135", "v136", "v137", "v138",
      "v139", "v140", "v141", "v142", "v143", "v144", "v145", "v146", "v147", "v148", "v149", "v150", "v151", "v152", "v153", "v154", "v155", "v156",
      "v157", "v158", "v159", "v160", "v161", "v162", "v163", "v164", "v165", "v166", "v167", "v168", "v169", "v170", "v171", "v172", "v173", "v174",
      "v175", "v176", "v177", "v178", "v179", "v180", "v181", "v182", "v183", "v184", "v185", "v186", "v187", "v188", "v189", "v190", "v191", "v192",
      "v193", "v194", "v195", "v196", "v197", "v198", "v199", "v216", "v217", "v218", "v219", "v220", "v221", "v222", "v223", "v224", "v225", "v226",
      "v227", "v228", "v229", "v230", "v231", "v232", "v233", "v234", "v235", "v236", "v237", "v240", "v241", "v242", "v243", "v252", "v253", "v254",
      "v255",
      "a0", "a1", "a2", "a3", "a4", "a5", "a6", "a7", "a8", "a9", "a10", "a11", "a12", "a13", "a14", "a15", "a16", "a17", "a18", "a19", "a20", "a21",
      "a22", "a23", "a24", "a25", "a26", "a27", "a28", "a29", "a30", "a31", "a32", "a33", "a34", "a35", "a36", "a37", "a38", "a39", "a40", "a41",
      "a42", "a43", "a44", "a45", "a46", "a47", "a48", "a49", "a50", "a51", "a52", "a53", "a54", "a55", "a56", "a57", "a58", "a59", "a60", "a61",
      "a62", "a63", "a64", "a65", "a66", "a67", "a68", "a69", "a70", "a71", "a72", "a73", "a74", "a75", "a76", "a77", "a78", "a79", "a80", "a81",
      "a82", "a83", "a84", "a85", "a86", "a87", "a88", "a89", "a90", "a91", "a92", "a93", "a94", "a95", "a96", "a97", "a98", "a99", "a100", "a101",
      "a102", "a103", "a104", "a105", "a106", "a107", "a108", "a109", "a110", "a111", "a112", "a113", "a114", "a115", "a116", "a117", "a118", "a119",
      "a120", "a121", "a122", "a123", "a124", "a125", "a126", "a127", "a128", "a129", "a130", "a131", "a132", "a133", "a134", "a135", "a136", "a137",
      "a138", "a139", "a140", "a141", "a142", "a143", "a144", "a145", "a146", "a147", "a148", "a149", "a150", "a151", "a152", "a153", "a154", "a155",
      "a156", "a157", "a158", "a159", "a160", "a161", "a162", "a163", "a164", "a165", "a166", "a167", "a168", "a169", "a170", "a171", "a172", "a173",
      "a174", "a175", "a176", "a177", "a178", "a179", "a180", "a181", "a182", "a183", "a184", "a185", "a186", "a187", "a188", "a189", "a190", "a191",
      "a192", "a193", "a194", "a195", "a196", "a197", "a198", "a199", "a200", "a201", "a202", "a203", "a204", "a205", "a206", "a207", "a208", "a209",
      "a210", "a211", "a212", "a213", "a214", "a215", "a216", "a217", "a218", "a219", "a220", "a221", "a222", "a223", "a224", "a225", "a226", "a227",
      "a228", "a229", "a230", "a231", "a232", "a233", "a234", "a235", "a236", "a237", "a238", "a239", "a240", "a241", "a242", "a243", "a244", "a245",
      "a246", "a247", "a248", "a249", "a250", "a251", "a252", "a253", "a254", "a255");
}

extern "C" void kernel_launch(void* const* d_in, const int* in_sizes, int n_in,
                              void* d_out, int out_size, void* d_ws, size_t ws_size,
                              hipStream_t stream) {
  const uint16_t* x = (const uint16_t*)d_in[0];
  const uint16_t* w = (const uint16_t*)d_in[1];
  const uint16_t* b = (const uint16_t*)d_in[2];
  uint16_t* out = (uint16_t*)d_out;
  const PaceTab pt = {{
    0x205d4eu, 0x205a4cu, 0x184944u, 0u,
    0x205d4eu, 0x205a4cu, 0x184944u, 0u,
    0x20564eu, 0x20534cu, 0x184944u, 0u,
    0x204f4eu, 0x204b48u, 0x184944u, 0u,
    0x184f4eu, 0x184b48u, 0x184944u, 0u,
    0x184f4eu, 0x184b48u, 0x184944u, 0u,
    0x184f4eu, 0x184b48u, 0x184944u, 0u,
    0x11514eu, 0x114e48u, 0x114944u, 0u,
    0x13534eu, 0x135049u, 0x134b44u, 0u,
    0x16544eu, 0x16514au, 0x164d46u, 0u,
    0x1b544eu, 0x1b514au, 0x1b4d46u, 0u,
    0x22544eu, 0x22514au, 0x224d46u, 0u,
    0x2f544eu, 0x2f514au, 0x2f4d46u, 0u,
    0x49544eu, 0x49514au, 0x494d46u, 0u,
    0xab544eu, 0xab514au, 0xab4d46u, 0u,
    0x1004400u, 0x1004400u, 0x1004400u, 0u,
    0x1004400u, 0x1004400u, 0x1004400u, 0u,
    0x1004400u, 0x1004400u, 0x1004400u, 0u}};
  conv1x1_kernel<<<256, 256, 0, stream>>>(x, w, b, out, (unsigned long long*)d_ws, pt);
}
```

```cpp
#include <hip/hip_runtime.h>
#include <stdint.h>

#define LAS __attribute__((address_space(3)))

struct PaceTab { unsigned v[72]; };

__global__ __launch_bounds__(256, 1) void conv1x1_kernel(const uint16_t* __restrict__ x,
                                                         const uint16_t* __restrict__ w,
                                                         const uint16_t* __restrict__ bias,
                                                         uint16_t* __restrict__ out, unsigned long long* ws, PaceTab pt) {
  __shared__ __attribute__((aligned(1024))) unsigned char lds[161792];

  unsigned long long xa, wa, ba, oa, sa, rs;
  asm volatile(
      "s_load_dwordx2 s[16:17], %6, 0x8\n\t"
      "s_load_dwordx2 s[20:21], %6, 0x0\n\t"
      "s_load_dwordx2 s[36:37], %6, 0x10\n\t"
      "s_load_dwordx2 s[28:29], %6, 0x18\n\t"
      "s_load_dwordx2 s[24:25], %6, 0x20\n\t"
      "s_memrealtime s[52:53]\n\t"
      : "={s[20:21]}"(xa), "={s[16:17]}"(wa), "={s[36:37]}"(ba), "={s[28:29]}"(oa), "={s[24:25]}"(sa), "={s[52:53]}"(rs)
      : "s"(__builtin_amdgcn_kernarg_segment_ptr())
      : "memory");
  __builtin_amdgcn_sched_barrier(0);
  const unsigned tid  = threadIdx.x;
  unsigned lane = tid & 63;
  unsigned wv   = __builtin_amdgcn_readfirstlane(tid >> 6);
  const unsigned lds0 = (unsigned)(size_t)(LAS unsigned char*)lds;
  const unsigned blk  = blockIdx.x;
  const unsigned role = (blk >> 2) & 1u;
  unsigned pk = (blk & 3u) * 32u + (blk >> 3);
  const unsigned sst  = (role ^ (blk >> 4)) & 1u;
  const unsigned t0   = pk * 49152u + sst * 24576u;
  const unsigned p0   = lds0 + 3072u * wv;
  const unsigned lq = lane >> 4, ph = (lane >> 2) & 3, pl = lane & 3;
  const unsigned gs = 256u * lq + 64u * ((ph - lq) & 3) + 16u * (pl ^ ((4u - wv) & 3));
  const unsigned ga = gs + 3072u * (wv < 3u ? wv : 5u);
  const unsigned gb = gs + 3072u * (wv < 2u ? wv + 6u : wv + 1u);
  const unsigned pv = blk >> 6, ps = (blk & 63u) ^ (4u * (pv & 1u)), pgr = (ps >> 2) & 7u, pi = (ps & 3u) * 2u + (pv >> 1);
  const unsigned ppg = min(pgr * 8u, 52u) + (pi & (0x13777777u >> (4u * pgr)) & 7u);
  const unsigned pop = (ppg << 21) + ((ps & 3u) * 4u + pv) * 0x1c080u + 0x8000u;
  unsigned pfire = ((role ^ ppg) & 1u) << 13;
  const unsigned pfo = (blk >> 3) * 9216u;
  const unsigned pfv = tid * 16u;
  const unsigned ty = (blk >> 3) & 1u;
  const unsigned tidx = (blk >> 3) * 4u + wv, tsel = tidx >> 6;
  const unsigned toff = (tidx & 63u) < 54u ? (tidx & 63u) << 21 : 113246204u;
  unsigned tres, pre1;
  unsigned long long pres, ep;
  asm volatile(
      "s_add_u32 m0, s43, 0x27000\n\t"
      "s_add_u32 s51, s42, 0x1000\n\t"
      "s_waitcnt lgkmcnt(0)\n\t"
      "buffer_load_dwordx4 v216, s[16:19], s42 offen lds\n\t"
      "buffer_load_dwordx4 v216, s[16:19], s51 offen lds\n\t"
      "s_add_u32 s51, s42, 0x2000\n\t"
      "buffer_load_dwordx4 v216, s[16:19], s51 offen lds\n\t"
      "s_cmp_lg_u32 s41, s43\n\t"
      "s_cbranch_scc1 .Lw0pi_%=\n\t"
      "s_cmp_eq_u32 s78, 0\n\t"
      "s_cselect_b64 s[96:97], s[20:21], s[28:29]\n\t"
      "s_load_dword s92, s[96:97], s76 offset:0x0\n\t"
      "s_load_dwordx2 s[60:61], s[96:97], s76 offset:0x3300\n\t"
      ".Lw0pi_%=:\n\t"
      "s_mov_b32 m0, s41\n\t"
      "s_nop 0\n\t"
      "buffer_load_dwordx4 v208, s[20:23], s40 offen lds\n\t"
      "buffer_load_dwordx4 v208, s[20:23], s40 offen offset:1024 lds\n\t"
      "buffer_load_dwordx4 v208, s[20:23], s40 offen offset:2048 lds\n\t"
      "s_add_u32 m0, s41, 0x3000\n\t"
      "s_nop 0\n\t"
      "buffer_load_dwordx4 v209, s[20:23], s40 offen lds\n\t"
      "buffer_load_dwordx4 v209, s[20:23], s40 offen offset:1024 lds\n\t"
      "buffer_load_dwordx4 v209, s[20:23], s40 offen offset:2048 lds\n\t"
      "s_cmp_lg_u32 s41, s43\n\t"
      "s_cbranch_scc1 .Lw0pe_%=\n\t"
      "s_waitcnt lgkmcnt(2)\n\t"
      "s_nop 7\n\t"
      "s_getreg_b32 s51, hwreg(HW_REG_IB_STS, 8, 4)\n\t"
      "s_lshl_b32 s96, s51, 13\n\t"
      "s_xor_b32 s77, s77, s96\n\t"
      "s_and_b32 s77, s77, 0x2000\n\t"
      "s_cmp_lg_u32 s51, 0\n\t"
      "s_cselect_b32 s77, s77, 0x1000\n\t"
      ".Lw0pe_%=:\n\t"
      "s_load_dwordx2 s[62:63], s[24:25], 0x800\n\t"
      "s_cmp_eq_u32 s99, 0\n\t"
      "s_cselect_b64 s[96:97], s[20:21], s[28:29]\n\t"
      "s_load_dword s95, s[96:97], s98\n\t"
      : "={s95}"(tres), "={s92}"(pre1), "={s[60:61]}"(pres), "={s[62:63]}"(ep), "+{s77}"(pfire), "+v"(lane), "+s"(wv), "+s"(pk)
      : "{s[20:21]}"(xa), "{s[16:17]}"(wa), "{s[36:37]}"(ba), "{s[28:29]}"(oa), "{s[24:25]}"(sa), "{s[52:53]}"(rs),
        "{s[22:23]}"(0x0002000006c00000ull), "{s[26:27]}"(0x0002000000000808ull), "{s[30:31]}"(0x0002000006c00000ull),
        "{s[18:19]}"(0x0002000000048000ull), "{s[38:39]}"(0x0002000000000300ull),
        "{s40}"(t0), "{s41}"(p0), "{s43}"(lds0), "{s94}"(ty),
        "{v208}"(ga), "{v209}"(gb),
        "{s42}"(pfo), "{s98}"(toff), "{s99}"(tsel), "{s76}"(pop), "{s78}"(ps >> 5),
        "{v216}"(pfv)
      : "memory", "scc", "s51", "s96", "s97");
  __builtin_amdgcn_sched_barrier(0);
  const unsigned sp = lane < 18u ? lane : lane - 14u;
  const unsigned ob = (unsigned)(((sp < 12u ? 0x41ccd62ca418820ull >> (5u * sp) : 0x22a4c1eeull >> (5u * ((sp - 12u) & 7u)))) & 31u) * 6291456u + pk * 49152u;
  const unsigned pg = (ob + 24576u) >> 21;
  const unsigned ta = lane < 18u ? ob + 24576u * sst : 113246208u, t1 = lane < 32u ? ob + 24576u * (sst ^ 1u) : 113246208u;
  const unsigned mk = lane >= 4u && lane < 18u ? 1u << (pg < 52u ? (pg >> 3) * 4u : 28u) : 0u;
  const unsigned plm = (unsigned)__builtin_amdgcn_ballot_w64(((pg ^ role ^ sst) & 1u) != 0u), plb = wv < 2u ? plm : ~plm;
  const unsigned vgv = 0u + wv * 512u + lane * 8u;
  const unsigned vpub = vgv == 0u ? 0u + (pv * 64u + ps) * 8u : 0x40000000u, vbmp = vpub == 0u ? 2048u : 0x40000000u;
  const unsigned vxr = lds0 + 159744u + lane * 16u, vxw = vxr + wv * 4u;
  const unsigned vfw = lds0 + 160768u + lane * 4u + wv * 256u, vfr = lds0 + 160768u + lane * 4u + (lane & 3u) * 256u;
  const unsigned li = lane & 15, lg = lane >> 4, rl = li & 3, hi = li >> 2;
  const unsigned rb = 768u * li + 16u * (lg ^ ((4u - hi) & 3)) - 3072u * wv;
  unsigned cfa[4], csw[3];
#pragma unroll
  for (unsigned u = 0; u < 4; ++u) cfa[u] = rb + 64u * ((u - rl) & 3);
#pragma unroll
  for (unsigned j = 0; j < 3; ++j) {
    const unsigned e = 3u * wv + j;
    csw[j] = rb + 256u * (e >> 2) + 64u * (((e & 3) + (e >> 2) - rl) & 3);
  }
  const unsigned csr = 16u * lane;
  const unsigned tbase = p0 + (4u * lg + (li >> 2)) * 192u + 16u * (li & 3);
  unsigned woff[3];
#pragma unroll
  for (unsigned j = 0; j < 3; ++j) {
    const unsigned q = 64u * j + lane, rr = q / 12u;
    woff[j] = (8u * (rr >> 2) + (rr & 3)) * 768u + 192u * wv + 16u * (q % 12u) - 1024u * j;
  }
  const unsigned boff = 192u * wv + 16u * lg;
  asm volatile(
      "s_cmp_eq_u32 s94, 1\n\t"
      "s_cbranch_scc0 .Lc0_%=\n\t"
      "s_getpc_b64 s[96:97]\n\t"
      ".Lcp_%=:\n\t"
      "s_add_u32 s98, s98, .Lcq_%=-.Lcp_%=\n\t"
      "s_min_u32 s98, s98, .Lend_%=-.Lcp_%=-68\n\t"
      "s_load_dword s99, s[96:97], s98 offset:64\n\t"
      "s_load_dword s98, s[96:97], s98\n\t"
      "s_add_u32 m0, s41, 0x6000\n\t"
      "s_add_u32 s51, s40, 0x600000\n\t"
      "buffer_load_dwordx4 v208, s[20:23], s51 offen lds\n\t"
      "buffer_load_dwordx4 v208, s[20:23], s51 offen offset:1024 lds\n\t"
      "buffer_load_dwordx4 v208, s[20:23], s51 offen offset:2048 lds\n\t"
      "s_add_u32 m0, s41, 0x9000\n\t"
      "s_add_u32 s51, s40, 0x600000\n\t"
      "buffer_load_dwordx4 v209, s[20:23], s51 offen lds\n\t"
      "buffer_load_dwordx4 v209, s[20:23], s51 offen offset:1024 lds\n\t"
      "buffer_load_dwordx4 v209, s[20:23], s51 offen offset:2048 lds\n\t"
      ".Lc0_%=:\n\t"
      "buffer_load_dwordx4 v[226:229], v215, s[36:39], 0 offen offset:0\n\t"
      "buffer_load_dwordx4 v[230:233], v215, s[36:39], 0 offen offset:64\n\t"
      "buffer_load_dwordx4 v[234:237], v215, s[36:39], 0 offen offset:128\n\t"
      "s_add_u32 m0, s41, 0x1e000\n\t"
      "s_mov_b32 s51, 0x0\n\t"
      "buffer_load_dwordx4 v211, s[16:19], s51 offen lds\n\t"
      "buffer_load_dwordx4 v212, s[16:19], s51 offen offset:1024 lds\n\t"
      "buffer_load_dwordx4 v213, s[16:19], s51 offen offset:2048 lds\n\t"
      "s_add_u32 m0, s41, 0x21000\n\t"
      "s_mov_b32 s51, 0xc00\n\t"
      "buffer_load_dwordx4 v211, s[16:19], s51 offen lds\n\t"
      "buffer_load_dwordx4 v212, s[16:19], s51 offen offset:1024 lds\n\t"
      "buffer_load_dwordx4 v213, s[16:19], s51 offen offset:2048 lds\n\t"
      "s_add_u32 m0, s41, 0x24000\n\t"
      "s_mov_b32 s51, 0x6000\n\t"
      "buffer_load_dwordx4 v211, s[16:19], s51 offen lds\n\t"
      "buffer_load_dwordx4 v212, s[16:19], s51 offen offset:1024 lds\n\t"
      "buffer_load_dwordx4 v213, s[16:19], s51 offen offset:2048 lds\n\t"
      "s_add_u32 m0, s41, 0x12000\n\t"
      "s_mov_b32 s51, 0x6c00\n\t"
      "buffer_load_dwordx4 v211, s[16:19], s51 offen lds\n\t"
      "buffer_load_dwordx4 v212, s[16:19], s51 offen offset:1024 lds\n\t"
      "buffer_load_dwordx4 v213, s[16:19], s51 offen offset:2048 lds\n\t"
      "s_add_u32 m0, s41, 0x15000\n\t"
      "s_mov_b32 s51, 0xc000\n\t"
      "buffer_load_dwordx4 v211, s[16:19], s51 offen lds\n\t"
      "buffer_load_dwordx4 v212, s[16:19], s51 offen offset:1024 lds\n\t"
      "buffer_load_dwordx4 v213, s[16:19], s51 offen offset:2048 lds\n\t"
      "s_add_u32 m0, s41, 0x18000\n\t"
      "s_mov_b32 s51, 0xcc00\n\t"
      "buffer_load_dwordx4 v211, s[16:19], s51 offen lds\n\t"
      "buffer_load_dwordx4 v212, s[16:19], s51 offen offset:1024 lds\n\t"
      "buffer_load_dwordx4 v213, s[16:19], s51 offen offset:2048 lds\n\t"
      "s_add_u32 m0, s41, 0x1b000\n\t"
      "s_mov_b32 s51, 0x12000\n\t"
      "buffer_load_dwordx4 v211, s[16:19], s51 offen lds\n\t"
      "buffer_load_dwordx4 v212, s[16:19], s51 offen offset:1024 lds\n\t"
      "buffer_load_dwordx4 v213, s[16:19], s51 offen offset:2048 lds\n\t"
      "s_mov_b32 s44, 0\n\t"
      "s_mov_b32 s85, 3\n\t"
      "s_add_u32 s50, s41, 0x12000\n\t"
      "s_mov_b32 s45, s41\n\t"
      "s_add_u32 s46, s41, 0x6000\n\t"
      "s_add_u32 s47, s41, 0xc000\n\t"
      "s_mov_b32 s84, 96\n\t"
      "v_add_u32_e32 v196, s41, v200\n\t"
      "v_add_u32_e32 v197, s41, v201\n\t"
      "v_add_u32_e32 v198, s41, v202\n\t"
      "v_add_u32_e32 v199, s41, v203\n\t"
      "v_add_u32_e32 v217, s41, v204\n\t"
      "v_add_u32_e32 v218, s41, v205\n\t"
      "v_add_u32_e32 v219, s41, v206\n\t"
      "v_add_u32_e32 v220, s41, v207\n\t"
      "s_waitcnt vmcnt(18)\n\t"
      "s_waitcnt lgkmcnt(0)\n\t"
      "s_barrier\n\t"
      "s_memrealtime s[68:69]\n\t"
      "v_add_u32_e32 v221, 0x1e000, v214\n\t"
      "ds_read_b64_tr_b16 a[0:1], v221 offset:0\n\t"
      "ds_read_b64_tr_b16 a[4:5], v221 offset:8\n\t"
      "ds_read_b64_tr_b16 a[8:9], v221 offset:64\n\t"
      "ds_read_b64_tr_b16 a[12:13], v221 offset:72\n\t"
      "ds_read_b64_tr_b16 a[16:17], v221 offset:128\n\t"
      "ds_read_b64_tr_b16 a[20:21], v221 offset:136\n\t"
      "ds_read_b128 v[140:143], v196 offset:0\n\t"
      "v_lshlrev_b32_e32 v8, 16, v226\n\t"
      "v_and_b32_e32 v9, 0xffff0000, v226\n\t"
      "v_lshlrev_b32_e32 v10, 16, v227\n\t"
      "v_and_b32_e32 v11, 0xffff0000, v227\n\t"
      "v_lshlrev_b32_e32 v12, 16, v228\n\t"
      "v_and_b32_e32 v13, 0xffff0000, v228\n\t"
      "v_lshlrev_b32_e32 v14, 16, v229\n\t"
      "v_and_b32_e32 v15, 0xffff0000, v229\n\t"
      "v_lshlrev_b32_e32 v16, 16, v230\n\t"
      "v_and_b32_e32 v17, 0xffff0000, v230\n\t"
      "v_lshlrev_b32_e32 v18, 16, v231\n\t"
      "v_and_b32_e32 v19, 0xffff0000, v231\n\t"
      "v_lshlrev_b32_e32 v20, 16, v232\n\t"
      "v_and_b32_e32 v21, 0xffff0000, v232\n\t"
      "v_lshlrev_b32_e32 v22, 16, v233\n\t"
      "v_and_b32_e32 v23, 0xffff0000, v233\n\t"
      "v_lshlrev_b32_e32 v24, 16, v234\n\t"
      "v_and_b32_e32 v25, 0xffff0000, v234\n\t"
      "v_lshlrev_b32_e32 v26, 16, v235\n\t"
      "v_and_b32_e32 v27, 0xffff0000, v235\n\t"
      "v_lshlrev_b32_e32 v28, 16, v236\n\t"
      "v_and_b32_e32 v29, 0xffff0000, v236\n\t"
      "v_lshlrev_b32_e32 v30, 16, v237\n\t"
      "v_and_b32_e32 v31, 0xffff0000, v237\n\t"
      "s_add_u32 s62, s62, 0x7f4a7c15\n\t"
      "s_add_u32 s63, s63, 0x9e3779b9\n\t"
      "s_waitcnt vmcnt(15)\n\t"
      "v_add_u32_e32 v221, 0x21000, v214\n\t"
      "ds_read_b64_tr_b16 a[2:3], v221 offset:0\n\t"
      "ds_read_b64_tr_b16 a[6:7], v221 offset:8\n\t"
      "ds_read_b64_tr_b16 a[10:11], v221 offset:64\n\t"
      "ds_read_b64_tr_b16 a[14:15], v221 offset:72\n\t"
      "ds_read_b64_tr_b16 a[18:19], v221 offset:128\n\t"
      "ds_read_b64_tr_b16 a[22:23], v221 offset:136\n\t"
      "ds_read_b128 v[152:155], v196 offset:12288\n\t"
      "s_waitcnt lgkmcnt(8)\n\t"
      "s_add_u32 m0, s41, 0x1e000\n\t"
      "s_mov_b32 s51, 0x12c00\n\t"
      "buffer_load_dwordx4 v211, s[16:19], s51 offen lds\n\t"
      "buffer_load_dwordx4 v212, s[16:19], s51 offen offset:1024 lds\n\t"
      "buffer_load_dwordx4 v213, s[16:19], s51 offen offset:2048 lds\n\t"
      "s_lshl_b32 s67, s68, 18\n\t"
      "s_bfe_u32 s51, s62, 0xc0000\n\t"
      "s_and_b32 s67, s67, 0xfff00000\n\t"
      "s_or_b32 s67, s67, s51\n\t"
      "s_cmp_lg_u32 s41, s43\n\t"
      "s_cbranch_scc1 .Lw0pb_%=\n\t"
      "s_or_b32 s77, s77, s67\n\t"
      "v_mov_b32_e32 v241, s63\n\t"
      "v_mov_b32_e32 v240, s77\n\t"
      "buffer_store_dwordx2 v[240:241], v244, s[24:27], 0 offen sc1\n\t"
      ".Lw0pb_%=:\n\t"
      "s_waitcnt vmcnt(15)\n\t"
      "v_add_u32_e32 v221, 0x24000, v214\n\t"
      "ds_read_b64_tr_b16 a[24:25], v221 offset:0\n\t"
      "ds_read_b64_tr_b16 a[28:29], v221 offset:8\n\t"
      "ds_read_b64_tr_b16 a[32:33], v221 offset:64\n\t"
      "ds_read_b64_tr_b16 a[36:37], v221 offset:72\n\t"
      "ds_read_b64_tr_b16 a[40:41], v221 offset:128\n\t"
      "ds_read_b64_tr_b16 a[44:45], v221 offset:136\n\t"
      "ds_read_b128 v[144:147], v197 offset:0\n\t"
      "s_waitcnt lgkmcnt(8)\n\t"
      "v_mfma_f32_16x16x32_bf16 v[80:83], a[0:3], v[140:143], v[8:11]\n\t"
      "s_add_u32 m0, s41, 0x21000\n\t"
      "s_mov_b32 s51, 0x18000\n\t"
      "buffer_load_dwordx4 v211, s[16:19], s51 offen lds\n\t"
      "buffer_load_dwordx4 v212, s[16:19], s51 offen offset:1024 lds\n\t"
      "v_mfma_f32_16x16x32_bf16 v[84:87], a[4:7], v[140:143], v[12:15]\n\t"
      "buffer_load_dwordx4 v213, s[16:19], s51 offen offset:2048 lds\n\t"
      "v_mfma_f32_16x16x32_bf16 v[88:91], a[8:11], v[140:143], v[16:19]\n\t"
      "s_sub_u32 s67, s67, 0x20000000\n\t"
      "s_andn2_b32 s68, s68, 3\n\t"
      "s_sub_u32 s68, s68, s52\n\t"
      "s_waitcnt vmcnt(15)\n\t"
      "v_add_u32_e32 v221, 0x12000, v214\n\t"
      "ds_read_b64_tr_b16 a[26:27], v221 offset:0\n\t"
      "v_mfma_f32_16x16x32_bf16 v[92:95], a[12:15], v[140:143], v[20:23]\n\t"
      "ds_read_b64_tr_b16 a[30:31], v221 offset:8\n\t"
      "ds_read_b64_tr_b16 a[34:35], v221 offset:64\n\t"
      "v_mfma_f32_16x16x32_bf16 v[96:99], a[16:19], v[140:143], v[24:27]\n\t"
      "ds_read_b64_tr_b16 a[38:39], v221 offset:72\n\t"
      "ds_read_b64_tr_b16 a[42:43], v221 offset:128\n\t"
      "v_mfma_f32_16x16x32_bf16 v[100:103], a[20:23], v[140:143], v[28:31]\n\t"
      "ds_read_b64_tr_b16 a[46:47], v221 offset:136\n\t"
      "ds_read_b128 v[156:159], v197 offset:12288\n\t"
      "s_waitcnt lgkmcnt(8)\n\t"
      "v_mfma_f32_16x16x32_bf16 v[104:107], a[0:3], v[152:155], v[8:11]\n\t"
      "s_add_u32 m0, s41, 0x24000\n\t"
      "s_mov_b32 s51, 0x18c00\n\t"
      "buffer_load_dwordx4 v211, s[16:19], s51 offen lds\n\t"
      "v_mfma_f32_16x16x32_bf16 v[108:111], a[4:7], v[152:155], v[12:15]\n\t"
      "buffer_load_dwordx4 v212, s[16:19], s51 offen offset:1024 lds\n\t"
      "v_mfma_f32_16x16x32_bf16 v[112:115], a[8:11], v[152:155], v[16:19]\n\t"
      "buffer_load_dwordx4 v213, s[16:19], s51 offen offset:2048 lds\n\t"
      "s_waitcnt vmcnt(15)\n\t"
      "v_add_u32_e32 v221, 0x15000, v214\n\t"
      "ds_read_b64_tr_b16 a[48:49], v221 offset:0\n\t"
      "v_mfma_f32_16x16x32_bf16 v[116:119], a[12:15], v[152:155], v[20:23]\n\t"
      "ds_read_b64_tr_b16 a[52:53], v221 offset:8\n\t"
      "ds_read_b64_tr_b16 a[56:57], v221 offset:64\n\t"
      "v_mfma_f32_16x16x32_bf16 v[120:123], a[16:19], v[152:155], v[24:27]\n\t"
      "ds_read_b64_tr_b16 a[60:61], v221 offset:72\n\t"
      "ds_read_b64_tr_b16 a[64:65], v221 offset:128\n\t"
      "v_mfma_f32_16x16x32_bf16 v[132:135], a[20:23], v[152:155], v[28:31]\n\t"
      "ds_read_b64_tr_b16 a[68:69], v221 offset:136\n\t"
      "ds_read_b128 v[148:151], v198 offset:0\n\t"
      "s_waitcnt lgkmcnt(8)\n\t"
      "v_mfma_f32_16x16x32_bf16 v[80:83], a[24:27], v[144:147], v[80:83]\n\t"
      "s_add_u32 m0, s41, 0x12000\n\t"
      "s_mov_b32 s51, 0x1e000\n\t"
      "buffer_load_dwordx4 v211, s[16:19], s51 offen lds\n\t"
      "v_mfma_f32_16x16x32_bf16 v[84:87], a[28:31], v[144:147], v[84:87]\n\t"
      "buffer_load_dwordx4 v212, s[16:19], s51 offen offset:1024 lds\n\t"
      "v_mfma_f32_16x16x32_bf16 v[88:91], a[32:35], v[144:147], v[88:91]\n\t"
      "buffer_load_dwordx4 v213, s[16:19], s51 offen offset:2048 lds\n\t"
      "s_waitcnt vmcnt(15)\n\t"
      "v_add_u32_e32 v221, 0x18000, v214\n\t"
      "ds_read_b64_tr_b16 a[50:51], v221 offset:0\n\t"
      "v_mfma_f32_16x16x32_bf16 v[92:95], a[36:39], v[144:147], v[92:95]\n\t"
      "ds_read_b64_tr_b16 a[54:55], v221 offset:8\n\t"
      "ds_read_b64_tr_b16 a[58:59], v221 offset:64\n\t"
      "v_mfma_f32_16x16x32_bf16 v[96:99], a[40:43], v[144:147], v[96:99]\n\t"
      "ds_read_b64_tr_b16 a[62:63], v221 offset:72\n\t"
      "ds_read_b64_tr_b16 a[66:67], v221 offset:128\n\t"
      "v_mfma_f32_16x16x32_bf16 v[100:103], a[44:47], v[144:147], v[100:103]\n\t"
      "ds_read_b64_tr_b16 a[70:71], v221 offset:136\n\t"
      "ds_read_b128 v[160:163], v198 offset:12288\n\t"
      "s_waitcnt lgkmcnt(8)\n\t"
      "v_mfma_f32_16x16x32_bf16 v[104:107], a[24:27], v[156:159], v[104:107]\n\t"
      "s_add_u32 m0, s41, 0x15000\n\t"
      "s_mov_b32 s51, 0x1ec00\n\t"
      "buffer_load_dwordx4 v211, s[16:19], s51 offen lds\n\t"
      "v_mfma_f32_16x16x32_bf16 v[108:111], a[28:31], v[156:159], v[108:111]\n\t"
      "buffer_load_dwordx4 v212, s[16:19], s51 offen offset:1024 lds\n\t"
      "v_mfma_f32_16x16x32_bf16 v[112:115], a[32:35], v[156:159], v[112:115]\n\t"
      "buffer_load_dwordx4 v213, s[16:19], s51 offen offset:2048 lds\n\t"
      "s_waitcnt vmcnt(15)\n\t"
      "v_add_u32_e32 v221, 0x1b000, v214\n\t"
      "ds_read_b64_tr_b16 a[72:73], v221 offset:0\n\t"
      "v_mfma_f32_16x16x32_bf16 v[116:119], a[36:39], v[156:159], v[116:119]\n\t"
      "ds_read_b64_tr_b16 a[76:77], v221 offset:8\n\t"
      "ds_read_b64_tr_b16 a[80:81], v221 offset:64\n\t"
      "v_mfma_f32_16x16x32_bf16 v[120:123], a[40:43], v[156:159], v[120:123]\n\t"
      "ds_read_b64_tr_b16 a[84:85], v221 offset:72\n\t"
      "ds_read_b64_tr_b16 a[88:89], v221 offset:128\n\t"
      "v_mfma_f32_16x16x32_bf16 v[132:135], a[44:47], v[156:159], v[132:135]\n\t"
      "ds_read_b64_tr_b16 a[92:93], v221 offset:136\n\t"
      "ds_read_b128 v[140:143], v199 offset:0\n\t"
      "s_waitcnt lgkmcnt(8)\n\t"
      "v_mfma_f32_16x16x32_bf16 v[80:83], a[48:51], v[148:151], v[80:83]\n\t"
      "s_add_u32 m0, s41, 0x18000\n\t"
      "s_mov_b32 s51, 0x24000\n\t"
      "buffer_load_dwordx4 v211, s[16:19], s51 offen lds\n\t"
      "v_mfma_f32_16x16x32_bf16 v[84:87], a[52:55], v[148:151], v[84:87]\n\t"
      "buffer_load_dwordx4 v212, s[16:19], s51 offen offset:1024 lds\n\t"
      "v_mfma_f32_16x16x32_bf16 v[88:91], a[56:59], v[148:151], v[88:91]\n\t"
      "buffer_load_dwordx4 v213, s[16:19], s51 offen offset:2048 lds\n\t"
      "s_waitcnt vmcnt(15)\n\t"
      "v_add_u32_e32 v221, 0x1e000, v214\n\t"
      "ds_read_b64_tr_b16 a[74:75], v221 offset:0\n\t"
      "v_mfma_f32_16x16x32_bf16 v[92:95], a[60:63], v[148:151], v[92:95]\n\t"
      "ds_read_b64_tr_b16 a[78:79], v221 offset:8\n\t"
      "ds_read_b64_tr_b16 a[82:83], v221 offset:64\n\t"
      "v_mfma_f32_16x16x32_bf16 v[96:99], a[64:67], v[148:151], v[96:99]\n\t"
      "ds_read_b64_tr_b16 a[86:87], v221 offset:72\n\t"
      "ds_read_b64_tr_b16 a[90:91], v221 offset:128\n\t"
      "v_mfma_f32_16x16x32_bf16 v[100:103], a[68:71], v[148:151], v[100:103]\n\t"
      "ds_read_b64_tr_b16 a[94:95], v221 offset:136\n\t"
      "ds_read_b128 v[152:155], v199 offset:12288\n\t"
      "s_waitcnt lgkmcnt(8)\n\t"
      "v_mfma_f32_16x16x32_bf16 v[104:107], a[48:51], v[160:163], v[104:107]\n\t"
      "s_add_u32 m0, s41, 0x1b000\n\t"
      "s_mov_b32 s51, 0x24c00\n\t"
      "buffer_load_dwordx4 v211, s[16:19], s51 offen lds\n\t"
      "v_mfma_f32_16x16x32_bf16 v[108:111], a[52:55], v[160:163], v[108:111]\n\t"
      "buffer_load_dwordx4 v212, s[16:19], s51 offen offset:1024 lds\n\t"
      "v_mfma_f32_16x16x32_bf16 v[112:115], a[56:59], v[160:163], v[112:115]\n\t"
      "buffer_load_dwordx4 v213, s[16:19], s51 offen offset:2048 lds\n\t"
      "s_waitcnt vmcnt(15)\n\t"
      "v_add_u32_e32 v221, 0x21000, v214\n\t"
      "ds_read_b64_tr_b16 a[96:97], v221 offset:0\n\t"
      "v_mfma_f32_16x16x32_bf16 v[116:119], a[60:63], v[160:163], v[116:119]\n\t"
      "ds_read_b64_tr_b16 a[100:101], v221 offset:8\n\t"
      "ds_read_b64_tr_b16 a[104:105], v221 offset:64\n\t"
      "v_mfma_f32_16x16x32_bf16 v[120:123], a[64:67], v[160:163], v[120:123]\n\t"
      "ds_read_b64_tr_b16 a[108:109], v221 offset:72\n\t"
      "ds_read_b64_tr_b16 a[112:113], v221 offset:128\n\t"
      "v_mfma_f32_16x16x32_bf16 v[132:135], a[68:71], v[160:163], v[132:135]\n\t"
      "ds_read_b64_tr_b16 a[116:117], v221 offset:136\n\t"
      "ds_read_b128 v[144:147], v197 offset:256\n\t"
      "s_waitcnt lgkmcnt(8)\n\t"
      "v_mfma_f32_16x16x32_bf16 v[80:83], a[72:75], v[140:143], v[80:83]\n\t"
      "s_add_u32 m0, s41, 0x1e000\n\t"
      "s_mov_b32 s51, 0x2a000\n\t"
      "buffer_load_dwordx4 v211, s[16:19], s51 offen lds\n\t"
      "v_mfma_f32_16x16x32_bf16 v[84:87], a[76:79], v[140:143], v[84:87]\n\t"
      "buffer_load_dwordx4 v212, s[16:19], s51 offen offset:1024 lds\n\t"
      "v_mfma_f32_16x16x32_bf16 v[88:91], a[80:83], v[140:143], v[88:91]\n\t"
      "buffer_load_dwordx4 v213, s[16:19], s51 offen offset:2048 lds\n\t"
      "s_waitcnt vmcnt(15)\n\t"
      "v_add_u32_e32 v221, 0x24000, v214\n\t"
      "ds_read_b64_tr_b16 a[98:99], v221 offset:0\n\t"
      "v_mfma_f32_16x16x32_bf16 v[92:95], a[84:87], v[140:143], v[92:95]\n\t"
      "ds_read_b64_tr_b16 a[102:103], v221 offset:8\n\t"
      "ds_read_b64_tr_b16 a[106:107], v221 offset:64\n\t"
      "v_mfma_f32_16x16x32_bf16 v[96:99], a[88:91], v[140:143], v[96:99]\n\t"
      "ds_read_b64_tr_b16 a[110:111], v221 offset:72\n\t"
      "ds_read_b64_tr_b16 a[114:115], v221 offset:128\n\t"
      "v_mfma_f32_16x16x32_bf16 v[100:103], a[92:95], v[140:143], v[100:103]\n\t"
      "ds_read_b64_tr_b16 a[118:119], v221 offset:136\n\t"
      "ds_read_b128 v[156:159], v197 offset:12544\n\t"
      "s_waitcnt lgkmcnt(8)\n\t"
      "v_mfma_f32_16x16x32_bf16 v[104:107], a[72:75], v[152:155], v[104:107]\n\t"
      "s_add_u32 m0, s41, 0x21000\n\t"
      "s_mov_b32 s51, 0x2ac00\n\t"
      "buffer_load_dwordx4 v211, s[16:19], s51 offen lds\n\t"
      "v_mfma_f32_16x16x32_bf16 v[108:111], a[76:79], v[152:155], v[108:111]\n\t"
      "buffer_load_dwordx4 v212, s[16:19], s51 offen offset:1024 lds\n\t"
      "v_mfma_f32_16x16x32_bf16 v[112:115], a[80:83], v[152:155], v[112:115]\n\t"
      "buffer_load_dwordx4 v213, s[16:19], s51 offen offset:2048 lds\n\t"
      "s_waitcnt vmcnt(15)\n\t"
      "v_add_u32_e32 v221, 0x12000, v214\n\t"
      "ds_read_b64_tr_b16 a[120:121], v221 offset:0\n\t"
      "v_mfma_f32_16x16x32_bf16 v[116:119], a[84:87], v[152:155], v[116:119]\n\t"
      "ds_read_b64_tr_b16 a[124:125], v221 offset:8\n\t"
      "ds_read_b64_tr_b16 a[128:129], v221 offset:64\n\t"
      "v_mfma_f32_16x16x32_bf16 v[120:123], a[88:91], v[152:155], v[120:123]\n\t"
      "ds_read_b64_tr_b16 a[132:133], v221 offset:72\n\t"
      "ds_read_b64_tr_b16 a[136:137], v221 offset:128\n\t"
      "v_mfma_f32_16x16x32_bf16 v[132:135], a[92:95], v[152:155], v[132:135]\n\t"
      "ds_read_b64_tr_b16 a[140:141], v221 offset:136\n\t"
      "ds_read_b128 v[148:151], v198 offset:256\n\t"
      "s_waitcnt lgkmcnt(8)\n\t"
      "v_mfma_f32_16x16x32_bf16 v[80:83], a[96:99], v[144:147], v[80:83]\n\t"
      "s_add_u32 m0, s41, 0x24000\n\t"
      "s_mov_b32 s51, 0x30000\n\t"
      "buffer_load_dwordx4 v211, s[16:19], s51 offen lds\n\t"
      "v_mfma_f32_16x16x32_bf16 v[84:87], a[100:103], v[144:147], v[84:87]\n\t"
      "buffer_load_dwordx4 v212, s[16:19], s51 offen offset:1024 lds\n\t"
      "v_mfma_f32_16x16x32_bf16 v[88:91], a[104:107], v[144:147], v[88:91]\n\t"
      "buffer_load_dwordx4 v213, s[16:19], s51 offen offset:2048 lds\n\t"
      "s_waitcnt vmcnt(15)\n\t"
      "v_add_u32_e32 v221, 0x15000, v214\n\t"
      "ds_read_b64_tr_b16 a[122:123], v221 offset:0\n\t"
      "v_mfma_f32_16x16x32_bf16 v[92:95], a[108:111], v[144:147], v[92:95]\n\t"
      "ds_read_b64_tr_b16 a[126:127], v221 offset:8\n\t"
      "ds_read_b64_tr_b16 a[130:131], v221 offset:64\n\t"
      "v_mfma_f32_16x16x32_bf16 v[96:99], a[112:115], v[144:147], v[96:99]\n\t"
      "ds_read_b64_tr_b16 a[134:135], v221 offset:72\n\t"
      "ds_read_b64_tr_b16 a[138:139], v221 offset:128\n\t"
      "v_mfma_f32_16x16x32_bf16 v[100:103], a[116:119], v[144:147], v[100:103]\n\t"
      "ds_read_b64_tr_b16 a[142:143], v221 offset:136\n\t"
      "ds_read_b128 v[160:163], v198 offset:12544\n\t"
      "s_waitcnt lgkmcnt(8)\n\t"
      "v_mfma_f32_16x16x32_bf16 v[104:107], a[96:99], v[156:159], v[104:107]\n\t"
      "s_add_u32 m0, s41, 0x12000\n\t"
      "s_mov_b32 s51, 0x30c00\n\t"
      "buffer_load_dwordx4 v211, s[16:19], s51 offen lds\n\t"
      "v_mfma_f32_16x16x32_bf16 v[108:111], a[100:103], v[156:159], v[108:111]\n\t"
      "buffer_load_dwordx4 v212, s[16:19], s51 offen offset:1024 lds\n\t"
      "v_mfma_f32_16x16x32_bf16 v[112:115], a[104:107], v[156:159], v[112:115]\n\t"
      "buffer_load_dwordx4 v213, s[16:19], s51 offen offset:2048 lds\n\t"
      "s_waitcnt vmcnt(15)\n\t"
      "v_add_u32_e32 v221, 0x18000, v214\n\t"
      "ds_read_b64_tr_b16 a[144:145], v221 offset:0\n\t"
      "v_mfma_f32_16x16x32_bf16 v[116:119], a[108:111], v[156:159], v[116:119]\n\t"
      "ds_read_b64_tr_b16 a[148:149], v221 offset:8\n\t"
      "ds_read_b64_tr_b16 a[152:153], v221 offset:64\n\t"
      "v_mfma_f32_16x16x32_bf16 v[120:123], a[112:115], v[156:159], v[120:123]\n\t"
      "ds_read_b64_tr_b16 a[156:157], v221 offset:72\n\t"
      "ds_read_b64_tr_b16 a[160:161], v221 offset:128\n\t"
      "v_mfma_f32_16x16x32_bf16 v[132:135], a[116:119], v[156:159], v[132:135]\n\t"
      "ds_read_b64_tr_b16 a[164:165], v221 offset:136\n\t"
      "ds_read_b128 v[140:143], v199 offset:256\n\t"
      "s_waitcnt lgkmcnt(8)\n\t"
      "v_mfma_f32_16x16x32_bf16 v[80:83], a[120:123], v[148:151], v[80:83]\n\t"
      "s_add_u32 m0, s41, 0x15000\n\t"
      "s_mov_b32 s51, 0x36000\n\t"
      "buffer_load_dwordx4 v211, s[16:19], s51 offen lds\n\t"
      "v_mfma_f32_16x16x32_bf16 v[84:87], a[124:127], v[148:151], v[84:87]\n\t"
      "buffer_load_dwordx4 v212, s[16:19], s51 offen offset:1024 lds\n\t"
      "v_mfma_f32_16x16x32_bf16 v[88:91], a[128:131], v[148:151], v[88:91]\n\t"
      "buffer_load_dwordx4 v213, s[16:19], s51 offen offset:2048 lds\n\t"
      "s_waitcnt vmcnt(15)\n\t"
      "v_add_u32_e32 v221, 0x1b000, v214\n\t"
      "ds_read_b64_tr_b16 a[146:147], v221 offset:0\n\t"
      "v_mfma_f32_16x16x32_bf16 v[92:95], a[132:135], v[148:151], v[92:95]\n\t"
      "ds_read_b64_tr_b16 a[150:151], v221 offset:8\n\t"
      "ds_read_b64_tr_b16 a[154:155], v221 offset:64\n\t"
      "v_mfma_f32_16x16x32_bf16 v[96:99], a[136:139], v[148:151], v[96:99]\n\t"
      "ds_read_b64_tr_b16 a[158:159], v221 offset:72\n\t"
      "ds_read_b64_tr_b16 a[162:163], v221 offset:128\n\t"
      "v_mfma_f32_16x16x32_bf16 v[100:103], a[140:143], v[148:151], v[100:103]\n\t"
      "ds_read_b64_tr_b16 a[166:167], v221 offset:136\n\t"
      "ds_read_b128 v[152:155], v199 offset:12544\n\t"
      "s_waitcnt lgkmcnt(8)\n\t"
      "v_mfma_f32_16x16x32_bf16 v[104:107], a[120:123], v[160:163], v[104:107]\n\t"
      "s_add_u32 m0, s41, 0x18000\n\t"
      "s_mov_b32 s51, 0x36c00\n\t"
      "buffer_load_dwordx4 v211, s[16:19], s51 offen lds\n\t"
      "v_mfma_f32_16x16x32_bf16 v[108:111], a[124:127], v[160:163], v[108:111]\n\t"
      "buffer_load_dwordx4 v212, s[16:19], s51 offen offset:1024 lds\n\t"
      "v_mfma_f32_16x16x32_bf16 v[112:115], a[128:131], v[160:163], v[112:115]\n\t"
      "buffer_load_dwordx4 v213, s[16:19], s51 offen offset:2048 lds\n\t"
      "s_waitcnt vmcnt(15)\n\t"
      "v_add_u32_e32 v221, 0x1e000, v214\n\t"
      "ds_read_b64_tr_b16 a[168:169], v221 offset:0\n\t"
      "v_mfma_f32_16x16x32_bf16 v[116:119], a[132:135], v[160:163], v[116:119]\n\t"
      "ds_read_b64_tr_b16 a[172:173], v221 offset:8\n\t"
      "ds_read_b64_tr_b16 a[176:177], v221 offset:64\n\t"
      "v_mfma_f32_16x16x32_bf16 v[120:123], a[136:139], v[160:163], v[120:123]\n\t"
      "ds_read_b64_tr_b16 a[180:181], v221 offset:72\n\t"
      "ds_read_b64_tr_b16 a[184:185], v221 offset:128\n\t"
      "v_mfma_f32_16x16x32_bf16 v[132:135], a[140:143], v[160:163], v[132:135]\n\t"
      "ds_read_b64_tr_b16 a[188:189], v221 offset:136\n\t"
      "ds_read_b128 v[144:147], v196 offset:256\n\t"
      "s_waitcnt lgkmcnt(8)\n\t"
      "v_mfma_f32_16x16x32_bf16 v[80:83], a[144:147], v[140:143], v[80:83]\n\t"
      "s_add_u32 m0, s41, 0x1b000\n\t"
      "s_mov_b32 s51, 0x3c000\n\t"
      "buffer_load_dwordx4 v211, s[16:19], s51 offen lds\n\t"
      "v_mfma_f32_16x16x32_bf16 v[84:87], a[148:151], v[140:143], v[84:87]\n\t"
      "buffer_load_dwordx4 v212, s[16:19], s51 offen offset:1024 lds\n\t"
      "v_mfma_f32_16x16x32_bf16 v[88:91], a[152:155], v[140:143], v[88:91]\n\t"
      "buffer_load_dwordx4 v213, s[16:19], s51 offen offset:2048 lds\n\t"
      "s_waitcnt vmcnt(15)\n\t"
      "v_add_u32_e32 v221, 0x21000, v214\n\t"
      "ds_read_b64_tr_b16 a[170:171], v221 offset:0\n\t"
      "v_mfma_f32_16x16x32_bf16 v[92:95], a[156:159], v[140:143], v[92:95]\n\t"
      "ds_read_b64_tr_b16 a[174:175], v221 offset:8\n\t"
      "ds_read_b64_tr_b16 a[178:179], v221 offset:64\n\t"
      "v_mfma_f32_16x16x32_bf16 v[96:99], a[160:163], v[140:143], v[96:99]\n\t"
      "ds_read_b64_tr_b16 a[182:183], v221 offset:72\n\t"
      "ds_read_b64_tr_b16 a[186:187], v221 offset:128\n\t"
      "v_mfma_f32_16x16x32_bf16 v[100:103], a[164:167], v[140:143], v[100:103]\n\t"
      "ds_read_b64_tr_b16 a[190:191], v221 offset:136\n\t"
      "ds_read_b128 v[156:159], v196 offset:12544\n\t"
      "s_waitcnt lgkmcnt(8)\n\t"
      "v_mfma_f32_16x16x32_bf16 v[104:107], a[144:147], v[152:155], v[104:107]\n\t"
      "s_add_u32 m0, s41, 0x1e000\n\t"
      "s_mov_b32 s51, 0x3cc00\n\t"
      "buffer_load_dwordx4 v211, s[16:19], s51 offen lds\n\t"
      "v_mfma_f32_16x16x32_bf16 v[108:111], a[148:151], v[152:155], v[108:111]\n\t"
      "buffer_load_dwordx4 v212, s[16:19], s51 offen offset:1024 lds\n\t"
      "v_mfma_f32_16x16x32_bf16 v[112:115], a[152:155], v[152:155], v[112:115]\n\t"
      "buffer_load_dwordx4 v213, s[16:19], s51 offen offset:2048 lds\n\t"
      "s_waitcnt vmcnt(15)\n\t"
      "v_add_u32_e32 v221, 0x24000, v214\n\t"
      "ds_read_b64_tr_b16 a[192:193], v221 offset:0\n\t"
      "v_mfma_f32_16x16x32_bf16 v[116:119], a[156:159], v[152:155], v[116:119]\n\t"
      "ds_read_b64_tr_b16 a[196:197], v221 offset:8\n\t"
      "ds_read_b64_tr_b16 a[200:201], v221 offset:64\n\t"
      "v_mfma_f32_16x16x32_bf16 v[120:123], a[160:163], v[152:155], v[120:123]\n\t"
      "ds_read_b64_tr_b16 a[204:205], v221 offset:72\n\t"
      "ds_read_b64_tr_b16 a[208:209], v221 offset:128\n\t"
      "v_mfma_f32_16x16x32_bf16 v[132:135], a[164:167], v[152:155], v[132:135]\n\t"
      "ds_read_b64_tr_b16 a[212:213], v221 offset:136\n\t"
      "ds_read_b128 v[148:151], v198 offset:512\n\t"
      "s_waitcnt lgkmcnt(8)\n\t"
      "v_mfma_f32_16x16x32_bf16 v[80:83], a[168:171], v[144:147], v[80:83]\n\t"
      "s_add_u32 m0, s41, 0x21000\n\t"
      "s_mov_b32 s51, 0x42000\n\t"
      "buffer_load_dwordx4 v211, s[16:19], s51 offen lds\n\t"
      "v_mfma_f32_16x16x32_bf16 v[84:87], a[172:175], v[144:147], v[84:87]\n\t"
      "buffer_load_dwordx4 v212, s[16:19], s51 offen offset:1024 lds\n\t"
      "v_mfma_f32_16x16x32_bf16 v[88:91], a[176:179], v[144:147], v[88:91]\n\t"
      "buffer_load_dwordx4 v213, s[16:19], s51 offen offset:2048 lds\n\t"
      "s_waitcnt vmcnt(15)\n\t"
      "v_add_u32_e32 v221, 0x12000, v214\n\t"
      "ds_read_b64_tr_b16 a[194:195], v221 offset:0\n\t"
      "v_mfma_f32_16x16x32_bf16 v[92:95], a[180:183], v[144:147], v[92:95]\n\t"
      "ds_read_b64_tr_b16 a[198:199], v221 offset:8\n\t"
      "ds_read_b64_tr_b16 a[202:203], v221 offset:64\n\t"
      "v_mfma_f32_16x16x32_bf16 v[96:99], a[184:187], v[144:147], v[96:99]\n\t"
      "ds_read_b64_tr_b16 a[206:207], v221 offset:72\n\t"
      "ds_read_b64_tr_b16 a[210:211], v221 offset:128\n\t"
      "v_mfma_f32_16x16x32_bf16 v[100:103], a[188:191], v[144:147], v[100:103]\n\t"
      "ds_read_b64_tr_b16 a[214:215], v221 offset:136\n\t"
      "ds_read_b128 v[160:163], v198 offset:12800\n\t"
      "s_waitcnt lgkmcnt(8)\n\t"
      "v_mfma_f32_16x16x32_bf16 v[104:107], a[168:171], v[156:159], v[104:107]\n\t"
      "s_add_u32 m0, s41, 0x24000\n\t"
      "s_mov_b32 s51, 0x42c00\n\t"
      "buffer_load_dwordx4 v211, s[16:19], s51 offen lds\n\t"
      "v_mfma_f32_16x16x32_bf16 v[108:111], a[172:175], v[156:159], v[108:111]\n\t"
      "buffer_load_dwordx4 v212, s[16:19], s51 offen offset:1024 lds\n\t"
      "v_mfma_f32_16x16x32_bf16 v[112:115], a[176:179], v[156:159], v[112:115]\n\t"
      "buffer_load_dwordx4 v213, s[16:19], s51 offen offset:2048 lds\n\t"
      "s_waitcnt vmcnt(15)\n\t"
      "v_add_u32_e32 v221, 0x15000, v214\n\t"
      "ds_read_b64_tr_b16 a[216:217], v221 offset:0\n\t"
      "v_mfma_f32_16x16x32_bf16 v[116:119], a[180:183], v[156:159], v[116:119]\n\t"
      "ds_read_b64_tr_b16 a[220:221], v221 offset:8\n\t"
      "ds_read_b64_tr_b16 a[224:225], v221 offset:64\n\t"
      "v_mfma_f32_16x16x32_bf16 v[120:123], a[184:187], v[156:159], v[120:123]\n\t"
      "ds_read_b64_tr_b16 a[228:229], v221 offset:72\n\t"
      "ds_read_b64_tr_b16 a[232:233], v221 offset:128\n\t"
      "v_mfma_f32_16x16x32_bf16 v[132:135], a[188:191], v[156:159], v[132:135]\n\t"
      "ds_read_b64_tr_b16 a[236:237], v221 offset:136\n\t"
      "ds_read_b128 v[140:143], v199 offset:512\n\t"
      "s_waitcnt lgkmcnt(8)\n\t"
      "v_mfma_f32_16x16x32_bf16 v[80:83], a[192:195], v[148:151], v[80:83]\n\t"
      "s_cmp_eq_u32 s94, 0\n\t"
      "s_cbranch_scc0 .Lc1_%=\n\t"
      "s_add_u32 m0, s41, 0x6000\n\t"
      "s_add_u32 s51, s40, 0x600000\n\t"
      "buffer_load_dwordx4 v208, s[20:23], s51 offen lds\n\t"
      "buffer_load_dwordx4 v208, s[20:23], s51 offen offset:1024 lds\n\t"
      "buffer_load_dwordx4 v208, s[20:23], s51 offen offset:2048 lds\n\t"
      ".Lc1_%=:\n\t"
      "v_mfma_f32_16x16x32_bf16 v[84:87], a[196:199], v[148:151], v[84:87]\n\t"
      "v_mfma_f32_16x16x32_bf16 v[88:91], a[200:203], v[148:151], v[88:91]\n\t"
      "s_waitcnt vmcnt(15)\n\t"
      "s_cmp_eq_u32 s94, 1\n\t"
      "s_cbranch_scc0 .Lwpw19_1_%=\n\t"
      "s_waitcnt vmcnt(12)\n\t"
      ".Lwpw19_1_%=:\n\t"
      "v_add_u32_e32 v221, 0x18000, v214\n\t"
      "ds_read_b64_tr_b16 a[218:219], v221 offset:0\n\t"
      "v_mfma_f32_16x16x32_bf16 v[92:95], a[204:207], v[148:151], v[92:95]\n\t"
      "ds_read_b64_tr_b16 a[222:223], v221 offset:8\n\t"
      "ds_read_b64_tr_b16 a[226:227], v221 offset:64\n\t"
      "v_mfma_f32_16x16x32_bf16 v[96:99], a[208:211], v[148:151], v[96:99]\n\t"
      "ds_read_b64_tr_b16 a[230:231], v221 offset:72\n\t"
      "ds_read_b64_tr_b16 a[234:235], v221 offset:128\n\t"
      "v_mfma_f32_16x16x32_bf16 v[100:103], a[212:215], v[148:151], v[100:103]\n\t"
      "ds_read_b64_tr_b16 a[238:239], v221 offset:136\n\t"
      "ds_read_b128 v[152:155], v199 offset:12800\n\t"
      "s_waitcnt lgkmcnt(14)\n\t"
      "v_mfma_f32_16x16x32_bf16 v[104:107], a[192:195], v[160:163], v[104:107]\n\t"
      "s_cmp_eq_u32 s94, 0\n\t"
      "s_cbranch_scc0 .Lc2_%=\n\t"
      "s_add_u32 m0, s41, 0x9000\n\t"
      "s_add_u32 s51, s40, 0x600000\n\t"
      "buffer_load_dwordx4 v209, s[20:23], s51 offen lds\n\t"
      "buffer_load_dwordx4 v209, s[20:23], s51 offen offset:1024 lds\n\t"
      "buffer_load_dwordx4 v209, s[20:23], s51 offen offset:2048 lds\n\t"
      ".Lc2_%=:\n\t"
      "v_mfma_f32_16x16x32_bf16 v[108:111], a[196:199], v[160:163], v[108:111]\n\t"
      "v_mfma_f32_16x16x32_bf16 v[112:115], a[200:203], v[160:163], v[112:115]\n\t"
      "s_waitcnt vmcnt(15)\n\t"
      "s_cmp_eq_u32 s94, 1\n\t"
      "s_cbranch_scc0 .Lwpw20_1_%=\n\t"
      "s_waitcnt vmcnt(9)\n\t"
      ".Lwpw20_1_%=:\n\t"
      "v_add_u32_e32 v221, 0x1b000, v214\n\t"
      "ds_read_b64_tr_b16 a[240:241], v221 offset:0\n\t"
      "v_mfma_f32_16x16x32_bf16 v[116:119], a[204:207], v[160:163], v[116:119]\n\t"
      "ds_read_b64_tr_b16 a[244:245], v221 offset:8\n\t"
      "ds_read_b64_tr_b16 a[248:249], v221 offset:64\n\t"
      "v_mfma_f32_16x16x32_bf16 v[120:123], a[208:211], v[160:163], v[120:123]\n\t"
      "ds_read_b64_tr_b16 a[252:253], v221 offset:72\n\t"
      "ds_read_b64_tr_b16 v[164:165], v221 offset:128\n\t"
      "v_mfma_f32_16x16x32_bf16 v[132:135], a[212:215], v[160:163], v[132:135]\n\t"
      "ds_read_b64_tr_b16 v[168:169], v221 offset:136\n\t"
      "ds_read_b128 v[144:147], v196 offset:512\n\t"
      "s_waitcnt lgkmcnt(8)\n\t"
      "v_mfma_f32_16x16x32_bf16 v[80:83], a[216:219], v[140:143], v[80:83]\n\t"
      "s_cmp_eq_u32 s94, 1\n\t"
      "s_cbranch_scc0 .Lc3_%=\n\t"
      "s_add_u32 m0, s41, 0xc000\n\t"
      "s_add_u32 s51, s40, 0xc00000\n\t"
      "buffer_load_dwordx4 v208, s[20:23], s51 offen lds\n\t"
      "buffer_load_dwordx4 v208, s[20:23], s51 offen offset:1024 lds\n\t"
      "buffer_load_dwordx4 v208, s[20:23], s51 offen offset:2048 lds\n\t"
      ".Lc3_%=:\n\t"
      "v_mfma_f32_16x16x32_bf16 v[84:87], a[220:223], v[140:143], v[84:87]\n\t"
      "v_mfma_f32_16x16x32_bf16 v[88:91], a[224:227], v[140:143], v[88:91]\n\t"
      "s_waitcnt vmcnt(12)\n\t"
      "s_cmp_eq_u32 s94, 1\n\t"
      "s_cbranch_scc0 .Lwpw21_1_%=\n\t"
      "s_waitcnt vmcnt(9)\n\t"
      ".Lwpw21_1_%=:\n\t"
      "v_add_u32_e32 v221, 0x1e000, v214\n\t"
      "ds_read_b64_tr_b16 a[242:243], v221 offset:0\n\t"
      "v_mfma_f32_16x16x32_bf16 v[92:95], a[228:231], v[140:143], v[92:95]\n\t"
      "ds_read_b64_tr_b16 a[246:247], v221 offset:8\n\t"
      "ds_read_b64_tr_b16 a[250:251], v221 offset:64\n\t"
      "v_mfma_f32_16x16x32_bf16 v[96:99], a[232:235], v[140:143], v[96:99]\n\t"
      "ds_read_b64_tr_b16 a[254:255], v221 offset:72\n\t"
      "ds_read_b64_tr_b16 v[166:167], v221 offset:128\n\t"
      "v_mfma_f32_16x16x32_bf16 v[100:103], a[236:239], v[140:143], v[100:103]\n\t"
      "ds_read_b64_tr_b16 v[170:171], v221 offset:136\n\t"
      "ds_read_b128 v[156:159], v196 offset:12800\n\t"
      "s_waitcnt lgkmcnt(14)\n\t"
      "v_mfma_f32_16x16x32_bf16 v[104:107], a[216:219], v[152:155], v[104:107]\n\t"
      "s_cmp_eq_u32 s94, 1\n\t"
      "s_cbranch_scc0 .Lc4_%=\n\t"
      "s_add_u32 m0, s41, 0xf000\n\t"
      "s_add_u32 s51, s40, 0xc00000\n\t"
      "buffer_load_dwordx4 v209, s[20:23], s51 offen lds\n\t"
      "buffer_load_dwordx4 v209, s[20:23], s51 offen offset:1024 lds\n\t"
      "buffer_load_dwordx4 v209, s[20:23], s51 offen offset:2048 lds\n\t"
      ".Lc4_%=:\n\t"
      "v_mfma_f32_16x16x32_bf16 v[108:111], a[220:223], v[152:155], v[108:111]\n\t"
      "v_mfma_f32_16x16x32_bf16 v[112:115], a[224:227], v[152:155], v[112:115]\n\t"
      "s_waitcnt vmcnt(9)\n\t"
      "v_add_u32_e32 v221, 0x21000, v214\n\t"
      "ds_read_b64_tr_b16 v[172:173], v221 offset:0\n\t"
      "v_mfma_f32_16x16x32_bf16 v[116:119], a[228:231], v[152:155], v[116:119]\n\t"
      "ds_read_b64_tr_b16 v[176:177], v221 offset:8\n\t"
      "ds_read_b64_tr_b16 v[180:181], v221 offset:64\n\t"
      "ds_read_b64_tr_b16 v[184:185], v221 offset:72\n\t"
      "v_mfma_f32_16x16x32_bf16 v[120:123], a[232:235], v[152:155], v[120:123]\n\t"
      "ds_read_b64_tr_b16 v[188:189], v221 offset:128\n\t"
      "ds_read_b64_tr_b16 v[192:193], v221 offset:136\n\t"
      "v_mfma_f32_16x16x32_bf16 v[132:135], a[236:239], v[152:155], v[132:135]\n\t"
      "ds_read_b128 v[148:151], v197 offset:512\n\t"
      "ds_read_b128 v[160:163], v197 offset:12800\n\t"
      "s_waitcnt lgkmcnt(9)\n\t"
      "v_mfma_f32_16x16x32_bf16 v[80:83], a[240:243], v[144:147], v[80:83]\n\t"
      "v_mfma_f32_16x16x32_bf16 v[84:87], a[244:247], v[144:147], v[84:87]\n\t"
      "v_mfma_f32_16x16x32_bf16 v[88:91], a[248:251], v[144:147], v[88:91]\n\t"
      "s_waitcnt vmcnt(6)\n\t"
      "v_add_u32_e32 v221, 0x24000, v214\n\t"
      "ds_read_b64_tr_b16 v[174:175], v221 offset:0\n\t"
      "v_mfma_f32_16x16x32_bf16 v[92:95], a[252:255], v[144:147], v[92:95]\n\t"
      "ds_read_b64_tr_b16 v[178:179], v221 offset:8\n\t"
      "ds_read_b64_tr_b16 v[182:183], v221 offset:64\n\t"
      "v_mfma_f32_16x16x32_bf16 v[96:99], v[164:167], v[144:147], v[96:99]\n\t"
      "ds_read_b64_tr_b16 v[186:187], v221 offset:72\n\t"
      "ds_read_b64_tr_b16 v[190:191], v221 offset:128\n\t"
      "v_mfma_f32_16x16x32_bf16 v[100:103], v[168:171], v[144:147], v[100:103]\n\t"
      "ds_read_b64_tr_b16 v[194:195], v221 offset:136\n\t"
      ".Lcq_%=:\n\t"
      "s_waitcnt lgkmcnt(14)\n\t"
      "v_mfma_f32_16x16x32_bf16 v[104:107], a[240:243], v[156:159], v[104:107]\n\t"
      "v_add_u32_e32 v196, s46, v200\n\t"
      "v_add_u32_e32 v197, s46, v201\n\t"
      "v_mfma_f32_16x16x32_bf16 v[108:111], a[244:247], v[156:159], v[108:111]\n\t"
      "v_mfma_f32_16x16x32_bf16 v[112:115], a[248:251], v[156:159], v[112:115]\n\t"
      "v_mfma_f32_16x16x32_bf16 v[116:119], a[252:255], v[156:159], v[116:119]\n\t"
      "v_add_u32_e32 v198, s46, v202\n\t"
      "v_add_u32_e32 v199, s46, v203\n\t"
      "v_mfma_f32_16x16x32_bf16 v[120:123], v[164:167], v[156:159], v[120:123]\n\t"
      "v_mfma_f32_16x16x32_bf16 v[132:135], v[168:171], v[156:159], v[132:135]\n\t"
      "s_waitcnt lgkmcnt(0)\n\t"
      "v_mfma_f32_16x16x32_bf16 v[80:83], v[172:175], v[148:151], v[80:83]\n\t"
      "s_cmp_eq_u32 s94, 0\n\t"
      "s_cbranch_scc0 .Lc5_%=\n\t"
      "s_add_u32 m0, s41, 0xc000\n\t"
      "s_add_u32 s51, s40, 0xc00000\n\t"
      "buffer_load_dwordx4 v208, s[20:23], s51 offen lds\n\t"
      "buffer_load_dwordx4 v208, s[20:23], s51 offen offset:1024 lds\n\t"
      "buffer_load_dwordx4 v208, s[20:23], s51 offen offset:2048 lds\n\t"
      ".Lc5_%=:\n\t"
      "v_mfma_f32_16x16x32_bf16 v[84:87], v[176:179], v[148:151], v[84:87]\n\t"
      "v_mfma_f32_16x16x32_bf16 v[88:91], v[180:183], v[148:151], v[88:91]\n\t"
      "v_mfma_f32_16x16x32_bf16 v[92:95], v[184:187], v[148:151], v[92:95]\n\t"
      "v_mfma_f32_16x16x32_bf16 v[96:99], v[188:191], v[148:151], v[96:99]\n\t"
      "v_mfma_f32_16x16x32_bf16 v[100:103], v[192:195], v[148:151], v[100:103]\n\t"
      "s_cmp_eq_u32 s94, 0\n\t"
      "s_cbranch_scc0 .Lc6_%=\n\t"
      "s_add_u32 m0, s41, 0xf000\n\t"
      "s_add_u32 s51, s40, 0xc00000\n\t"
      "buffer_load_dwordx4 v209, s[20:23], s51 offen lds\n\t"
      "buffer_load_dwordx4 v209, s[20:23], s51 offen offset:1024 lds\n\t"
      "buffer_load_dwordx4 v209, s[20:23], s51 offen offset:2048 lds\n\t"
      ".Lc6_%=:\n\t"
      "s_mov_b64 s[96:97], s[34:35]\n\t"
      "s_movk_i32 s98, 0x148\n\t"
      "s_mov_b32 s99, s23\n\t"
      "buffer_load_dwordx3 v[252:254], v207, s[96:99], 0 offen offset:40\n\t"
      "buffer_load_dwordx2 v[240:241], v245, s[24:27], 0 offen sc1\n\t"
      "s_cmp_eq_u32 s94, 0\n\t"
      "s_cbranch_scc0 .Lwpx2_0_%=\n\t"
      "s_waitcnt vmcnt(11)\n\t"
      ".Lwpx2_0_%=:\n\t"
      "s_barrier\n\t"
      "ds_read_b128 v[32:35], v196 offset:0\n\t"
      "ds_read_b128 v[36:39], v197 offset:0\n\t"
      "v_mfma_f32_16x16x32_bf16 v[104:107], v[172:175], v[160:163], v[104:107]\n\t"
      "ds_read_b128 v[40:43], v198 offset:0\n\t"
      "ds_read_b128 v[44:47], v199 offset:0\n\t"
      "v_mfma_f32_16x16x32_bf16 v[108:111], v[176:179], v[160:163], v[108:111]\n\t"
      "ds_read_b128 v[48:51], v197 offset:256\n\t"
      "ds_read_b128 v[52:55], v198 offset:256\n\t"
      "v_mfma_f32_16x16x32_bf16 v[112:115], v[180:183], v[160:163], v[112:115]\n\t"
      "ds_read_b128 v[56:59], v199 offset:256\n\t"
      "ds_read_b128 v[60:63], v196 offset:256\n\t"
      "v_mfma_f32_16x16x32_bf16 v[116:119], v[184:187], v[160:163], v[116:119]\n\t"
      "ds_read_b128 v[64:67], v198 offset:512\n\t"
      "ds_read_b128 v[68:71], v199 offset:512\n\t"
      "v_mfma_f32_16x16x32_bf16 v[120:123], v[188:191], v[160:163], v[120:123]\n\t"
      "ds_read_b128 v[72:75], v196 offset:512\n\t"
      "ds_read_b128 v[76:79], v197 offset:512\n\t"
      "v_mfma_f32_16x16x32_bf16 v[132:135], v[192:195], v[160:163], v[132:135]\n\t"
      "s_waitcnt lgkmcnt(8)\n\t"
      "v_mfma_f32_16x16x32_bf16 v[128:131], a[0:3], v[32:35], v[8:11]\n\t"
      "v_cvt_pk_bf16_f32 v80, v80, v81\n\t"
      "v_cvt_pk_bf16_f32 v81, v82, v83\n\t"
      "v_mfma_f32_16x16x32_bf16 v[128:131], a[24:27], v[36:39], v[128:131]\n\t"
      "v_cvt_pk_bf16_f32 v136, v120, v121\n\t"
      "v_cvt_pk_bf16_f32 v137, v122, v123\n\t"
      "v_mfma_f32_16x16x32_bf16 v[128:131], a[48:51], v[40:43], v[128:131]\n\t"
      "v_cvt_pk_bf16_f32 v138, v132, v133\n\t"
      "v_mfma_f32_16x16x32_bf16 v[128:131], a[72:75], v[44:47], v[128:131]\n\t"
      "v_cvt_pk_bf16_f32 v139, v134, v135\n\t"
      "s_waitcnt lgkmcnt(4)\n\t"
      "v_mfma_f32_16x16x32_bf16 v[128:131], a[96:99], v[48:51], v[128:131]\n\t"
      "ds_write_b128 v219, v[136:139] offset:12288\n\t"
      "v_mfma_f32_16x16x32_bf16 v[128:131], a[120:123], v[52:55], v[128:131]\n\t"
      "v_cvt_pk_bf16_f32 v82, v84, v85\n\t"
      "v_cvt_pk_bf16_f32 v83, v86, v87\n\t"
      "v_mfma_f32_16x16x32_bf16 v[128:131], a[144:147], v[56:59], v[128:131]\n\t"
      "v_cvt_pk_bf16_f32 v88, v88, v89\n\t"
      "v_cvt_pk_bf16_f32 v89, v90, v91\n\t"
      "v_mfma_f32_16x16x32_bf16 v[128:131], a[168:171], v[60:63], v[128:131]\n\t"
      "ds_write_b128 v217, v[80:83] offset:0\n\t"
      "v_cvt_pk_bf16_f32 v90, v92, v93\n\t"
      "s_waitcnt lgkmcnt(2)\n\t"
      "v_mfma_f32_16x16x32_bf16 v[128:131], a[192:195], v[64:67], v[128:131]\n\t"
      "v_cvt_pk_bf16_f32 v91, v94, v95\n\t"
      "v_cvt_pk_bf16_f32 v96, v96, v97\n\t"
      "v_mfma_f32_16x16x32_bf16 v[128:131], a[216:219], v[68:71], v[128:131]\n\t"
      "ds_write_b128 v218, v[88:91] offset:0\n\t"
      "v_cvt_pk_bf16_f32 v97, v98, v99\n\t"
      "v_mfma_f32_16x16x32_bf16 v[128:131], a[240:243], v[72:75], v[128:131]\n\t"
      "v_cvt_pk_bf16_f32 v98, v100, v101\n\t"
      "v_cvt_pk_bf16_f32 v99, v102, v103\n\t"
      "v_mfma_f32_16x16x32_bf16 v[128:131], v[172:175], v[76:79], v[128:131]\n\t"
      "v_cvt_pk_bf16_f32 v104, v104, v105\n\t"
      "v_cvt_pk_bf16_f32 v105, v106, v107\n\t"
      "v_mfma_f32_16x16x32_bf16 v[132:135], a[4:7], v[32:35], v[12:15]\n\t"
      "ds_write_b128 v219, v[96:99] offset:0\n\t"
      "v_cvt_pk_bf16_f32 v106, v108, v109\n\t"
      "v_mfma_f32_16x16x32_bf16 v[132:135], a[28:31], v[36:39], v[132:135]\n\t"
      "v_cvt_pk_bf16_f32 v107, v110, v111\n\t"
      "v_cvt_pk_bf16_f32 v112, v112, v113\n\t"
      "v_mfma_f32_16x16x32_bf16 v[132:135], a[52:55], v[40:43], v[132:135]\n\t"
      "v_cvt_pk_bf16_f32 v136, v128, v129\n\t"
      "v_mfma_f32_16x16x32_bf16 v[132:135], a[76:79], v[44:47], v[132:135]\n\t"
      "v_cvt_pk_bf16_f32 v137, v130, v131\n\t"
      "v_mfma_f32_16x16x32_bf16 v[132:135], a[100:103], v[48:51], v[132:135]\n\t"
      "ds_write_b128 v217, v[104:107] offset:12288\n\t"
      "v_cvt_pk_bf16_f32 v113, v114, v115\n\t"
      "s_add_u32 s48, s47, 0x6000\n\t"
      "v_mfma_f32_16x16x32_bf16 v[132:135], a[124:127], v[52:55], v[132:135]\n\t"
      "v_cvt_pk_bf16_f32 v114, v116, v117\n\t"
      "v_cvt_pk_bf16_f32 v115, v118, v119\n\t"
      "s_cmp_eq_u32 s48, s50\n\t"
      "s_cselect_b32 s48, s41, s48\n\t"
      "v_mfma_f32_16x16x32_bf16 v[132:135], a[148:151], v[56:59], v[132:135]\n\t"
      "ds_write_b128 v218, v[112:115] offset:12288\n\t"
      "v_mfma_f32_16x16x32_bf16 v[132:135], a[172:175], v[60:63], v[132:135]\n\t"
      "v_add_u32_e32 v217, s46, v204\n\t"
      "v_mfma_f32_16x16x32_bf16 v[132:135], a[196:199], v[64:67], v[132:135]\n\t"
      "v_add_u32_e32 v218, s46, v205\n\t"
      "v_mfma_f32_16x16x32_bf16 v[132:135], a[220:223], v[68:71], v[132:135]\n\t"
      "v_readlane_b32 s73, v248, 3\n\t"
      "v_add_u32_e32 v219, s46, v206\n\t"
      "v_mfma_f32_16x16x32_bf16 v[132:135], a[244:247], v[72:75], v[132:135]\n\t"
      "v_mfma_f32_16x16x32_bf16 v[132:135], v[176:179], v[76:79], v[132:135]\n\t"
      "s_waitcnt lgkmcnt(0)\n\t"
      "s_barrier\n\t"
      "v_mfma_f32_16x16x32_bf16 v[128:131], a[8:11], v[32:35], v[16:19]\n\t"
      "ds_read_b128 v[140:143], v220 offset:0\n\t"
      "v_mfma_f32_16x16x32_bf16 v[128:131], a[32:35], v[36:39], v[128:131]\n\t"
      "ds_read_b128 v[144:147], v220 offset:1024\n\t"
      "s_memrealtime s[80:81]\n\t"
      "v_mfma_f32_16x16x32_bf16 v[128:131], a[56:59], v[40:43], v[128:131]\n\t"
      "v_cvt_pk_bf16_f32 v138, v132, v133\n\t"
      "s_mov_b32 m0, s45\n\t"
      "v_mfma_f32_16x16x32_bf16 v[128:131], a[80:83], v[44:47], v[128:131]\n\t"
      "v_cvt_pk_bf16_f32 v139, v134, v135\n\t"
      "v_mfma_f32_16x16x32_bf16 v[128:131], a[104:107], v[48:51], v[128:131]\n\t"
      "ds_write_b128 v217, v[136:139] offset:0\n\t"
      "v_mfma_f32_16x16x32_bf16 v[128:131], a[128:131], v[52:55], v[128:131]\n\t"
      "s_waitcnt lgkmcnt(2)\n\t"
      "buffer_store_dwordx4 v[140:143], v208, s[28:31], s40 offen sc1\n\t"
      "v_mfma_f32_16x16x32_bf16 v[128:131], a[152:155], v[56:59], v[128:131]\n\t"
      "ds_read_b128 v[148:151], v220 offset:2048\n\t"
      "v_mfma_f32_16x16x32_bf16 v[128:131], a[176:179], v[60:63], v[128:131]\n\t"
      "ds_read_b128 v[152:155], v220 offset:12288\n\t"
      "v_mfma_f32_16x16x32_bf16 v[128:131], a[200:203], v[64:67], v[128:131]\n\t"
      "ds_read_b128 v[156:159], v220 offset:13312\n\t"
      "v_mfma_f32_16x16x32_bf16 v[128:131], a[224:227], v[68:71], v[128:131]\n\t"
      "s_waitcnt lgkmcnt(4)\n\t"
      "buffer_store_dwordx4 v[144:147], v208, s[28:31], s40 offen offset:1024 sc1\n\t"
      "v_mfma_f32_16x16x32_bf16 v[128:131], a[248:251], v[72:75], v[128:131]\n\t"
      "ds_read_b128 v[160:163], v220 offset:14336\n\t"
      "v_mfma_f32_16x16x32_bf16 v[128:131], v[180:183], v[76:79], v[128:131]\n\t"
      "v_mfma_f32_16x16x32_bf16 v[132:135], a[12:15], v[32:35], v[20:23]\n\t"
      "v_mfma_f32_16x16x32_bf16 v[132:135], a[36:39], v[36:39], v[132:135]\n\t"
      "s_waitcnt lgkmcnt(3)\n\t"
      "buffer_store_dwordx4 v[148:151], v208, s[28:31], s40 offen offset:2048 sc1\n\t"
      "v_mfma_f32_16x16x32_bf16 v[132:135], a[60:63], v[40:43], v[132:135]\n\t"
      "v_cvt_pk_bf16_f32 v136, v128, v129\n\t"
      "v_mfma_f32_16x16x32_bf16 v[132:135], a[84:87], v[44:47], v[132:135]\n\t"
      "v_cvt_pk_bf16_f32 v137, v130, v131\n\t"
      "v_mfma_f32_16x16x32_bf16 v[132:135], a[108:111], v[48:51], v[132:135]\n\t"
      "v_mov_b32_e32 v6, 0\n\t"
      "v_mfma_f32_16x16x32_bf16 v[132:135], a[132:135], v[52:55], v[132:135]\n\t"
      "buffer_load_dwordx4 v208, s[20:23], s73 offen lds\n\t"
      "v_mfma_f32_16x16x32_bf16 v[132:135], a[156:159], v[56:59], v[132:135]\n\t"
      "ds_write_b32 v238, v6\n\t"
      "v_mfma_f32_16x16x32_bf16 v[132:135], a[180:183], v[60:63], v[132:135]\n\t"
      "v_mfma_f32_16x16x32_bf16 v[132:135], a[204:207], v[64:67], v[132:135]\n\t"
      "v_mfma_f32_16x16x32_bf16 v[132:135], a[228:231], v[68:71], v[132:135]\n\t"
      "buffer_load_dwordx4 v208, s[20:23], s73 offen offset:1024 lds\n\t"
      "v_mfma_f32_16x16x32_bf16 v[132:135], a[252:255], v[72:75], v[132:135]\n\t"
      "v_mfma_f32_16x16x32_bf16 v[132:135], v[184:187], v[76:79], v[132:135]\n\t"
      "s_waitcnt vmcnt(13)\n\t"
      "s_barrier\n\t"
      "v_mfma_f32_16x16x32_bf16 v[128:131], a[16:19], v[32:35], v[24:27]\n\t"
      "ds_read_b128 v[80:83], v196 offset:12288\n\t"
      "v_mfma_f32_16x16x32_bf16 v[128:131], a[40:43], v[36:39], v[128:131]\n\t"
      "buffer_load_dwordx4 v208, s[20:23], s73 offen offset:2048 lds\n\t"
      "v_mfma_f32_16x16x32_bf16 v[128:131], a[64:67], v[40:43], v[128:131]\n\t"
      "ds_read_b128 v[84:87], v197 offset:12288\n\t"
      "v_cvt_pk_bf16_f32 v138, v132, v133\n\t"
      "s_add_u32 m0, s45, 0x3000\n\t"
      "v_mfma_f32_16x16x32_bf16 v[128:131], a[88:91], v[44:47], v[128:131]\n\t"
      "ds_read_b128 v[88:91], v198 offset:12288\n\t"
      "v_cvt_pk_bf16_f32 v139, v134, v135\n\t"
      "v_mfma_f32_16x16x32_bf16 v[128:131], a[112:115], v[48:51], v[128:131]\n\t"
      "ds_write_b128 v218, v[136:139] offset:0\n\t"
      "v_mfma_f32_16x16x32_bf16 v[128:131], a[136:139], v[52:55], v[128:131]\n\t"
      "s_waitcnt lgkmcnt(7)\n\t"
      "buffer_store_dwordx4 v[152:155], v209, s[28:31], s40 offen sc1\n\t"
      "s_waitcnt vmcnt(7)\n\t"
      "v_subrev_u32_e32 v242, s67, v240\n\t"
      "v_xor_b32_e32 v241, s63, v241\n\t"
      "v_mfma_f32_16x16x32_bf16 v[128:131], a[160:163], v[56:59], v[128:131]\n\t"
      "ds_read_b128 v[92:95], v199 offset:12288\n\t"
      "v_lshl_or_b32 v240, v242, 20, v241\n\t"
      "v_mfma_f32_16x16x32_bf16 v[128:131], a[184:187], v[60:63], v[128:131]\n\t"
      "ds_read_b128 v[96:99], v197 offset:12544\n\t"
      "v_mfma_f32_16x16x32_bf16 v[128:131], a[208:211], v[64:67], v[128:131]\n\t"
      "v_lshrrev_b32_e32 v242, 12, v242\n\t"
      "v_cmp_ne_u32_e32 vcc, 0, v240\n\t"
      "v_mfma_f32_16x16x32_bf16 v[128:131], a[232:235], v[68:71], v[128:131]\n\t"
      "s_waitcnt lgkmcnt(8)\n\t"
      "buffer_store_dwordx4 v[156:159], v209, s[28:31], s40 offen offset:1024 sc1\n\t"
      "v_mfma_f32_16x16x32_bf16 v[128:131], v[164:167], v[72:75], v[128:131]\n\t"
      "ds_read_b128 v[100:103], v198 offset:12544\n\t"
      "s_cbranch_vccnz .Lgs_%=\n\t"
      ".Lgr_%=:\n\t"
      "ds_write_b32 v246, v242\n\t"
      "v_mfma_f32_16x16x32_bf16 v[128:131], v[188:191], v[76:79], v[128:131]\n\t"
      "ds_read_b128 v[104:107], v199 offset:12544\n\t"
      "v_mfma_f32_16x16x32_bf16 v[132:135], a[20:23], v[32:35], v[28:31]\n\t"
      "ds_read_b128 v[108:111], v196 offset:12544\n\t"
      "v_mfma_f32_16x16x32_bf16 v[132:135], a[44:47], v[36:39], v[132:135]\n\t"
      "s_waitcnt lgkmcnt(11)\n\t"
      "buffer_store_dwordx4 v[160:163], v209, s[28:31], s40 offen offset:2048 sc1\n\t"
      "v_mfma_f32_16x16x32_bf16 v[132:135], a[68:71], v[40:43], v[132:135]\n\t"
      "ds_read_b128 v[112:115], v198 offset:12800\n\t"
      "v_cvt_pk_bf16_f32 v136, v128, v129\n\t"
      "v_add_u32_e32 v220, s46, v207\n\t"
      "v_mfma_f32_16x16x32_bf16 v[132:135], a[92:95], v[44:47], v[132:135]\n\t"
      "ds_read_b128 v[116:119], v199 offset:12800\n\t"
      "v_cvt_pk_bf16_f32 v137, v130, v131\n\t"
      "v_mfma_f32_16x16x32_bf16 v[132:135], a[116:119], v[48:51], v[132:135]\n\t"
      "s_sub_u32 s87, s80, s52\n\t"
      "v_mfma_f32_16x16x32_bf16 v[132:135], a[140:143], v[52:55], v[132:135]\n\t"
      "buffer_load_dwordx4 v209, s[20:23], s73 offen lds\n\t"
      "v_mfma_f32_16x16x32_bf16 v[132:135], a[164:167], v[56:59], v[132:135]\n\t"
      "ds_read_b128 v[120:123], v196 offset:12800\n\t"
      "v_mfma_f32_16x16x32_bf16 v[132:135], a[188:191], v[60:63], v[132:135]\n\t"
      "ds_read_b128 v[124:127], v197 offset:12800\n\t"
      "v_mfma_f32_16x16x32_bf16 v[132:135], a[212:215], v[64:67], v[132:135]\n\t"
      "s_add_u32 s44, s44, 1\n\t"
      "v_mfma_f32_16x16x32_bf16 v[132:135], a[236:239], v[68:71], v[132:135]\n\t"
      "buffer_load_dwordx4 v209, s[20:23], s73 offen offset:1024 lds\n\t"
      "v_mfma_f32_16x16x32_bf16 v[132:135], v[168:171], v[72:75], v[132:135]\n\t"
      "s_add_u32 s85, s85, 1\n\t"
      "s_add_u32 s82, s87, s84\n\t"
      "v_mfma_f32_16x16x32_bf16 v[132:135], v[192:195], v[76:79], v[132:135]\n\t"
      "s_waitcnt lgkmcnt(9)\n\t"
      "v_mfma_f32_16x16x32_bf16 v[128:131], a[0:3], v[80:83], v[8:11]\n\t"
      "v_mfma_f32_16x16x32_bf16 v[128:131], a[24:27], v[84:87], v[128:131]\n\t"
      "v_readlane_b32 s72, v248, s44\n\t"
      "v_mfma_f32_16x16x32_bf16 v[128:131], a[48:51], v[88:91], v[128:131]\n\t"
      "v_cvt_pk_bf16_f32 v138, v132, v133\n\t"
      "v_mfma_f32_16x16x32_bf16 v[128:131], a[72:75], v[92:95], v[128:131]\n\t"
      "v_cvt_pk_bf16_f32 v139, v134, v135\n\t"
      "s_waitcnt lgkmcnt(4)\n\t"
      "v_mfma_f32_16x16x32_bf16 v[128:131], a[96:99], v[96:99], v[128:131]\n\t"
      "ds_write_b128 v219, v[136:139] offset:0\n\t"
      "v_mfma_f32_16x16x32_bf16 v[128:131], a[120:123], v[100:103], v[128:131]\n\t"
      "v_mfma_f32_16x16x32_bf16 v[128:131], a[144:147], v[104:107], v[128:131]\n\t"
      "buffer_load_dwordx4 v209, s[20:23], s73 offen offset:2048 lds\n\t"
      "v_mfma_f32_16x16x32_bf16 v[128:131], a[168:171], v[108:111], v[128:131]\n\t"
      "v_add_u32_e32 v196, s47, v200\n\t"
      "s_waitcnt lgkmcnt(1)\n\t"
      "v_mfma_f32_16x16x32_bf16 v[128:131], a[192:195], v[112:115], v[128:131]\n\t"
      "v_add_u32_e32 v197, s47, v201\n\t"
      "v_mfma_f32_16x16x32_bf16 v[128:131], a[216:219], v[116:119], v[128:131]\n\t"
      "v_add_u32_e32 v198, s47, v202\n\t"
      "v_mfma_f32_16x16x32_bf16 v[128:131], a[240:243], v[120:123], v[128:131]\n\t"
      "v_add_u32_e32 v199, s47, v203\n\t"
      "v_mfma_f32_16x16x32_bf16 v[128:131], v[172:175], v[124:127], v[128:131]\n\t"
      "s_waitcnt vmcnt(17) lgkmcnt(0)\n\t"
      "s_barrier\n\t"
      "v_mfma_f32_16x16x32_bf16 v[132:135], a[4:7], v[80:83], v[12:15]\n\t"
      "ds_read_b128 v[140:143], v220 offset:0\n\t"
      "v_mfma_f32_16x16x32_bf16 v[132:135], a[28:31], v[84:87], v[132:135]\n\t"
      "ds_read_b128 v[240:243], v247 offset:0\n\t"
      "v_mfma_f32_16x16x32_bf16 v[132:135], a[52:55], v[88:91], v[132:135]\n\t"
      "v_cvt_pk_bf16_f32 v136, v128, v129\n\t"
      "v_mfma_f32_16x16x32_bf16 v[132:135], a[76:79], v[92:95], v[132:135]\n\t"
      "ds_read_b128 v[32:35], v196 offset:0\n\t"
      "v_cvt_pk_bf16_f32 v137, v130, v131\n\t"
      "v_mfma_f32_16x16x32_bf16 v[132:135], a[100:103], v[96:99], v[132:135]\n\t"
      "v_mfma_f32_16x16x32_bf16 v[132:135], a[124:127], v[100:103], v[132:135]\n\t"
      "v_mfma_f32_16x16x32_bf16 v[132:135], a[148:151], v[104:107], v[132:135]\n\t"
      "ds_read_b128 v[36:39], v197 offset:0\n\t"
      "v_mfma_f32_16x16x32_bf16 v[132:135], a[172:175], v[108:111], v[132:135]\n\t"
      "v_mfma_f32_16x16x32_bf16 v[132:135], a[196:199], v[112:115], v[132:135]\n\t"
      "s_waitcnt lgkmcnt(2)\n\t"
      "v_add3_u32 v240, v240, v241, v242\n\t"
      "v_mfma_f32_16x16x32_bf16 v[132:135], a[220:223], v[116:119], v[132:135]\n\t"
      "ds_read_b128 v[40:43], v198 offset:0\n\t"
      "v_mfma_f32_16x16x32_bf16 v[132:135], a[244:247], v[120:123], v[132:135]\n\t"
      "v_add_u32_e32 v240, v240, v243\n\t"
      "v_mfma_f32_16x16x32_bf16 v[132:135], v[176:179], v[124:127], v[132:135]\n\t"
      "buffer_store_dwordx4 v[140:143], v208, s[28:31], s72 offen sc1\n\t"
      "v_mfma_f32_16x16x32_bf16 v[128:131], a[8:11], v[80:83], v[16:19]\n\t"
      "ds_read_b128 v[44:47], v199 offset:0\n\t"
      "v_mfma_f32_16x16x32_bf16 v[128:131], a[32:35], v[84:87], v[128:131]\n\t"
      "v_add_u32_dpp v241, v240, v240 quad_perm:[1,0,3,2] row_mask:0xf bank_mask:0xf\n\t"
      "s_memrealtime s[80:81]\n\t"
      "v_mfma_f32_16x16x32_bf16 v[128:131], a[56:59], v[88:91], v[128:131]\n\t"
      "v_cvt_pk_bf16_f32 v138, v132, v133\n\t"
      "v_mfma_f32_16x16x32_bf16 v[128:131], a[80:83], v[92:95], v[128:131]\n\t"
      "v_cvt_pk_bf16_f32 v139, v134, v135\n\t"
      "v_mfma_f32_16x16x32_bf16 v[128:131], a[104:107], v[96:99], v[128:131]\n\t"
      "ds_write_b128 v217, v[136:139] offset:12288\n\t"
      "v_add_u32_dpp v242, v241, v241 quad_perm:[2,3,0,1] row_mask:0xf bank_mask:0xf\n\t"
      "v_mfma_f32_16x16x32_bf16 v[128:131], a[128:131], v[100:103], v[128:131]\n\t"
      "s_mov_b32 m0, s46\n\t"
      "v_and_b32_e32 v240, 0xff, v242\n\t"
      "v_mfma_f32_16x16x32_bf16 v[128:131], a[152:155], v[104:107], v[128:131]\n\t"
      "v_cmp_le_u32_e64 s[76:77], 19, v240\n\t"
      "v_cmp_ge_u32_e64 s[78:79], 13, v240\n\t"
      "v_mfma_f32_16x16x32_bf16 v[128:131], a[176:179], v[108:111], v[128:131]\n\t"
      "ds_read_b128 v[48:51], v197 offset:256\n\t"
      "v_readlane_b32 s70, v242, 0\n\t"
      "v_mfma_f32_16x16x32_bf16 v[128:131], a[200:203], v[112:115], v[128:131]\n\t"
      "v_cmp_lt_u32_e32 vcc, 32, v240\n\t"
      "s_mov_b32 s66, s76\n\t"
      "v_mfma_f32_16x16x32_bf16 v[128:131], a[224:227], v[116:119], v[128:131]\n\t"
      "ds_read_b128 v[52:55], v198 offset:256\n\t"
      "v_mov_b32_e32 v242, s62\n\t"
      "v_mfma_f32_16x16x32_bf16 v[128:131], a[248:251], v[120:123], v[128:131]\n\t"
      "s_and_b32 s65, s76, s77\n\t"
      "s_and_b32 s78, s78, s79\n\t"
      "v_mfma_f32_16x16x32_bf16 v[128:131], v[180:183], v[124:127], v[128:131]\n\t"
      "v_mov_b32_e32 v243, s63\n\t"
      "s_or_b32 s65, s65, s78\n\t"
      "v_mfma_f32_16x16x32_bf16 v[132:135], a[12:15], v[80:83], v[20:23]\n\t"
      "ds_read_b128 v[56:59], v199 offset:256\n\t"
      "s_cmp_lg_u64 vcc, 0\n\t"
      "s_cselect_b32 s65, 0, s65\n\t"
      "s_cselect_b32 s64, 0xfffc0000, 0\n\t"
      "s_cselect_b32 s49, 31, 17\n\t"
      "v_mfma_f32_16x16x32_bf16 v[132:135], a[36:39], v[84:87], v[132:135]\n\t"
      "v_and_b32_e32 v240, s66, v250\n\t"
      "v_and_b32_e32 v241, s65, v250\n\t"
      "v_mfma_f32_16x16x32_bf16 v[132:135], a[60:63], v[88:91], v[132:135]\n\t"
      "v_cvt_pk_bf16_f32 v136, v128, v129\n\t"
      "s_andn2_b32 s67, 0x40000000, s64\n\t"
      "v_mfma_f32_16x16x32_bf16 v[132:135], a[84:87], v[92:95], v[132:135]\n\t"
      "v_cvt_pk_bf16_f32 v137, v130, v131\n\t"
      "ds_read_b128 v[144:147], v220 offset:1024\n\t"
      "v_mfma_f32_16x16x32_bf16 v[132:135], a[108:111], v[96:99], v[132:135]\n\t"
      "v_cmp_ne_u32_e64 s[76:77], 0, v240\n\t"
      "v_cmp_ne_u32_e64 s[78:79], 0, v241\n\t"
      "v_mfma_f32_16x16x32_bf16 v[132:135], a[132:135], v[100:103], v[132:135]\n\t"
      "buffer_store_dwordx2 v[242:243], v210, s[24:27], 0 offen sc1\n\t"
      "v_mfma_f32_16x16x32_bf16 v[132:135], a[156:159], v[104:107], v[132:135]\n\t"
      "ds_read_b128 v[60:63], v196 offset:256\n\t"
      "s_lshr_b32 s70, s70, 10\n\t"
      "v_mfma_f32_16x16x32_bf16 v[132:135], a[180:183], v[108:111], v[132:135]\n\t"
      "s_xor_b32 s75, s75, s76\n\t"
      "s_xor_b32 s74, s74, s76\n\t"
      "v_mfma_f32_16x16x32_bf16 v[132:135], a[204:207], v[112:115], v[132:135]\n\t"
      "s_and_b32 s76, s75, s78\n\t"
      "s_or_b32 s76, s76, s64\n\t"
      "v_cndmask_b32_e64 v249, v248, v251, s[76:77]\n\t"
      "v_mfma_f32_16x16x32_bf16 v[132:135], a[228:231], v[116:119], v[132:135]\n\t"
      "ds_read_b128 v[64:67], v198 offset:512\n\t"
      "s_add_u32 s70, s70, s68\n\t"
      "v_mfma_f32_16x16x32_bf16 v[132:135], a[252:255], v[120:123], v[132:135]\n\t"
      "s_and_b32 s76, s74, s78\n\t"
      "s_or_b32 s76, s76, s64\n\t"
      "v_cndmask_b32_e64 v248, v248, v251, s[76:77]\n\t"
      "v_mfma_f32_16x16x32_bf16 v[132:135], v[184:187], v[124:127], v[132:135]\n\t"
      "s_waitcnt lgkmcnt(2)\n\t"
      "buffer_store_dwordx4 v[144:147], v208, s[28:31], s72 offen offset:1024 sc1\n\t"
      "v_mfma_f32_16x16x32_bf16 v[128:131], a[16:19], v[80:83], v[24:27]\n\t"
      "ds_read_b128 v[68:71], v199 offset:512\n\t"
      "v_mfma_f32_16x16x32_bf16 v[128:131], a[40:43], v[84:87], v[128:131]\n\t"
      "s_lshr_b32 s74, s78, 3\n\t"
      "s_or_b32 s76, s74, s78\n\t"
      "v_mfma_f32_16x16x32_bf16 v[128:131], a[64:67], v[88:91], v[128:131]\n\t"
      "v_cvt_pk_bf16_f32 v138, v132, v133\n\t"
      "v_readlane_b32 s73, v248, s85\n\t"
      "v_mfma_f32_16x16x32_bf16 v[128:131], a[88:91], v[92:95], v[128:131]\n\t"
      "v_cvt_pk_bf16_f32 v139, v134, v135\n\t"
      "s_sub_u32 s87, s80, s52\n\t"
      "v_mfma_f32_16x16x32_bf16 v[128:131], a[112:115], v[96:99], v[128:131]\n\t"
      "ds_write_b128 v218, v[136:139] offset:12288\n\t"
      "s_sub_u32 s83, s82, s87\n\t"
      "v_mfma_f32_16x16x32_bf16 v[128:131], a[136:139], v[100:103], v[128:131]\n\t"
      "v_cndmask_b32_e64 v252, v252, v253, s[76:77]\n\t"
      "s_and_b32 s78, s74, s78\n\t"
      "v_mfma_f32_16x16x32_bf16 v[128:131], a[160:163], v[104:107], v[128:131]\n\t"
      "v_cndmask_b32_e64 v252, v252, v254, s[78:79]\n\t"
      "v_mfma_f32_16x16x32_bf16 v[128:131], a[184:187], v[108:111], v[128:131]\n\t"
      "ds_read_b128 v[148:151], v220 offset:2048\n\t"
      "s_add_u32 s70, s70, -1574\n\t"
      "v_mfma_f32_16x16x32_bf16 v[128:131], a[208:211], v[112:115], v[128:131]\n\t"
      "buffer_load_dwordx4 v208, s[20:23], s73 offen lds\n\t"
      "v_mfma_f32_16x16x32_bf16 v[128:131], a[232:235], v[116:119], v[128:131]\n\t"
      "ds_read_b128 v[72:75], v196 offset:512\n\t"
      "v_readlane_b32 s71, v252, s44\n\t"
      "s_max_i32 s82, s82, s87\n\t"
      "v_mfma_f32_16x16x32_bf16 v[128:131], v[164:167], v[120:123], v[128:131]\n\t"
      "s_sub_u32 s61, s70, s82\n\t"
      "v_mfma_f32_16x16x32_bf16 v[128:131], v[188:191], v[124:127], v[128:131]\n\t"
      "v_subrev_u32_e32 v240, 0x9e3779b9, v243\n\t"
      "s_sub_u32 s84, s84, 19\n\t"
      "v_mfma_f32_16x16x32_bf16 v[132:135], a[20:23], v[80:83], v[28:31]\n\t"
      "ds_read_b128 v[76:79], v197 offset:512\n\t"
      "s_lshr_b32 s60, s71, 16\n\t"
      "v_mfma_f32_16x16x32_bf16 v[132:135], a[44:47], v[84:87], v[132:135]\n\t"
      "buffer_load_dwordx4 v208, s[20:23], s73 offen offset:1024 lds\n\t"
      "s_and_b32 s86, s71, 0xff\n\t"
      "v_mfma_f32_16x16x32_bf16 v[132:135], a[68:71], v[88:91], v[132:135]\n\t"
      "v_cvt_pk_bf16_f32 v136, v128, v129\n\t"
      "s_bfe_u32 s71, s71, 0x80008\n\t"
      "v_mfma_f32_16x16x32_bf16 v[132:135], a[92:95], v[92:95], v[132:135]\n\t"
      "v_cvt_pk_bf16_f32 v137, v130, v131\n\t"
      "s_max_i32 s84, s84, s86\n\t"
      "v_mfma_f32_16x16x32_bf16 v[132:135], a[116:119], v[96:99], v[132:135]\n\t"
      "s_mul_i32 s61, s61, s60\n\t"
      "s_ashr_i32 s61, s61, 8\n\t"
      "v_mfma_f32_16x16x32_bf16 v[132:135], a[140:143], v[100:103], v[132:135]\n\t"
      "buffer_store_dword v240, v244, s[24:27], s67 offen offset:4 sc1\n\t"
      "v_mfma_f32_16x16x32_bf16 v[132:135], a[164:167], v[104:107], v[132:135]\n\t"
      "s_add_u32 s61, s61, s71\n\t"
      "s_mov_b32 s45, s46\n\t"
      "v_mfma_f32_16x16x32_bf16 v[132:135], a[188:191], v[108:111], v[132:135]\n\t"
      "s_max_i32 s61, s61, s84\n\t"
      "s_mov_b32 s46, s47\n\t"
      "v_mfma_f32_16x16x32_bf16 v[132:135], a[212:215], v[112:115], v[132:135]\n\t"
      "s_add_u32 s70, s70, s71\n\t"
      "s_mov_b32 s47, s48\n\t"
      "v_mfma_f32_16x16x32_bf16 v[132:135], a[236:239], v[116:119], v[132:135]\n\t"
      "s_add_u32 s82, s82, s61\n\t"
      "ds_write_b64 v219, v[136:137] offset:12288\n\t"
      "v_mfma_f32_16x16x32_bf16 v[132:135], v[168:171], v[120:123], v[132:135]\n\t"
      "v_add_u32_e32 v6, 1, v6\n\t"
      "v_mfma_f32_16x16x32_bf16 v[132:135], v[192:195], v[124:127], v[132:135]\n\t"
      "s_waitcnt lgkmcnt(3)\n\t"
      "buffer_store_dwordx4 v[148:151], v208, s[28:31], s72 offen offset:2048 sc1\n\t"
      ".p2align 6\n\t"
      ".Lloop_%=:\n\t"
      "v_mfma_f32_16x16x32_bf16 v[128:131], a[0:3], v[32:35], v[8:11]\n\t"
      "s_waitcnt vmcnt(17)\n\t"
      "v_mfma_f32_16x16x32_bf16 v[128:131], a[24:27], v[36:39], v[128:131]\n\t"
      "v_readlane_b32 s72, v249, s44\n\t"
      "v_mfma_f32_16x16x32_bf16 v[128:131], a[48:51], v[40:43], v[128:131]\n\t"
      "v_cvt_pk_bf16_f32 v138, v132, v133\n\t"
      "v_cvt_pk_bf16_f32 v139, v134, v135\n\t"
      "v_mfma_f32_16x16x32_bf16 v[128:131], a[72:75], v[44:47], v[128:131]\n\t"
      "ds_write_b32 v219, v138 offset:12296\n\t"
      "v_mfma_f32_16x16x32_bf16 v[128:131], a[96:99], v[48:51], v[128:131]\n\t"
      "ds_write_b32 v219, v139 offset:12300\n\t"
      "v_mfma_f32_16x16x32_bf16 v[128:131], a[120:123], v[52:55], v[128:131]\n\t"
      "ds_write_b32 v238, v6\n\t"
      "v_mfma_f32_16x16x32_bf16 v[128:131], a[144:147], v[56:59], v[128:131]\n\t"
      "buffer_load_dwordx4 v208, s[20:23], s73 offen offset:2048 lds\n\t"
      "v_mfma_f32_16x16x32_bf16 v[128:131], a[168:171], v[60:63], v[128:131]\n\t"
      "s_waitcnt lgkmcnt(4)\n\t"
      "v_add_u32_e32 v217, s46, v204\n\t"
      "v_mfma_f32_16x16x32_bf16 v[128:131], a[192:195], v[64:67], v[128:131]\n\t"
      "v_add_u32_e32 v218, s46, v205\n\t"
      "v_mfma_f32_16x16x32_bf16 v[128:131], a[216:219], v[68:71], v[128:131]\n\t"
      "v_add_u32_e32 v219, s46, v206\n\t"
      "v_mfma_f32_16x16x32_bf16 v[128:131], a[240:243], v[72:75], v[128:131]\n\t"
      "v_mfma_f32_16x16x32_bf16 v[128:131], v[172:175], v[76:79], v[128:131]\n\t"
      "s_cmp_gt_i32 s83, 1\n\t"
      "s_cbranch_scc1 .LslpA_%=\n\t"
      ".LretA_%=:\n\t"
      "v_mfma_f32_16x16x32_bf16 v[132:135], a[4:7], v[32:35], v[12:15]\n\t"
      "ds_read_b32 v7, v239\n\t"
      "v_mfma_f32_16x16x32_bf16 v[132:135], a[28:31], v[36:39], v[132:135]\n\t"
      "v_mfma_f32_16x16x32_bf16 v[132:135], a[52:55], v[40:43], v[132:135]\n\t"
      "v_cvt_pk_bf16_f32 v136, v128, v129\n\t"
      "v_cvt_pk_bf16_f32 v137, v130, v131\n\t"
      "v_mfma_f32_16x16x32_bf16 v[132:135], a[76:79], v[44:47], v[132:135]\n\t"
      "v_mfma_f32_16x16x32_bf16 v[132:135], a[100:103], v[48:51], v[132:135]\n\t"
      "s_waitcnt lgkmcnt(0)\n\t"
      "v_cmp_gt_u32_e32 vcc, v6, v7\n\t"
      "v_mfma_f32_16x16x32_bf16 v[132:135], a[124:127], v[52:55], v[132:135]\n\t"
      "s_cbranch_vccnz .LsbA_%=\n\t"
      ".LsrA_%=:\n\t"
      "v_mfma_f32_16x16x32_bf16 v[132:135], a[148:151], v[56:59], v[132:135]\n\t"
      "ds_read_b128 v[140:143], v220 offset:12288\n\t"
      "v_mfma_f32_16x16x32_bf16 v[132:135], a[172:175], v[60:63], v[132:135]\n\t"
      "ds_write_b32 v217, v136 offset:0\n\t"
      "v_mfma_f32_16x16x32_bf16 v[132:135], a[196:199], v[64:67], v[132:135]\n\t"
      "ds_write_b32 v217, v137 offset:4\n\t"
      "v_mfma_f32_16x16x32_bf16 v[132:135], a[220:223], v[68:71], v[132:135]\n\t"
      "ds_read_b128 v[80:83], v196 offset:12288\n\t"
      "v_mfma_f32_16x16x32_bf16 v[132:135], a[244:247], v[72:75], v[132:135]\n\t"
      "v_readlane_b32 s73, v249, s85\n\t"
      "v_mfma_f32_16x16x32_bf16 v[132:135], v[176:179], v[76:79], v[132:135]\n\t"
      "s_waitcnt lgkmcnt(3)\n\t"
      "buffer_store_dwordx4 v[140:143], v209, s[28:31], s72 offen sc1\n\t"
      "v_mfma_f32_16x16x32_bf16 v[128:131], a[8:11], v[32:35], v[16:19]\n\t"
      "ds_read_b128 v[84:87], v197 offset:12288\n\t"
      "v_mfma_f32_16x16x32_bf16 v[128:131], a[32:35], v[36:39], v[128:131]\n\t"
      "s_memrealtime s[80:81]\n\t"
      "v_mfma_f32_16x16x32_bf16 v[128:131], a[56:59], v[40:43], v[128:131]\n\t"
      "v_cvt_pk_bf16_f32 v138, v132, v133\n\t"
      "v_cvt_pk_bf16_f32 v139, v134, v135\n\t"
      "v_mfma_f32_16x16x32_bf16 v[128:131], a[80:83], v[44:47], v[128:131]\n\t"
      "ds_write_b32 v217, v138 offset:8\n\t"
      "v_mfma_f32_16x16x32_bf16 v[128:131], a[104:107], v[48:51], v[128:131]\n\t"
      "ds_write_b32 v217, v139 offset:12\n\t"
      "v_mfma_f32_16x16x32_bf16 v[128:131], a[128:131], v[52:55], v[128:131]\n\t"
      "ds_read_b128 v[88:91], v198 offset:12288\n\t"
      "v_mfma_f32_16x16x32_bf16 v[128:131], a[152:155], v[56:59], v[128:131]\n\t"
      "s_add_u32 m0, s45, 0x3000\n\t"
      "v_mfma_f32_16x16x32_bf16 v[128:131], a[176:179], v[60:63], v[128:131]\n\t"
      "ds_read_b128 v[92:95], v199 offset:12288\n\t"
      "v_mfma_f32_16x16x32_bf16 v[128:131], a[200:203], v[64:67], v[128:131]\n\t"
      "s_add_u32 s48, s47, 0x6000\n\t"
      "v_mfma_f32_16x16x32_bf16 v[128:131], a[224:227], v[68:71], v[128:131]\n\t"
      "ds_read_b128 v[96:99], v197 offset:12544\n\t"
      "v_mfma_f32_16x16x32_bf16 v[128:131], a[248:251], v[72:75], v[128:131]\n\t"
      "s_cmp_eq_u32 s48, s50\n\t"
      "s_cselect_b32 s48, s41, s48\n\t"
      "v_mfma_f32_16x16x32_bf16 v[128:131], v[180:183], v[76:79], v[128:131]\n\t"
      "buffer_load_dwordx4 v209, s[20:23], s73 offen lds\n\t"
      "v_mfma_f32_16x16x32_bf16 v[132:135], a[12:15], v[32:35], v[20:23]\n\t"
      "ds_read_b128 v[100:103], v198 offset:12544\n\t"
      "v_mfma_f32_16x16x32_bf16 v[132:135], a[36:39], v[36:39], v[132:135]\n\t"
      "v_mfma_f32_16x16x32_bf16 v[132:135], a[60:63], v[40:43], v[132:135]\n\t"
      "v_cvt_pk_bf16_f32 v136, v128, v129\n\t"
      "v_cvt_pk_bf16_f32 v137, v130, v131\n\t"
      "v_mfma_f32_16x16x32_bf16 v[132:135], a[84:87], v[44:47], v[132:135]\n\t"
      "ds_write_b32 v218, v136 offset:0\n\t"
      "v_mfma_f32_16x16x32_bf16 v[132:135], a[108:111], v[48:51], v[132:135]\n\t"
      "ds_write_b32 v218, v137 offset:4\n\t"
      "v_mfma_f32_16x16x32_bf16 v[132:135], a[132:135], v[52:55], v[132:135]\n\t"
      "ds_read_b128 v[144:147], v220 offset:13312\n\t"
      "v_mfma_f32_16x16x32_bf16 v[132:135], a[156:159], v[56:59], v[132:135]\n\t"
      "ds_read_b128 v[104:107], v199 offset:12544\n\t"
      "v_mfma_f32_16x16x32_bf16 v[132:135], a[180:183], v[60:63], v[132:135]\n\t"
      "s_sub_u32 s84, s84, 19\n\t"
      "v_mfma_f32_16x16x32_bf16 v[132:135], a[204:207], v[64:67], v[132:135]\n\t"
      "s_max_i32 s84, s84, s86\n\t"
      "v_mfma_f32_16x16x32_bf16 v[132:135], a[228:231], v[68:71], v[132:135]\n\t"
      "ds_read_b128 v[108:111], v196 offset:12544\n\t"
      "v_mfma_f32_16x16x32_bf16 v[132:135], a[252:255], v[72:75], v[132:135]\n\t"
      "v_mfma_f32_16x16x32_bf16 v[132:135], v[184:187], v[76:79], v[132:135]\n\t"
      "s_waitcnt lgkmcnt(2)\n\t"
      "buffer_store_dwordx4 v[144:147], v209, s[28:31], s72 offen offset:1024 sc1\n\t"
      "v_mfma_f32_16x16x32_bf16 v[128:131], a[16:19], v[32:35], v[24:27]\n\t"
      "ds_read_b128 v[112:115], v198 offset:12800\n\t"
      "v_mfma_f32_16x16x32_bf16 v[128:131], a[40:43], v[36:39], v[128:131]\n\t"
      "v_mfma_f32_16x16x32_bf16 v[128:131], a[64:67], v[40:43], v[128:131]\n\t"
      "v_cvt_pk_bf16_f32 v138, v132, v133\n\t"
      "v_cvt_pk_bf16_f32 v139, v134, v135\n\t"
      "v_mfma_f32_16x16x32_bf16 v[128:131], a[88:91], v[44:47], v[128:131]\n\t"
      "ds_write_b32 v218, v138 offset:8\n\t"
      "v_mfma_f32_16x16x32_bf16 v[128:131], a[112:115], v[48:51], v[128:131]\n\t"
      "ds_write_b32 v218, v139 offset:12\n\t"
      "v_mfma_f32_16x16x32_bf16 v[128:131], a[136:139], v[52:55], v[128:131]\n\t"
      "s_sub_u32 s87, s80, s52\n\t"
      "v_mfma_f32_16x16x32_bf16 v[128:131], a[160:163], v[56:59], v[128:131]\n\t"
      "s_sub_u32 s83, s82, s87\n\t"
      "ds_read_b128 v[116:119], v199 offset:12800\n\t"
      "v_mfma_f32_16x16x32_bf16 v[128:131], a[184:187], v[60:63], v[128:131]\n\t"
      "s_max_i32 s82, s82, s87\n\t"
      "v_mfma_f32_16x16x32_bf16 v[128:131], a[208:211], v[64:67], v[128:131]\n\t"
      "s_sub_u32 s61, s70, s82\n\t"
      "v_mfma_f32_16x16x32_bf16 v[128:131], a[232:235], v[68:71], v[128:131]\n\t"
      "s_mul_i32 s61, s61, s60\n\t"
      "ds_read_b128 v[120:123], v196 offset:12800\n\t"
      "v_mfma_f32_16x16x32_bf16 v[128:131], v[164:167], v[72:75], v[128:131]\n\t"
      "s_ashr_i32 s61, s61, 8\n\t"
      "v_mfma_f32_16x16x32_bf16 v[128:131], v[188:191], v[76:79], v[128:131]\n\t"
      "buffer_load_dwordx4 v209, s[20:23], s73 offen offset:1024 lds\n\t"
      "v_mfma_f32_16x16x32_bf16 v[132:135], a[20:23], v[32:35], v[28:31]\n\t"
      "s_add_u32 s61, s61, s71\n\t"
      "ds_read_b128 v[124:127], v197 offset:12800\n\t"
      "v_mfma_f32_16x16x32_bf16 v[132:135], a[44:47], v[36:39], v[132:135]\n\t"
      "s_max_i32 s61, s61, s84\n\t"
      "ds_read_b128 v[148:151], v220 offset:14336\n\t"
      "v_mfma_f32_16x16x32_bf16 v[132:135], a[68:71], v[40:43], v[132:135]\n\t"
      "s_add_u32 s82, s82, s61\n\t"
      "v_cvt_pk_bf16_f32 v136, v128, v129\n\t"
      "v_cvt_pk_bf16_f32 v137, v130, v131\n\t"
      "v_mfma_f32_16x16x32_bf16 v[132:135], a[92:95], v[44:47], v[132:135]\n\t"
      "ds_write_b32 v219, v136 offset:0\n\t"
      "v_mfma_f32_16x16x32_bf16 v[132:135], a[116:119], v[48:51], v[132:135]\n\t"
      "ds_write_b32 v219, v137 offset:4\n\t"
      "v_mfma_f32_16x16x32_bf16 v[132:135], a[140:143], v[52:55], v[132:135]\n\t"
      "s_add_u32 s70, s70, s71\n\t"
      "v_add_u32_e32 v220, s46, v207\n\t"
      "v_mfma_f32_16x16x32_bf16 v[132:135], a[164:167], v[56:59], v[132:135]\n\t"
      "v_add_u32_e32 v6, 1, v6\n\t"
      "v_mfma_f32_16x16x32_bf16 v[132:135], a[188:191], v[60:63], v[132:135]\n\t"
      "v_mfma_f32_16x16x32_bf16 v[132:135], a[212:215], v[64:67], v[132:135]\n\t"
      "s_add_u32 s44, s44, 1\n\t"
      "v_mfma_f32_16x16x32_bf16 v[132:135], a[236:239], v[68:71], v[132:135]\n\t"
      "s_add_u32 s85, s85, 1\n\t"
      "v_mfma_f32_16x16x32_bf16 v[132:135], v[168:171], v[72:75], v[132:135]\n\t"
      "v_mfma_f32_16x16x32_bf16 v[132:135], v[192:195], v[76:79], v[132:135]\n\t"
      "s_waitcnt lgkmcnt(2)\n\t"
      "buffer_store_dwordx4 v[148:151], v209, s[28:31], s72 offen offset:2048 sc1\n\t"
      "v_mfma_f32_16x16x32_bf16 v[128:131], a[0:3], v[80:83], v[8:11]\n\t"
      "s_waitcnt vmcnt(17)\n\t"
      "v_mfma_f32_16x16x32_bf16 v[128:131], a[24:27], v[84:87], v[128:131]\n\t"
      "v_readlane_b32 s72, v248, s44\n\t"
      "v_mfma_f32_16x16x32_bf16 v[128:131], a[48:51], v[88:91], v[128:131]\n\t"
      "v_cvt_pk_bf16_f32 v138, v132, v133\n\t"
      "v_cvt_pk_bf16_f32 v139, v134, v135\n\t"
      "v_mfma_f32_16x16x32_bf16 v[128:131], a[72:75], v[92:95], v[128:131]\n\t"
      "ds_write_b32 v219, v138 offset:8\n\t"
      "v_mfma_f32_16x16x32_bf16 v[128:131], a[96:99], v[96:99], v[128:131]\n\t"
      "ds_write_b32 v219, v139 offset:12\n\t"
      "v_mfma_f32_16x16x32_bf16 v[128:131], a[120:123], v[100:103], v[128:131]\n\t"
      "ds_write_b32 v238, v6\n\t"
      "v_mfma_f32_16x16x32_bf16 v[128:131], a[144:147], v[104:107], v[128:131]\n\t"
      "buffer_load_dwordx4 v209, s[20:23], s73 offen offset:2048 lds\n\t"
      "v_mfma_f32_16x16x32_bf16 v[128:131], a[168:171], v[108:111], v[128:131]\n\t"
      "v_add_u32_e32 v196, s47, v200\n\t"
      "v_mfma_f32_16x16x32_bf16 v[128:131], a[192:195], v[112:115], v[128:131]\n\t"
      "v_add_u32_e32 v197, s47, v201\n\t"
      "v_mfma_f32_16x16x32_bf16 v[128:131], a[216:219], v[116:119], v[128:131]\n\t"
      "v_add_u32_e32 v198, s47, v202\n\t"
      "v_mfma_f32_16x16x32_bf16 v[128:131], a[240:243], v[120:123], v[128:131]\n\t"
      "v_add_u32_e32 v199, s47, v203\n\t"
      "v_mfma_f32_16x16x32_bf16 v[128:131], v[172:175], v[124:127], v[128:131]\n\t"
      "s_cmp_gt_i32 s83, 1\n\t"
      "s_cbranch_scc1 .LslpB_%=\n\t"
      ".LretB_%=:\n\t"
      "v_mfma_f32_16x16x32_bf16 v[132:135], a[4:7], v[80:83], v[12:15]\n\t"
      "ds_read_b32 v7, v239\n\t"
      "v_mfma_f32_16x16x32_bf16 v[132:135], a[28:31], v[84:87], v[132:135]\n\t"
      "v_mfma_f32_16x16x32_bf16 v[132:135], a[52:55], v[88:91], v[132:135]\n\t"
      "v_cvt_pk_bf16_f32 v136, v128, v129\n\t"
      "v_cvt_pk_bf16_f32 v137, v130, v131\n\t"
      "v_mfma_f32_16x16x32_bf16 v[132:135], a[76:79], v[92:95], v[132:135]\n\t"
      "v_mfma_f32_16x16x32_bf16 v[132:135], a[100:103], v[96:99], v[132:135]\n\t"
      "s_waitcnt lgkmcnt(0)\n\t"
      "v_cmp_gt_u32_e32 vcc, v6, v7\n\t"
      "v_mfma_f32_16x16x32_bf16 v[132:135], a[124:127], v[100:103], v[132:135]\n\t"
      "s_cbranch_vccnz .LsbB_%=\n\t"
      ".LsrB_%=:\n\t"
      "v_mfma_f32_16x16x32_bf16 v[132:135], a[148:151], v[104:107], v[132:135]\n\t"
      "ds_read_b128 v[140:143], v220 offset:0\n\t"
      "v_mfma_f32_16x16x32_bf16 v[132:135], a[172:175], v[108:111], v[132:135]\n\t"
      "ds_write_b32 v217, v136 offset:12288\n\t"
      "v_mfma_f32_16x16x32_bf16 v[132:135], a[196:199], v[112:115], v[132:135]\n\t"
      "ds_write_b32 v217, v137 offset:12292\n\t"
      "v_mfma_f32_16x16x32_bf16 v[132:135], a[220:223], v[116:119], v[132:135]\n\t"
      "ds_read_b128 v[32:35], v196 offset:0\n\t"
      "v_mfma_f32_16x16x32_bf16 v[132:135], a[244:247], v[120:123], v[132:135]\n\t"
      "v_readlane_b32 s73, v248, s85\n\t"
      "v_mfma_f32_16x16x32_bf16 v[132:135], v[176:179], v[124:127], v[132:135]\n\t"
      "s_waitcnt lgkmcnt(3)\n\t"
      "buffer_store_dwordx4 v[140:143], v208, s[28:31], s72 offen sc1\n\t"
      "v_mfma_f32_16x16x32_bf16 v[128:131], a[8:11], v[80:83], v[16:19]\n\t"
      "ds_read_b128 v[36:39], v197 offset:0\n\t"
      "v_mfma_f32_16x16x32_bf16 v[128:131], a[32:35], v[84:87], v[128:131]\n\t"
      "s_memrealtime s[80:81]\n\t"
      "v_mfma_f32_16x16x32_bf16 v[128:131], a[56:59], v[88:91], v[128:131]\n\t"
      "v_cvt_pk_bf16_f32 v138, v132, v133\n\t"
      "v_cvt_pk_bf16_f32 v139, v134, v135\n\t"
      "v_mfma_f32_16x16x32_bf16 v[128:131], a[80:83], v[92:95], v[128:131]\n\t"
      "ds_write_b32 v217, v138 offset:12296\n\t"
      "v_mfma_f32_16x16x32_bf16 v[128:131], a[104:107], v[96:99], v[128:131]\n\t"
      "ds_write_b32 v217, v139 offset:12300\n\t"
      "v_mfma_f32_16x16x32_bf16 v[128:131], a[128:131], v[100:103], v[128:131]\n\t"
      "ds_read_b128 v[40:43], v198 offset:0\n\t"
      "v_mfma_f32_16x16x32_bf16 v[128:131], a[152:155], v[104:107], v[128:131]\n\t"
      "s_mov_b32 m0, s46\n\t"
      "v_mfma_f32_16x16x32_bf16 v[128:131], a[176:179], v[108:111], v[128:131]\n\t"
      "ds_read_b128 v[44:47], v199 offset:0\n\t"
      "v_mfma_f32_16x16x32_bf16 v[128:131], a[200:203], v[112:115], v[128:131]\n\t"
      "v_mfma_f32_16x16x32_bf16 v[128:131], a[224:227], v[116:119], v[128:131]\n\t"
      "ds_read_b128 v[48:51], v197 offset:256\n\t"
      "v_mfma_f32_16x16x32_bf16 v[128:131], a[248:251], v[120:123], v[128:131]\n\t"
      "v_mfma_f32_16x16x32_bf16 v[128:131], v[180:183], v[124:127], v[128:131]\n\t"
      "buffer_load_dwordx4 v208, s[20:23], s73 offen lds\n\t"
      "v_mfma_f32_16x16x32_bf16 v[132:135], a[12:15], v[80:83], v[20:23]\n\t"
      "ds_read_b128 v[52:55], v198 offset:256\n\t"
      "v_mfma_f32_16x16x32_bf16 v[132:135], a[36:39], v[84:87], v[132:135]\n\t"
      "v_readlane_b32 s71, v252, s44\n\t"
      "v_mfma_f32_16x16x32_bf16 v[132:135], a[60:63], v[88:91], v[132:135]\n\t"
      "v_cvt_pk_bf16_f32 v136, v128, v129\n\t"
      "v_cvt_pk_bf16_f32 v137, v130, v131\n\t"
      "v_mfma_f32_16x16x32_bf16 v[132:135], a[84:87], v[92:95], v[132:135]\n\t"
      "ds_write_b32 v218, v136 offset:12288\n\t"
      "v_mfma_f32_16x16x32_bf16 v[132:135], a[108:111], v[96:99], v[132:135]\n\t"
      "s_lshr_b32 s60, s71, 16\n\t"
      "ds_write_b32 v218, v137 offset:12292\n\t"
      "v_mfma_f32_16x16x32_bf16 v[132:135], a[132:135], v[100:103], v[132:135]\n\t"
      "s_and_b32 s86, s71, 0xff\n\t"
      "ds_read_b128 v[144:147], v220 offset:1024\n\t"
      "v_mfma_f32_16x16x32_bf16 v[132:135], a[156:159], v[104:107], v[132:135]\n\t"
      "s_bfe_u32 s71, s71, 0x80008\n\t"
      "ds_read_b128 v[56:59], v199 offset:256\n\t"
      "v_mfma_f32_16x16x32_bf16 v[132:135], a[180:183], v[108:111], v[132:135]\n\t"
      "s_sub_u32 s84, s84, 19\n\t"
      "v_mfma_f32_16x16x32_bf16 v[132:135], a[204:207], v[112:115], v[132:135]\n\t"
      "s_max_i32 s84, s84, s86\n\t"
      "v_mfma_f32_16x16x32_bf16 v[132:135], a[228:231], v[116:119], v[132:135]\n\t"
      "ds_read_b128 v[60:63], v196 offset:256\n\t"
      "v_mfma_f32_16x16x32_bf16 v[132:135], a[252:255], v[120:123], v[132:135]\n\t"
      "v_mfma_f32_16x16x32_bf16 v[132:135], v[184:187], v[124:127], v[132:135]\n\t"
      "s_waitcnt lgkmcnt(2)\n\t"
      "buffer_store_dwordx4 v[144:147], v208, s[28:31], s72 offen offset:1024 sc1\n\t"
      "v_mfma_f32_16x16x32_bf16 v[128:131], a[16:19], v[80:83], v[24:27]\n\t"
      "ds_read_b128 v[64:67], v198 offset:512\n\t"
      "v_mfma_f32_16x16x32_bf16 v[128:131], a[40:43], v[84:87], v[128:131]\n\t"
      "v_mfma_f32_16x16x32_bf16 v[128:131], a[64:67], v[88:91], v[128:131]\n\t"
      "v_cvt_pk_bf16_f32 v138, v132, v133\n\t"
      "v_cvt_pk_bf16_f32 v139, v134, v135\n\t"
      "v_mfma_f32_16x16x32_bf16 v[128:131], a[88:91], v[92:95], v[128:131]\n\t"
      "ds_write_b32 v218, v138 offset:12296\n\t"
      "v_mfma_f32_16x16x32_bf16 v[128:131], a[112:115], v[96:99], v[128:131]\n\t"
      "ds_write_b32 v218, v139 offset:12300\n\t"
      "v_mfma_f32_16x16x32_bf16 v[128:131], a[136:139], v[100:103], v[128:131]\n\t"
      "s_sub_u32 s87, s80, s52\n\t"
      "v_mfma_f32_16x16x32_bf16 v[128:131], a[160:163], v[104:107], v[128:131]\n\t"
      "s_sub_u32 s83, s82, s87\n\t"
      "ds_read_b128 v[68:71], v199 offset:512\n\t"
      "v_mfma_f32_16x16x32_bf16 v[128:131], a[184:187], v[108:111], v[128:131]\n\t"
      "s_max_i32 s82, s82, s87\n\t"
      "v_mfma_f32_16x16x32_bf16 v[128:131], a[208:211], v[112:115], v[128:131]\n\t"
      "s_sub_u32 s61, s70, s82\n\t"
      "v_mfma_f32_16x16x32_bf16 v[128:131], a[232:235], v[116:119], v[128:131]\n\t"
      "s_mul_i32 s61, s61, s60\n\t"
      "ds_read_b128 v[72:75], v196 offset:512\n\t"
      "v_mfma_f32_16x16x32_bf16 v[128:131], v[164:167], v[120:123], v[128:131]\n\t"
      "s_ashr_i32 s61, s61, 8\n\t"
      "v_mfma_f32_16x16x32_bf16 v[128:131], v[188:191], v[124:127], v[128:131]\n\t"
      "buffer_load_dwordx4 v208, s[20:23], s73 offen offset:1024 lds\n\t"
      "v_mfma_f32_16x16x32_bf16 v[132:135], a[20:23], v[80:83], v[28:31]\n\t"
      "s_add_u32 s61, s61, s71\n\t"
      "ds_read_b128 v[76:79], v197 offset:512\n\t"
      "v_mfma_f32_16x16x32_bf16 v[132:135], a[44:47], v[84:87], v[132:135]\n\t"
      "s_max_i32 s61, s61, s84\n\t"
      "ds_read_b128 v[148:151], v220 offset:2048\n\t"
      "v_mfma_f32_16x16x32_bf16 v[132:135], a[68:71], v[88:91], v[132:135]\n\t"
      "s_add_u32 s82, s82, s61\n\t"
      "v_cvt_pk_bf16_f32 v136, v128, v129\n\t"
      "v_cvt_pk_bf16_f32 v137, v130, v131\n\t"
      "v_mfma_f32_16x16x32_bf16 v[132:135], a[92:95], v[92:95], v[132:135]\n\t"
      "ds_write_b32 v219, v136 offset:12288\n\t"
      "v_mfma_f32_16x16x32_bf16 v[132:135], a[116:119], v[96:99], v[132:135]\n\t"
      "ds_write_b32 v219, v137 offset:12292\n\t"
      "v_mfma_f32_16x16x32_bf16 v[132:135], a[140:143], v[100:103], v[132:135]\n\t"
      "s_add_u32 s70, s70, s71\n\t"
      "v_mfma_f32_16x16x32_bf16 v[132:135], a[164:167], v[104:107], v[132:135]\n\t"
      "s_mov_b32 s45, s46\n\t"
      "v_add_u32_e32 v6, 1, v6\n\t"
      "v_mfma_f32_16x16x32_bf16 v[132:135], a[188:191], v[108:111], v[132:135]\n\t"
      "s_mov_b32 s46, s47\n\t"
      "v_mfma_f32_16x16x32_bf16 v[132:135], a[212:215], v[112:115], v[132:135]\n\t"
      "s_mov_b32 s47, s48\n\t"
      "v_mfma_f32_16x16x32_bf16 v[132:135], a[236:239], v[116:119], v[132:135]\n\t"
      "v_mfma_f32_16x16x32_bf16 v[132:135], v[168:171], v[120:123], v[132:135]\n\t"
      "s_cmp_lg_u32 s44, s49\n\t"
      "v_mfma_f32_16x16x32_bf16 v[132:135], v[192:195], v[124:127], v[132:135]\n\t"
      "s_waitcnt lgkmcnt(2)\n\t"
      "buffer_store_dwordx4 v[148:151], v208, s[28:31], s72 offen offset:2048 sc1\n\t"
      "s_cbranch_scc1 .Lloop_%=\n\t"
      "v_readlane_b32 s72, v249, s44\n\t"
      "s_nop 6\n\t"
      "v_cvt_pk_bf16_f32 v138, v132, v133\n\t"
      "v_cvt_pk_bf16_f32 v139, v134, v135\n\t"
      "ds_write_b64 v219, v[138:139] offset:12296\n\t"
      "s_waitcnt lgkmcnt(0)\n\t"
      "s_barrier\n\t"
      "ds_read_b128 v[32:35], v220 offset:12288\n\t"
      "ds_read_b128 v[36:39], v220 offset:13312\n\t"
      "ds_read_b128 v[40:43], v220 offset:14336\n\t"
      "s_waitcnt lgkmcnt(2)\n\t"
      "buffer_store_dwordx4 v[32:35], v209, s[28:31], s72 offen sc1\n\t"
      "s_waitcnt lgkmcnt(1)\n\t"
      "buffer_store_dwordx4 v[36:39], v209, s[28:31], s72 offen offset:1024 sc1\n\t"
      "s_waitcnt lgkmcnt(0)\n\t"
      "buffer_store_dwordx4 v[40:43], v209, s[28:31], s72 offen offset:2048 sc1\n\t"
      "s_endpgm\n\t"
      ".LslpA_%=:\n\t"
      "s_min_i32 s83, s83, 0x190\n\t"
      ".Lsl2A_%=:\n\t"
      "s_sleep 1\n\t"
      "s_sub_u32 s83, s83, 3\n\t"
      "s_cmp_gt_i32 s83, 1\n\t"
      "s_cbranch_scc1 .Lsl2A_%=\n\t"
      "s_branch .LretA_%=\n\t"
      ".LslpB_%=:\n\t"
      "s_min_i32 s83, s83, 0x190\n\t"
      ".Lsl2B_%=:\n\t"
      "s_sleep 1\n\t"
      "s_sub_u32 s83, s83, 3\n\t"
      "s_cmp_gt_i32 s83, 1\n\t"
      "s_cbranch_scc1 .Lsl2B_%=\n\t"
      "s_branch .LretB_%=\n\t"
      ".Lgs_%=:\n\t"
      "s_sleep 8\n\t"
      "buffer_load_dwordx2 v[240:241], v245, s[24:27], 0 offen sc1\n\t"
      "s_memrealtime s[76:77]\n\t"
      "s_waitcnt vmcnt(0) lgkmcnt(0)\n\t"
      "v_subrev_u32_e32 v242, s67, v240\n\t"
      "v_xor_b32_e32 v241, s63, v241\n\t"
      "v_lshl_or_b32 v240, v242, 20, v241\n\t"
      "v_lshrrev_b32_e32 v242, 12, v242\n\t"
      "v_cmp_ne_u32_e32 vcc, 0, v240\n\t"
      "s_nop 0\n\t"
      "s_cbranch_vccz .Lgr_%=\n\t"
      "s_sub_u32 s76, s76, s52\n\t"
      "s_cmp_lt_u32 s76, 2000\n\t"
      "s_cbranch_scc1 .Lgs_%=\n\t"
      "v_mov_b32_e32 v242, 10\n\t"
      "s_branch .Lgr_%=\n\t"
      ".LsbA_%=:\n\t"
      "ds_read_b32 v7, v239\n\t"
      "s_waitcnt lgkmcnt(0)\n\t"
      "v_cmp_gt_u32_e32 vcc, v6, v7\n\t"
      "s_nop 0\n\t"
      "s_cbranch_vccnz .LsbA_%=\n\t"
      "s_branch .LsrA_%=\n\t"
      ".LsbB_%=:\n\t"
      "ds_read_b32 v7, v239\n\t"
      "s_waitcnt lgkmcnt(0)\n\t"
      "v_cmp_gt_u32_e32 vcc, v6, v7\n\t"
      "s_nop 0\n\t"
      "s_cbranch_vccnz .LsbB_%=\n\t"
      "s_branch .LsrB_%=\n\t"
      ".Lend_%=:\n\t"
      :
      : "{s[20:21]}"(xa), "{s[16:17]}"(wa), "{s[36:37]}"(ba), "{s[28:29]}"(oa), "{s[24:25]}"(sa), "{s[52:53]}"(rs),
        "{s[22:23]}"(0x0002000006c00000ull), "{s[26:27]}"(0x0002000000000808ull), "{s[30:31]}"(0x0002000006c00000ull),
        "{s[18:19]}"(0x0002000000048000ull), "{s[38:39]}"(0x0002000000000300ull),
        "{s40}"(t0), "{s41}"(p0), "{s43}"(lds0), "{s94}"(ty),
        "{v208}"(ga), "{v209}"(gb),
        "{v211}"(woff[0]), "{v212}"(woff[1]), "{v213}"(woff[2]), "{v215}"(boff),
        "{s95}"(tres), "{s92}"(pre1), "{s[60:61]}"(pres), "{s[62:63]}"(ep), "{s77}"(pfire), "{s98}"(((tidx >> 3) * 4u + (tidx & 3u)) * 128u), "{v238}"(vfw), "{v239}"(vfr),
        "{v248}"(ta), "{v249}"(ta), "{v251}"(t1), "{v250}"(mk), "{s74}"(plm), "{s75}"(plb),
        "{s[34:35]}"(__builtin_amdgcn_kernarg_segment_ptr()),
        "{v244}"(vpub), "{v210}"(vbmp), "{v245}"(vgv), "{v246}"(vxw), "{v247}"(vxr),
        "{v200}"(cfa[0]), "{v201}"(cfa[1]), "{v202}"(cfa[2]), "{v203}"(cfa[3]),
        "{v204}"(csw[0]), "{v205}"(csw[1]), "{v206}"(csw[2]), "{v207}"(csr),
        "{v214}"(tbase)
      : "memory", "scc", "vcc",
      "s42", "s44", "s45", "s46", "s47", "s48", "s49", "s50", "s51", "s54", "s55", "s56", "s57", "s58", "s59", "s64", "s65", "s66", "s67", "s68",
      "s69", "s70", "s71", "s72", "s73", "s76", "s78", "s79", "s80", "s81", "s82", "s83", "s84", "s85", "s86", "s87", "s88", "s89", "s90", "s91",
      "s93", "s96", "s97", "s99",
      "v1", "v2", "v3", "v4", "v5", "v6", "v7", "v8", "v9", "v10", "v11", "v12", "v13", "v14", "v15", "v16", "v17", "v18", "v19", "v20", "v21", "v22",
      "v23", "v24", "v25", "v26", "v27", "v28", "v29", "v30", "v31", "v32", "v33", "v34", "v35", "v36", "v37", "v38", "v39", "v40", "v41", "v42",
      "v43", "v44", "v45", "v46", "v47", "v48", "v49", "v50", "v51", "v52", "v53", "v54", "v55", "v56", "v57", "v58", "v59", "v60", "v61", "v62",
      "v63", "v64", "v65", "v66", "v67", "v68", "v69", "v70", "v71", "v72", "v73", "v74", "v75", "v76", "v77", "v78", "v79", "v80", "v81", "v82",
      "v83", "v84", "v85", "v86", "v87", "v88", "v89", "v90", "v91", "v92", "v93", "v94", "v95", "v96", "v97", "v98", "v99", "v100", "v101", "v102",
      "v103", "v104", "v105", "v106", "v107", "v108", "v109", "v110", "v111", "v112", "v113", "v114", "v115", "v116", "v117", "v118", "v119", "v120",
      "v121", "v122", "v123", "v124", "v125", "v126", "v127", "v128", "v129", "v130", "v131", "v132", "v133", "v134", "v135", "v136", "v137", "v138",
      "v139", "v140", "v141", "v142", "v143", "v144", "v145", "v146", "v147", "v148", "v149", "v150", "v151", "v152", "v153", "v154", "v155", "v156",
      "v157", "v158", "v159", "v160", "v161", "v162", "v163", "v164", "v165", "v166", "v167", "v168", "v169", "v170", "v171", "v172", "v173", "v174",
      "v175", "v176", "v177", "v178", "v179", "v180", "v181", "v182", "v183", "v184", "v185", "v186", "v187", "v188", "v189", "v190", "v191", "v192",
      "v193", "v194", "v195", "v196", "v197", "v198", "v199", "v216", "v217", "v218", "v219", "v220", "v221", "v222", "v223", "v224", "v225", "v226",
      "v227", "v228", "v229", "v230", "v231", "v232", "v233", "v234", "v235", "v236", "v237", "v240", "v241", "v242", "v243", "v252", "v253", "v254",
      "v255",
      "a0", "a1", "a2", "a3", "a4", "a5", "a6", "a7", "a8", "a9", "a10", "a11", "a12", "a13", "a14", "a15", "a16", "a17", "a18", "a19", "a20", "a21",
      "a22", "a23", "a24", "a25", "a26", "a27", "a28", "a29", "a30", "a31", "a32", "a33", "a34", "a35", "a36", "a37", "a38", "a39", "a40", "a41",
      "a42", "a43", "a44", "a45", "a46", "a47", "a48", "a49", "a50", "a51", "a52", "a53", "a54", "a55", "a56", "a57", "a58", "a59", "a60", "a61",
      "a62", "a63", "a64", "a65", "a66", "a67", "a68", "a69", "a70", "a71", "a72", "a73", "a74", "a75", "a76", "a77", "a78", "a79", "a80", "a81",
      "a82", "a83", "a84", "a85", "a86", "a87", "a88", "a89", "a90", "a91", "a92", "a93", "a94", "a95", "a96", "a97", "a98", "a99", "a100", "a101",
      "a102", "a103", "a104", "a105", "a106", "a107", "a108", "a109", "a110", "a111", "a112", "a113", "a114", "a115", "a116", "a117", "a118", "a119",
      "a120", "a121", "a122", "a123", "a124", "a125", "a126", "a127", "a128", "a129", "a130", "a131", "a132", "a133", "a134", "a135", "a136", "a137",
      "a138", "a139", "a140", "a141", "a142", "a143", "a144", "a145", "a146", "a147", "a148", "a149", "a150", "a151", "a152", "a153", "a154", "a155",
      "a156", "a157", "a158", "a159", "a160", "a161", "a162", "a163", "a164", "a165", "a166", "a167", "a168", "a169", "a170", "a171", "a172", "a173",
      "a174", "a175", "a176", "a177", "a178", "a179", "a180", "a181", "a182", "a183", "a184", "a185", "a186", "a187", "a188", "a189", "a190", "a191",
      "a192", "a193", "a194", "a195", "a196", "a197", "a198", "a199", "a200", "a201", "a202", "a203", "a204", "a205", "a206", "a207", "a208", "a209",
      "a210", "a211", "a212", "a213", "a214", "a215", "a216", "a217", "a218", "a219", "a220", "a221", "a222", "a223", "a224", "a225", "a226", "a227",
      "a228", "a229", "a230", "a231", "a232", "a233", "a234", "a235", "a236", "a237", "a238", "a239", "a240", "a241", "a242", "a243", "a244", "a245",
      "a246", "a247", "a248", "a249", "a250", "a251", "a252", "a253", "a254", "a255");
}

extern "C" void kernel_launch(void* const* d_in, const int* in_sizes, int n_in,
                              void* d_out, int out_size, void* d_ws, size_t ws_size,
                              hipStream_t stream) {
  const uint16_t* x = (const uint16_t*)d_in[0];
  const uint16_t* w = (const uint16_t*)d_in[1];
  const uint16_t* b = (const uint16_t*)d_in[2];
  uint16_t* out = (uint16_t*)d_out;
  const PaceTab pt = {{
    0x205d4eu, 0x205a4cu, 0x184944u, 0u,
    0x205d4eu, 0x205a4cu, 0x184944u, 0u,
    0x20564eu, 0x20534cu, 0x184944u, 0u,
    0x204f4eu, 0x204b48u, 0x184944u, 0u,
    0x184f4eu, 0x184b48u, 0x184944u, 0u,
    0x184f4eu, 0x184b48u, 0x184944u, 0u,
    0x184f4eu, 0x184b48u, 0x184944u, 0u,
    0x11514eu, 0x114e48u, 0x114944u, 0u,
    0x13534eu, 0x135049u, 0x134b44u, 0u,
    0x16544eu, 0x16514au, 0x164d46u, 0u,
    0x1b544eu, 0x1b514au, 0x1b4d46u, 0u,
    0x22544eu, 0x22514au, 0x224d46u, 0u,
    0x2f544eu, 0x2f514au, 0x2f4d46u, 0u,
    0x49544eu, 0x49514au, 0x494d46u, 0u,
    0xab544eu, 0xab514au, 0xab4d46u, 0u,
    0x1004400u, 0x1004400u, 0x1004400u, 0u,
    0x1004400u, 0x1004400u, 0x1004400u, 0u,
    0x1004400u, 0x1004400u, 0x1004400u, 0u}};
  conv1x1_kernel<<<256, 256, 0, stream>>>(x, w, b, out, (unsigned long long*)d_ws, pt);
}
```

```cpp
#include <hip/hip_runtime.h>
#include <stdint.h>

#define LAS __attribute__((address_space(3)))

struct PaceTab { unsigned v[72]; };

__global__ __launch_bounds__(256, 1) void conv1x1_kernel(const uint16_t* __restrict__ x,
                                                         const uint16_t* __restrict__ w,
                                                         const uint16_t* __restrict__ bias,
                                                         uint16_t* __restrict__ out, unsigned long long* ws, PaceTab pt) {
  __shared__ __attribute__((aligned(1024))) unsigned char lds[161792];

  unsigned long long xa, wa, ba, oa, sa, rs;
  asm volatile(
      "s_load_dwordx2 s[16:17], %6, 0x8\n\t"
      "s_load_dwordx2 s[20:21], %6, 0x0\n\t"
      "s_load_dwordx2 s[36:37], %6, 0x10\n\t"
      "s_load_dwordx2 s[28:29], %6, 0x18\n\t"
      "s_load_dwordx2 s[24:25], %6, 0x20\n\t"
      "s_memrealtime s[52:53]\n\t"
      : "={s[20:21]}"(xa), "={s[16:17]}"(wa), "={s[36:37]}"(ba), "={s[28:29]}"(oa), "={s[24:25]}"(sa), "={s[52:53]}"(rs)
      : "s"(__builtin_amdgcn_kernarg_segment_ptr())
      : "memory");
  __builtin_amdgcn_sched_barrier(0);
  const unsigned tid  = threadIdx.x;
  unsigned lane = tid & 63;
  unsigned wv   = __builtin_amdgcn_readfirstlane(tid >> 6);
  const unsigned lds0 = (unsigned)(size_t)(LAS unsigned char*)lds;
  const unsigned blk  = blockIdx.x;
  const unsigned role = (blk >> 2) & 1u;
  unsigned pk = (blk & 3u) * 32u + (blk >> 3);
  const unsigned sst  = (role ^ (blk >> 4)) & 1u;
  const unsigned t0   = pk * 49152u + sst * 24576u;
  const unsigned p0   = lds0 + 3072u * wv;
  const unsigned lq = lane >> 4, ph = (lane >> 2) & 3, pl = lane & 3;
  const unsigned gs = 256u * lq + 64u * ((ph - lq) & 3) + 16u * (pl ^ ((4u - wv) & 3));
  const unsigned ga = gs + 3072u * (wv < 3u ? wv : 5u);
  const unsigned gb = gs + 3072u * (wv < 2u ? wv + 6u : wv + 1u);
  const unsigned pv = blk >> 6, ps = (blk & 63u) ^ (4u * (pv & 1u)), pgr = (ps >> 2) & 7u, pi = (ps & 3u) * 2u + (pv >> 1);
  const unsigned ppg = min(pgr * 8u, 52u) + (pi & (0x13777777u >> (4u * pgr)) & 7u);
  const unsigned pop = (ppg << 21) + ((ps & 3u) * 4u + pv) * 0x1c080u + 0x8000u;
  unsigned pfire = ((role ^ ppg) & 1u) << 13;
  const unsigned pfo = (blk >> 3) * 9216u;
  const unsigned pfv = tid * 16u;
  const unsigned ty = (blk >> 3) & 1u;
  const unsigned tidx = (blk >> 3) * 4u + wv, tsel = tidx >> 6;
  const unsigned toff = (tidx & 63u) < 54u ? (tidx & 63u) << 21 : 113246204u;
  unsigned tres, pre1;
  unsigned long long pres, ep;
  asm volatile(
      "s_add_u32 m0, s43, 0x27000\n\t"
      "s_add_u32 s51, s42, 0x1000\n\t"
      "s_waitcnt lgkmcnt(0)\n\t"
      "buffer_load_dwordx4 v216, s[16:19], s42 offen lds\n\t"
      "buffer_load_dwordx4 v216, s[16:19], s51 offen lds\n\t"
      "s_add_u32 s51, s42, 0x2000\n\t"
      "buffer_load_dwordx4 v216, s[16:19], s51 offen lds\n\t"
      "s_cmp_lg_u32 s41, s43\n\t"
      "s_cbranch_scc1 .Lw0pi_%=\n\t"
      "s_cmp_eq_u32 s78, 0\n\t"
      "s_cselect_b64 s[96:97], s[20:21], s[28:29]\n\t"
      "s_load_dword s92, s[96:97], s76 offset:0x0\n\t"
      "s_load_dwordx2 s[60:61], s[96:97], s76 offset:0x3300\n\t"
      ".Lw0pi_%=:\n\t"
      "s_mov_b32 m0, s41\n\t"
      "s_nop 0\n\t"
      "buffer_load_dwordx4 v208, s[20:23], s40 offen lds\n\t"
      "buffer_load_dwordx4 v208, s[20:23], s40 offen offset:1024 lds\n\t"
      "buffer_load_dwordx4 v208, s[20:23], s40 offen offset:2048 lds\n\t"
      "s_add_u32 m0, s41, 0x3000\n\t"
      "s_nop 0\n\t"
      "buffer_load_dwordx4 v209, s[20:23], s40 offen lds\n\t"
      "buffer_load_dwordx4 v209, s[20:23], s40 offen offset:1024 lds\n\t"
      "buffer_load_dwordx4 v209, s[20:23], s40 offen offset:2048 lds\n\t"
      "s_add_u32 s51, s43, 0x2400\n\t"
      "s_cmp_lg_u32 s41, s51\n\t"
      "s_cbranch_scc1 .Lbd_%=\n\t"
      "s_add_u32 m0, s43, 0x27400\n\t"
      "s_sub_u32 s36, s36, 0xc00\n\t"
      "s_subb_u32 s37, s37, 0\n\t"
      "s_mov_b32 s51, 0\n\t"
      "buffer_load_dwordx4 v216, s[36:39], s51 offen lds\n\t"
      ".Lbd_%=:\n\t"
      "s_cmp_lg_u32 s41, s43\n\t"
      "s_cbranch_scc1 .Lw0pe_%=\n\t"
      "s_waitcnt lgkmcnt(2)\n\t"
      "s_nop 7\n\t"
      "s_getreg_b32 s51, hwreg(HW_REG_IB_STS, 8, 4)\n\t"
      "s_lshl_b32 s96, s51, 13\n\t"
      "s_xor_b32 s77, s77, s96\n\t"
      "s_and_b32 s77, s77, 0x2000\n\t"
      "s_cmp_lg_u32 s51, 0\n\t"
      "s_cselect_b32 s77, s77, 0x1000\n\t"
      ".Lw0pe_%=:\n\t"
      "s_load_dwordx2 s[62:63], s[24:25], 0x800\n\t"
      "s_cmp_eq_u32 s99, 0\n\t"
      "s_cselect_b64 s[96:97], s[20:21], s[28:29]\n\t"
      "s_load_dword s95, s[96:97], s98\n\t"
      : "={s95}"(tres), "={s92}"(pre1), "={s[60:61]}"(pres), "={s[62:63]}"(ep), "+{s77}"(pfire), "+v"(lane), "+s"(wv), "+s"(pk)
      : "{s[20:21]}"(xa), "{s[16:17]}"(wa), "{s[36:37]}"(ba), "{s[28:29]}"(oa), "{s[24:25]}"(sa), "{s[52:53]}"(rs),
        "{s[22:23]}"(0x0002000006c00000ull), "{s[26:27]}"(0x0002000000000808ull), "{s[30:31]}"(0x0002000006c00000ull),
        "{s[18:19]}"(0x0002000000048000ull), "{s[38:39]}"(0x0002000000000f00ull),
        "{s40}"(t0), "{s41}"(p0), "{s43}"(lds0), "{s94}"(ty),
        "{v208}"(ga), "{v209}"(gb),
        "{s42}"(pfo), "{s98}"(toff), "{s99}"(tsel), "{s76}"(pop), "{s78}"(ps >> 5),
        "{v216}"(pfv)
      : "memory", "scc", "s51", "s96", "s97");
  __builtin_amdgcn_sched_barrier(0);
  const unsigned sp = lane < 18u ? lane : lane - 14u;
  const unsigned ob = (unsigned)(((sp < 12u ? 0x41ccd62ca418820ull >> (5u * sp) : 0x22a4c1eeull >> (5u * ((sp - 12u) & 7u)))) & 31u) * 6291456u + pk * 49152u;
  const unsigned pg = (ob + 24576u) >> 21;
  const unsigned ta = lane < 18u ? ob + 24576u * sst : 113246208u, t1 = lane < 32u ? ob + 24576u * (sst ^ 1u) : 113246208u;
  const unsigned mk = lane >= 4u && lane < 18u ? 1u << (pg < 52u ? (pg >> 3) * 4u : 28u) : 0u;
  const unsigned plm = (unsigned)__builtin_amdgcn_ballot_w64(((pg ^ role ^ sst) & 1u) != 0u), plb = wv < 2u ? plm : ~plm;
  const unsigned vgv = 0u + wv * 512u + lane * 8u;
  const unsigned vpub = vgv == 0u ? 0u + (pv * 64u + ps) * 8u : 0x40000000u, vbmp = vpub == 0u ? 2048u : 0x40000000u;
  const unsigned vxr = lds0 + 159744u + lane * 16u, vxw = vxr + wv * 4u;
  const unsigned vfw = lds0 + 160768u + lane * 4u + wv * 256u, vfr = lds0 + 160768u + lane * 4u + (lane & 3u) * 256u;
  const unsigned li = lane & 15, lg = lane >> 4, rl = li & 3, hi = li >> 2;
  const unsigned rb = 768u * li + 16u * (lg ^ ((4u - hi) & 3)) - 3072u * wv;
  unsigned cfa[4], csw[3];
#pragma unroll
  for (unsigned u = 0; u < 4; ++u) cfa[u] = rb + 64u * ((u - rl) & 3);
#pragma unroll
  for (unsigned j = 0; j < 3; ++j) {
    const unsigned e = 3u * wv + j;
    csw[j] = rb + 256u * (e >> 2) + 64u * (((e & 3) + (e >> 2) - rl) & 3);
  }
  const unsigned csr = 16u * lane;
  const unsigned tbase = p0 + (4u * lg + (li >> 2)) * 192u + 16u * (li & 3);
  unsigned woff[3];
#pragma unroll
  for (unsigned j = 0; j < 3; ++j) {
    const unsigned q = 64u * j + lane, rr = q / 12u;
    woff[j] = (8u * (rr >> 2) + (rr & 3)) * 768u + 192u * wv + 16u * (q % 12u) - 1024u * j;
  }
  const unsigned boff = lds0 + 160768u + 192u * wv + 16u * lg;
  asm volatile(
      "s_cmp_eq_u32 s94, 1\n\t"
      "s_cbranch_scc0 .Lc0_%=\n\t"
      "s_getpc_b64 s[96:97]\n\t"
      ".Lcp_%=:\n\t"
      "s_add_u32 s98, s98, .Lcq_%=-.Lcp_%=\n\t"
      "s_min_u32 s98, s98, .Lend_%=-.Lcp_%=-68\n\t"
      "s_load_dword s99, s[96:97], s98 offset:64\n\t"
      "s_load_dword s98, s[96:97], s98\n\t"
      "s_add_u32 m0, s41, 0x6000\n\t"
      "s_add_u32 s51, s40, 0x600000\n\t"
      "buffer_load_dwordx4 v208, s[20:23], s51 offen lds\n\t"
      "buffer_load_dwordx4 v208, s[20:23], s51 offen offset:1024 lds\n\t"
      "buffer_load_dwordx4 v208, s[20:23], s51 offen offset:2048 lds\n\t"
      "s_add_u32 m0, s41, 0x9000\n\t"
      "s_add_u32 s51, s40, 0x600000\n\t"
      "buffer_load_dwordx4 v209, s[20:23], s51 offen lds\n\t"
      "buffer_load_dwordx4 v209, s[20:23], s51 offen offset:1024 lds\n\t"
      "buffer_load_dwordx4 v209, s[20:23], s51 offen offset:2048 lds\n\t"
      ".Lc0_%=:\n\t"
      "s_add_u32 m0, s41, 0x1e000\n\t"
      "s_mov_b32 s51, 0x0\n\t"
      "buffer_load_dwordx4 v211, s[16:19], s51 offen lds\n\t"
      "buffer_load_dwordx4 v212, s[16:19], s51 offen offset:1024 lds\n\t"
      "buffer_load_dwordx4 v213, s[16:19], s51 offen offset:2048 lds\n\t"
      "s_add_u32 m0, s41, 0x21000\n\t"
      "s_mov_b32 s51, 0xc00\n\t"
      "buffer_load_dwordx4 v211, s[16:19], s51 offen lds\n\t"
      "buffer_load_dwordx4 v212, s[16:19], s51 offen offset:1024 lds\n\t"
      "buffer_load_dwordx4 v213, s[16:19], s51 offen offset:2048 lds\n\t"
      "s_add_u32 m0, s41, 0x24000\n\t"
      "s_mov_b32 s51, 0x6000\n\t"
      "buffer_load_dwordx4 v211, s[16:19], s51 offen lds\n\t"
      "buffer_load_dwordx4 v212, s[16:19], s51 offen offset:1024 lds\n\t"
      "buffer_load_dwordx4 v213, s[16:19], s51 offen offset:2048 lds\n\t"
      "s_add_u32 m0, s41, 0x12000\n\t"
      "s_mov_b32 s51, 0x6c00\n\t"
      "buffer_load_dwordx4 v211, s[16:19], s51 offen lds\n\t"
      "buffer_load_dwordx4 v212, s[16:19], s51 offen offset:1024 lds\n\t"
      "buffer_load_dwordx4 v213, s[16:19], s51 offen offset:2048 lds\n\t"
      "s_add_u32 m0, s41, 0x15000\n\t"
      "s_mov_b32 s51, 0xc000\n\t"
      "buffer_load_dwordx4 v211, s[16:19], s51 offen lds\n\t"
      "buffer_load_dwordx4 v212, s[16:19], s51 offen offset:1024 lds\n\t"
      "buffer_load_dwordx4 v213, s[16:19], s51 offen offset:2048 lds\n\t"
      "s_add_u32 m0, s41, 0x18000\n\t"
      "s_mov_b32 s51, 0xcc00\n\t"
      "buffer_load_dwordx4 v211, s[16:19], s51 offen lds\n\t"
      "buffer_load_dwordx4 v212, s[16:19], s51 offen offset:1024 lds\n\t"
      "buffer_load_dwordx4 v213, s[16:19], s51 offen offset:2048 lds\n\t"
      "s_add_u32 m0, s41, 0x1b000\n\t"
      "s_mov_b32 s51, 0x12000\n\t"
      "buffer_load_dwordx4 v211, s[16:19], s51 offen lds\n\t"
      "buffer_load_dwordx4 v212, s[16:19], s51 offen offset:1024 lds\n\t"
      "buffer_load_dwordx4 v213, s[16:19], s51 offen offset:2048 lds\n\t"
      "s_mov_b32 s44, 0\n\t"
      "s_mov_b32 s85, 3\n\t"
      "s_add_u32 s50, s41, 0x12000\n\t"
      "s_mov_b32 s45, s41\n\t"
      "s_add_u32 s46, s41, 0x6000\n\t"
      "s_add_u32 s47, s41, 0xc000\n\t"
      "s_mov_b32 s84, 96\n\t"
      "v_add_u32_e32 v196, s41, v200\n\t"
      "v_add_u32_e32 v197, s41, v201\n\t"
      "v_add_u32_e32 v198, s41, v202\n\t"
      "v_add_u32_e32 v199, s41, v203\n\t"
      "v_add_u32_e32 v217, s41, v204\n\t"
      "v_add_u32_e32 v218, s41, v205\n\t"
      "v_add_u32_e32 v219, s41, v206\n\t"
      "v_add_u32_e32 v220, s41, v207\n\t"
      "s_waitcnt vmcnt(18)\n\t"
      "s_waitcnt lgkmcnt(0)\n\t"
      "s_barrier\n\t"
      "s_memrealtime s[68:69]\n\t"
      "ds_read_b128 v[226:229], v215 offset:0\n\t"
      "ds_read_b128 v[230:233], v215 offset:64\n\t"
      "ds_read_b128 v[234:237], v215 offset:128\n\t"
      "v_add_u32_e32 v221, 0x1e000, v214\n\t"
      "ds_read_b64_tr_b16 a[0:1], v221 offset:0\n\t"
      "ds_read_b64_tr_b16 a[4:5], v221 offset:8\n\t"
      "ds_read_b64_tr_b16 a[8:9], v221 offset:64\n\t"
      "ds_read_b64_tr_b16 a[12:13], v221 offset:72\n\t"
      "ds_read_b64_tr_b16 a[16:17], v221 offset:128\n\t"
      "ds_read_b64_tr_b16 a[20:21], v221 offset:136\n\t"
      "ds_read_b128 v[140:143], v196 offset:0\n\t"
      "s_waitcnt lgkmcnt(7)\n\t"
      "v_lshlrev_b32_e32 v8, 16, v226\n\t"
      "v_and_b32_e32 v9, 0xffff0000, v226\n\t"
      "v_lshlrev_b32_e32 v10, 16, v227\n\t"
      "v_and_b32_e32 v11, 0xffff0000, v227\n\t"
      "v_lshlrev_b32_e32 v12, 16, v228\n\t"
      "v_and_b32_e32 v13, 0xffff0000, v228\n\t"
      "v_lshlrev_b32_e32 v14, 16, v229\n\t"
      "v_and_b32_e32 v15, 0xffff0000, v229\n\t"
      "v_lshlrev_b32_e32 v16, 16, v230\n\t"
      "v_and_b32_e32 v17, 0xffff0000, v230\n\t"
      "v_lshlrev_b32_e32 v18, 16, v231\n\t"
      "v_and_b32_e32 v19, 0xffff0000, v231\n\t"
      "v_lshlrev_b32_e32 v20, 16, v232\n\t"
      "v_and_b32_e32 v21, 0xffff0000, v232\n\t"
      "v_lshlrev_b32_e32 v22, 16, v233\n\t"
      "v_and_b32_e32 v23, 0xffff0000, v233\n\t"
      "v_lshlrev_b32_e32 v24, 16, v234\n\t"
      "v_and_b32_e32 v25, 0xffff0000, v234\n\t"
      "v_lshlrev_b32_e32 v26, 16, v235\n\t"
      "v_and_b32_e32 v27, 0xffff0000, v235\n\t"
      "v_lshlrev_b32_e32 v28, 16, v236\n\t"
      "v_and_b32_e32 v29, 0xffff0000, v236\n\t"
      "v_lshlrev_b32_e32 v30, 16, v237\n\t"
      "v_and_b32_e32 v31, 0xffff0000, v237\n\t"
      "s_add_u32 s62, s62, 0x7f4a7c15\n\t"
      "s_add_u32 s63, s63, 0x9e3779b9\n\t"
      "s_waitcnt vmcnt(15)\n\t"
      "v_add_u32_e32 v221, 0x21000, v214\n\t"
      "ds_read_b64_tr_b16 a[2:3], v221 offset:0\n\t"
      "ds_read_b64_tr_b16 a[6:7], v221 offset:8\n\t"
      "ds_read_b64_tr_b16 a[10:11], v221 offset:64\n\t"
      "ds_read_b64_tr_b16 a[14:15], v221 offset:72\n\t"
      "ds_read_b64_tr_b16 a[18:19], v221 offset:128\n\t"
      "ds_read_b64_tr_b16 a[22:23], v221 offset:136\n\t"
      "ds_read_b128 v[152:155], v196 offset:12288\n\t"
      "s_waitcnt lgkmcnt(8)\n\t"
      "s_add_u32 m0, s41, 0x1e000\n\t"
      "s_mov_b32 s51, 0x12c00\n\t"
      "buffer_load_dwordx4 v211, s[16:19], s51 offen lds\n\t"
      "buffer_load_dwordx4 v212, s[16:19], s51 offen offset:1024 lds\n\t"
      "buffer_load_dwordx4 v213, s[16:19], s51 offen offset:2048 lds\n\t"
      "s_lshl_b32 s67, s68, 18\n\t"
      "s_bfe_u32 s51, s62, 0xc0000\n\t"
      "s_and_b32 s67, s67, 0xfff00000\n\t"
      "s_or_b32 s67, s67, s51\n\t"
      "s_cmp_lg_u32 s41, s43\n\t"
      "s_cbranch_scc1 .Lw0pb_%=\n\t"
      "s_or_b32 s77, s77, s67\n\t"
      "v_mov_b32_e32 v241, s63\n\t"
      "v_mov_b32_e32 v240, s77\n\t"
      "buffer_store_dwordx2 v[240:241], v244, s[24:27], 0 offen sc1\n\t"
      ".Lw0pb_%=:\n\t"
      "s_waitcnt vmcnt(15)\n\t"
      "v_add_u32_e32 v221, 0x24000, v214\n\t"
      "ds_read_b64_tr_b16 a[24:25], v221 offset:0\n\t"
      "ds_read_b64_tr_b16 a[28:29], v221 offset:8\n\t"
      "ds_read_b64_tr_b16 a[32:33], v221 offset:64\n\t"
      "ds_read_b64_tr_b16 a[36:37], v221 offset:72\n\t"
      "ds_read_b64_tr_b16 a[40:41], v221 offset:128\n\t"
      "ds_read_b64_tr_b16 a[44:45], v221 offset:136\n\t"
      "ds_read_b128 v[144:147], v197 offset:0\n\t"
      "s_waitcnt lgkmcnt(8)\n\t"
      "v_mfma_f32_16x16x32_bf16 v[80:83], a[0:3], v[140:143], v[8:11]\n\t"
      "s_add_u32 m0, s41, 0x21000\n\t"
      "s_mov_b32 s51, 0x18000\n\t"
      "buffer_load_dwordx4 v211, s[16:19], s51 offen lds\n\t"
      "buffer_load_dwordx4 v212, s[16:19], s51 offen offset:1024 lds\n\t"
      "v_mfma_f32_16x16x32_bf16 v[84:87], a[4:7], v[140:143], v[12:15]\n\t"
      "buffer_load_dwordx4 v213, s[16:19], s51 offen offset:2048 lds\n\t"
      "v_mfma_f32_16x16x32_bf16 v[88:91], a[8:11], v[140:143], v[16:19]\n\t"
      "s_sub_u32 s67, s67, 0x20000000\n\t"
      "s_andn2_b32 s68, s68, 3\n\t"
      "s_sub_u32 s68, s68, s52\n\t"
      "s_waitcnt vmcnt(15)\n\t"
      "v_add_u32_e32 v221, 0x12000, v214\n\t"
      "ds_read_b64_tr_b16 a[26:27], v221 offset:0\n\t"
      "v_mfma_f32_16x16x32_bf16 v[92:95], a[12:15], v[140:143], v[20:23]\n\t"
      "ds_read_b64_tr_b16 a[30:31], v221 offset:8\n\t"
      "ds_read_b64_tr_b16 a[34:35], v221 offset:64\n\t"
      "v_mfma_f32_16x16x32_bf16 v[96:99], a[16:19], v[140:143], v[24:27]\n\t"
      "ds_read_b64_tr_b16 a[38:39], v221 offset:72\n\t"
      "ds_read_b64_tr_b16 a[42:43], v221 offset:128\n\t"
      "v_mfma_f32_16x16x32_bf16 v[100:103], a[20:23], v[140:143], v[28:31]\n\t"
      "ds_read_b64_tr_b16 a[46:47], v221 offset:136\n\t"
      "ds_read_b128 v[156:159], v197 offset:12288\n\t"
      "s_waitcnt lgkmcnt(8)\n\t"
      "v_mfma_f32_16x16x32_bf16 v[104:107], a[0:3], v[152:155], v[8:11]\n\t"
      "s_add_u32 m0, s41, 0x24000\n\t"
      "s_mov_b32 s51, 0x18c00\n\t"
      "buffer_load_dwordx4 v211, s[16:19], s51 offen lds\n\t"
      "v_mfma_f32_16x16x32_bf16 v[108:111], a[4:7], v[152:155], v[12:15]\n\t"
      "buffer_load_dwordx4 v212, s[16:19], s51 offen offset:1024 lds\n\t"
      "v_mfma_f32_16x16x32_bf16 v[112:115], a[8:11], v[152:155], v[16:19]\n\t"
      "buffer_load_dwordx4 v213, s[16:19], s51 offen offset:2048 lds\n\t"
      "s_waitcnt vmcnt(15)\n\t"
      "v_add_u32_e32 v221, 0x15000, v214\n\t"
      "ds_read_b64_tr_b16 a[48:49], v221 offset:0\n\t"
      "v_mfma_f32_16x16x32_bf16 v[116:119], a[12:15], v[152:155], v[20:23]\n\t"
      "ds_read_b64_tr_b16 a[52:53], v221 offset:8\n\t"
      "ds_read_b64_tr_b16 a[56:57], v221 offset:64\n\t"
      "v_mfma_f32_16x16x32_bf16 v[120:123], a[16:19], v[152:155], v[24:27]\n\t"
      "ds_read_b64_tr_b16 a[60:61], v221 offset:72\n\t"
      "ds_read_b64_tr_b16 a[64:65], v221 offset:128\n\t"
      "v_mfma_f32_16x16x32_bf16 v[132:135], a[20:23], v[152:155], v[28:31]\n\t"
      "ds_read_b64_tr_b16 a[68:69], v221 offset:136\n\t"
      "ds_read_b128 v[148:151], v198 offset:0\n\t"
      "s_waitcnt lgkmcnt(8)\n\t"
      "v_mfma_f32_16x16x32_bf16 v[80:83], a[24:27], v[144:147], v[80:83]\n\t"
      "s_add_u32 m0, s41, 0x12000\n\t"
      "s_mov_b32 s51, 0x1e000\n\t"
      "buffer_load_dwordx4 v211, s[16:19], s51 offen lds\n\t"
      "v_mfma_f32_16x16x32_bf16 v[84:87], a[28:31], v[144:147], v[84:87]\n\t"
      "buffer_load_dwordx4 v212, s[16:19], s51 offen offset:1024 lds\n\t"
      "v_mfma_f32_16x16x32_bf16 v[88:91], a[32:35], v[144:147], v[88:91]\n\t"
      "buffer_load_dwordx4 v213, s[16:19], s51 offen offset:2048 lds\n\t"
      "s_waitcnt vmcnt(15)\n\t"
      "v_add_u32_e32 v221, 0x18000, v214\n\t"
      "ds_read_b64_tr_b16 a[50:51], v221 offset:0\n\t"
      "v_mfma_f32_16x16x32_bf16 v[92:95], a[36:39], v[144:147], v[92:95]\n\t"
      "ds_read_b64_tr_b16 a[54:55], v221 offset:8\n\t"
      "ds_read_b64_tr_b16 a[58:59], v221 offset:64\n\t"
      "v_mfma_f32_16x16x32_bf16 v[96:99], a[40:43], v[144:147], v[96:99]\n\t"
      "ds_read_b64_tr_b16 a[62:63], v221 offset:72\n\t"
      "ds_read_b64_tr_b16 a[66:67], v221 offset:128\n\t"
      "v_mfma_f32_16x16x32_bf16 v[100:103], a[44:47], v[144:147], v[100:103]\n\t"
      "ds_read_b64_tr_b16 a[70:71], v221 offset:136\n\t"
      "ds_read_b128 v[160:163], v198 offset:12288\n\t"
      "s_waitcnt lgkmcnt(8)\n\t"
      "v_mfma_f32_16x16x32_bf16 v[104:107], a[24:27], v[156:159], v[104:107]\n\t"
      "s_add_u32 m0, s41, 0x15000\n\t"
      "s_mov_b32 s51, 0x1ec00\n\t"
      "buffer_load_dwordx4 v211, s[16:19], s51 offen lds\n\t"
      "v_mfma_f32_16x16x32_bf16 v[108:111], a[28:31], v[156:159], v[108:111]\n\t"
      "buffer_load_dwordx4 v212, s[16:19], s51 offen offset:1024 lds\n\t"
      "v_mfma_f32_16x16x32_bf16 v[112:115], a[32:35], v[156:159], v[112:115]\n\t"
      "buffer_load_dwordx4 v213, s[16:19], s51 offen offset:2048 lds\n\t"
      "s_waitcnt vmcnt(15)\n\t"
      "v_add_u32_e32 v221, 0x1b000, v214\n\t"
      "ds_read_b64_tr_b16 a[72:73], v221 offset:0\n\t"
      "v_mfma_f32_16x16x32_bf16 v[116:119], a[36:39], v[156:159], v[116:119]\n\t"
      "ds_read_b64_tr_b16 a[76:77], v221 offset:8\n\t"
      "ds_read_b64_tr_b16 a[80:81], v221 offset:64\n\t"
      "v_mfma_f32_16x16x32_bf16 v[120:123], a[40:43], v[156:159], v[120:123]\n\t"
      "ds_read_b64_tr_b16 a[84:85], v221 offset:72\n\t"
      "ds_read_b64_tr_b16 a[88:89], v221 offset:128\n\t"
      "v_mfma_f32_16x16x32_bf16 v[132:135], a[44:47], v[156:159], v[132:135]\n\t"
      "ds_read_b64_tr_b16 a[92:93], v221 offset:136\n\t"
      "ds_read_b128 v[140:143], v199 offset:0\n\t"
      "s_waitcnt lgkmcnt(8)\n\t"
      "v_mfma_f32_16x16x32_bf16 v[80:83], a[48:51], v[148:151], v[80:83]\n\t"
      "s_add_u32 m0, s41, 0x18000\n\t"
      "s_mov_b32 s51, 0x24000\n\t"
      "buffer_load_dwordx4 v211, s[16:19], s51 offen lds\n\t"
      "v_mfma_f32_16x16x32_bf16 v[84:87], a[52:55], v[148:151], v[84:87]\n\t"
      "buffer_load_dwordx4 v212, s[16:19], s51 offen offset:1024 lds\n\t"
      "v_mfma_f32_16x16x32_bf16 v[88:91], a[56:59], v[148:151], v[88:91]\n\t"
      "buffer_load_dwordx4 v213, s[16:19], s51 offen offset:2048 lds\n\t"
      "s_waitcnt vmcnt(15)\n\t"
      "v_add_u32_e32 v221, 0x1e000, v214\n\t"
      "ds_read_b64_tr_b16 a[74:75], v221 offset:0\n\t"
      "v_mfma_f32_16x16x32_bf16 v[92:95], a[60:63], v[148:151], v[92:95]\n\t"
      "ds_read_b64_tr_b16 a[78:79], v221 offset:8\n\t"
      "ds_read_b64_tr_b16 a[82:83], v221 offset:64\n\t"
      "v_mfma_f32_16x16x32_bf16 v[96:99], a[64:67], v[148:151], v[96:99]\n\t"
      "ds_read_b64_tr_b16 a[86:87], v221 offset:72\n\t"
      "ds_read_b64_tr_b16 a[90:91], v221 offset:128\n\t"
      "v_mfma_f32_16x16x32_bf16 v[100:103], a[68:71], v[148:151], v[100:103]\n\t"
      "ds_read_b64_tr_b16 a[94:95], v221 offset:136\n\t"
      "ds_read_b128 v[152:155], v199 offset:12288\n\t"
      "s_waitcnt lgkmcnt(8)\n\t"
      "v_mfma_f32_16x16x32_bf16 v[104:107], a[48:51], v[160:163], v[104:107]\n\t"
      "s_add_u32 m0, s41, 0x1b000\n\t"
      "s_mov_b32 s51, 0x24c00\n\t"
      "buffer_load_dwordx4 v211, s[16:19], s51 offen lds\n\t"
      "v_mfma_f32_16x16x32_bf16 v[108:111], a[52:55], v[160:163], v[108:111]\n\t"
      "buffer_load_dwordx4 v212, s[16:19], s51 offen offset:1024 lds\n\t"
      "v_mfma_f32_16x16x32_bf16 v[112:115], a[56:59], v[160:163], v[112:115]\n\t"
      "buffer_load_dwordx4 v213, s[16:19], s51 offen offset:2048 lds\n\t"
      "s_waitcnt vmcnt(15)\n\t"
      "v_add_u32_e32 v221, 0x21000, v214\n\t"
      "ds_read_b64_tr_b16 a[96:97], v221 offset:0\n\t"
      "v_mfma_f32_16x16x32_bf16 v[116:119], a[60:63], v[160:163], v[116:119]\n\t"
      "ds_read_b64_tr_b16 a[100:101], v221 offset:8\n\t"
      "ds_read_b64_tr_b16 a[104:105], v221 offset:64\n\t"
      "v_mfma_f32_16x16x32_bf16 v[120:123], a[64:67], v[160:163], v[120:123]\n\t"
      "ds_read_b64_tr_b16 a[108:109], v221 offset:72\n\t"
      "ds_read_b64_tr_b16 a[112:113], v221 offset:128\n\t"
      "v_mfma_f32_16x16x32_bf16 v[132:135], a[68:71], v[160:163], v[132:135]\n\t"
      "ds_read_b64_tr_b16 a[116:117], v221 offset:136\n\t"
      "ds_read_b128 v[144:147], v197 offset:256\n\t"
      "s_waitcnt lgkmcnt(8)\n\t"
      "v_mfma_f32_16x16x32_bf16 v[80:83], a[72:75], v[140:143], v[80:83]\n\t"
      "s_add_u32 m0, s41, 0x1e000\n\t"
      "s_mov_b32 s51, 0x2a000\n\t"
      "buffer_load_dwordx4 v211, s[16:19], s51 offen lds\n\t"
      "v_mfma_f32_16x16x32_bf16 v[84:87], a[76:79], v[140:143], v[84:87]\n\t"
      "buffer_load_dwordx4 v212, s[16:19], s51 offen offset:1024 lds\n\t"
      "v_mfma_f32_16x16x32_bf16 v[88:91], a[80:83], v[140:143], v[88:91]\n\t"
      "buffer_load_dwordx4 v213, s[16:19], s51 offen offset:2048 lds\n\t"
      "s_waitcnt vmcnt(15)\n\t"
      "v_add_u32_e32 v221, 0x24000, v214\n\t"
      "ds_read_b64_tr_b16 a[98:99], v221 offset:0\n\t"
      "v_mfma_f32_16x16x32_bf16 v[92:95], a[84:87], v[140:143], v[92:95]\n\t"
      "ds_read_b64_tr_b16 a[102:103], v221 offset:8\n\t"
      "ds_read_b64_tr_b16 a[106:107], v221 offset:64\n\t"
      "v_mfma_f32_16x16x32_bf16 v[96:99], a[88:91], v[140:143], v[96:99]\n\t"
      "ds_read_b64_tr_b16 a[110:111], v221 offset:72\n\t"
      "ds_read_b64_tr_b16 a[114:115], v221 offset:128\n\t"
      "v_mfma_f32_16x16x32_bf16 v[100:103], a[92:95], v[140:143], v[100:103]\n\t"
      "ds_read_b64_tr_b16 a[118:119], v221 offset:136\n\t"
      "ds_read_b128 v[156:159], v197 offset:12544\n\t"
      "s_waitcnt lgkmcnt(8)\n\t"
      "v_mfma_f32_16x16x32_bf16 v[104:107], a[72:75], v[152:155], v[104:107]\n\t"
      "s_add_u32 m0, s41, 0x21000\n\t"
      "s_mov_b32 s51, 0x2ac00\n\t"
      "buffer_load_dwordx4 v211, s[16:19], s51 offen lds\n\t"
      "v_mfma_f32_16x16x32_bf16 v[108:111], a[76:79], v[152:155], v[108:111]\n\t"
      "buffer_load_dwordx4 v212, s[16:19], s51 offen offset:1024 lds\n\t"
      "v_mfma_f32_16x16x32_bf16 v[112:115], a[80:83], v[152:155], v[112:115]\n\t"
      "buffer_load_dwordx4 v213, s[16:19], s51 offen offset:2048 lds\n\t"
      "s_waitcnt vmcnt(15)\n\t"
      "v_add_u32_e32 v221, 0x12000, v214\n\t"
      "ds_read_b64_tr_b16 a[120:121], v221 offset:0\n\t"
      "v_mfma_f32_16x16x32_bf16 v[116:119], a[84:87], v[152:155], v[116:119]\n\t"
      "ds_read_b64_tr_b16 a[124:125], v221 offset:8\n\t"
      "ds_read_b64_tr_b16 a[128:129], v221 offset:64\n\t"
      "v_mfma_f32_16x16x32_bf16 v[120:123], a[88:91], v[152:155], v[120:123]\n\t"
      "ds_read_b64_tr_b16 a[132:133], v221 offset:72\n\t"
      "ds_read_b64_tr_b16 a[136:137], v221 offset:128\n\t"
      "v_mfma_f32_16x16x32_bf16 v[132:135], a[92:95], v[152:155], v[132:135]\n\t"
      "ds_read_b64_tr_b16 a[140:141], v221 offset:136\n\t"
      "ds_read_b128 v[148:151], v198 offset:256\n\t"
      "s_waitcnt lgkmcnt(8)\n\t"
      "v_mfma_f32_16x16x32_bf16 v[80:83], a[96:99], v[144:147], v[80:83]\n\t"
      "s_add_u32 m0, s41, 0x24000\n\t"
      "s_mov_b32 s51, 0x30000\n\t"
      "buffer_load_dwordx4 v211, s[16:19], s51 offen lds\n\t"
      "v_mfma_f32_16x16x32_bf16 v[84:87], a[100:103], v[144:147], v[84:87]\n\t"
      "buffer_load_dwordx4 v212, s[16:19], s51 offen offset:1024 lds\n\t"
      "v_mfma_f32_16x16x32_bf16 v[88:91], a[104:107], v[144:147], v[88:91]\n\t"
      "buffer_load_dwordx4 v213, s[16:19], s51 offen offset:2048 lds\n\t"
      "s_waitcnt vmcnt(15)\n\t"
      "v_add_u32_e32 v221, 0x15000, v214\n\t"
      "ds_read_b64_tr_b16 a[122:123], v221 offset:0\n\t"
      "v_mfma_f32_16x16x32_bf16 v[92:95], a[108:111], v[144:147], v[92:95]\n\t"
      "ds_read_b64_tr_b16 a[126:127], v221 offset:8\n\t"
      "ds_read_b64_tr_b16 a[130:131], v221 offset:64\n\t"
      "v_mfma_f32_16x16x32_bf16 v[96:99], a[112:115], v[144:147], v[96:99]\n\t"
      "ds_read_b64_tr_b16 a[134:135], v221 offset:72\n\t"
      "ds_read_b64_tr_b16 a[138:139], v221 offset:128\n\t"
      "v_mfma_f32_16x16x32_bf16 v[100:103], a[116:119], v[144:147], v[100:103]\n\t"
      "ds_read_b64_tr_b16 a[142:143], v221 offset:136\n\t"
      "ds_read_b128 v[160:163], v198 offset:12544\n\t"
      "s_waitcnt lgkmcnt(8)\n\t"
      "v_mfma_f32_16x16x32_bf16 v[104:107], a[96:99], v[156:159], v[104:107]\n\t"
      "s_add_u32 m0, s41, 0x12000\n\t"
      "s_mov_b32 s51, 0x30c00\n\t"
      "buffer_load_dwordx4 v211, s[16:19], s51 offen lds\n\t"
      "v_mfma_f32_16x16x32_bf16 v[108:111], a[100:103], v[156:159], v[108:111]\n\t"
      "buffer_load_dwordx4 v212, s[16:19], s51 offen offset:1024 lds\n\t"
      "v_mfma_f32_16x16x32_bf16 v[112:115], a[104:107], v[156:159], v[112:115]\n\t"
      "buffer_load_dwordx4 v213, s[16:19], s51 offen offset:2048 lds\n\t"
      "s_waitcnt vmcnt(15)\n\t"
      "v_add_u32_e32 v221, 0x18000, v214\n\t"
      "ds_read_b64_tr_b16 a[144:145], v221 offset:0\n\t"
      "v_mfma_f32_16x16x32_bf16 v[116:119], a[108:111], v[156:159], v[116:119]\n\t"
      "ds_read_b64_tr_b16 a[148:149], v221 offset:8\n\t"
      "ds_read_b64_tr_b16 a[152:153], v221 offset:64\n\t"
      "v_mfma_f32_16x16x32_bf16 v[120:123], a[112:115], v[156:159], v[120:123]\n\t"
      "ds_read_b64_tr_b16 a[156:157], v221 offset:72\n\t"
      "ds_read_b64_tr_b16 a[160:161], v221 offset:128\n\t"
      "v_mfma_f32_16x16x32_bf16 v[132:135], a[116:119], v[156:159], v[132:135]\n\t"
      "ds_read_b64_tr_b16 a[164:165], v221 offset:136\n\t"
      "ds_read_b128 v[140:143], v199 offset:256\n\t"
      "s_waitcnt lgkmcnt(8)\n\t"
      "v_mfma_f32_16x16x32_bf16 v[80:83], a[120:123], v[148:151], v[80:83]\n\t"
      "s_add_u32 m0, s41, 0x15000\n\t"
      "s_mov_b32 s51, 0x36000\n\t"
      "buffer_load_dwordx4 v211, s[16:19], s51 offen lds\n\t"
      "v_mfma_f32_16x16x32_bf16 v[84:87], a[124:127], v[148:151], v[84:87]\n\t"
      "buffer_load_dwordx4 v212, s[16:19], s51 offen offset:1024 lds\n\t"
      "v_mfma_f32_16x16x32_bf16 v[88:91], a[128:131], v[148:151], v[88:91]\n\t"
      "buffer_load_dwordx4 v213, s[16:19], s51 offen offset:2048 lds\n\t"
      "s_waitcnt vmcnt(15)\n\t"
      "v_add_u32_e32 v221, 0x1b000, v214\n\t"
      "ds_read_b64_tr_b16 a[146:147], v221 offset:0\n\t"
      "v_mfma_f32_16x16x32_bf16 v[92:95], a[132:135], v[148:151], v[92:95]\n\t"
      "ds_read_b64_tr_b16 a[150:151], v221 offset:8\n\t"
      "ds_read_b64_tr_b16 a[154:155], v221 offset:64\n\t"
      "v_mfma_f32_16x16x32_bf16 v[96:99], a[136:139], v[148:151], v[96:99]\n\t"
      "ds_read_b64_tr_b16 a[158:159], v221 offset:72\n\t"
      "ds_read_b64_tr_b16 a[162:163], v221 offset:128\n\t"
      "v_mfma_f32_16x16x32_bf16 v[100:103], a[140:143], v[148:151], v[100:103]\n\t"
      "ds_read_b64_tr_b16 a[166:167], v221 offset:136\n\t"
      "ds_read_b128 v[152:155], v199 offset:12544\n\t"
      "s_waitcnt lgkmcnt(8)\n\t"
      "v_mfma_f32_16x16x32_bf16 v[104:107], a[120:123], v[160:163], v[104:107]\n\t"
      "s_add_u32 m0, s41, 0x18000\n\t"
      "s_mov_b32 s51, 0x36c00\n\t"
      "buffer_load_dwordx4 v211, s[16:19], s51 offen lds\n\t"
      "v_mfma_f32_16x16x32_bf16 v[108:111], a[124:127], v[160:163], v[108:111]\n\t"
      "buffer_load_dwordx4 v212, s[16:19], s51 offen offset:1024 lds\n\t"
      "v_mfma_f32_16x16x32_bf16 v[112:115], a[128:131], v[160:163], v[112:115]\n\t"
      "buffer_load_dwordx4 v213, s[16:19], s51 offen offset:2048 lds\n\t"
      "s_waitcnt vmcnt(15)\n\t"
      "v_add_u32_e32 v221, 0x1e000, v214\n\t"
      "ds_read_b64_tr_b16 a[168:169], v221 offset:0\n\t"
      "v_mfma_f32_16x16x32_bf16 v[116:119], a[132:135], v[160:163], v[116:119]\n\t"
      "ds_read_b64_tr_b16 a[172:173], v221 offset:8\n\t"
      "ds_read_b64_tr_b16 a[176:177], v221 offset:64\n\t"
      "v_mfma_f32_16x16x32_bf16 v[120:123], a[136:139], v[160:163], v[120:123]\n\t"
      "ds_read_b64_tr_b16 a[180:181], v221 offset:72\n\t"
      "ds_read_b64_tr_b16 a[184:185], v221 offset:128\n\t"
      "v_mfma_f32_16x16x32_bf16 v[132:135], a[140:143], v[160:163], v[132:135]\n\t"
      "ds_read_b64_tr_b16 a[188:189], v221 offset:136\n\t"
      "ds_read_b128 v[144:147], v196 offset:256\n\t"
      "s_waitcnt lgkmcnt(8)\n\t"
      "v_mfma_f32_16x16x32_bf16 v[80:83], a[144:147], v[140:143], v[80:83]\n\t"
      "s_add_u32 m0, s41, 0x1b000\n\t"
      "s_mov_b32 s51, 0x3c000\n\t"
      "buffer_load_dwordx4 v211, s[16:19], s51 offen lds\n\t"
      "v_mfma_f32_16x16x32_bf16 v[84:87], a[148:151], v[140:143], v[84:87]\n\t"
      "buffer_load_dwordx4 v212, s[16:19], s51 offen offset:1024 lds\n\t"
      "v_mfma_f32_16x16x32_bf16 v[88:91], a[152:155], v[140:143], v[88:91]\n\t"
      "buffer_load_dwordx4 v213, s[16:19], s51 offen offset:2048 lds\n\t"
      "s_waitcnt vmcnt(15)\n\t"
      "v_add_u32_e32 v221, 0x21000, v214\n\t"
      "ds_read_b64_tr_b16 a[170:171], v221 offset:0\n\t"
      "v_mfma_f32_16x16x32_bf16 v[92:95], a[156:159], v[140:143], v[92:95]\n\t"
      "ds_read_b64_tr_b16 a[174:175], v221 offset:8\n\t"
      "ds_read_b64_tr_b16 a[178:179], v221 offset:64\n\t"
      "v_mfma_f32_16x16x32_bf16 v[96:99], a[160:163], v[140:143], v[96:99]\n\t"
      "ds_read_b64_tr_b16 a[182:183], v221 offset:72\n\t"
      "ds_read_b64_tr_b16 a[186:187], v221 offset:128\n\t"
      "v_mfma_f32_16x16x32_bf16 v[100:103], a[164:167], v[140:143], v[100:103]\n\t"
      "ds_read_b64_tr_b16 a[190:191], v221 offset:136\n\t"
      "ds_read_b128 v[156:159], v196 offset:12544\n\t"
      "s_waitcnt lgkmcnt(8)\n\t"
      "v_mfma_f32_16x16x32_bf16 v[104:107], a[144:147], v[152:155], v[104:107]\n\t"
      "s_add_u32 m0, s41, 0x1e000\n\t"
      "s_mov_b32 s51, 0x3cc00\n\t"
      "buffer_load_dwordx4 v211, s[16:19], s51 offen lds\n\t"
      "v_mfma_f32_16x16x32_bf16 v[108:111], a[148:151], v[152:155], v[108:111]\n\t"
      "buffer_load_dwordx4 v212, s[16:19], s51 offen offset:1024 lds\n\t"
      "v_mfma_f32_16x16x32_bf16 v[112:115], a[152:155], v[152:155], v[112:115]\n\t"
      "buffer_load_dwordx4 v213, s[16:19], s51 offen offset:2048 lds\n\t"
      "s_waitcnt vmcnt(15)\n\t"
      "v_add_u32_e32 v221, 0x24000, v214\n\t"
      "ds_read_b64_tr_b16 a[192:193], v221 offset:0\n\t"
      "v_mfma_f32_16x16x32_bf16 v[116:119], a[156:159], v[152:155], v[116:119]\n\t"
      "ds_read_b64_tr_b16 a[196:197], v221 offset:8\n\t"
      "ds_read_b64_tr_b16 a[200:201], v221 offset:64\n\t"
      "v_mfma_f32_16x16x32_bf16 v[120:123], a[160:163], v[152:155], v[120:123]\n\t"
      "ds_read_b64_tr_b16 a[204:205], v221 offset:72\n\t"
      "ds_read_b64_tr_b16 a[208:209], v221 offset:128\n\t"
      "v_mfma_f32_16x16x32_bf16 v[132:135], a[164:167], v[152:155], v[132:135]\n\t"
      "ds_read_b64_tr_b16 a[212:213], v221 offset:136\n\t"
      "ds_read_b128 v[148:151], v198 offset:512\n\t"
      "s_waitcnt lgkmcnt(8)\n\t"
      "v_mfma_f32_16x16x32_bf16 v[80:83], a[168:171], v[144:147], v[80:83]\n\t"
      "s_add_u32 m0, s41, 0x21000\n\t"
      "s_mov_b32 s51, 0x42000\n\t"
      "buffer_load_dwordx4 v211, s[16:19], s51 offen lds\n\t"
      "v_mfma_f32_16x16x32_bf16 v[84:87], a[172:175], v[144:147], v[84:87]\n\t"
      "buffer_load_dwordx4 v212, s[16:19], s51 offen offset:1024 lds\n\t"
      "v_mfma_f32_16x16x32_bf16 v[88:91], a[176:179], v[144:147], v[88:91]\n\t"
      "buffer_load_dwordx4 v213, s[16:19], s51 offen offset:2048 lds\n\t"
      "s_waitcnt vmcnt(15)\n\t"
      "v_add_u32_e32 v221, 0x12000, v214\n\t"
      "ds_read_b64_tr_b16 a[194:195], v221 offset:0\n\t"
      "v_mfma_f32_16x16x32_bf16 v[92:95], a[180:183], v[144:147], v[92:95]\n\t"
      "ds_read_b64_tr_b16 a[198:199], v221 offset:8\n\t"
      "ds_read_b64_tr_b16 a[202:203], v221 offset:64\n\t"
      "v_mfma_f32_16x16x32_bf16 v[96:99], a[184:187], v[144:147], v[96:99]\n\t"
      "ds_read_b64_tr_b16 a[206:207], v221 offset:72\n\t"
      "ds_read_b64_tr_b16 a[210:211], v221 offset:128\n\t"
      "v_mfma_f32_16x16x32_bf16 v[100:103], a[188:191], v[144:147], v[100:103]\n\t"
      "ds_read_b64_tr_b16 a[214:215], v221 offset:136\n\t"
      "ds_read_b128 v[160:163], v198 offset:12800\n\t"
      "s_waitcnt lgkmcnt(8)\n\t"
      "v_mfma_f32_16x16x32_bf16 v[104:107], a[168:171], v[156:159], v[104:107]\n\t"
      "s_add_u32 m0, s41, 0x24000\n\t"
      "s_mov_b32 s51, 0x42c00\n\t"
      "buffer_load_dwordx4 v211, s[16:19], s51 offen lds\n\t"
      "v_mfma_f32_16x16x32_bf16 v[108:111], a[172:175], v[156:159], v[108:111]\n\t"
      "buffer_load_dwordx4 v212, s[16:19], s51 offen offset:1024 lds\n\t"
      "v_mfma_f32_16x16x32_bf16 v[112:115], a[176:179], v[156:159], v[112:115]\n\t"
      "buffer_load_dwordx4 v213, s[16:19], s51 offen offset:2048 lds\n\t"
      "s_waitcnt vmcnt(15)\n\t"
      "v_add_u32_e32 v221, 0x15000, v214\n\t"
      "ds_read_b64_tr_b16 a[216:217], v221 offset:0\n\t"
      "v_mfma_f32_16x16x32_bf16 v[116:119], a[180:183], v[156:159], v[116:119]\n\t"
      "ds_read_b64_tr_b16 a[220:221], v221 offset:8\n\t"
      "ds_read_b64_tr_b16 a[224:225], v221 offset:64\n\t"
      "v_mfma_f32_16x16x32_bf16 v[120:123], a[184:187], v[156:159], v[120:123]\n\t"
      "ds_read_b64_tr_b16 a[228:229], v221 offset:72\n\t"
      "ds_read_b64_tr_b16 a[232:233], v221 offset:128\n\t"
      "v_mfma_f32_16x16x32_bf16 v[132:135], a[188:191], v[156:159], v[132:135]\n\t"
      "ds_read_b64_tr_b16 a[236:237], v221 offset:136\n\t"
      "ds_read_b128 v[140:143], v199 offset:512\n\t"
      "s_waitcnt lgkmcnt(8)\n\t"
      "v_mfma_f32_16x16x32_bf16 v[80:83], a[192:195], v[148:151], v[80:83]\n\t"
      "s_cmp_eq_u32 s94, 0\n\t"
      "s_cbranch_scc0 .Lc1_%=\n\t"
      "s_add_u32 m0, s41, 0x6000\n\t"
      "s_add_u32 s51, s40, 0x600000\n\t"
      "buffer_load_dwordx4 v208, s[20:23], s51 offen lds\n\t"
      "buffer_load_dwordx4 v208, s[20:23], s51 offen offset:1024 lds\n\t"
      "buffer_load_dwordx4 v208, s[20:23], s51 offen offset:2048 lds\n\t"
      ".Lc1_%=:\n\t"
      "v_mfma_f32_16x16x32_bf16 v[84:87], a[196:199], v[148:151], v[84:87]\n\t"
      "v_mfma_f32_16x16x32_bf16 v[88:91], a[200:203], v[148:151], v[88:91]\n\t"
      "s_waitcnt vmcnt(15)\n\t"
      "s_cmp_eq_u32 s94, 1\n\t"
      "s_cbranch_scc0 .Lwpw19_1_%=\n\t"
      "s_waitcnt vmcnt(12)\n\t"
      ".Lwpw19_1_%=:\n\t"
      "v_add_u32_e32 v221, 0x18000, v214\n\t"
      "ds_read_b64_tr_b16 a[218:219], v221 offset:0\n\t"
      "v_mfma_f32_16x16x32_bf16 v[92:95], a[204:207], v[148:151], v[92:95]\n\t"
      "ds_read_b64_tr_b16 a[222:223], v221 offset:8\n\t"
      "ds_read_b64_tr_b16 a[226:227], v221 offset:64\n\t"
      "v_mfma_f32_16x16x32_bf16 v[96:99], a[208:211], v[148:151], v[96:99]\n\t"
      "ds_read_b64_tr_b16 a[230:231], v221 offset:72\n\t"
      "ds_read_b64_tr_b16 a[234:235], v221 offset:128\n\t"
      "v_mfma_f32_16x16x32_bf16 v[100:103], a[212:215], v[148:151], v[100:103]\n\t"
      "ds_read_b64_tr_b16 a[238:239], v221 offset:136\n\t"
      "ds_read_b128 v[152:155], v199 offset:12800\n\t"
      "s_waitcnt lgkmcnt(14)\n\t"
      "v_mfma_f32_16x16x32_bf16 v[104:107], a[192:195], v[160:163], v[104:107]\n\t"
      "s_cmp_eq_u32 s94, 0\n\t"
      "s_cbranch_scc0 .Lc2_%=\n\t"
      "s_add_u32 m0, s41, 0x9000\n\t"
      "s_add_u32 s51, s40, 0x600000\n\t"
      "buffer_load_dwordx4 v209, s[20:23], s51 offen lds\n\t"
      "buffer_load_dwordx4 v209, s[20:23], s51 offen offset:1024 lds\n\t"
      "buffer_load_dwordx4 v209, s[20:23], s51 offen offset:2048 lds\n\t"
      ".Lc2_%=:\n\t"
      "v_mfma_f32_16x16x32_bf16 v[108:111], a[196:199], v[160:163], v[108:111]\n\t"
      "v_mfma_f32_16x16x32_bf16 v[112:115], a[200:203], v[160:163], v[112:115]\n\t"
      "s_waitcnt vmcnt(15)\n\t"
      "s_cmp_eq_u32 s94, 1\n\t"
      "s_cbranch_scc0 .Lwpw20_1_%=\n\t"
      "s_waitcnt vmcnt(9)\n\t"
      ".Lwpw20_1_%=:\n\t"
      "v_add_u32_e32 v221, 0x1b000, v214\n\t"
      "ds_read_b64_tr_b16 a[240:241], v221 offset:0\n\t"
      "v_mfma_f32_16x16x32_bf16 v[116:119], a[204:207], v[160:163], v[116:119]\n\t"
      "ds_read_b64_tr_b16 a[244:245], v221 offset:8\n\t"
      "ds_read_b64_tr_b16 a[248:249], v221 offset:64\n\t"
      "v_mfma_f32_16x16x32_bf16 v[120:123], a[208:211], v[160:163], v[120:123]\n\t"
      "ds_read_b64_tr_b16 a[252:253], v221 offset:72\n\t"
      "ds_read_b64_tr_b16 v[164:165], v221 offset:128\n\t"
      "v_mfma_f32_16x16x32_bf16 v[132:135], a[212:215], v[160:163], v[132:135]\n\t"
      "ds_read_b64_tr_b16 v[168:169], v221 offset:136\n\t"
      "ds_read_b128 v[144:147], v196 offset:512\n\t"
      "s_waitcnt lgkmcnt(8)\n\t"
      "v_mfma_f32_16x16x32_bf16 v[80:83], a[216:219], v[140:143], v[80:83]\n\t"
      "s_cmp_eq_u32 s94, 1\n\t"
      "s_cbranch_scc0 .Lc3_%=\n\t"
      "s_add_u32 m0, s41, 0xc000\n\t"
      "s_add_u32 s51, s40, 0xc00000\n\t"
      "buffer_load_dwordx4 v208, s[20:23], s51 offen lds\n\t"
      "buffer_load_dwordx4 v208, s[20:23], s51 offen offset:1024 lds\n\t"
      "buffer_load_dwordx4 v208, s[20:23], s51 offen offset:2048 lds\n\t"
      ".Lc3_%=:\n\t"
      "v_mfma_f32_16x16x32_bf16 v[84:87], a[220:223], v[140:143], v[84:87]\n\t"
      "v_mfma_f32_16x16x32_bf16 v[88:91], a[224:227], v[140:143], v[88:91]\n\t"
      "s_waitcnt vmcnt(12)\n\t"
      "s_cmp_eq_u32 s94, 1\n\t"
      "s_cbranch_scc0 .Lwpw21_1_%=\n\t"
      "s_waitcnt vmcnt(9)\n\t"
      ".Lwpw21_1_%=:\n\t"
      "v_add_u32_e32 v221, 0x1e000, v214\n\t"
      "ds_read_b64_tr_b16 a[242:243], v221 offset:0\n\t"
      "v_mfma_f32_16x16x32_bf16 v[92:95], a[228:231], v[140:143], v[92:95]\n\t"
      "ds_read_b64_tr_b16 a[246:247], v221 offset:8\n\t"
      "ds_read_b64_tr_b16 a[250:251], v221 offset:64\n\t"
      "v_mfma_f32_16x16x32_bf16 v[96:99], a[232:235], v[140:143], v[96:99]\n\t"
      "ds_read_b64_tr_b16 a[254:255], v221 offset:72\n\t"
      "ds_read_b64_tr_b16 v[166:167], v221 offset:128\n\t"
      "v_mfma_f32_16x16x32_bf16 v[100:103], a[236:239], v[140:143], v[100:103]\n\t"
      "ds_read_b64_tr_b16 v[170:171], v221 offset:136\n\t"
      "ds_read_b128 v[156:159], v196 offset:12800\n\t"
      "s_waitcnt lgkmcnt(14)\n\t"
      "v_mfma_f32_16x16x32_bf16 v[104:107], a[216:219], v[152:155], v[104:107]\n\t"
      "s_cmp_eq_u32 s94, 1\n\t"
      "s_cbranch_scc0 .Lc4_%=\n\t"
      "s_add_u32 m0, s41, 0xf000\n\t"
      "s_add_u32 s51, s40, 0xc00000\n\t"
      "buffer_load_dwordx4 v209, s[20:23], s51 offen lds\n\t"
      "buffer_load_dwordx4 v209, s[20:23], s51 offen offset:1024 lds\n\t"
      "buffer_load_dwordx4 v209, s[20:23], s51 offen offset:2048 lds\n\t"
      ".Lc4_%=:\n\t"
      "v_mfma_f32_16x16x32_bf16 v[108:111], a[220:223], v[152:155], v[108:111]\n\t"
      "v_mfma_f32_16x16x32_bf16 v[112:115], a[224:227], v[152:155], v[112:115]\n\t"
      "s_waitcnt vmcnt(9)\n\t"
      "v_add_u32_e32 v221, 0x21000, v214\n\t"
      "ds_read_b64_tr_b16 v[172:173], v221 offset:0\n\t"
      "v_mfma_f32_16x16x32_bf16 v[116:119], a[228:231], v[152:155], v[116:119]\n\t"
      "ds_read_b64_tr_b16 v[176:177], v221 offset:8\n\t"
      "ds_read_b64_tr_b16 v[180:181], v221 offset:64\n\t"
      "ds_read_b64_tr_b16 v[184:185], v221 offset:72\n\t"
      "v_mfma_f32_16x16x32_bf16 v[120:123], a[232:235], v[152:155], v[120:123]\n\t"
      "ds_read_b64_tr_b16 v[188:189], v221 offset:128\n\t"
      "ds_read_b64_tr_b16 v[192:193], v221 offset:136\n\t"
      "v_mfma_f32_16x16x32_bf16 v[132:135], a[236:239], v[152:155], v[132:135]\n\t"
      "ds_read_b128 v[148:151], v197 offset:512\n\t"
      "ds_read_b128 v[160:163], v197 offset:12800\n\t"
      "s_waitcnt lgkmcnt(9)\n\t"
      "v_mfma_f32_16x16x32_bf16 v[80:83], a[240:243], v[144:147], v[80:83]\n\t"
      "v_mfma_f32_16x16x32_bf16 v[84:87], a[244:247], v[144:147], v[84:87]\n\t"
      "v_mfma_f32_16x16x32_bf16 v[88:91], a[248:251], v[144:147], v[88:91]\n\t"
      "s_waitcnt vmcnt(6)\n\t"
      "v_add_u32_e32 v221, 0x24000, v214\n\t"
      "ds_read_b64_tr_b16 v[174:175], v221 offset:0\n\t"
      "v_mfma_f32_16x16x32_bf16 v[92:95], a[252:255], v[144:147], v[92:95]\n\t"
      "ds_read_b64_tr_b16 v[178:179], v221 offset:8\n\t"
      "ds_read_b64_tr_b16 v[182:183], v221 offset:64\n\t"
      "v_mfma_f32_16x16x32_bf16 v[96:99], v[164:167], v[144:147], v[96:99]\n\t"
      "ds_read_b64_tr_b16 v[186:187], v221 offset:72\n\t"
      "ds_read_b64_tr_b16 v[190:191], v221 offset:128\n\t"
      "v_mfma_f32_16x16x32_bf16 v[100:103], v[168:171], v[144:147], v[100:103]\n\t"
      "ds_read_b64_tr_b16 v[194:195], v221 offset:136\n\t"
      ".Lcq_%=:\n\t"
      "s_waitcnt lgkmcnt(14)\n\t"
      "v_mfma_f32_16x16x32_bf16 v[104:107], a[240:243], v[156:159], v[104:107]\n\t"
      "v_add_u32_e32 v196, s46, v200\n\t"
      "v_add_u32_e32 v197, s46, v201\n\t"
      "v_mfma_f32_16x16x32_bf16 v[108:111], a[244:247], v[156:159], v[108:111]\n\t"
      "v_mfma_f32_16x16x32_bf16 v[112:115], a[248:251], v[156:159], v[112:115]\n\t"
      "v_mfma_f32_16x16x32_bf16 v[116:119], a[252:255], v[156:159], v[116:119]\n\t"
      "v_add_u32_e32 v198, s46, v202\n\t"
      "v_add_u32_e32 v199, s46, v203\n\t"
      "v_mfma_f32_16x16x32_bf16 v[120:123], v[164:167], v[156:159], v[120:123]\n\t"
      "v_mfma_f32_16x16x32_bf16 v[132:135], v[168:171], v[156:159], v[132:135]\n\t"
      "s_waitcnt lgkmcnt(0)\n\t"
      "v_mfma_f32_16x16x32_bf16 v[80:83], v[172:175], v[148:151], v[80:83]\n\t"
      "s_cmp_eq_u32 s94, 0\n\t"
      "s_cbranch_scc0 .Lc5_%=\n\t"
      "s_add_u32 m0, s41, 0xc000\n\t"
      "s_add_u32 s51, s40, 0xc00000\n\t"
      "buffer_load_dwordx4 v208, s[20:23], s51 offen lds\n\t"
      "buffer_load_dwordx4 v208, s[20:23], s51 offen offset:1024 lds\n\t"
      "buffer_load_dwordx4 v208, s[20:23], s51 offen offset:2048 lds\n\t"
      ".Lc5_%=:\n\t"
      "v_mfma_f32_16x16x32_bf16 v[84:87], v[176:179], v[148:151], v[84:87]\n\t"
      "v_mfma_f32_16x16x32_bf16 v[88:91], v[180:183], v[148:151], v[88:91]\n\t"
      "v_mfma_f32_16x16x32_bf16 v[92:95], v[184:187], v[148:151], v[92:95]\n\t"
      "v_mfma_f32_16x16x32_bf16 v[96:99], v[188:191], v[148:151], v[96:99]\n\t"
      "v_mfma_f32_16x16x32_bf16 v[100:103], v[192:195], v[148:151], v[100:103]\n\t"
      "s_cmp_eq_u32 s94, 0\n\t"
      "s_cbranch_scc0 .Lc6_%=\n\t"
      "s_add_u32 m0, s41, 0xf000\n\t"
      "s_add_u32 s51, s40, 0xc00000\n\t"
      "buffer_load_dwordx4 v209, s[20:23], s51 offen lds\n\t"
      "buffer_load_dwordx4 v209, s[20:23], s51 offen offset:1024 lds\n\t"
      "buffer_load_dwordx4 v209, s[20:23], s51 offen offset:2048 lds\n\t"
      ".Lc6_%=:\n\t"
      "s_mov_b64 s[96:97], s[34:35]\n\t"
      "s_movk_i32 s98, 0x148\n\t"
      "s_mov_b32 s99, s23\n\t"
      "buffer_load_dwordx3 v[252:254], v207, s[96:99], 0 offen offset:40\n\t"
      "buffer_load_dwordx2 v[240:241], v245, s[24:27], 0 offen sc1\n\t"
      "s_cmp_eq_u32 s94, 0\n\t"
      "s_cbranch_scc0 .Lwpx2_0_%=\n\t"
      "s_waitcnt vmcnt(11)\n\t"
      ".Lwpx2_0_%=:\n\t"
      "s_barrier\n\t"
      "ds_read_b128 v[32:35], v196 offset:0\n\t"
      "ds_read_b128 v[36:39], v197 offset:0\n\t"
      "v_mfma_f32_16x16x32_bf16 v[104:107], v[172:175], v[160:163], v[104:107]\n\t"
      "ds_read_b128 v[40:43], v198 offset:0\n\t"
      "ds_read_b128 v[44:47], v199 offset:0\n\t"
      "v_mfma_f32_16x16x32_bf16 v[108:111], v[176:179], v[160:163], v[108:111]\n\t"
      "ds_read_b128 v[48:51], v197 offset:256\n\t"
      "ds_read_b128 v[52:55], v198 offset:256\n\t"
      "v_mfma_f32_16x16x32_bf16 v[112:115], v[180:183], v[160:163], v[112:115]\n\t"
      "ds_read_b128 v[56:59], v199 offset:256\n\t"
      "ds_read_b128 v[60:63], v196 offset:256\n\t"
      "v_mfma_f32_16x16x32_bf16 v[116:119], v[184:187], v[160:163], v[116:119]\n\t"
      "ds_read_b128 v[64:67], v198 offset:512\n\t"
      "ds_read_b128 v[68:71], v199 offset:512\n\t"
      "v_mfma_f32_16x16x32_bf16 v[120:123], v[188:191], v[160:163], v[120:123]\n\t"
      "ds_read_b128 v[72:75], v196 offset:512\n\t"
      "ds_read_b128 v[76:79], v197 offset:512\n\t"
      "v_mfma_f32_16x16x32_bf16 v[132:135], v[192:195], v[160:163], v[132:135]\n\t"
      "s_waitcnt lgkmcnt(8)\n\t"
      "v_mfma_f32_16x16x32_bf16 v[128:131], a[0:3], v[32:35], v[8:11]\n\t"
      "v_cvt_pk_bf16_f32 v80, v80, v81\n\t"
      "v_cvt_pk_bf16_f32 v81, v82, v83\n\t"
      "v_mfma_f32_16x16x32_bf16 v[128:131], a[24:27], v[36:39], v[128:131]\n\t"
      "v_cvt_pk_bf16_f32 v136, v120, v121\n\t"
      "v_cvt_pk_bf16_f32 v137, v122, v123\n\t"
      "v_mfma_f32_16x16x32_bf16 v[128:131], a[48:51], v[40:43], v[128:131]\n\t"
      "v_cvt_pk_bf16_f32 v138, v132, v133\n\t"
      "v_mfma_f32_16x16x32_bf16 v[128:131], a[72:75], v[44:47], v[128:131]\n\t"
      "v_cvt_pk_bf16_f32 v139, v134, v135\n\t"
      "s_waitcnt lgkmcnt(4)\n\t"
      "v_mfma_f32_16x16x32_bf16 v[128:131], a[96:99], v[48:51], v[128:131]\n\t"
      "ds_write_b128 v219, v[136:139] offset:12288\n\t"
      "v_mfma_f32_16x16x32_bf16 v[128:131], a[120:123], v[52:55], v[128:131]\n\t"
      "v_cvt_pk_bf16_f32 v82, v84, v85\n\t"
      "v_cvt_pk_bf16_f32 v83, v86, v87\n\t"
      "v_mfma_f32_16x16x32_bf16 v[128:131], a[144:147], v[56:59], v[128:131]\n\t"
      "v_cvt_pk_bf16_f32 v88, v88, v89\n\t"
      "v_cvt_pk_bf16_f32 v89, v90, v91\n\t"
      "v_mfma_f32_16x16x32_bf16 v[128:131], a[168:171], v[60:63], v[128:131]\n\t"
      "ds_write_b128 v217, v[80:83] offset:0\n\t"
      "v_cvt_pk_bf16_f32 v90, v92, v93\n\t"
      "s_waitcnt lgkmcnt(2)\n\t"
      "v_mfma_f32_16x16x32_bf16 v[128:131], a[192:195], v[64:67], v[128:131]\n\t"
      "v_cvt_pk_bf16_f32 v91, v94, v95\n\t"
      "v_cvt_pk_bf16_f32 v96, v96, v97\n\t"
      "v_mfma_f32_16x16x32_bf16 v[128:131], a[216:219], v[68:71], v[128:131]\n\t"
      "ds_write_b128 v218, v[88:91] offset:0\n\t"
      "v_cvt_pk_bf16_f32 v97, v98, v99\n\t"
      "v_mfma_f32_16x16x32_bf16 v[128:131], a[240:243], v[72:75], v[128:131]\n\t"
      "v_cvt_pk_bf16_f32 v98, v100, v101\n\t"
      "v_cvt_pk_bf16_f32 v99, v102, v103\n\t"
      "v_mfma_f32_16x16x32_bf16 v[128:131], v[172:175], v[76:79], v[128:131]\n\t"
      "v_cvt_pk_bf16_f32 v104, v104, v105\n\t"
      "v_cvt_pk_bf16_f32 v105, v106, v107\n\t"
      "v_mfma_f32_16x16x32_bf16 v[132:135], a[4:7], v[32:35], v[12:15]\n\t"
      "ds_write_b128 v219, v[96:99] offset:0\n\t"
      "v_cvt_pk_bf16_f32 v106, v108, v109\n\t"
      "v_mfma_f32_16x16x32_bf16 v[132:135], a[28:31], v[36:39], v[132:135]\n\t"
      "v_cvt_pk_bf16_f32 v107, v110, v111\n\t"
      "v_cvt_pk_bf16_f32 v112, v112, v113\n\t"
      "v_mfma_f32_16x16x32_bf16 v[132:135], a[52:55], v[40:43], v[132:135]\n\t"
      "v_cvt_pk_bf16_f32 v136, v128, v129\n\t"
      "v_mfma_f32_16x16x32_bf16 v[132:135], a[76:79], v[44:47], v[132:135]\n\t"
      "v_cvt_pk_bf16_f32 v137, v130, v131\n\t"
      "v_mfma_f32_16x16x32_bf16 v[132:135], a[100:103], v[48:51], v[132:135]\n\t"
      "ds_write_b128 v217, v[104:107] offset:12288\n\t"
      "v_cvt_pk_bf16_f32 v113, v114, v115\n\t"
      "s_add_u32 s48, s47, 0x6000\n\t"
      "v_mfma_f32_16x16x32_bf16 v[132:135], a[124:127], v[52:55], v[132:135]\n\t"
      "v_cvt_pk_bf16_f32 v114, v116, v117\n\t"
      "v_cvt_pk_bf16_f32 v115, v118, v119\n\t"
      "s_cmp_eq_u32 s48, s50\n\t"
      "s_cselect_b32 s48, s41, s48\n\t"
      "v_mfma_f32_16x16x32_bf16 v[132:135], a[148:151], v[56:59], v[132:135]\n\t"
      "ds_write_b128 v218, v[112:115] offset:12288\n\t"
      "v_mfma_f32_16x16x32_bf16 v[132:135], a[172:175], v[60:63], v[132:135]\n\t"
      "v_add_u32_e32 v217, s46, v204\n\t"
      "v_mfma_f32_16x16x32_bf16 v[132:135], a[196:199], v[64:67], v[132:135]\n\t"
      "v_add_u32_e32 v218, s46, v205\n\t"
      "v_mfma_f32_16x16x32_bf16 v[132:135], a[220:223], v[68:71], v[132:135]\n\t"
      "v_readlane_b32 s73, v248, 3\n\t"
      "v_add_u32_e32 v219, s46, v206\n\t"
      "v_mfma_f32_16x16x32_bf16 v[132:135], a[244:247], v[72:75], v[132:135]\n\t"
      "v_mfma_f32_16x16x32_bf16 v[132:135], v[176:179], v[76:79], v[132:135]\n\t"
      "s_waitcnt lgkmcnt(0)\n\t"
      "s_barrier\n\t"
      "v_mfma_f32_16x16x32_bf16 v[128:131], a[8:11], v[32:35], v[16:19]\n\t"
      "ds_read_b128 v[140:143], v220 offset:0\n\t"
      "v_mfma_f32_16x16x32_bf16 v[128:131], a[32:35], v[36:39], v[128:131]\n\t"
      "ds_read_b128 v[144:147], v220 offset:1024\n\t"
      "s_memrealtime s[80:81]\n\t"
      "v_mfma_f32_16x16x32_bf16 v[128:131], a[56:59], v[40:43], v[128:131]\n\t"
      "v_cvt_pk_bf16_f32 v138, v132, v133\n\t"
      "s_mov_b32 m0, s45\n\t"
      "v_mfma_f32_16x16x32_bf16 v[128:131], a[80:83], v[44:47], v[128:131]\n\t"
      "v_cvt_pk_bf16_f32 v139, v134, v135\n\t"
      "v_mfma_f32_16x16x32_bf16 v[128:131], a[104:107], v[48:51], v[128:131]\n\t"
      "ds_write_b128 v217, v[136:139] offset:0\n\t"
      "v_mfma_f32_16x16x32_bf16 v[128:131], a[128:131], v[52:55], v[128:131]\n\t"
      "s_waitcnt lgkmcnt(2)\n\t"
      "buffer_store_dwordx4 v[140:143], v208, s[28:31], s40 offen sc1\n\t"
      "v_mfma_f32_16x16x32_bf16 v[128:131], a[152:155], v[56:59], v[128:131]\n\t"
      "ds_read_b128 v[148:151], v220 offset:2048\n\t"
      "v_mfma_f32_16x16x32_bf16 v[128:131], a[176:179], v[60:63], v[128:131]\n\t"
      "ds_read_b128 v[152:155], v220 offset:12288\n\t"
      "v_mfma_f32_16x16x32_bf16 v[128:131], a[200:203], v[64:67], v[128:131]\n\t"
      "ds_read_b128 v[156:159], v220 offset:13312\n\t"
      "v_mfma_f32_16x16x32_bf16 v[128:131], a[224:227], v[68:71], v[128:131]\n\t"
      "s_waitcnt lgkmcnt(4)\n\t"
      "buffer_store_dwordx4 v[144:147], v208, s[28:31], s40 offen offset:1024 sc1\n\t"
      "v_mfma_f32_16x16x32_bf16 v[128:131], a[248:251], v[72:75], v[128:131]\n\t"
      "ds_read_b128 v[160:163], v220 offset:14336\n\t"
      "v_mfma_f32_16x16x32_bf16 v[128:131], v[180:183], v[76:79], v[128:131]\n\t"
      "v_mfma_f32_16x16x32_bf16 v[132:135], a[12:15], v[32:35], v[20:23]\n\t"
      "v_mfma_f32_16x16x32_bf16 v[132:135], a[36:39], v[36:39], v[132:135]\n\t"
      "s_waitcnt lgkmcnt(3)\n\t"
      "buffer_store_dwordx4 v[148:151], v208, s[28:31], s40 offen offset:2048 sc1\n\t"
      "v_mfma_f32_16x16x32_bf16 v[132:135], a[60:63], v[40:43], v[132:135]\n\t"
      "v_cvt_pk_bf16_f32 v136, v128, v129\n\t"
      "v_mfma_f32_16x16x32_bf16 v[132:135], a[84:87], v[44:47], v[132:135]\n\t"
      "v_cvt_pk_bf16_f32 v137, v130, v131\n\t"
      "v_mfma_f32_16x16x32_bf16 v[132:135], a[108:111], v[48:51], v[132:135]\n\t"
      "v_mov_b32_e32 v6, 0\n\t"
      "v_mfma_f32_16x16x32_bf16 v[132:135], a[132:135], v[52:55], v[132:135]\n\t"
      "buffer_load_dwordx4 v208, s[20:23], s73 offen lds\n\t"
      "v_mfma_f32_16x16x32_bf16 v[132:135], a[156:159], v[56:59], v[132:135]\n\t"
      "ds_write_b32 v238, v6\n\t"
      "v_mfma_f32_16x16x32_bf16 v[132:135], a[180:183], v[60:63], v[132:135]\n\t"
      "v_mfma_f32_16x16x32_bf16 v[132:135], a[204:207], v[64:67], v[132:135]\n\t"
      "v_mfma_f32_16x16x32_bf16 v[132:135], a[228:231], v[68:71], v[132:135]\n\t"
      "buffer_load_dwordx4 v208, s[20:23], s73 offen offset:1024 lds\n\t"
      "v_mfma_f32_16x16x32_bf16 v[132:135], a[252:255], v[72:75], v[132:135]\n\t"
      "v_mfma_f32_16x16x32_bf16 v[132:135], v[184:187], v[76:79], v[132:135]\n\t"
      "s_waitcnt vmcnt(13)\n\t"
      "s_barrier\n\t"
      "v_mfma_f32_16x16x32_bf16 v[128:131], a[16:19], v[32:35], v[24:27]\n\t"
      "ds_read_b128 v[80:83], v196 offset:12288\n\t"
      "v_mfma_f32_16x16x32_bf16 v[128:131], a[40:43], v[36:39], v[128:131]\n\t"
      "buffer_load_dwordx4 v208, s[20:23], s73 offen offset:2048 lds\n\t"
      "v_mfma_f32_16x16x32_bf16 v[128:131], a[64:67], v[40:43], v[128:131]\n\t"
      "ds_read_b128 v[84:87], v197 offset:12288\n\t"
      "v_cvt_pk_bf16_f32 v138, v132, v133\n\t"
      "s_add_u32 m0, s45, 0x3000\n\t"
      "v_mfma_f32_16x16x32_bf16 v[128:131], a[88:91], v[44:47], v[128:131]\n\t"
      "ds_read_b128 v[88:91], v198 offset:12288\n\t"
      "v_cvt_pk_bf16_f32 v139, v134, v135\n\t"
      "v_mfma_f32_16x16x32_bf16 v[128:131], a[112:115], v[48:51], v[128:131]\n\t"
      "ds_write_b128 v218, v[136:139] offset:0\n\t"
      "v_mfma_f32_16x16x32_bf16 v[128:131], a[136:139], v[52:55], v[128:131]\n\t"
      "s_waitcnt lgkmcnt(7)\n\t"
      "buffer_store_dwordx4 v[152:155], v209, s[28:31], s40 offen sc1\n\t"
      "s_waitcnt vmcnt(7)\n\t"
      "v_subrev_u32_e32 v242, s67, v240\n\t"
      "v_xor_b32_e32 v241, s63, v241\n\t"
      "v_mfma_f32_16x16x32_bf16 v[128:131], a[160:163], v[56:59], v[128:131]\n\t"
      "ds_read_b128 v[92:95], v199 offset:12288\n\t"
      "v_lshl_or_b32 v240, v242, 20, v241\n\t"
      "v_mfma_f32_16x16x32_bf16 v[128:131], a[184:187], v[60:63], v[128:131]\n\t"
      "ds_read_b128 v[96:99], v197 offset:12544\n\t"
      "v_mfma_f32_16x16x32_bf16 v[128:131], a[208:211], v[64:67], v[128:131]\n\t"
      "v_lshrrev_b32_e32 v242, 12, v242\n\t"
      "v_cmp_ne_u32_e32 vcc, 0, v240\n\t"
      "v_mfma_f32_16x16x32_bf16 v[128:131], a[232:235], v[68:71], v[128:131]\n\t"
      "s_waitcnt lgkmcnt(8)\n\t"
      "buffer_store_dwordx4 v[156:159], v209, s[28:31], s40 offen offset:1024 sc1\n\t"
      "v_mfma_f32_16x16x32_bf16 v[128:131], v[164:167], v[72:75], v[128:131]\n\t"
      "ds_read_b128 v[100:103], v198 offset:12544\n\t"
      "s_cbranch_vccnz .Lgs_%=\n\t"
      ".Lgr_%=:\n\t"
      "ds_write_b32 v246, v242\n\t"
      "v_mfma_f32_16x16x32_bf16 v[128:131], v[188:191], v[76:79], v[128:131]\n\t"
      "ds_read_b128 v[104:107], v199 offset:12544\n\t"
      "v_mfma_f32_16x16x32_bf16 v[132:135], a[20:23], v[32:35], v[28:31]\n\t"
      "ds_read_b128 v[108:111], v196 offset:12544\n\t"
      "v_mfma_f32_16x16x32_bf16 v[132:135], a[44:47], v[36:39], v[132:135]\n\t"
      "s_waitcnt lgkmcnt(11)\n\t"
      "buffer_store_dwordx4 v[160:163], v209, s[28:31], s40 offen offset:2048 sc1\n\t"
      "v_mfma_f32_16x16x32_bf16 v[132:135], a[68:71], v[40:43], v[132:135]\n\t"
      "ds_read_b128 v[112:115], v198 offset:12800\n\t"
      "v_cvt_pk_bf16_f32 v136, v128, v129\n\t"
      "v_add_u32_e32 v220, s46, v207\n\t"
      "v_mfma_f32_16x16x32_bf16 v[132:135], a[92:95], v[44:47], v[132:135]\n\t"
      "ds_read_b128 v[116:119], v199 offset:12800\n\t"
      "v_cvt_pk_bf16_f32 v137, v130, v131\n\t"
      "v_mfma_f32_16x16x32_bf16 v[132:135], a[116:119], v[48:51], v[132:135]\n\t"
      "s_sub_u32 s87, s80, s52\n\t"
      "v_mfma_f32_16x16x32_bf16 v[132:135], a[140:143], v[52:55], v[132:135]\n\t"
      "buffer_load_dwordx4 v209, s[20:23], s73 offen lds\n\t"
      "v_mfma_f32_16x16x32_bf16 v[132:135], a[164:167], v[56:59], v[132:135]\n\t"
      "ds_read_b128 v[120:123], v196 offset:12800\n\t"
      "v_mfma_f32_16x16x32_bf16 v[132:135], a[188:191], v[60:63], v[132:135]\n\t"
      "ds_read_b128 v[124:127], v197 offset:12800\n\t"
      "v_mfma_f32_16x16x32_bf16 v[132:135], a[212:215], v[64:67], v[132:135]\n\t"
      "s_add_u32 s44, s44, 1\n\t"
      "v_mfma_f32_16x16x32_bf16 v[132:135], a[236:239], v[68:71], v[132:135]\n\t"
      "buffer_load_dwordx4 v209, s[20:23], s73 offen offset:1024 lds\n\t"
      "v_mfma_f32_16x16x32_bf16 v[132:135], v[168:171], v[72:75], v[132:135]\n\t"
      "s_add_u32 s85, s85, 1\n\t"
      "s_add_u32 s82, s87, s84\n\t"
      "v_mfma_f32_16x16x32_bf16 v[132:135], v[192:195], v[76:79], v[132:135]\n\t"
      "s_waitcnt lgkmcnt(9)\n\t"
      "v_mfma_f32_16x16x32_bf16 v[128:131], a[0:3], v[80:83], v[8:11]\n\t"
      "v_mfma_f32_16x16x32_bf16 v[128:131], a[24:27], v[84:87], v[128:131]\n\t"
      "v_readlane_b32 s72, v248, s44\n\t"
      "v_mfma_f32_16x16x32_bf16 v[128:131], a[48:51], v[88:91], v[128:131]\n\t"
      "v_cvt_pk_bf16_f32 v138, v132, v133\n\t"
      "v_mfma_f32_16x16x32_bf16 v[128:131], a[72:75], v[92:95], v[128:131]\n\t"
      "v_cvt_pk_bf16_f32 v139, v134, v135\n\t"
      "s_waitcnt lgkmcnt(4)\n\t"
      "v_mfma_f32_16x16x32_bf16 v[128:131], a[96:99], v[96:99], v[128:131]\n\t"
      "ds_write_b128 v219, v[136:139] offset:0\n\t"
      "v_mfma_f32_16x16x32_bf16 v[128:131], a[120:123], v[100:103], v[128:131]\n\t"
      "v_mfma_f32_16x16x32_bf16 v[128:131], a[144:147], v[104:107], v[128:131]\n\t"
      "buffer_load_dwordx4 v209, s[20:23], s73 offen offset:2048 lds\n\t"
      "v_mfma_f32_16x16x32_bf16 v[128:131], a[168:171], v[108:111], v[128:131]\n\t"
      "v_add_u32_e32 v196, s47, v200\n\t"
      "s_waitcnt lgkmcnt(1)\n\t"
      "v_mfma_f32_16x16x32_bf16 v[128:131], a[192:195], v[112:115], v[128:131]\n\t"
      "v_add_u32_e32 v197, s47, v201\n\t"
      "v_mfma_f32_16x16x32_bf16 v[128:131], a[216:219], v[116:119], v[128:131]\n\t"
      "v_add_u32_e32 v198, s47, v202\n\t"
      "v_mfma_f32_16x16x32_bf16 v[128:131], a[240:243], v[120:123], v[128:131]\n\t"
      "v_add_u32_e32 v199, s47, v203\n\t"
      "v_mfma_f32_16x16x32_bf16 v[128:131], v[172:175], v[124:127], v[128:131]\n\t"
      "s_waitcnt vmcnt(17) lgkmcnt(0)\n\t"
      "s_barrier\n\t"
      "v_mfma_f32_16x16x32_bf16 v[132:135], a[4:7], v[80:83], v[12:15]\n\t"
      "ds_read_b128 v[140:143], v220 offset:0\n\t"
      "v_mfma_f32_16x16x32_bf16 v[132:135], a[28:31], v[84:87], v[132:135]\n\t"
      "ds_read_b128 v[240:243], v247 offset:0\n\t"
      "v_mfma_f32_16x16x32_bf16 v[132:135], a[52:55], v[88:91], v[132:135]\n\t"
      "v_cvt_pk_bf16_f32 v136, v128, v129\n\t"
      "v_mfma_f32_16x16x32_bf16 v[132:135], a[76:79], v[92:95], v[132:135]\n\t"
      "ds_read_b128 v[32:35], v196 offset:0\n\t"
      "v_cvt_pk_bf16_f32 v137, v130, v131\n\t"
      "v_mfma_f32_16x16x32_bf16 v[132:135], a[100:103], v[96:99], v[132:135]\n\t"
      "v_mfma_f32_16x16x32_bf16 v[132:135], a[124:127], v[100:103], v[132:135]\n\t"
      "v_mfma_f32_16x16x32_bf16 v[132:135], a[148:151], v[104:107], v[132:135]\n\t"
      "ds_read_b128 v[36:39], v197 offset:0\n\t"
      "v_mfma_f32_16x16x32_bf16 v[132:135], a[172:175], v[108:111], v[132:135]\n\t"
      "v_mfma_f32_16x16x32_bf16 v[132:135], a[196:199], v[112:115], v[132:135]\n\t"
      "s_waitcnt lgkmcnt(2)\n\t"
      "v_add3_u32 v240, v240, v241, v242\n\t"
      "v_mfma_f32_16x16x32_bf16 v[132:135], a[220:223], v[116:119], v[132:135]\n\t"
      "ds_read_b128 v[40:43], v198 offset:0\n\t"
      "v_mfma_f32_16x16x32_bf16 v[132:135], a[244:247], v[120:123], v[132:135]\n\t"
      "v_add_u32_e32 v240, v240, v243\n\t"
      "v_mfma_f32_16x16x32_bf16 v[132:135], v[176:179], v[124:127], v[132:135]\n\t"
      "buffer_store_dwordx4 v[140:143], v208, s[28:31], s72 offen sc1\n\t"
      "v_mfma_f32_16x16x32_bf16 v[128:131], a[8:11], v[80:83], v[16:19]\n\t"
      "ds_read_b128 v[44:47], v199 offset:0\n\t"
      "v_mfma_f32_16x16x32_bf16 v[128:131], a[32:35], v[84:87], v[128:131]\n\t"
      "v_add_u32_dpp v241, v240, v240 quad_perm:[1,0,3,2] row_mask:0xf bank_mask:0xf\n\t"
      "s_memrealtime s[80:81]\n\t"
      "v_mfma_f32_16x16x32_bf16 v[128:131], a[56:59], v[88:91], v[128:131]\n\t"
      "v_cvt_pk_bf16_f32 v138, v132, v133\n\t"
      "v_mfma_f32_16x16x32_bf16 v[128:131], a[80:83], v[92:95], v[128:131]\n\t"
      "v_cvt_pk_bf16_f32 v139, v134, v135\n\t"
      "v_mfma_f32_16x16x32_bf16 v[128:131], a[104:107], v[96:99], v[128:131]\n\t"
      "ds_write_b128 v217, v[136:139] offset:12288\n\t"
      "v_add_u32_dpp v242, v241, v241 quad_perm:[2,3,0,1] row_mask:0xf bank_mask:0xf\n\t"
      "v_mfma_f32_16x16x32_bf16 v[128:131], a[128:131], v[100:103], v[128:131]\n\t"
      "s_mov_b32 m0, s46\n\t"
      "v_and_b32_e32 v240, 0xff, v242\n\t"
      "v_mfma_f32_16x16x32_bf16 v[128:131], a[152:155], v[104:107], v[128:131]\n\t"
      "v_cmp_le_u32_e64 s[76:77], 19, v240\n\t"
      "v_cmp_ge_u32_e64 s[78:79], 13, v240\n\t"
      "v_mfma_f32_16x16x32_bf16 v[128:131], a[176:179], v[108:111], v[128:131]\n\t"
      "ds_read_b128 v[48:51], v197 offset:256\n\t"
      "v_readlane_b32 s70, v242, 0\n\t"
      "v_mfma_f32_16x16x32_bf16 v[128:131], a[200:203], v[112:115], v[128:131]\n\t"
      "v_cmp_lt_u32_e32 vcc, 32, v240\n\t"
      "s_mov_b32 s66, s76\n\t"
      "v_mfma_f32_16x16x32_bf16 v[128:131], a[224:227], v[116:119], v[128:131]\n\t"
      "ds_read_b128 v[52:55], v198 offset:256\n\t"
      "v_mov_b32_e32 v242, s62\n\t"
      "v_mfma_f32_16x16x32_bf16 v[128:131], a[248:251], v[120:123], v[128:131]\n\t"
      "s_and_b32 s65, s76, s77\n\t"
      "s_and_b32 s78, s78, s79\n\t"
      "v_mfma_f32_16x16x32_bf16 v[128:131], v[180:183], v[124:127], v[128:131]\n\t"
      "v_mov_b32_e32 v243, s63\n\t"
      "s_or_b32 s65, s65, s78\n\t"
      "v_mfma_f32_16x16x32_bf16 v[132:135], a[12:15], v[80:83], v[20:23]\n\t"
      "ds_read_b128 v[56:59], v199 offset:256\n\t"
      "s_cmp_lg_u64 vcc, 0\n\t"
      "s_cselect_b32 s65, 0, s65\n\t"
      "s_cselect_b32 s64, 0xfffc0000, 0\n\t"
      "s_cselect_b32 s49, 31, 17\n\t"
      "v_mfma_f32_16x16x32_bf16 v[132:135], a[36:39], v[84:87], v[132:135]\n\t"
      "v_and_b32_e32 v240, s66, v250\n\t"
      "v_and_b32_e32 v241, s65, v250\n\t"
      "v_mfma_f32_16x16x32_bf16 v[132:135], a[60:63], v[88:91], v[132:135]\n\t"
      "v_cvt_pk_bf16_f32 v136, v128, v129\n\t"
      "s_andn2_b32 s67, 0x40000000, s64\n\t"
      "v_mfma_f32_16x16x32_bf16 v[132:135], a[84:87], v[92:95], v[132:135]\n\t"
      "v_cvt_pk_bf16_f32 v137, v130, v131\n\t"
      "ds_read_b128 v[144:147], v220 offset:1024\n\t"
      "v_mfma_f32_16x16x32_bf16 v[132:135], a[108:111], v[96:99], v[132:135]\n\t"
      "v_cmp_ne_u32_e64 s[76:77], 0, v240\n\t"
      "v_cmp_ne_u32_e64 s[78:79], 0, v241\n\t"
      "v_mfma_f32_16x16x32_bf16 v[132:135], a[132:135], v[100:103], v[132:135]\n\t"
      "buffer_store_dwordx2 v[242:243], v210, s[24:27], 0 offen sc1\n\t"
      "v_mfma_f32_16x16x32_bf16 v[132:135], a[156:159], v[104:107], v[132:135]\n\t"
      "ds_read_b128 v[60:63], v196 offset:256\n\t"
      "s_lshr_b32 s70, s70, 10\n\t"
      "v_mfma_f32_16x16x32_bf16 v[132:135], a[180:183], v[108:111], v[132:135]\n\t"
      "s_xor_b32 s75, s75, s76\n\t"
      "s_xor_b32 s74, s74, s76\n\t"
      "v_mfma_f32_16x16x32_bf16 v[132:135], a[204:207], v[112:115], v[132:135]\n\t"
      "s_and_b32 s76, s75, s78\n\t"
      "s_or_b32 s76, s76, s64\n\t"
      "v_cndmask_b32_e64 v249, v248, v251, s[76:77]\n\t"
      "v_mfma_f32_16x16x32_bf16 v[132:135], a[228:231], v[116:119], v[132:135]\n\t"
      "ds_read_b128 v[64:67], v198 offset:512\n\t"
      "s_add_u32 s70, s70, s68\n\t"
      "v_mfma_f32_16x16x32_bf16 v[132:135], a[252:255], v[120:123], v[132:135]\n\t"
      "s_and_b32 s76, s74, s78\n\t"
      "s_or_b32 s76, s76, s64\n\t"
      "v_cndmask_b32_e64 v248, v248, v251, s[76:77]\n\t"
      "v_mfma_f32_16x16x32_bf16 v[132:135], v[184:187], v[124:127], v[132:135]\n\t"
      "s_waitcnt lgkmcnt(2)\n\t"
      "buffer_store_dwordx4 v[144:147], v208, s[28:31], s72 offen offset:1024 sc1\n\t"
      "v_mfma_f32_16x16x32_bf16 v[128:131], a[16:19], v[80:83], v[24:27]\n\t"
      "ds_read_b128 v[68:71], v199 offset:512\n\t"
      "v_mfma_f32_16x16x32_bf16 v[128:131], a[40:43], v[84:87], v[128:131]\n\t"
      "s_lshr_b32 s74, s78, 3\n\t"
      "s_or_b32 s76, s74, s78\n\t"
      "v_mfma_f32_16x16x32_bf16 v[128:131], a[64:67], v[88:91], v[128:131]\n\t"
      "v_cvt_pk_bf16_f32 v138, v132, v133\n\t"
      "v_readlane_b32 s73, v248, s85\n\t"
      "v_mfma_f32_16x16x32_bf16 v[128:131], a[88:91], v[92:95], v[128:131]\n\t"
      "v_cvt_pk_bf16_f32 v139, v134, v135\n\t"
      "s_sub_u32 s87, s80, s52\n\t"
      "v_mfma_f32_16x16x32_bf16 v[128:131], a[112:115], v[96:99], v[128:131]\n\t"
      "ds_write_b128 v218, v[136:139] offset:12288\n\t"
      "s_sub_u32 s83, s82, s87\n\t"
      "v_mfma_f32_16x16x32_bf16 v[128:131], a[136:139], v[100:103], v[128:131]\n\t"
      "v_cndmask_b32_e64 v252, v252, v253, s[76:77]\n\t"
      "s_and_b32 s78, s74, s78\n\t"
      "v_mfma_f32_16x16x32_bf16 v[128:131], a[160:163], v[104:107], v[128:131]\n\t"
      "v_cndmask_b32_e64 v252, v252, v254, s[78:79]\n\t"
      "v_mfma_f32_16x16x32_bf16 v[128:131], a[184:187], v[108:111], v[128:131]\n\t"
      "ds_read_b128 v[148:151], v220 offset:2048\n\t"
      "s_add_u32 s70, s70, -1574\n\t"
      "v_mfma_f32_16x16x32_bf16 v[128:131], a[208:211], v[112:115], v[128:131]\n\t"
      "buffer_load_dwordx4 v208, s[20:23], s73 offen lds\n\t"
      "v_mfma_f32_16x16x32_bf16 v[128:131], a[232:235], v[116:119], v[128:131]\n\t"
      "ds_read_b128 v[72:75], v196 offset:512\n\t"
      "v_readlane_b32 s71, v252, s44\n\t"
      "s_max_i32 s82, s82, s87\n\t"
      "v_mfma_f32_16x16x32_bf16 v[128:131], v[164:167], v[120:123], v[128:131]\n\t"
      "s_sub_u32 s61, s70, s82\n\t"
      "v_mfma_f32_16x16x32_bf16 v[128:131], v[188:191], v[124:127], v[128:131]\n\t"
      "v_subrev_u32_e32 v240, 0x9e3779b9, v243\n\t"
      "s_sub_u32 s84, s84, 19\n\t"
      "v_mfma_f32_16x16x32_bf16 v[132:135], a[20:23], v[80:83], v[28:31]\n\t"
      "ds_read_b128 v[76:79], v197 offset:512\n\t"
      "s_lshr_b32 s60, s71, 16\n\t"
      "v_mfma_f32_16x16x32_bf16 v[132:135], a[44:47], v[84:87], v[132:135]\n\t"
      "buffer_load_dwordx4 v208, s[20:23], s73 offen offset:1024 lds\n\t"
      "s_and_b32 s86, s71, 0xff\n\t"
      "v_mfma_f32_16x16x32_bf16 v[132:135], a[68:71], v[88:91], v[132:135]\n\t"
      "v_cvt_pk_bf16_f32 v136, v128, v129\n\t"
      "s_bfe_u32 s71, s71, 0x80008\n\t"
      "v_mfma_f32_16x16x32_bf16 v[132:135], a[92:95], v[92:95], v[132:135]\n\t"
      "v_cvt_pk_bf16_f32 v137, v130, v131\n\t"
      "s_max_i32 s84, s84, s86\n\t"
      "v_mfma_f32_16x16x32_bf16 v[132:135], a[116:119], v[96:99], v[132:135]\n\t"
      "s_mul_i32 s61, s61, s60\n\t"
      "s_ashr_i32 s61, s61, 8\n\t"
      "v_mfma_f32_16x16x32_bf16 v[132:135], a[140:143], v[100:103], v[132:135]\n\t"
      "buffer_store_dword v240, v244, s[24:27], s67 offen offset:4 sc1\n\t"
      "v_mfma_f32_16x16x32_bf16 v[132:135], a[164:167], v[104:107], v[132:135]\n\t"
      "s_add_u32 s61, s61, s71\n\t"
      "s_mov_b32 s45, s46\n\t"
      "v_mfma_f32_16x16x32_bf16 v[132:135], a[188:191], v[108:111], v[132:135]\n\t"
      "s_max_i32 s61, s61, s84\n\t"
      "s_mov_b32 s46, s47\n\t"
      "v_mfma_f32_16x16x32_bf16 v[132:135], a[212:215], v[112:115], v[132:135]\n\t"
      "s_add_u32 s70, s70, s71\n\t"
      "s_mov_b32 s47, s48\n\t"
      "v_mfma_f32_16x16x32_bf16 v[132:135], a[236:239], v[116:119], v[132:135]\n\t"
      "s_add_u32 s82, s82, s61\n\t"
      "ds_write_b64 v219, v[136:137] offset:12288\n\t"
      "v_mfma_f32_16x16x32_bf16 v[132:135], v[168:171], v[120:123], v[132:135]\n\t"
      "v_add_u32_e32 v6, 1, v6\n\t"
      "v_mfma_f32_16x16x32_bf16 v[132:135], v[192:195], v[124:127], v[132:135]\n\t"
      "s_waitcnt lgkmcnt(3)\n\t"
      "buffer_store_dwordx4 v[148:151], v208, s[28:31], s72 offen offset:2048 sc1\n\t"
      ".p2align 6\n\t"
      ".Lloop_%=:\n\t"
      "v_mfma_f32_16x16x32_bf16 v[128:131], a[0:3], v[32:35], v[8:11]\n\t"
      "s_waitcnt vmcnt(17)\n\t"
      "v_mfma_f32_16x16x32_bf16 v[128:131], a[24:27], v[36:39], v[128:131]\n\t"
      "v_readlane_b32 s72, v249, s44\n\t"
      "v_mfma_f32_16x16x32_bf16 v[128:131], a[48:51], v[40:43], v[128:131]\n\t"
      "v_cvt_pk_bf16_f32 v138, v132, v133\n\t"
      "v_cvt_pk_bf16_f32 v139, v134, v135\n\t"
      "v_mfma_f32_16x16x32_bf16 v[128:131], a[72:75], v[44:47], v[128:131]\n\t"
      "ds_write_b32 v219, v138 offset:12296\n\t"
      "v_mfma_f32_16x16x32_bf16 v[128:131], a[96:99], v[48:51], v[128:131]\n\t"
      "ds_write_b32 v219, v139 offset:12300\n\t"
      "v_mfma_f32_16x16x32_bf16 v[128:131], a[120:123], v[52:55], v[128:131]\n\t"
      "ds_write_b32 v238, v6\n\t"
      "v_mfma_f32_16x16x32_bf16 v[128:131], a[144:147], v[56:59], v[128:131]\n\t"
      "buffer_load_dwordx4 v208, s[20:23], s73 offen offset:2048 lds\n\t"
      "v_mfma_f32_16x16x32_bf16 v[128:131], a[168:171], v[60:63], v[128:131]\n\t"
      "s_waitcnt lgkmcnt(4)\n\t"
      "v_add_u32_e32 v217, s46, v204\n\t"
      "v_mfma_f32_16x16x32_bf16 v[128:131], a[192:195], v[64:67], v[128:131]\n\t"
      "v_add_u32_e32 v218, s46, v205\n\t"
      "v_mfma_f32_16x16x32_bf16 v[128:131], a[216:219], v[68:71], v[128:131]\n\t"
      "v_add_u32_e32 v219, s46, v206\n\t"
      "v_mfma_f32_16x16x32_bf16 v[128:131], a[240:243], v[72:75], v[128:131]\n\t"
      "v_mfma_f32_16x16x32_bf16 v[128:131], v[172:175], v[76:79], v[128:131]\n\t"
      "s_cmp_gt_i32 s83, 1\n\t"
      "s_cbranch_scc1 .LslpA_%=\n\t"
      ".LretA_%=:\n\t"
      "v_mfma_f32_16x16x32_bf16 v[132:135], a[4:7], v[32:35], v[12:15]\n\t"
      "ds_read_b32 v7, v239\n\t"
      "v_mfma_f32_16x16x32_bf16 v[132:135], a[28:31], v[36:39], v[132:135]\n\t"
      "v_mfma_f32_16x16x32_bf16 v[132:135], a[52:55], v[40:43], v[132:135]\n\t"
      "v_cvt_pk_bf16_f32 v136, v128, v129\n\t"
      "v_cvt_pk_bf16_f32 v137, v130, v131\n\t"
      "v_mfma_f32_16x16x32_bf16 v[132:135], a[76:79], v[44:47], v[132:135]\n\t"
      "v_mfma_f32_16x16x32_bf16 v[132:135], a[100:103], v[48:51], v[132:135]\n\t"
      "s_waitcnt lgkmcnt(0)\n\t"
      "v_cmp_gt_u32_e32 vcc, v6, v7\n\t"
      "v_mfma_f32_16x16x32_bf16 v[132:135], a[124:127], v[52:55], v[132:135]\n\t"
      "s_cbranch_vccnz .LsbA_%=\n\t"
      ".LsrA_%=:\n\t"
      "v_mfma_f32_16x16x32_bf16 v[132:135], a[148:151], v[56:59], v[132:135]\n\t"
      "ds_read_b128 v[140:143], v220 offset:12288\n\t"
      "v_mfma_f32_16x16x32_bf16 v[132:135], a[172:175], v[60:63], v[132:135]\n\t"
      "ds_write_b32 v217, v136 offset:0\n\t"
      "v_mfma_f32_16x16x32_bf16 v[132:135], a[196:199], v[64:67], v[132:135]\n\t"
      "ds_write_b32 v217, v137 offset:4\n\t"
      "v_mfma_f32_16x16x32_bf16 v[132:135], a[220:223], v[68:71], v[132:135]\n\t"
      "ds_read_b128 v[80:83], v196 offset:12288\n\t"
      "v_mfma_f32_16x16x32_bf16 v[132:135], a[244:247], v[72:75], v[132:135]\n\t"
      "v_readlane_b32 s73, v249, s85\n\t"
      "v_mfma_f32_16x16x32_bf16 v[132:135], v[176:179], v[76:79], v[132:135]\n\t"
      "s_waitcnt lgkmcnt(3)\n\t"
      "buffer_store_dwordx4 v[140:143], v209, s[28:31], s72 offen sc1\n\t"
      "v_mfma_f32_16x16x32_bf16 v[128:131], a[8:11], v[32:35], v[16:19]\n\t"
      "ds_read_b128 v[84:87], v197 offset:12288\n\t"
      "v_mfma_f32_16x16x32_bf16 v[128:131], a[32:35], v[36:39], v[128:131]\n\t"
      "s_memrealtime s[80:81]\n\t"
      "v_mfma_f32_16x16x32_bf16 v[128:131], a[56:59], v[40:43], v[128:131]\n\t"
      "v_cvt_pk_bf16_f32 v138, v132, v133\n\t"
      "v_cvt_pk_bf16_f32 v139, v134, v135\n\t"
      "v_mfma_f32_16x16x32_bf16 v[128:131], a[80:83], v[44:47], v[128:131]\n\t"
      "ds_write_b32 v217, v138 offset:8\n\t"
      "v_mfma_f32_16x16x32_bf16 v[128:131], a[104:107], v[48:51], v[128:131]\n\t"
      "ds_write_b32 v217, v139 offset:12\n\t"
      "v_mfma_f32_16x16x32_bf16 v[128:131], a[128:131], v[52:55], v[128:131]\n\t"
      "ds_read_b128 v[88:91], v198 offset:12288\n\t"
      "v_mfma_f32_16x16x32_bf16 v[128:131], a[152:155], v[56:59], v[128:131]\n\t"
      "s_add_u32 m0, s45, 0x3000\n\t"
      "v_mfma_f32_16x16x32_bf16 v[128:131], a[176:179], v[60:63], v[128:131]\n\t"
      "ds_read_b128 v[92:95], v199 offset:12288\n\t"
      "v_mfma_f32_16x16x32_bf16 v[128:131], a[200:203], v[64:67], v[128:131]\n\t"
      "s_add_u32 s48, s47, 0x6000\n\t"
      "v_mfma_f32_16x16x32_bf16 v[128:131], a[224:227], v[68:71], v[128:131]\n\t"
      "ds_read_b128 v[96:99], v197 offset:12544\n\t"
      "v_mfma_f32_16x16x32_bf16 v[128:131], a[248:251], v[72:75], v[128:131]\n\t"
      "s_cmp_eq_u32 s48, s50\n\t"
      "s_cselect_b32 s48, s41, s48\n\t"
      "v_mfma_f32_16x16x32_bf16 v[128:131], v[180:183], v[76:79], v[128:131]\n\t"
      "buffer_load_dwordx4 v209, s[20:23], s73 offen lds\n\t"
      "v_mfma_f32_16x16x32_bf16 v[132:135], a[12:15], v[32:35], v[20:23]\n\t"
      "ds_read_b128 v[100:103], v198 offset:12544\n\t"
      "v_mfma_f32_16x16x32_bf16 v[132:135], a[36:39], v[36:39], v[132:135]\n\t"
      "v_mfma_f32_16x16x32_bf16 v[132:135], a[60:63], v[40:43], v[132:135]\n\t"
      "v_cvt_pk_bf16_f32 v136, v128, v129\n\t"
      "v_cvt_pk_bf16_f32 v137, v130, v131\n\t"
      "v_mfma_f32_16x16x32_bf16 v[132:135], a[84:87], v[44:47], v[132:135]\n\t"
      "ds_write_b32 v218, v136 offset:0\n\t"
      "v_mfma_f32_16x16x32_bf16 v[132:135], a[108:111], v[48:51], v[132:135]\n\t"
      "ds_write_b32 v218, v137 offset:4\n\t"
      "v_mfma_f32_16x16x32_bf16 v[132:135], a[132:135], v[52:55], v[132:135]\n\t"
      "ds_read_b128 v[144:147], v220 offset:13312\n\t"
      "v_mfma_f32_16x16x32_bf16 v[132:135], a[156:159], v[56:59], v[132:135]\n\t"
      "ds_read_b128 v[104:107], v199 offset:12544\n\t"
      "v_mfma_f32_16x16x32_bf16 v[132:135], a[180:183], v[60:63], v[132:135]\n\t"
      "s_sub_u32 s84, s84, 19\n\t"
      "v_mfma_f32_16x16x32_bf16 v[132:135], a[204:207], v[64:67], v[132:135]\n\t"
      "s_max_i32 s84, s84, s86\n\t"
      "v_mfma_f32_16x16x32_bf16 v[132:135], a[228:231], v[68:71], v[132:135]\n\t"
      "ds_read_b128 v[108:111], v196 offset:12544\n\t"
      "v_mfma_f32_16x16x32_bf16 v[132:135], a[252:255], v[72:75], v[132:135]\n\t"
      "v_mfma_f32_16x16x32_bf16 v[132:135], v[184:187], v[76:79], v[132:135]\n\t"
      "s_waitcnt lgkmcnt(2)\n\t"
      "buffer_store_dwordx4 v[144:147], v209, s[28:31], s72 offen offset:1024 sc1\n\t"
      "v_mfma_f32_16x16x32_bf16 v[128:131], a[16:19], v[32:35], v[24:27]\n\t"
      "ds_read_b128 v[112:115], v198 offset:12800\n\t"
      "v_mfma_f32_16x16x32_bf16 v[128:131], a[40:43], v[36:39], v[128:131]\n\t"
      "v_mfma_f32_16x16x32_bf16 v[128:131], a[64:67], v[40:43], v[128:131]\n\t"
      "v_cvt_pk_bf16_f32 v138, v132, v133\n\t"
      "v_cvt_pk_bf16_f32 v139, v134, v135\n\t"
      "v_mfma_f32_16x16x32_bf16 v[128:131], a[88:91], v[44:47], v[128:131]\n\t"
      "ds_write_b32 v218, v138 offset:8\n\t"
      "v_mfma_f32_16x16x32_bf16 v[128:131], a[112:115], v[48:51], v[128:131]\n\t"
      "ds_write_b32 v218, v139 offset:12\n\t"
      "v_mfma_f32_16x16x32_bf16 v[128:131], a[136:139], v[52:55], v[128:131]\n\t"
      "s_sub_u32 s87, s80, s52\n\t"
      "v_mfma_f32_16x16x32_bf16 v[128:131], a[160:163], v[56:59], v[128:131]\n\t"
      "s_sub_u32 s83, s82, s87\n\t"
      "ds_read_b128 v[116:119], v199 offset:12800\n\t"
      "v_mfma_f32_16x16x32_bf16 v[128:131], a[184:187], v[60:63], v[128:131]\n\t"
      "s_max_i32 s82, s82, s87\n\t"
      "v_mfma_f32_16x16x32_bf16 v[128:131], a[208:211], v[64:67], v[128:131]\n\t"
      "s_sub_u32 s61, s70, s82\n\t"
      "v_mfma_f32_16x16x32_bf16 v[128:131], a[232:235], v[68:71], v[128:131]\n\t"
      "s_mul_i32 s61, s61, s60\n\t"
      "ds_read_b128 v[120:123], v196 offset:12800\n\t"
      "v_mfma_f32_16x16x32_bf16 v[128:131], v[164:167], v[72:75], v[128:131]\n\t"
      "s_ashr_i32 s61, s61, 8\n\t"
      "v_mfma_f32_16x16x32_bf16 v[128:131], v[188:191], v[76:79], v[128:131]\n\t"
      "buffer_load_dwordx4 v209, s[20:23], s73 offen offset:1024 lds\n\t"
      "v_mfma_f32_16x16x32_bf16 v[132:135], a[20:23], v[32:35], v[28:31]\n\t"
      "s_add_u32 s61, s61, s71\n\t"
      "ds_read_b128 v[124:127], v197 offset:12800\n\t"
      "v_mfma_f32_16x16x32_bf16 v[132:135], a[44:47], v[36:39], v[132:135]\n\t"
      "s_max_i32 s61, s61, s84\n\t"
      "ds_read_b128 v[148:151], v220 offset:14336\n\t"
      "v_mfma_f32_16x16x32_bf16 v[132:135], a[68:71], v[40:43], v[132:135]\n\t"
      "s_add_u32 s82, s82, s61\n\t"
      "v_cvt_pk_bf16_f32 v136, v128, v129\n\t"
      "v_cvt_pk_bf16_f32 v137, v130, v131\n\t"
      "v_mfma_f32_16x16x32_bf16 v[132:135], a[92:95], v[44:47], v[132:135]\n\t"
      "ds_write_b32 v219, v136 offset:0\n\t"
      "v_mfma_f32_16x16x32_bf16 v[132:135], a[116:119], v[48:51], v[132:135]\n\t"
      "ds_write_b32 v219, v137 offset:4\n\t"
      "v_mfma_f32_16x16x32_bf16 v[132:135], a[140:143], v[52:55], v[132:135]\n\t"
      "s_add_u32 s70, s70, s71\n\t"
      "v_add_u32_e32 v220, s46, v207\n\t"
      "v_mfma_f32_16x16x32_bf16 v[132:135], a[164:167], v[56:59], v[132:135]\n\t"
      "v_add_u32_e32 v6, 1, v6\n\t"
      "v_mfma_f32_16x16x32_bf16 v[132:135], a[188:191], v[60:63], v[132:135]\n\t"
      "v_mfma_f32_16x16x32_bf16 v[132:135], a[212:215], v[64:67], v[132:135]\n\t"
      "s_add_u32 s44, s44, 1\n\t"
      "v_mfma_f32_16x16x32_bf16 v[132:135], a[236:239], v[68:71], v[132:135]\n\t"
      "s_add_u32 s85, s85, 1\n\t"
      "v_mfma_f32_16x16x32_bf16 v[132:135], v[168:171], v[72:75], v[132:135]\n\t"
      "v_mfma_f32_16x16x32_bf16 v[132:135], v[192:195], v[76:79], v[132:135]\n\t"
      "s_waitcnt lgkmcnt(2)\n\t"
      "buffer_store_dwordx4 v[148:151], v209, s[28:31], s72 offen offset:2048 sc1\n\t"
      "v_mfma_f32_16x16x32_bf16 v[128:131], a[0:3], v[80:83], v[8:11]\n\t"
      "s_waitcnt vmcnt(17)\n\t"
      "v_mfma_f32_16x16x32_bf16 v[128:131], a[24:27], v[84:87], v[128:131]\n\t"
      "v_readlane_b32 s72, v248, s44\n\t"
      "v_mfma_f32_16x16x32_bf16 v[128:131], a[48:51], v[88:91], v[128:131]\n\t"
      "v_cvt_pk_bf16_f32 v138, v132, v133\n\t"
      "v_cvt_pk_bf16_f32 v139, v134, v135\n\t"
      "v_mfma_f32_16x16x32_bf16 v[128:131], a[72:75], v[92:95], v[128:131]\n\t"
      "ds_write_b32 v219, v138 offset:8\n\t"
      "v_mfma_f32_16x16x32_bf16 v[128:131], a[96:99], v[96:99], v[128:131]\n\t"
      "ds_write_b32 v219, v139 offset:12\n\t"
      "v_mfma_f32_16x16x32_bf16 v[128:131], a[120:123], v[100:103], v[128:131]\n\t"
      "ds_write_b32 v238, v6\n\t"
      "v_mfma_f32_16x16x32_bf16 v[128:131], a[144:147], v[104:107], v[128:131]\n\t"
      "buffer_load_dwordx4 v209, s[20:23], s73 offen offset:2048 lds\n\t"
      "v_mfma_f32_16x16x32_bf16 v[128:131], a[168:171], v[108:111], v[128:131]\n\t"
      "v_add_u32_e32 v196, s47, v200\n\t"
      "v_mfma_f32_16x16x32_bf16 v[128:131], a[192:195], v[112:115], v[128:131]\n\t"
      "v_add_u32_e32 v197, s47, v201\n\t"
      "v_mfma_f32_16x16x32_bf16 v[128:131], a[216:219], v[116:119], v[128:131]\n\t"
      "v_add_u32_e32 v198, s47, v202\n\t"
      "v_mfma_f32_16x16x32_bf16 v[128:131], a[240:243], v[120:123], v[128:131]\n\t"
      "v_add_u32_e32 v199, s47, v203\n\t"
      "v_mfma_f32_16x16x32_bf16 v[128:131], v[172:175], v[124:127], v[128:131]\n\t"
      "s_cmp_gt_i32 s83, 1\n\t"
      "s_cbranch_scc1 .LslpB_%=\n\t"
      ".LretB_%=:\n\t"
      "v_mfma_f32_16x16x32_bf16 v[132:135], a[4:7], v[80:83], v[12:15]\n\t"
      "ds_read_b32 v7, v239\n\t"
      "v_mfma_f32_16x16x32_bf16 v[132:135], a[28:31], v[84:87], v[132:135]\n\t"
      "v_mfma_f32_16x16x32_bf16 v[132:135], a[52:55], v[88:91], v[132:135]\n\t"
      "v_cvt_pk_bf16_f32 v136, v128, v129\n\t"
      "v_cvt_pk_bf16_f32 v137, v130, v131\n\t"
      "v_mfma_f32_16x16x32_bf16 v[132:135], a[76:79], v[92:95], v[132:135]\n\t"
      "v_mfma_f32_16x16x32_bf16 v[132:135], a[100:103], v[96:99], v[132:135]\n\t"
      "s_waitcnt lgkmcnt(0)\n\t"
      "v_cmp_gt_u32_e32 vcc, v6, v7\n\t"
      "v_mfma_f32_16x16x32_bf16 v[132:135], a[124:127], v[100:103], v[132:135]\n\t"
      "s_cbranch_vccnz .LsbB_%=\n\t"
      ".LsrB_%=:\n\t"
      "v_mfma_f32_16x16x32_bf16 v[132:135], a[148:151], v[104:107], v[132:135]\n\t"
      "ds_read_b128 v[140:143], v220 offset:0\n\t"
      "v_mfma_f32_16x16x32_bf16 v[132:135], a[172:175], v[108:111], v[132:135]\n\t"
      "ds_write_b32 v217, v136 offset:12288\n\t"
      "v_mfma_f32_16x16x32_bf16 v[132:135], a[196:199], v[112:115], v[132:135]\n\t"
      "ds_write_b32 v217, v137 offset:12292\n\t"
      "v_mfma_f32_16x16x32_bf16 v[132:135], a[220:223], v[116:119], v[132:135]\n\t"
      "ds_read_b128 v[32:35], v196 offset:0\n\t"
      "v_mfma_f32_16x16x32_bf16 v[132:135], a[244:247], v[120:123], v[132:135]\n\t"
      "v_readlane_b32 s73, v248, s85\n\t"
      "v_mfma_f32_16x16x32_bf16 v[132:135], v[176:179], v[124:127], v[132:135]\n\t"
      "s_waitcnt lgkmcnt(3)\n\t"
      "buffer_store_dwordx4 v[140:143], v208, s[28:31], s72 offen sc1\n\t"
      "v_mfma_f32_16x16x32_bf16 v[128:131], a[8:11], v[80:83], v[16:19]\n\t"
      "ds_read_b128 v[36:39], v197 offset:0\n\t"
      "v_mfma_f32_16x16x32_bf16 v[128:131], a[32:35], v[84:87], v[128:131]\n\t"
      "s_memrealtime s[80:81]\n\t"
      "v_mfma_f32_16x16x32_bf16 v[128:131], a[56:59], v[88:91], v[128:131]\n\t"
      "v_cvt_pk_bf16_f32 v138, v132, v133\n\t"
      "v_cvt_pk_bf16_f32 v139, v134, v135\n\t"
      "v_mfma_f32_16x16x32_bf16 v[128:131], a[80:83], v[92:95], v[128:131]\n\t"
      "ds_write_b32 v217, v138 offset:12296\n\t"
      "v_mfma_f32_16x16x32_bf16 v[128:131], a[104:107], v[96:99], v[128:131]\n\t"
      "ds_write_b32 v217, v139 offset:12300\n\t"
      "v_mfma_f32_16x16x32_bf16 v[128:131], a[128:131], v[100:103], v[128:131]\n\t"
      "ds_read_b128 v[40:43], v198 offset:0\n\t"
      "v_mfma_f32_16x16x32_bf16 v[128:131], a[152:155], v[104:107], v[128:131]\n\t"
      "s_mov_b32 m0, s46\n\t"
      "v_mfma_f32_16x16x32_bf16 v[128:131], a[176:179], v[108:111], v[128:131]\n\t"
      "ds_read_b128 v[44:47], v199 offset:0\n\t"
      "v_mfma_f32_16x16x32_bf16 v[128:131], a[200:203], v[112:115], v[128:131]\n\t"
      "v_mfma_f32_16x16x32_bf16 v[128:131], a[224:227], v[116:119], v[128:131]\n\t"
      "ds_read_b128 v[48:51], v197 offset:256\n\t"
      "v_mfma_f32_16x16x32_bf16 v[128:131], a[248:251], v[120:123], v[128:131]\n\t"
      "v_mfma_f32_16x16x32_bf16 v[128:131], v[180:183], v[124:127], v[128:131]\n\t"
      "buffer_load_dwordx4 v208, s[20:23], s73 offen lds\n\t"
      "v_mfma_f32_16x16x32_bf16 v[132:135], a[12:15], v[80:83], v[20:23]\n\t"
      "ds_read_b128 v[52:55], v198 offset:256\n\t"
      "v_mfma_f32_16x16x32_bf16 v[132:135], a[36:39], v[84:87], v[132:135]\n\t"
      "v_readlane_b32 s71, v252, s44\n\t"
      "v_mfma_f32_16x16x32_bf16 v[132:135], a[60:63], v[88:91], v[132:135]\n\t"
      "v_cvt_pk_bf16_f32 v136, v128, v129\n\t"
      "v_cvt_pk_bf16_f32 v137, v130, v131\n\t"
      "v_mfma_f32_16x16x32_bf16 v[132:135], a[84:87], v[92:95], v[132:135]\n\t"
      "ds_write_b32 v218, v136 offset:12288\n\t"
      "v_mfma_f32_16x16x32_bf16 v[132:135], a[108:111], v[96:99], v[132:135]\n\t"
      "s_lshr_b32 s60, s71, 16\n\t"
      "ds_write_b32 v218, v137 offset:12292\n\t"
      "v_mfma_f32_16x16x32_bf16 v[132:135], a[132:135], v[100:103], v[132:135]\n\t"
      "s_and_b32 s86, s71, 0xff\n\t"
      "ds_read_b128 v[144:147], v220 offset:1024\n\t"
      "v_mfma_f32_16x16x32_bf16 v[132:135], a[156:159], v[104:107], v[132:135]\n\t"
      "s_bfe_u32 s71, s71, 0x80008\n\t"
      "ds_read_b128 v[56:59], v199 offset:256\n\t"
      "v_mfma_f32_16x16x32_bf16 v[132:135], a[180:183], v[108:111], v[132:135]\n\t"
      "s_sub_u32 s84, s84, 19\n\t"
      "v_mfma_f32_16x16x32_bf16 v[132:135], a[204:207], v[112:115], v[132:135]\n\t"
      "s_max_i32 s84, s84, s86\n\t"
      "v_mfma_f32_16x16x32_bf16 v[132:135], a[228:231], v[116:119], v[132:135]\n\t"
      "ds_read_b128 v[60:63], v196 offset:256\n\t"
      "v_mfma_f32_16x16x32_bf16 v[132:135], a[252:255], v[120:123], v[132:135]\n\t"
      "v_mfma_f32_16x16x32_bf16 v[132:135], v[184:187], v[124:127], v[132:135]\n\t"
      "s_waitcnt lgkmcnt(2)\n\t"
      "buffer_store_dwordx4 v[144:147], v208, s[28:31], s72 offen offset:1024 sc1\n\t"
      "v_mfma_f32_16x16x32_bf16 v[128:131], a[16:19], v[80:83], v[24:27]\n\t"
      "ds_read_b128 v[64:67], v198 offset:512\n\t"
      "v_mfma_f32_16x16x32_bf16 v[128:131], a[40:43], v[84:87], v[128:131]\n\t"
      "v_mfma_f32_16x16x32_bf16 v[128:131], a[64:67], v[88:91], v[128:131]\n\t"
      "v_cvt_pk_bf16_f32 v138, v132, v133\n\t"
      "v_cvt_pk_bf16_f32 v139, v134, v135\n\t"
      "v_mfma_f32_16x16x32_bf16 v[128:131], a[88:91], v[92:95], v[128:131]\n\t"
      "ds_write_b32 v218, v138 offset:12296\n\t"
      "v_mfma_f32_16x16x32_bf16 v[128:131], a[112:115], v[96:99], v[128:131]\n\t"
      "ds_write_b32 v218, v139 offset:12300\n\t"
      "v_mfma_f32_16x16x32_bf16 v[128:131], a[136:139], v[100:103], v[128:131]\n\t"
      "s_sub_u32 s87, s80, s52\n\t"
      "v_mfma_f32_16x16x32_bf16 v[128:131], a[160:163], v[104:107], v[128:131]\n\t"
      "s_sub_u32 s83, s82, s87\n\t"
      "ds_read_b128 v[68:71], v199 offset:512\n\t"
      "v_mfma_f32_16x16x32_bf16 v[128:131], a[184:187], v[108:111], v[128:131]\n\t"
      "s_max_i32 s82, s82, s87\n\t"
      "v_mfma_f32_16x16x32_bf16 v[128:131], a[208:211], v[112:115], v[128:131]\n\t"
      "s_sub_u32 s61, s70, s82\n\t"
      "v_mfma_f32_16x16x32_bf16 v[128:131], a[232:235], v[116:119], v[128:131]\n\t"
      "s_mul_i32 s61, s61, s60\n\t"
      "ds_read_b128 v[72:75], v196 offset:512\n\t"
      "v_mfma_f32_16x16x32_bf16 v[128:131], v[164:167], v[120:123], v[128:131]\n\t"
      "s_ashr_i32 s61, s61, 8\n\t"
      "v_mfma_f32_16x16x32_bf16 v[128:131], v[188:191], v[124:127], v[128:131]\n\t"
      "buffer_load_dwordx4 v208, s[20:23], s73 offen offset:1024 lds\n\t"
      "v_mfma_f32_16x16x32_bf16 v[132:135], a[20:23], v[80:83], v[28:31]\n\t"
      "s_add_u32 s61, s61, s71\n\t"
      "ds_read_b128 v[76:79], v197 offset:512\n\t"
      "v_mfma_f32_16x16x32_bf16 v[132:135], a[44:47], v[84:87], v[132:135]\n\t"
      "s_max_i32 s61, s61, s84\n\t"
      "ds_read_b128 v[148:151], v220 offset:2048\n\t"
      "v_mfma_f32_16x16x32_bf16 v[132:135], a[68:71], v[88:91], v[132:135]\n\t"
      "s_add_u32 s82, s82, s61\n\t"
      "v_cvt_pk_bf16_f32 v136, v128, v129\n\t"
      "v_cvt_pk_bf16_f32 v137, v130, v131\n\t"
      "v_mfma_f32_16x16x32_bf16 v[132:135], a[92:95], v[92:95], v[132:135]\n\t"
      "ds_write_b32 v219, v136 offset:12288\n\t"
      "v_mfma_f32_16x16x32_bf16 v[132:135], a[116:119], v[96:99], v[132:135]\n\t"
      "ds_write_b32 v219, v137 offset:12292\n\t"
      "v_mfma_f32_16x16x32_bf16 v[132:135], a[140:143], v[100:103], v[132:135]\n\t"
      "s_add_u32 s70, s70, s71\n\t"
      "v_mfma_f32_16x16x32_bf16 v[132:135], a[164:167], v[104:107], v[132:135]\n\t"
      "s_mov_b32 s45, s46\n\t"
      "v_add_u32_e32 v6, 1, v6\n\t"
      "v_mfma_f32_16x16x32_bf16 v[132:135], a[188:191], v[108:111], v[132:135]\n\t"
      "s_mov_b32 s46, s47\n\t"
      "v_mfma_f32_16x16x32_bf16 v[132:135], a[212:215], v[112:115], v[132:135]\n\t"
      "s_mov_b32 s47, s48\n\t"
      "v_mfma_f32_16x16x32_bf16 v[132:135], a[236:239], v[116:119], v[132:135]\n\t"
      "v_mfma_f32_16x16x32_bf16 v[132:135], v[168:171], v[120:123], v[132:135]\n\t"
      "s_cmp_lg_u32 s44, s49\n\t"
      "v_mfma_f32_16x16x32_bf16 v[132:135], v[192:195], v[124:127], v[132:135]\n\t"
      "s_waitcnt lgkmcnt(2)\n\t"
      "buffer_store_dwordx4 v[148:151], v208, s[28:31], s72 offen offset:2048 sc1\n\t"
      "s_cbranch_scc1 .Lloop_%=\n\t"
      "v_readlane_b32 s72, v249, s44\n\t"
      "s_nop 6\n\t"
      "v_cvt_pk_bf16_f32 v138, v132, v133\n\t"
      "v_cvt_pk_bf16_f32 v139, v134, v135\n\t"
      "ds_write_b64 v219, v[138:139] offset:12296\n\t"
      "s_waitcnt lgkmcnt(0)\n\t"
      "s_barrier\n\t"
      "ds_read_b128 v[32:35], v220 offset:12288\n\t"
      "ds_read_b128 v[36:39], v220 offset:13312\n\t"
      "ds_read_b128 v[40:43], v220 offset:14336\n\t"
      "s_waitcnt lgkmcnt(2)\n\t"
      "buffer_store_dwordx4 v[32:35], v209, s[28:31], s72 offen sc1\n\t"
      "s_waitcnt lgkmcnt(1)\n\t"
      "buffer_store_dwordx4 v[36:39], v209, s[28:31], s72 offen offset:1024 sc1\n\t"
      "s_waitcnt lgkmcnt(0)\n\t"
      "buffer_store_dwordx4 v[40:43], v209, s[28:31], s72 offen offset:2048 sc1\n\t"
      "s_endpgm\n\t"
      ".LslpA_%=:\n\t"
      "s_min_i32 s83, s83, 0x190\n\t"
      ".Lsl2A_%=:\n\t"
      "s_sleep 1\n\t"
      "s_sub_u32 s83, s83, 3\n\t"
      "s_cmp_gt_i32 s83, 1\n\t"
      "s_cbranch_scc1 .Lsl2A_%=\n\t"
      "s_branch .LretA_%=\n\t"
      ".LslpB_%=:\n\t"
      "s_min_i32 s83, s83, 0x190\n\t"
      ".Lsl2B_%=:\n\t"
      "s_sleep 1\n\t"
      "s_sub_u32 s83, s83, 3\n\t"
      "s_cmp_gt_i32 s83, 1\n\t"
      "s_cbranch_scc1 .Lsl2B_%=\n\t"
      "s_branch .LretB_%=\n\t"
      ".Lgs_%=:\n\t"
      "s_sleep 8\n\t"
      "buffer_load_dwordx2 v[240:241], v245, s[24:27], 0 offen sc1\n\t"
      "s_memrealtime s[76:77]\n\t"
      "s_waitcnt vmcnt(0) lgkmcnt(0)\n\t"
      "v_subrev_u32_e32 v242, s67, v240\n\t"
      "v_xor_b32_e32 v241, s63, v241\n\t"
      "v_lshl_or_b32 v240, v242, 20, v241\n\t"
      "v_lshrrev_b32_e32 v242, 12, v242\n\t"
      "v_cmp_ne_u32_e32 vcc, 0, v240\n\t"
      "s_nop 0\n\t"
      "s_cbranch_vccz .Lgr_%=\n\t"
      "s_sub_u32 s76, s76, s52\n\t"
      "s_cmp_lt_u32 s76, 2000\n\t"
      "s_cbranch_scc1 .Lgs_%=\n\t"
      "v_mov_b32_e32 v242, 10\n\t"
      "s_branch .Lgr_%=\n\t"
      ".LsbA_%=:\n\t"
      "ds_read_b32 v7, v239\n\t"
      "s_waitcnt lgkmcnt(0)\n\t"
      "v_cmp_gt_u32_e32 vcc, v6, v7\n\t"
      "s_nop 0\n\t"
      "s_cbranch_vccnz .LsbA_%=\n\t"
      "s_branch .LsrA_%=\n\t"
      ".LsbB_%=:\n\t"
      "ds_read_b32 v7, v239\n\t"
      "s_waitcnt lgkmcnt(0)\n\t"
      "v_cmp_gt_u32_e32 vcc, v6, v7\n\t"
      "s_nop 0\n\t"
      "s_cbranch_vccnz .LsbB_%=\n\t"
      "s_branch .LsrB_%=\n\t"
      ".Lend_%=:\n\t"
      :
      : "{s[20:21]}"(xa), "{s[16:17]}"(wa), "{s[36:37]}"(ba), "{s[28:29]}"(oa), "{s[24:25]}"(sa), "{s[52:53]}"(rs),
        "{s[22:23]}"(0x0002000006c00000ull), "{s[26:27]}"(0x0002000000000808ull), "{s[30:31]}"(0x0002000006c00000ull),
        "{s[18:19]}"(0x0002000000048000ull), "{s[38:39]}"(0x0002000000000f00ull),
        "{s40}"(t0), "{s41}"(p0), "{s43}"(lds0), "{s94}"(ty),
        "{v208}"(ga), "{v209}"(gb),
        "{v211}"(woff[0]), "{v212}"(woff[1]), "{v213}"(woff[2]), "{v215}"(boff),
        "{s95}"(tres), "{s92}"(pre1), "{s[60:61]}"(pres), "{s[62:63]}"(ep), "{s77}"(pfire), "{s98}"(((tidx >> 3) * 4u + (tidx & 3u)) * 128u), "{v238}"(vfw), "{v239}"(vfr),
        "{v248}"(ta), "{v249}"(ta), "{v251}"(t1), "{v250}"(mk), "{s74}"(plm), "{s75}"(plb),
        "{s[34:35]}"(__builtin_amdgcn_kernarg_segment_ptr()),
        "{v244}"(vpub), "{v210}"(vbmp), "{v245}"(vgv), "{v246}"(vxw), "{v247}"(vxr),
        "{v200}"(cfa[0]), "{v201}"(cfa[1]), "{v202}"(cfa[2]), "{v203}"(cfa[3]),
        "{v204}"(csw[0]), "{v205}"(csw[1]), "{v206}"(csw[2]), "{v207}"(csr),
        "{v214}"(tbase)
      : "memory", "scc", "vcc",
      "s42", "s44", "s45", "s46", "s47", "s48", "s49", "s50", "s51", "s54", "s55", "s56", "s57", "s58", "s59", "s64", "s65", "s66", "s67", "s68",
      "s69", "s70", "s71", "s72", "s73", "s76", "s78", "s79", "s80", "s81", "s82", "s83", "s84", "s85", "s86", "s87", "s88", "s89", "s90", "s91",
      "s93", "s96", "s97", "s99",
      "v1", "v2", "v3", "v4", "v5", "v6", "v7", "v8", "v9", "v10", "v11", "v12", "v13", "v14", "v15", "v16", "v17", "v18", "v19", "v20", "v21", "v22",
      "v23", "v24", "v25", "v26", "v27", "v28", "v29", "v30", "v31", "v32", "v33", "v34", "v35", "v36", "v37", "v38", "v39", "v40", "v41", "v42",
      "v43", "v44", "v45", "v46", "v47", "v48", "v49", "v50", "v51", "v52", "v53", "v54", "v55", "v56", "v57", "v58", "v59", "v60", "v61", "v62",
      "v63", "v64", "v65", "v66", "v67", "v68", "v69", "v70", "v71", "v72", "v73", "v74", "v75", "v76", "v77", "v78", "v79", "v80", "v81", "v82",
      "v83", "v84", "v85", "v86", "v87", "v88", "v89", "v90", "v91", "v92", "v93", "v94", "v95", "v96", "v97", "v98", "v99", "v100", "v101", "v102",
      "v103", "v104", "v105", "v106", "v107", "v108", "v109", "v110", "v111", "v112", "v113", "v114", "v115", "v116", "v117", "v118", "v119", "v120",
      "v121", "v122", "v123", "v124", "v125", "v126", "v127", "v128", "v129", "v130", "v131", "v132", "v133", "v134", "v135", "v136", "v137", "v138",
      "v139", "v140", "v141", "v142", "v143", "v144", "v145", "v146", "v147", "v148", "v149", "v150", "v151", "v152", "v153", "v154", "v155", "v156",
      "v157", "v158", "v159", "v160", "v161", "v162", "v163", "v164", "v165", "v166", "v167", "v168", "v169", "v170", "v171", "v172", "v173", "v174",
      "v175", "v176", "v177", "v178", "v179", "v180", "v181", "v182", "v183", "v184", "v185", "v186", "v187", "v188", "v189", "v190", "v191", "v192",
      "v193", "v194", "v195", "v196", "v197", "v198", "v199", "v216", "v217", "v218", "v219", "v220", "v221", "v222", "v223", "v224", "v225", "v226",
      "v227", "v228", "v229", "v230", "v231", "v232", "v233", "v234", "v235", "v236", "v237", "v240", "v241", "v242", "v243", "v252", "v253", "v254",
      "v255",
      "a0", "a1", "a2", "a3", "a4", "a5", "a6", "a7", "a8", "a9", "a10", "a11", "a12", "a13", "a14", "a15", "a16", "a17", "a18", "a19", "a20", "a21",
      "a22", "a23", "a24", "a25", "a26", "a27", "a28", "a29", "a30", "a31", "a32", "a33", "a34", "a35", "a36", "a37", "a38", "a39", "a40", "a41",
      "a42", "a43", "a44", "a45", "a46", "a47", "a48", "a49", "a50", "a51", "a52", "a53", "a54", "a55", "a56", "a57", "a58", "a59", "a60", "a61",
      "a62", "a63", "a64", "a65", "a66", "a67", "a68", "a69", "a70", "a71", "a72", "a73", "a74", "a75", "a76", "a77", "a78", "a79", "a80", "a81",
      "a82", "a83", "a84", "a85", "a86", "a87", "a88", "a89", "a90", "a91", "a92", "a93", "a94", "a95", "a96", "a97", "a98", "a99", "a100", "a101",
      "a102", "a103", "a104", "a105", "a106", "a107", "a108", "a109", "a110", "a111", "a112", "a113", "a114", "a115", "a116", "a117", "a118", "a119",
      "a120", "a121", "a122", "a123", "a124", "a125", "a126", "a127", "a128", "a129", "a130", "a131", "a132", "a133", "a134", "a135", "a136", "a137",
      "a138", "a139", "a140", "a141", "a142", "a143", "a144", "a145", "a146", "a147", "a148", "a149", "a150", "a151", "a152", "a153", "a154", "a155",
      "a156", "a157", "a158", "a159", "a160", "a161", "a162", "a163", "a164", "a165", "a166", "a167", "a168", "a169", "a170", "a171", "a172", "a173",
      "a174", "a175", "a176", "a177", "a178", "a179", "a180", "a181", "a182", "a183", "a184", "a185", "a186", "a187", "a188", "a189", "a190", "a191",
      "a192", "a193", "a194", "a195", "a196", "a197", "a198", "a199", "a200", "a201", "a202", "a203", "a204", "a205", "a206", "a207", "a208", "a209",
      "a210", "a211", "a212", "a213", "a214", "a215", "a216", "a217", "a218", "a219", "a220", "a221", "a222", "a223", "a224", "a225", "a226", "a227",
      "a228", "a229", "a230", "a231", "a232", "a233", "a234", "a235", "a236", "a237", "a238", "a239", "a240", "a241", "a242", "a243", "a244", "a245",
      "a246", "a247", "a248", "a249", "a250", "a251", "a252", "a253", "a254", "a255");
}

extern "C" void kernel_launch(void* const* d_in, const int* in_sizes, int n_in,
                              void* d_out, int out_size, void* d_ws, size_t ws_size,
                              hipStream_t stream) {
  const uint16_t* x = (const uint16_t*)d_in[0];
  const uint16_t* w = (const uint16_t*)d_in[1];
  const uint16_t* b = (const uint16_t*)d_in[2];
  uint16_t* out = (uint16_t*)d_out;
  const PaceTab pt = {{
    0x205d4eu, 0x205a4cu, 0x184944u, 0u,
    0x205d4eu, 0x205a4cu, 0x184944u, 0u,
    0x20564eu, 0x20534cu, 0x184944u, 0u,
    0x204f4eu, 0x204b48u, 0x184944u, 0u,
    0x184f4eu, 0x184b48u, 0x184944u, 0u,
    0x184f4eu, 0x184b48u, 0x184944u, 0u,
    0x184f4eu, 0x184b48u, 0x184944u, 0u,
    0x11514eu, 0x114e48u, 0x114944u, 0u,
    0x13534eu, 0x135049u, 0x134b44u, 0u,
    0x16544eu, 0x16514au, 0x164d46u, 0u,
    0x1b544eu, 0x1b514au, 0x1b4d46u, 0u,
    0x22544eu, 0x22514au, 0x224d46u, 0u,
    0x2f544eu, 0x2f514au, 0x2f4d46u, 0u,
    0x49544eu, 0x49514au, 0x494d46u, 0u,
    0xab544eu, 0xab514au, 0xab4d46u, 0u,
    0x1004400u, 0x1004400u, 0x1004400u, 0u,
    0x1004400u, 0x1004400u, 0x1004400u, 0u,
    0x1004400u, 0x1004400u, 0x1004400u, 0u}};
  conv1x1_kernel<<<256, 256, 0, stream>>>(x, w, b, out, (unsigned long long*)d_ws, pt);
}
```

```cpp
#include <hip/hip_runtime.h>
#include <stdint.h>

#define LAS __attribute__((address_space(3)))

struct PaceTab { unsigned v[72]; };

__global__ __launch_bounds__(256, 1) void conv1x1_kernel(const uint16_t* __restrict__ x,
                                                         const uint16_t* __restrict__ w,
                                                         const uint16_t* __restrict__ bias,
                                                         uint16_t* __restrict__ out, unsigned long long* ws, PaceTab pt) {
  __shared__ __attribute__((aligned(1024))) unsigned char lds[161792];

  unsigned long long xa, wa, ba, oa, sa, rs;
  asm volatile(
      "s_load_dwordx2 s[16:17], %6, 0x8\n\t"
      "s_load_dwordx2 s[20:21], %6, 0x0\n\t"
      "s_load_dwordx2 s[36:37], %6, 0x10\n\t"
      "s_load_dwordx2 s[28:29], %6, 0x18\n\t"
      "s_load_dwordx2 s[24:25], %6, 0x20\n\t"
      "s_memrealtime s[52:53]\n\t"
      : "={s[20:21]}"(xa), "={s[16:17]}"(wa), "={s[36:37]}"(ba), "={s[28:29]}"(oa), "={s[24:25]}"(sa), "={s[52:53]}"(rs)
      : "s"(__builtin_amdgcn_kernarg_segment_ptr())
      : "memory");
  __builtin_amdgcn_sched_barrier(0);
  const unsigned tid  = threadIdx.x;
  unsigned lane = tid & 63;
  unsigned wv   = __builtin_amdgcn_readfirstlane(tid >> 6);
  const unsigned lds0 = (unsigned)(size_t)(LAS unsigned char*)lds;
  const unsigned blk  = blockIdx.x;
  const unsigned role = (blk >> 2) & 1u;
  unsigned pk = (blk & 3u) * 32u + (blk >> 3);
  const unsigned sst  = (role ^ (blk >> 4)) & 1u;
  const unsigned t0   = pk * 49152u + sst * 24576u;
  const unsigned p0   = lds0 + 3072u * wv;
  const unsigned lq = lane >> 4, ph = (lane >> 2) & 3, pl = lane & 3;
  const unsigned gs = 256u * lq + 64u * ((ph - lq) & 3) + 16u * (pl ^ ((4u - wv) & 3));
  const unsigned ga = gs + 3072u * (wv < 3u ? wv : 5u);
  const unsigned gb = gs + 3072u * (wv < 2u ? wv + 6u : wv + 1u);
  const unsigned pv = blk >> 6, ps = (blk & 63u) ^ (4u * (pv & 1u)), pgr = (ps >> 2) & 7u, pi = (ps & 3u) * 2u + (pv >> 1);
  const unsigned ppg = min(pgr * 8u, 52u) + (pi & (0x13777777u >> (4u * pgr)) & 7u);
  const unsigned pop = (ppg << 21) + ((ps & 3u) * 4u + pv) * 0x1c080u + 0x8000u;
  unsigned pfire = ((role ^ ppg) & 1u) << 13;
  const unsigned pfo = (blk >> 3) * 9216u;
  const unsigned pfv = tid * 16u;
  const unsigned ty = (blk >> 3) & 1u;
  const unsigned tidx = (blk >> 3) * 4u + wv, tsel = tidx >> 6;
  const unsigned toff = (tidx & 63u) < 54u ? (tidx & 63u) << 21 : 113246204u;
  unsigned tres, pre1;
  unsigned long long pres, ep;
  asm volatile(
      "s_add_u32 m0, s43, 0x27000\n\t"
      "s_add_u32 s51, s42, 0x1000\n\t"
      "s_waitcnt lgkmcnt(0)\n\t"
      "buffer_load_dwordx4 v216, s[16:19], s42 offen lds\n\t"
      "buffer_load_dwordx4 v216, s[16:19], s51 offen lds\n\t"
      "s_add_u32 s51, s42, 0x2000\n\t"
      "buffer_load_dwordx4 v216, s[16:19], s51 offen lds\n\t"
      "s_cmp_lg_u32 s41, s43\n\t"
      "s_cbranch_scc1 .Lw0pi_%=\n\t"
      "s_cmp_eq_u32 s78, 0\n\t"
      "s_cselect_b64 s[96:97], s[20:21], s[28:29]\n\t"
      "s_load_dword s92, s[96:97], s76 offset:0x0\n\t"
      "s_load_dwordx2 s[60:61], s[96:97], s76 offset:0x3300\n\t"
      ".Lw0pi_%=:\n\t"
      "s_mov_b32 m0, s41\n\t"
      "s_nop 0\n\t"
      "buffer_load_dwordx4 v208, s[20:23], s40 offen lds\n\t"
      "buffer_load_dwordx4 v208, s[20:23], s40 offen offset:1024 lds\n\t"
      "buffer_load_dwordx4 v208, s[20:23], s40 offen offset:2048 lds\n\t"
      "s_add_u32 m0, s41, 0x3000\n\t"
      "s_nop 0\n\t"
      "buffer_load_dwordx4 v209, s[20:23], s40 offen lds\n\t"
      "buffer_load_dwordx4 v209, s[20:23], s40 offen offset:1024 lds\n\t"
      "buffer_load_dwordx4 v209, s[20:23], s40 offen offset:2048 lds\n\t"
      "s_add_u32 s51, s43, 0x2400\n\t"
      "s_cmp_lg_u32 s41, s51\n\t"
      "s_cbranch_scc1 .Lbd_%=\n\t"
      "s_add_u32 m0, s43, 0x27400\n\t"
      "s_sub_u32 s36, s36, 0xc00\n\t"
      "s_subb_u32 s37, s37, 0\n\t"
      "s_mov_b32 s51, 0\n\t"
      "buffer_load_dwordx4 v216, s[36:39], s51 offen lds\n\t"
      ".Lbd_%=:\n\t"
      "s_cmp_lg_u32 s41, s43\n\t"
      "s_cbranch_scc1 .Lw0pe_%=\n\t"
      "s_waitcnt lgkmcnt(2)\n\t"
      "s_nop 7\n\t"
      "s_getreg_b32 s51, hwreg(HW_REG_IB_STS, 8, 4)\n\t"
      "s_lshl_b32 s96, s51, 13\n\t"
      "s_xor_b32 s77, s77, s96\n\t"
      "s_and_b32 s77, s77, 0x2000\n\t"
      "s_cmp_lg_u32 s51, 0\n\t"
      "s_cselect_b32 s77, s77, 0x1000\n\t"
      ".Lw0pe_%=:\n\t"
      "s_load_dwordx2 s[62:63], s[24:25], 0x800\n\t"
      "s_cmp_eq_u32 s99, 0\n\t"
      "s_cselect_b64 s[96:97], s[20:21], s[28:29]\n\t"
      "s_load_dword s95, s[96:97], s98\n\t"
      : "={s95}"(tres), "={s92}"(pre1), "={s[60:61]}"(pres), "={s[62:63]}"(ep), "+{s77}"(pfire), "+v"(lane), "+s"(wv), "+s"(pk)
      : "{s[20:21]}"(xa), "{s[16:17]}"(wa), "{s[36:37]}"(ba), "{s[28:29]}"(oa), "{s[24:25]}"(sa), "{s[52:53]}"(rs),
        "{s[22:23]}"(0x0002000006c00000ull), "{s[26:27]}"(0x0002000000000808ull), "{s[30:31]}"(0x0002000006c00000ull),
        "{s[18:19]}"(0x0002000000048000ull), "{s[38:39]}"(0x0002000000000f00ull),
        "{s40}"(t0), "{s41}"(p0), "{s43}"(lds0), "{s94}"(ty),
        "{v208}"(ga), "{v209}"(gb),
        "{s42}"(pfo), "{s98}"(toff), "{s99}"(tsel), "{s76}"(pop), "{s78}"(ps >> 5),
        "{v216}"(pfv)
      : "memory", "scc", "s51", "s96", "s97");
  __builtin_amdgcn_sched_barrier(0);
  const unsigned sp = lane < 18u ? lane : lane - 14u;
  const unsigned ob = (unsigned)(((sp < 12u ? 0x41ccd62ca418820ull >> (5u * sp) : 0x22a4c1eeull >> (5u * ((sp - 12u) & 7u)))) & 31u) * 6291456u + pk * 49152u;
  const unsigned pg = (ob + 24576u) >> 21;
  const unsigned ta = lane < 18u ? ob + 24576u * sst : 113246208u, t1 = lane < 32u ? ob + 24576u * (sst ^ 1u) : 113246208u;
  const unsigned mk = lane >= 4u && lane < 18u ? 1u << (pg < 52u ? (pg >> 3) * 4u : 28u) : 0u;
  const unsigned plm = (unsigned)__builtin_amdgcn_ballot_w64(((pg ^ role ^ sst) & 1u) != 0u), plb = wv < 2u ? plm : ~plm;
  const unsigned vgv = 0u + wv * 512u + lane * 8u;
  const unsigned vpub = vgv == 0u ? 0u + (pv * 64u + ps) * 8u : 0x40000000u, vbmp = vpub == 0u ? 2048u : 0x40000000u;
  const unsigned vxr = lds0 + 159744u + lane * 16u, vxw = vxr + wv * 4u;
  const unsigned vfw = lds0 + 160768u + lane * 4u + wv * 256u, vfr = lds0 + 160768u + lane * 4u + (lane & 3u) * 256u;
  const unsigned li = lane & 15, lg = lane >> 4, rl = li & 3, hi = li >> 2;
  const unsigned rb = 768u * li + 16u * (lg ^ ((4u - hi) & 3)) - 3072u * wv;
  unsigned cfa[4], csw[3];
#pragma unroll
  for (unsigned u = 0; u < 4; ++u) cfa[u] = rb + 64u * ((u - rl) & 3);
#pragma unroll
  for (unsigned j = 0; j < 3; ++j) {
    const unsigned e = 3u * wv + j;
    csw[j] = rb + 256u * (e >> 2) + 64u * (((e & 3) + (e >> 2) - rl) & 3);
  }
  const unsigned csr = 16u * lane;
  const unsigned tbase = p0 + (4u * lg + (li >> 2)) * 192u + 16u * (li & 3);
  unsigned woff[3];
#pragma unroll
  for (unsigned j = 0; j < 3; ++j) {
    const unsigned q = 64u * j + lane, rr = q / 12u;
    woff[j] = (8u * (rr >> 2) + (rr & 3)) * 768u + 192u * wv + 16u * (q % 12u) - 1024u * j;
  }
  const unsigned boff = lds0 + 160768u + 192u * wv + 16u * lg;
  asm volatile(
      "s_cmp_eq_u32 s94, 1\n\t"
      "s_cbranch_scc0 .Lc0_%=\n\t"
      "s_getpc_b64 s[96:97]\n\t"
      ".Lcp_%=:\n\t"
      "s_add_u32 s98, s98, .Lcq_%=-.Lcp_%=\n\t"
      "s_min_u32 s98, s98, .Lend_%=-.Lcp_%=-68\n\t"
      "s_load_dword s99, s[96:97], s98 offset:64\n\t"
      "s_load_dword s98, s[96:97], s98\n\t"
      "s_add_u32 m0, s41, 0x6000\n\t"
      "s_add_u32 s51, s40, 0x600000\n\t"
      "buffer_load_dwordx4 v208, s[20:23], s51 offen lds\n\t"
      "buffer_load_dwordx4 v208, s[20:23], s51 offen offset:1024 lds\n\t"
      "buffer_load_dwordx4 v208, s[20:23], s51 offen offset:2048 lds\n\t"
      "s_add_u32 m0, s41, 0x9000\n\t"
      "s_add_u32 s51, s40, 0x600000\n\t"
      "buffer_load_dwordx4 v209, s[20:23], s51 offen lds\n\t"
      "buffer_load_dwordx4 v209, s[20:23], s51 offen offset:1024 lds\n\t"
      "buffer_load_dwordx4 v209, s[20:23], s51 offen offset:2048 lds\n\t"
      ".Lc0_%=:\n\t"
      "s_add_u32 m0, s41, 0x1e000\n\t"
      "s_mov_b32 s51, 0x0\n\t"
      "buffer_load_dwordx4 v211, s[16:19], s51 offen lds\n\t"
      "buffer_load_dwordx4 v212, s[16:19], s51 offen offset:1024 lds\n\t"
      "buffer_load_dwordx4 v213, s[16:19], s51 offen offset:2048 lds\n\t"
      "s_add_u32 m0, s41, 0x21000\n\t"
      "s_mov_b32 s51, 0xc00\n\t"
      "buffer_load_dwordx4 v211, s[16:19], s51 offen lds\n\t"
      "buffer_load_dwordx4 v212, s[16:19], s51 offen offset:1024 lds\n\t"
      "buffer_load_dwordx4 v213, s[16:19], s51 offen offset:2048 lds\n\t"
      "s_add_u32 m0, s41, 0x24000\n\t"
      "s_mov_b32 s51, 0x6000\n\t"
      "buffer_load_dwordx4 v211, s[16:19], s51 offen lds\n\t"
      "buffer_load_dwordx4 v212, s[16:19], s51 offen offset:1024 lds\n\t"
      "buffer_load_dwordx4 v213, s[16:19], s51 offen offset:2048 lds\n\t"
      "s_add_u32 m0, s41, 0x12000\n\t"
      "s_mov_b32 s51, 0x6c00\n\t"
      "buffer_load_dwordx4 v211, s[16:19], s51 offen lds\n\t"
      "buffer_load_dwordx4 v212, s[16:19], s51 offen offset:1024 lds\n\t"
      "buffer_load_dwordx4 v213, s[16:19], s51 offen offset:2048 lds\n\t"
      "s_add_u32 m0, s41, 0x15000\n\t"
      "s_mov_b32 s51, 0xc000\n\t"
      "buffer_load_dwordx4 v211, s[16:19], s51 offen lds\n\t"
      "buffer_load_dwordx4 v212, s[16:19], s51 offen offset:1024 lds\n\t"
      "buffer_load_dwordx4 v213, s[16:19], s51 offen offset:2048 lds\n\t"
      "s_add_u32 m0, s41, 0x18000\n\t"
      "s_mov_b32 s51, 0xcc00\n\t"
      "buffer_load_dwordx4 v211, s[16:19], s51 offen lds\n\t"
      "buffer_load_dwordx4 v212, s[16:19], s51 offen offset:1024 lds\n\t"
      "buffer_load_dwordx4 v213, s[16:19], s51 offen offset:2048 lds\n\t"
      "s_add_u32 m0, s41, 0x1b000\n\t"
      "s_mov_b32 s51, 0x12000\n\t"
      "buffer_load_dwordx4 v211, s[16:19], s51 offen lds\n\t"
      "buffer_load_dwordx4 v212, s[16:19], s51 offen offset:1024 lds\n\t"
      "buffer_load_dwordx4 v213, s[16:19], s51 offen offset:2048 lds\n\t"
      "s_mov_b32 s44, 0\n\t"
      "s_mov_b32 s85, 3\n\t"
      "s_add_u32 s50, s41, 0x12000\n\t"
      "s_mov_b32 s45, s41\n\t"
      "s_add_u32 s46, s41, 0x6000\n\t"
      "s_add_u32 s47, s41, 0xc000\n\t"
      "s_mov_b32 s84, 98\n\t"
      "v_add_u32_e32 v196, s41, v200\n\t"
      "v_add_u32_e32 v197, s41, v201\n\t"
      "v_add_u32_e32 v198, s41, v202\n\t"
      "v_add_u32_e32 v199, s41, v203\n\t"
      "v_add_u32_e32 v217, s41, v204\n\t"
      "v_add_u32_e32 v218, s41, v205\n\t"
      "v_add_u32_e32 v219, s41, v206\n\t"
      "v_add_u32_e32 v220, s41, v207\n\t"
      "s_waitcnt vmcnt(18)\n\t"
      "s_waitcnt lgkmcnt(0)\n\t"
      "s_barrier\n\t"
      "s_memrealtime s[68:69]\n\t"
      "ds_read_b128 v[226:229], v215 offset:0\n\t"
      "ds_read_b128 v[230:233], v215 offset:64\n\t"
      "ds_read_b128 v[234:237], v215 offset:128\n\t"
      "v_add_u32_e32 v221, 0x1e000, v214\n\t"
      "ds_read_b64_tr_b16 a[0:1], v221 offset:0\n\t"
      "ds_read_b64_tr_b16 a[4:5], v221 offset:8\n\t"
      "ds_read_b64_tr_b16 a[8:9], v221 offset:64\n\t"
      "ds_read_b64_tr_b16 a[12:13], v221 offset:72\n\t"
      "ds_read_b64_tr_b16 a[16:17], v221 offset:128\n\t"
      "ds_read_b64_tr_b16 a[20:21], v221 offset:136\n\t"
      "ds_read_b128 v[140:143], v196 offset:0\n\t"
      "s_waitcnt lgkmcnt(7)\n\t"
      "v_lshlrev_b32_e32 v8, 16, v226\n\t"
      "v_and_b32_e32 v9, 0xffff0000, v226\n\t"
      "v_lshlrev_b32_e32 v10, 16, v227\n\t"
      "v_and_b32_e32 v11, 0xffff0000, v227\n\t"
      "v_lshlrev_b32_e32 v12, 16, v228\n\t"
      "v_and_b32_e32 v13, 0xffff0000, v228\n\t"
      "v_lshlrev_b32_e32 v14, 16, v229\n\t"
      "v_and_b32_e32 v15, 0xffff0000, v229\n\t"
      "v_lshlrev_b32_e32 v16, 16, v230\n\t"
      "v_and_b32_e32 v17, 0xffff0000, v230\n\t"
      "v_lshlrev_b32_e32 v18, 16, v231\n\t"
      "v_and_b32_e32 v19, 0xffff0000, v231\n\t"
      "v_lshlrev_b32_e32 v20, 16, v232\n\t"
      "v_and_b32_e32 v21, 0xffff0000, v232\n\t"
      "v_lshlrev_b32_e32 v22, 16, v233\n\t"
      "v_and_b32_e32 v23, 0xffff0000, v233\n\t"
      "v_lshlrev_b32_e32 v24, 16, v234\n\t"
      "v_and_b32_e32 v25, 0xffff0000, v234\n\t"
      "v_lshlrev_b32_e32 v26, 16, v235\n\t"
      "v_and_b32_e32 v27, 0xffff0000, v235\n\t"
      "v_lshlrev_b32_e32 v28, 16, v236\n\t"
      "v_and_b32_e32 v29, 0xffff0000, v236\n\t"
      "v_lshlrev_b32_e32 v30, 16, v237\n\t"
      "v_and_b32_e32 v31, 0xffff0000, v237\n\t"
      "s_add_u32 s62, s62, 0x7f4a7c15\n\t"
      "s_add_u32 s63, s63, 0x9e3779b9\n\t"
      "s_waitcnt vmcnt(15)\n\t"
      "v_add_u32_e32 v221, 0x21000, v214\n\t"
      "ds_read_b64_tr_b16 a[2:3], v221 offset:0\n\t"
      "ds_read_b64_tr_b16 a[6:7], v221 offset:8\n\t"
      "ds_read_b64_tr_b16 a[10:11], v221 offset:64\n\t"
      "ds_read_b64_tr_b16 a[14:15], v221 offset:72\n\t"
      "ds_read_b64_tr_b16 a[18:19], v221 offset:128\n\t"
      "ds_read_b64_tr_b16 a[22:23], v221 offset:136\n\t"
      "ds_read_b128 v[152:155], v196 offset:12288\n\t"
      "s_waitcnt lgkmcnt(8)\n\t"
      "s_add_u32 m0, s41, 0x1e000\n\t"
      "s_mov_b32 s51, 0x12c00\n\t"
      "buffer_load_dwordx4 v211, s[16:19], s51 offen lds\n\t"
      "buffer_load_dwordx4 v212, s[16:19], s51 offen offset:1024 lds\n\t"
      "buffer_load_dwordx4 v213, s[16:19], s51 offen offset:2048 lds\n\t"
      "s_lshl_b32 s67, s68, 18\n\t"
      "s_bfe_u32 s51, s62, 0xc0000\n\t"
      "s_and_b32 s67, s67, 0xfff00000\n\t"
      "s_or_b32 s67, s67, s51\n\t"
      "s_cmp_lg_u32 s41, s43\n\t"
      "s_cbranch_scc1 .Lw0pb_%=\n\t"
      "s_or_b32 s77, s77, s67\n\t"
      "v_mov_b32_e32 v241, s63\n\t"
      "v_mov_b32_e32 v240, s77\n\t"
      "buffer_store_dwordx2 v[240:241], v244, s[24:27], 0 offen sc1\n\t"
      ".Lw0pb_%=:\n\t"
      "s_waitcnt vmcnt(15)\n\t"
      "v_add_u32_e32 v221, 0x24000, v214\n\t"
      "ds_read_b64_tr_b16 a[24:25], v221 offset:0\n\t"
      "ds_read_b64_tr_b16 a[28:29], v221 offset:8\n\t"
      "ds_read_b64_tr_b16 a[32:33], v221 offset:64\n\t"
      "ds_read_b64_tr_b16 a[36:37], v221 offset:72\n\t"
      "ds_read_b64_tr_b16 a[40:41], v221 offset:128\n\t"
      "ds_read_b64_tr_b16 a[44:45], v221 offset:136\n\t"
      "ds_read_b128 v[144:147], v197 offset:0\n\t"
      "s_waitcnt lgkmcnt(8)\n\t"
      "v_mfma_f32_16x16x32_bf16 v[80:83], a[0:3], v[140:143], v[8:11]\n\t"
      "s_add_u32 m0, s41, 0x21000\n\t"
      "s_mov_b32 s51, 0x18000\n\t"
      "buffer_load_dwordx4 v211, s[16:19], s51 offen lds\n\t"
      "buffer_load_dwordx4 v212, s[16:19], s51 offen offset:1024 lds\n\t"
      "v_mfma_f32_16x16x32_bf16 v[84:87], a[4:7], v[140:143], v[12:15]\n\t"
      "buffer_load_dwordx4 v213, s[16:19], s51 offen offset:2048 lds\n\t"
      "v_mfma_f32_16x16x32_bf16 v[88:91], a[8:11], v[140:143], v[16:19]\n\t"
      "s_sub_u32 s67, s67, 0x20000000\n\t"
      "s_andn2_b32 s68, s68, 3\n\t"
      "s_sub_u32 s68, s68, s52\n\t"
      "s_waitcnt vmcnt(15)\n\t"
      "v_add_u32_e32 v221, 0x12000, v214\n\t"
      "ds_read_b64_tr_b16 a[26:27], v221 offset:0\n\t"
      "v_mfma_f32_16x16x32_bf16 v[92:95], a[12:15], v[140:143], v[20:23]\n\t"
      "ds_read_b64_tr_b16 a[30:31], v221 offset:8\n\t"
      "ds_read_b64_tr_b16 a[34:35], v221 offset:64\n\t"
      "v_mfma_f32_16x16x32_bf16 v[96:99], a[16:19], v[140:143], v[24:27]\n\t"
      "ds_read_b64_tr_b16 a[38:39], v221 offset:72\n\t"
      "ds_read_b64_tr_b16 a[42:43], v221 offset:128\n\t"
      "v_mfma_f32_16x16x32_bf16 v[100:103], a[20:23], v[140:143], v[28:31]\n\t"
      "ds_read_b64_tr_b16 a[46:47], v221 offset:136\n\t"
      "ds_read_b128 v[156:159], v197 offset:12288\n\t"
      "s_waitcnt lgkmcnt(8)\n\t"
      "v_mfma_f32_16x16x32_bf16 v[104:107], a[0:3], v[152:155], v[8:11]\n\t"
      "s_add_u32 m0, s41, 0x24000\n\t"
      "s_mov_b32 s51, 0x18c00\n\t"
      "buffer_load_dwordx4 v211, s[16:19], s51 offen lds\n\t"
      "v_mfma_f32_16x16x32_bf16 v[108:111], a[4:7], v[152:155], v[12:15]\n\t"
      "buffer_load_dwordx4 v212, s[16:19], s51 offen offset:1024 lds\n\t"
      "v_mfma_f32_16x16x32_bf16 v[112:115], a[8:11], v[152:155], v[16:19]\n\t"
      "buffer_load_dwordx4 v213, s[16:19], s51 offen offset:2048 lds\n\t"
      "s_waitcnt vmcnt(15)\n\t"
      "v_add_u32_e32 v221, 0x15000, v214\n\t"
      "ds_read_b64_tr_b16 a[48:49], v221 offset:0\n\t"
      "v_mfma_f32_16x16x32_bf16 v[116:119], a[12:15], v[152:155], v[20:23]\n\t"
      "ds_read_b64_tr_b16 a[52:53], v221 offset:8\n\t"
      "ds_read_b64_tr_b16 a[56:57], v221 offset:64\n\t"
      "v_mfma_f32_16x16x32_bf16 v[120:123], a[16:19], v[152:155], v[24:27]\n\t"
      "ds_read_b64_tr_b16 a[60:61], v221 offset:72\n\t"
      "ds_read_b64_tr_b16 a[64:65], v221 offset:128\n\t"
      "v_mfma_f32_16x16x32_bf16 v[132:135], a[20:23], v[152:155], v[28:31]\n\t"
      "ds_read_b64_tr_b16 a[68:69], v221 offset:136\n\t"
      "ds_read_b128 v[148:151], v198 offset:0\n\t"
      "s_waitcnt lgkmcnt(8)\n\t"
      "v_mfma_f32_16x16x32_bf16 v[80:83], a[24:27], v[144:147], v[80:83]\n\t"
      "s_add_u32 m0, s41, 0x12000\n\t"
      "s_mov_b32 s51, 0x1e000\n\t"
      "buffer_load_dwordx4 v211, s[16:19], s51 offen lds\n\t"
      "v_mfma_f32_16x16x32_bf16 v[84:87], a[28:31], v[144:147], v[84:87]\n\t"
      "buffer_load_dwordx4 v212, s[16:19], s51 offen offset:1024 lds\n\t"
      "v_mfma_f32_16x16x32_bf16 v[88:91], a[32:35], v[144:147], v[88:91]\n\t"
      "buffer_load_dwordx4 v213, s[16:19], s51 offen offset:2048 lds\n\t"
      "s_waitcnt vmcnt(15)\n\t"
      "v_add_u32_e32 v221, 0x18000, v214\n\t"
      "ds_read_b64_tr_b16 a[50:51], v221 offset:0\n\t"
      "v_mfma_f32_16x16x32_bf16 v[92:95], a[36:39], v[144:147], v[92:95]\n\t"
      "ds_read_b64_tr_b16 a[54:55], v221 offset:8\n\t"
      "ds_read_b64_tr_b16 a[58:59], v221 offset:64\n\t"
      "v_mfma_f32_16x16x32_bf16 v[96:99], a[40:43], v[144:147], v[96:99]\n\t"
      "ds_read_b64_tr_b16 a[62:63], v221 offset:72\n\t"
      "ds_read_b64_tr_b16 a[66:67], v221 offset:128\n\t"
      "v_mfma_f32_16x16x32_bf16 v[100:103], a[44:47], v[144:147], v[100:103]\n\t"
      "ds_read_b64_tr_b16 a[70:71], v221 offset:136\n\t"
      "ds_read_b128 v[160:163], v198 offset:12288\n\t"
      "s_waitcnt lgkmcnt(8)\n\t"
      "v_mfma_f32_16x16x32_bf16 v[104:107], a[24:27], v[156:159], v[104:107]\n\t"
      "s_add_u32 m0, s41, 0x15000\n\t"
      "s_mov_b32 s51, 0x1ec00\n\t"
      "buffer_load_dwordx4 v211, s[16:19], s51 offen lds\n\t"
      "v_mfma_f32_16x16x32_bf16 v[108:111], a[28:31], v[156:159], v[108:111]\n\t"
      "buffer_load_dwordx4 v212, s[16:19], s51 offen offset:1024 lds\n\t"
      "v_mfma_f32_16x16x32_bf16 v[112:115], a[32:35], v[156:159], v[112:115]\n\t"
      "buffer_load_dwordx4 v213, s[16:19], s51 offen offset:2048 lds\n\t"
      "s_waitcnt vmcnt(15)\n\t"
      "v_add_u32_e32 v221, 0x1b000, v214\n\t"
      "ds_read_b64_tr_b16 a[72:73], v221 offset:0\n\t"
      "v_mfma_f32_16x16x32_bf16 v[116:119], a[36:39], v[156:159], v[116:119]\n\t"
      "ds_read_b64_tr_b16 a[76:77], v221 offset:8\n\t"
      "ds_read_b64_tr_b16 a[80:81], v221 offset:64\n\t"
      "v_mfma_f32_16x16x32_bf16 v[120:123], a[40:43], v[156:159], v[120:123]\n\t"
      "ds_read_b64_tr_b16 a[84:85], v221 offset:72\n\t"
      "ds_read_b64_tr_b16 a[88:89], v221 offset:128\n\t"
      "v_mfma_f32_16x16x32_bf16 v[132:135], a[44:47], v[156:159], v[132:135]\n\t"
      "ds_read_b64_tr_b16 a[92:93], v221 offset:136\n\t"
      "ds_read_b128 v[140:143], v199 offset:0\n\t"
      "s_waitcnt lgkmcnt(8)\n\t"
      "v_mfma_f32_16x16x32_bf16 v[80:83], a[48:51], v[148:151], v[80:83]\n\t"
      "s_add_u32 m0, s41, 0x18000\n\t"
      "s_mov_b32 s51, 0x24000\n\t"
      "buffer_load_dwordx4 v211, s[16:19], s51 offen lds\n\t"
      "v_mfma_f32_16x16x32_bf16 v[84:87], a[52:55], v[148:151], v[84:87]\n\t"
      "buffer_load_dwordx4 v212, s[16:19], s51 offen offset:1024 lds\n\t"
      "v_mfma_f32_16x16x32_bf16 v[88:91], a[56:59], v[148:151], v[88:91]\n\t"
      "buffer_load_dwordx4 v213, s[16:19], s51 offen offset:2048 lds\n\t"
      "s_waitcnt vmcnt(15)\n\t"
      "v_add_u32_e32 v221, 0x1e000, v214\n\t"
      "ds_read_b64_tr_b16 a[74:75], v221 offset:0\n\t"
      "v_mfma_f32_16x16x32_bf16 v[92:95], a[60:63], v[148:151], v[92:95]\n\t"
      "ds_read_b64_tr_b16 a[78:79], v221 offset:8\n\t"
      "ds_read_b64_tr_b16 a[82:83], v221 offset:64\n\t"
      "v_mfma_f32_16x16x32_bf16 v[96:99], a[64:67], v[148:151], v[96:99]\n\t"
      "ds_read_b64_tr_b16 a[86:87], v221 offset:72\n\t"
      "ds_read_b64_tr_b16 a[90:91], v221 offset:128\n\t"
      "v_mfma_f32_16x16x32_bf16 v[100:103], a[68:71], v[148:151], v[100:103]\n\t"
      "ds_read_b64_tr_b16 a[94:95], v221 offset:136\n\t"
      "ds_read_b128 v[152:155], v199 offset:12288\n\t"
      "s_waitcnt lgkmcnt(8)\n\t"
      "v_mfma_f32_16x16x32_bf16 v[104:107], a[48:51], v[160:163], v[104:107]\n\t"
      "s_add_u32 m0, s41, 0x1b000\n\t"
      "s_mov_b32 s51, 0x24c00\n\t"
      "buffer_load_dwordx4 v211, s[16:19], s51 offen lds\n\t"
      "v_mfma_f32_16x16x32_bf16 v[108:111], a[52:55], v[160:163], v[108:111]\n\t"
      "buffer_load_dwordx4 v212, s[16:19], s51 offen offset:1024 lds\n\t"
      "v_mfma_f32_16x16x32_bf16 v[112:115], a[56:59], v[160:163], v[112:115]\n\t"
      "buffer_load_dwordx4 v213, s[16:19], s51 offen offset:2048 lds\n\t"
      "s_waitcnt vmcnt(15)\n\t"
      "v_add_u32_e32 v221, 0x21000, v214\n\t"
      "ds_read_b64_tr_b16 a[96:97], v221 offset:0\n\t"
      "v_mfma_f32_16x16x32_bf16 v[116:119], a[60:63], v[160:163], v[116:119]\n\t"
      "ds_read_b64_tr_b16 a[100:101], v221 offset:8\n\t"
      "ds_read_b64_tr_b16 a[104:105], v221 offset:64\n\t"
      "v_mfma_f32_16x16x32_bf16 v[120:123], a[64:67], v[160:163], v[120:123]\n\t"
      "ds_read_b64_tr_b16 a[108:109], v221 offset:72\n\t"
      "ds_read_b64_tr_b16 a[112:113], v221 offset:128\n\t"
      "v_mfma_f32_16x16x32_bf16 v[132:135], a[68:71], v[160:163], v[132:135]\n\t"
      "ds_read_b64_tr_b16 a[116:117], v221 offset:136\n\t"
      "ds_read_b128 v[144:147], v197 offset:256\n\t"
      "s_waitcnt lgkmcnt(8)\n\t"
      "v_mfma_f32_16x16x32_bf16 v[80:83], a[72:75], v[140:143], v[80:83]\n\t"
      "s_add_u32 m0, s41, 0x1e000\n\t"
      "s_mov_b32 s51, 0x2a000\n\t"
      "buffer_load_dwordx4 v211, s[16:19], s51 offen lds\n\t"
      "v_mfma_f32_16x16x32_bf16 v[84:87], a[76:79], v[140:143], v[84:87]\n\t"
      "buffer_load_dwordx4 v212, s[16:19], s51 offen offset:1024 lds\n\t"
      "v_mfma_f32_16x16x32_bf16 v[88:91], a[80:83], v[140:143], v[88:91]\n\t"
      "buffer_load_dwordx4 v213, s[16:19], s51 offen offset:2048 lds\n\t"
      "s_waitcnt vmcnt(15)\n\t"
      "v_add_u32_e32 v221, 0x24000, v214\n\t"
      "ds_read_b64_tr_b16 a[98:99], v221 offset:0\n\t"
      "v_mfma_f32_16x16x32_bf16 v[92:95], a[84:87], v[140:143], v[92:95]\n\t"
      "ds_read_b64_tr_b16 a[102:103], v221 offset:8\n\t"
      "ds_read_b64_tr_b16 a[106:107], v221 offset:64\n\t"
      "v_mfma_f32_16x16x32_bf16 v[96:99], a[88:91], v[140:143], v[96:99]\n\t"
      "ds_read_b64_tr_b16 a[110:111], v221 offset:72\n\t"
      "ds_read_b64_tr_b16 a[114:115], v221 offset:128\n\t"
      "v_mfma_f32_16x16x32_bf16 v[100:103], a[92:95], v[140:143], v[100:103]\n\t"
      "ds_read_b64_tr_b16 a[118:119], v221 offset:136\n\t"
      "ds_read_b128 v[156:159], v197 offset:12544\n\t"
      "s_waitcnt lgkmcnt(8)\n\t"
      "v_mfma_f32_16x16x32_bf16 v[104:107], a[72:75], v[152:155], v[104:107]\n\t"
      "s_add_u32 m0, s41, 0x21000\n\t"
      "s_mov_b32 s51, 0x2ac00\n\t"
      "buffer_load_dwordx4 v211, s[16:19], s51 offen lds\n\t"
      "v_mfma_f32_16x16x32_bf16 v[108:111], a[76:79], v[152:155], v[108:111]\n\t"
      "buffer_load_dwordx4 v212, s[16:19], s51 offen offset:1024 lds\n\t"
      "v_mfma_f32_16x16x32_bf16 v[112:115], a[80:83], v[152:155], v[112:115]\n\t"
      "buffer_load_dwordx4 v213, s[16:19], s51 offen offset:2048 lds\n\t"
      "s_waitcnt vmcnt(15)\n\t"
      "v_add_u32_e32 v221, 0x12000, v214\n\t"
      "ds_read_b64_tr_b16 a[120:121], v221 offset:0\n\t"
      "v_mfma_f32_16x16x32_bf16 v[116:119], a[84:87], v[152:155], v[116:119]\n\t"
      "ds_read_b64_tr_b16 a[124:125], v221 offset:8\n\t"
      "ds_read_b64_tr_b16 a[128:129], v221 offset:64\n\t"
      "v_mfma_f32_16x16x32_bf16 v[120:123], a[88:91], v[152:155], v[120:123]\n\t"
      "ds_read_b64_tr_b16 a[132:133], v221 offset:72\n\t"
      "ds_read_b64_tr_b16 a[136:137], v221 offset:128\n\t"
      "v_mfma_f32_16x16x32_bf16 v[132:135], a[92:95], v[152:155], v[132:135]\n\t"
      "ds_read_b64_tr_b16 a[140:141], v221 offset:136\n\t"
      "ds_read_b128 v[148:151], v198 offset:256\n\t"
      "s_waitcnt lgkmcnt(8)\n\t"
      "v_mfma_f32_16x16x32_bf16 v[80:83], a[96:99], v[144:147], v[80:83]\n\t"
      "s_add_u32 m0, s41, 0x24000\n\t"
      "s_mov_b32 s51, 0x30000\n\t"
      "buffer_load_dwordx4 v211, s[16:19], s51 offen lds\n\t"
      "v_mfma_f32_16x16x32_bf16 v[84:87], a[100:103], v[144:147], v[84:87]\n\t"
      "buffer_load_dwordx4 v212, s[16:19], s51 offen offset:1024 lds\n\t"
      "v_mfma_f32_16x16x32_bf16 v[88:91], a[104:107], v[144:147], v[88:91]\n\t"
      "buffer_load_dwordx4 v213, s[16:19], s51 offen offset:2048 lds\n\t"
      "s_waitcnt vmcnt(15)\n\t"
      "v_add_u32_e32 v221, 0x15000, v214\n\t"
      "ds_read_b64_tr_b16 a[122:123], v221 offset:0\n\t"
      "v_mfma_f32_16x16x32_bf16 v[92:95], a[108:111], v[144:147], v[92:95]\n\t"
      "ds_read_b64_tr_b16 a[126:127], v221 offset:8\n\t"
      "ds_read_b64_tr_b16 a[130:131], v221 offset:64\n\t"
      "v_mfma_f32_16x16x32_bf16 v[96:99], a[112:115], v[144:147], v[96:99]\n\t"
      "ds_read_b64_tr_b16 a[134:135], v221 offset:72\n\t"
      "ds_read_b64_tr_b16 a[138:139], v221 offset:128\n\t"
      "v_mfma_f32_16x16x32_bf16 v[100:103], a[116:119], v[144:147], v[100:103]\n\t"
      "ds_read_b64_tr_b16 a[142:143], v221 offset:136\n\t"
      "ds_read_b128 v[160:163], v198 offset:12544\n\t"
      "s_waitcnt lgkmcnt(8)\n\t"
      "v_mfma_f32_16x16x32_bf16 v[104:107], a[96:99], v[156:159], v[104:107]\n\t"
      "s_add_u32 m0, s41, 0x12000\n\t"
      "s_mov_b32 s51, 0x30c00\n\t"
      "buffer_load_dwordx4 v211, s[16:19], s51 offen lds\n\t"
      "v_mfma_f32_16x16x32_bf16 v[108:111], a[100:103], v[156:159], v[108:111]\n\t"
      "buffer_load_dwordx4 v212, s[16:19], s51 offen offset:1024 lds\n\t"
      "v_mfma_f32_16x16x32_bf16 v[112:115], a[104:107], v[156:159], v[112:115]\n\t"
      "buffer_load_dwordx4 v213, s[16:19], s51 offen offset:2048 lds\n\t"
      "s_waitcnt vmcnt(15)\n\t"
      "v_add_u32_e32 v221, 0x18000, v214\n\t"
      "ds_read_b64_tr_b16 a[144:145], v221 offset:0\n\t"
      "v_mfma_f32_16x16x32_bf16 v[116:119], a[108:111], v[156:159], v[116:119]\n\t"
      "ds_read_b64_tr_b16 a[148:149], v221 offset:8\n\t"
      "ds_read_b64_tr_b16 a[152:153], v221 offset:64\n\t"
      "v_mfma_f32_16x16x32_bf16 v[120:123], a[112:115], v[156:159], v[120:123]\n\t"
      "ds_read_b64_tr_b16 a[156:157], v221 offset:72\n\t"
      "ds_read_b64_tr_b16 a[160:161], v221 offset:128\n\t"
      "v_mfma_f32_16x16x32_bf16 v[132:135], a[116:119], v[156:159], v[132:135]\n\t"
      "ds_read_b64_tr_b16 a[164:165], v221 offset:136\n\t"
      "ds_read_b128 v[140:143], v199 offset:256\n\t"
      "s_waitcnt lgkmcnt(8)\n\t"
      "v_mfma_f32_16x16x32_bf16 v[80:83], a[120:123], v[148:151], v[80:83]\n\t"
      "s_add_u32 m0, s41, 0x15000\n\t"
      "s_mov_b32 s51, 0x36000\n\t"
      "buffer_load_dwordx4 v211, s[16:19], s51 offen lds\n\t"
      "v_mfma_f32_16x16x32_bf16 v[84:87], a[124:127], v[148:151], v[84:87]\n\t"
      "buffer_load_dwordx4 v212, s[16:19], s51 offen offset:1024 lds\n\t"
      "v_mfma_f32_16x16x32_bf16 v[88:91], a[128:131], v[148:151], v[88:91]\n\t"
      "buffer_load_dwordx4 v213, s[16:19], s51 offen offset:2048 lds\n\t"
      "s_waitcnt vmcnt(15)\n\t"
      "v_add_u32_e32 v221, 0x1b000, v214\n\t"
      "ds_read_b64_tr_b16 a[146:147], v221 offset:0\n\t"
      "v_mfma_f32_16x16x32_bf16 v[92:95], a[132:135], v[148:151], v[92:95]\n\t"
      "ds_read_b64_tr_b16 a[150:151], v221 offset:8\n\t"
      "ds_read_b64_tr_b16 a[154:155], v221 offset:64\n\t"
      "v_mfma_f32_16x16x32_bf16 v[96:99], a[136:139], v[148:151], v[96:99]\n\t"
      "ds_read_b64_tr_b16 a[158:159], v221 offset:72\n\t"
      "ds_read_b64_tr_b16 a[162:163], v221 offset:128\n\t"
      "v_mfma_f32_16x16x32_bf16 v[100:103], a[140:143], v[148:151], v[100:103]\n\t"
      "ds_read_b64_tr_b16 a[166:167], v221 offset:136\n\t"
      "ds_read_b128 v[152:155], v199 offset:12544\n\t"
      "s_waitcnt lgkmcnt(8)\n\t"
      "v_mfma_f32_16x16x32_bf16 v[104:107], a[120:123], v[160:163], v[104:107]\n\t"
      "s_add_u32 m0, s41, 0x18000\n\t"
      "s_mov_b32 s51, 0x36c00\n\t"
      "buffer_load_dwordx4 v211, s[16:19], s51 offen lds\n\t"
      "v_mfma_f32_16x16x32_bf16 v[108:111], a[124:127], v[160:163], v[108:111]\n\t"
      "buffer_load_dwordx4 v212, s[16:19], s51 offen offset:1024 lds\n\t"
      "v_mfma_f32_16x16x32_bf16 v[112:115], a[128:131], v[160:163], v[112:115]\n\t"
      "buffer_load_dwordx4 v213, s[16:19], s51 offen offset:2048 lds\n\t"
      "s_waitcnt vmcnt(15)\n\t"
      "v_add_u32_e32 v221, 0x1e000, v214\n\t"
      "ds_read_b64_tr_b16 a[168:169], v221 offset:0\n\t"
      "v_mfma_f32_16x16x32_bf16 v[116:119], a[132:135], v[160:163], v[116:119]\n\t"
      "ds_read_b64_tr_b16 a[172:173], v221 offset:8\n\t"
      "ds_read_b64_tr_b16 a[176:177], v221 offset:64\n\t"
      "v_mfma_f32_16x16x32_bf16 v[120:123], a[136:139], v[160:163], v[120:123]\n\t"
      "ds_read_b64_tr_b16 a[180:181], v221 offset:72\n\t"
      "ds_read_b64_tr_b16 a[184:185], v221 offset:128\n\t"
      "v_mfma_f32_16x16x32_bf16 v[132:135], a[140:143], v[160:163], v[132:135]\n\t"
      "ds_read_b64_tr_b16 a[188:189], v221 offset:136\n\t"
      "ds_read_b128 v[144:147], v196 offset:256\n\t"
      "s_waitcnt lgkmcnt(8)\n\t"
      "v_mfma_f32_16x16x32_bf16 v[80:83], a[144:147], v[140:143], v[80:83]\n\t"
      "s_add_u32 m0, s41, 0x1b000\n\t"
      "s_mov_b32 s51, 0x3c000\n\t"
      "buffer_load_dwordx4 v211, s[16:19], s51 offen lds\n\t"
      "v_mfma_f32_16x16x32_bf16 v[84:87], a[148:151], v[140:143], v[84:87]\n\t"
      "buffer_load_dwordx4 v212, s[16:19], s51 offen offset:1024 lds\n\t"
      "v_mfma_f32_16x16x32_bf16 v[88:91], a[152:155], v[140:143], v[88:91]\n\t"
      "buffer_load_dwordx4 v213, s[16:19], s51 offen offset:2048 lds\n\t"
      "s_waitcnt vmcnt(15)\n\t"
      "v_add_u32_e32 v221, 0x21000, v214\n\t"
      "ds_read_b64_tr_b16 a[170:171], v221 offset:0\n\t"
      "v_mfma_f32_16x16x32_bf16 v[92:95], a[156:159], v[140:143], v[92:95]\n\t"
      "ds_read_b64_tr_b16 a[174:175], v221 offset:8\n\t"
      "ds_read_b64_tr_b16 a[178:179], v221 offset:64\n\t"
      "v_mfma_f32_16x16x32_bf16 v[96:99], a[160:163], v[140:143], v[96:99]\n\t"
      "ds_read_b64_tr_b16 a[182:183], v221 offset:72\n\t"
      "ds_read_b64_tr_b16 a[186:187], v221 offset:128\n\t"
      "v_mfma_f32_16x16x32_bf16 v[100:103], a[164:167], v[140:143], v[100:103]\n\t"
      "ds_read_b64_tr_b16 a[190:191], v221 offset:136\n\t"
      "ds_read_b128 v[156:159], v196 offset:12544\n\t"
      "s_waitcnt lgkmcnt(8)\n\t"
      "v_mfma_f32_16x16x32_bf16 v[104:107], a[144:147], v[152:155], v[104:107]\n\t"
      "s_add_u32 m0, s41, 0x1e000\n\t"
      "s_mov_b32 s51, 0x3cc00\n\t"
      "buffer_load_dwordx4 v211, s[16:19], s51 offen lds\n\t"
      "v_mfma_f32_16x16x32_bf16 v[108:111], a[148:151], v[152:155], v[108:111]\n\t"
      "buffer_load_dwordx4 v212, s[16:19], s51 offen offset:1024 lds\n\t"
      "v_mfma_f32_16x16x32_bf16 v[112:115], a[152:155], v[152:155], v[112:115]\n\t"
      "buffer_load_dwordx4 v213, s[16:19], s51 offen offset:2048 lds\n\t"
      "s_waitcnt vmcnt(15)\n\t"
      "v_add_u32_e32 v221, 0x24000, v214\n\t"
      "ds_read_b64_tr_b16 a[192:193], v221 offset:0\n\t"
      "v_mfma_f32_16x16x32_bf16 v[116:119], a[156:159], v[152:155], v[116:119]\n\t"
      "ds_read_b64_tr_b16 a[196:197], v221 offset:8\n\t"
      "ds_read_b64_tr_b16 a[200:201], v221 offset:64\n\t"
      "v_mfma_f32_16x16x32_bf16 v[120:123], a[160:163], v[152:155], v[120:123]\n\t"
      "ds_read_b64_tr_b16 a[204:205], v221 offset:72\n\t"
      "ds_read_b64_tr_b16 a[208:209], v221 offset:128\n\t"
      "v_mfma_f32_16x16x32_bf16 v[132:135], a[164:167], v[152:155], v[132:135]\n\t"
      "ds_read_b64_tr_b16 a[212:213], v221 offset:136\n\t"
      "ds_read_b128 v[148:151], v198 offset:512\n\t"
      "s_waitcnt lgkmcnt(8)\n\t"
      "v_mfma_f32_16x16x32_bf16 v[80:83], a[168:171], v[144:147], v[80:83]\n\t"
      "s_add_u32 m0, s41, 0x21000\n\t"
      "s_mov_b32 s51, 0x42000\n\t"
      "buffer_load_dwordx4 v211, s[16:19], s51 offen lds\n\t"
      "v_mfma_f32_16x16x32_bf16 v[84:87], a[172:175], v[144:147], v[84:87]\n\t"
      "buffer_load_dwordx4 v212, s[16:19], s51 offen offset:1024 lds\n\t"
      "v_mfma_f32_16x16x32_bf16 v[88:91], a[176:179], v[144:147], v[88:91]\n\t"
      "buffer_load_dwordx4 v213, s[16:19], s51 offen offset:2048 lds\n\t"
      "s_waitcnt vmcnt(15)\n\t"
      "v_add_u32_e32 v221, 0x12000, v214\n\t"
      "ds_read_b64_tr_b16 a[194:195], v221 offset:0\n\t"
      "v_mfma_f32_16x16x32_bf16 v[92:95], a[180:183], v[144:147], v[92:95]\n\t"
      "ds_read_b64_tr_b16 a[198:199], v221 offset:8\n\t"
      "ds_read_b64_tr_b16 a[202:203], v221 offset:64\n\t"
      "v_mfma_f32_16x16x32_bf16 v[96:99], a[184:187], v[144:147], v[96:99]\n\t"
      "ds_read_b64_tr_b16 a[206:207], v221 offset:72\n\t"
      "ds_read_b64_tr_b16 a[210:211], v221 offset:128\n\t"
      "v_mfma_f32_16x16x32_bf16 v[100:103], a[188:191], v[144:147], v[100:103]\n\t"
      "ds_read_b64_tr_b16 a[214:215], v221 offset:136\n\t"
      "ds_read_b128 v[160:163], v198 offset:12800\n\t"
      "s_waitcnt lgkmcnt(8)\n\t"
      "v_mfma_f32_16x16x32_bf16 v[104:107], a[168:171], v[156:159], v[104:107]\n\t"
      "s_add_u32 m0, s41, 0x24000\n\t"
      "s_mov_b32 s51, 0x42c00\n\t"
      "buffer_load_dwordx4 v211, s[16:19], s51 offen lds\n\t"
      "v_mfma_f32_16x16x32_bf16 v[108:111], a[172:175], v[156:159], v[108:111]\n\t"
      "buffer_load_dwordx4 v212, s[16:19], s51 offen offset:1024 lds\n\t"
      "v_mfma_f32_16x16x32_bf16 v[112:115], a[176:179], v[156:159], v[112:115]\n\t"
      "buffer_load_dwordx4 v213, s[16:19], s51 offen offset:2048 lds\n\t"
      "s_waitcnt vmcnt(15)\n\t"
      "v_add_u32_e32 v221, 0x15000, v214\n\t"
      "ds_read_b64_tr_b16 a[216:217], v221 offset:0\n\t"
      "v_mfma_f32_16x16x32_bf16 v[116:119], a[180:183], v[156:159], v[116:119]\n\t"
      "ds_read_b64_tr_b16 a[220:221], v221 offset:8\n\t"
      "ds_read_b64_tr_b16 a[224:225], v221 offset:64\n\t"
      "v_mfma_f32_16x16x32_bf16 v[120:123], a[184:187], v[156:159], v[120:123]\n\t"
      "ds_read_b64_tr_b16 a[228:229], v221 offset:72\n\t"
      "ds_read_b64_tr_b16 a[232:233], v221 offset:128\n\t"
      "v_mfma_f32_16x16x32_bf16 v[132:135], a[188:191], v[156:159], v[132:135]\n\t"
      "ds_read_b64_tr_b16 a[236:237], v221 offset:136\n\t"
      "ds_read_b128 v[140:143], v199 offset:512\n\t"
      "s_waitcnt lgkmcnt(8)\n\t"
      "v_mfma_f32_16x16x32_bf16 v[80:83], a[192:195], v[148:151], v[80:83]\n\t"
      "s_cmp_eq_u32 s94, 0\n\t"
      "s_cbranch_scc0 .Lc1_%=\n\t"
      "s_add_u32 m0, s41, 0x6000\n\t"
      "s_add_u32 s51, s40, 0x600000\n\t"
      "buffer_load_dwordx4 v208, s[20:23], s51 offen lds\n\t"
      "buffer_load_dwordx4 v208, s[20:23], s51 offen offset:1024 lds\n\t"
      "buffer_load_dwordx4 v208, s[20:23], s51 offen offset:2048 lds\n\t"
      ".Lc1_%=:\n\t"
      "v_mfma_f32_16x16x32_bf16 v[84:87], a[196:199], v[148:151], v[84:87]\n\t"
      "v_mfma_f32_16x16x32_bf16 v[88:91], a[200:203], v[148:151], v[88:91]\n\t"
      "s_waitcnt vmcnt(15)\n\t"
      "s_cmp_eq_u32 s94, 1\n\t"
      "s_cbranch_scc0 .Lwpw19_1_%=\n\t"
      "s_waitcnt vmcnt(12)\n\t"
      ".Lwpw19_1_%=:\n\t"
      "v_add_u32_e32 v221, 0x18000, v214\n\t"
      "ds_read_b64_tr_b16 a[218:219], v221 offset:0\n\t"
      "v_mfma_f32_16x16x32_bf16 v[92:95], a[204:207], v[148:151], v[92:95]\n\t"
      "ds_read_b64_tr_b16 a[222:223], v221 offset:8\n\t"
      "ds_read_b64_tr_b16 a[226:227], v221 offset:64\n\t"
      "v_mfma_f32_16x16x32_bf16 v[96:99], a[208:211], v[148:151], v[96:99]\n\t"
      "ds_read_b64_tr_b16 a[230:231], v221 offset:72\n\t"
      "ds_read_b64_tr_b16 a[234:235], v221 offset:128\n\t"
      "v_mfma_f32_16x16x32_bf16 v[100:103], a[212:215], v[148:151], v[100:103]\n\t"
      "ds_read_b64_tr_b16 a[238:239], v221 offset:136\n\t"
      "ds_read_b128 v[152:155], v199 offset:12800\n\t"
      "s_waitcnt lgkmcnt(14)\n\t"
      "v_mfma_f32_16x16x32_bf16 v[104:107], a[192:195], v[160:163], v[104:107]\n\t"
      "s_cmp_eq_u32 s94, 0\n\t"
      "s_cbranch_scc0 .Lc2_%=\n\t"
      "s_add_u32 m0, s41, 0x9000\n\t"
      "s_add_u32 s51, s40, 0x600000\n\t"
      "buffer_load_dwordx4 v209, s[20:23], s51 offen lds\n\t"
      "buffer_load_dwordx4 v209, s[20:23], s51 offen offset:1024 lds\n\t"
      "buffer_load_dwordx4 v209, s[20:23], s51 offen offset:2048 lds\n\t"
      ".Lc2_%=:\n\t"
      "v_mfma_f32_16x16x32_bf16 v[108:111], a[196:199], v[160:163], v[108:111]\n\t"
      "v_mfma_f32_16x16x32_bf16 v[112:115], a[200:203], v[160:163], v[112:115]\n\t"
      "s_waitcnt vmcnt(15)\n\t"
      "s_cmp_eq_u32 s94, 1\n\t"
      "s_cbranch_scc0 .Lwpw20_1_%=\n\t"
      "s_waitcnt vmcnt(9)\n\t"
      ".Lwpw20_1_%=:\n\t"
      "v_add_u32_e32 v221, 0x1b000, v214\n\t"
      "ds_read_b64_tr_b16 a[240:241], v221 offset:0\n\t"
      "v_mfma_f32_16x16x32_bf16 v[116:119], a[204:207], v[160:163], v[116:119]\n\t"
      "ds_read_b64_tr_b16 a[244:245], v221 offset:8\n\t"
      "ds_read_b64_tr_b16 a[248:249], v221 offset:64\n\t"
      "v_mfma_f32_16x16x32_bf16 v[120:123], a[208:211], v[160:163], v[120:123]\n\t"
      "ds_read_b64_tr_b16 a[252:253], v221 offset:72\n\t"
      "ds_read_b64_tr_b16 v[164:165], v221 offset:128\n\t"
      "v_mfma_f32_16x16x32_bf16 v[132:135], a[212:215], v[160:163], v[132:135]\n\t"
      "ds_read_b64_tr_b16 v[168:169], v221 offset:136\n\t"
      "ds_read_b128 v[144:147], v196 offset:512\n\t"
      "s_waitcnt lgkmcnt(8)\n\t"
      "v_mfma_f32_16x16x32_bf16 v[80:83], a[216:219], v[140:143], v[80:83]\n\t"
      "s_cmp_eq_u32 s94, 1\n\t"
      "s_cbranch_scc0 .Lc3_%=\n\t"
      "s_add_u32 m0, s41, 0xc000\n\t"
      "s_add_u32 s51, s40, 0xc00000\n\t"
      "buffer_load_dwordx4 v208, s[20:23], s51 offen lds\n\t"
      "buffer_load_dwordx4 v208, s[20:23], s51 offen offset:1024 lds\n\t"
      "buffer_load_dwordx4 v208, s[20:23], s51 offen offset:2048 lds\n\t"
      ".Lc3_%=:\n\t"
      "v_mfma_f32_16x16x32_bf16 v[84:87], a[220:223], v[140:143], v[84:87]\n\t"
      "v_mfma_f32_16x16x32_bf16 v[88:91], a[224:227], v[140:143], v[88:91]\n\t"
      "s_waitcnt vmcnt(12)\n\t"
      "s_cmp_eq_u32 s94, 1\n\t"
      "s_cbranch_scc0 .Lwpw21_1_%=\n\t"
      "s_waitcnt vmcnt(9)\n\t"
      ".Lwpw21_1_%=:\n\t"
      "v_add_u32_e32 v221, 0x1e000, v214\n\t"
      "ds_read_b64_tr_b16 a[242:243], v221 offset:0\n\t"
      "v_mfma_f32_16x16x32_bf16 v[92:95], a[228:231], v[140:143], v[92:95]\n\t"
      "ds_read_b64_tr_b16 a[246:247], v221 offset:8\n\t"
      "ds_read_b64_tr_b16 a[250:251], v221 offset:64\n\t"
      "v_mfma_f32_16x16x32_bf16 v[96:99], a[232:235], v[140:143], v[96:99]\n\t"
      "ds_read_b64_tr_b16 a[254:255], v221 offset:72\n\t"
      "ds_read_b64_tr_b16 v[166:167], v221 offset:128\n\t"
      "v_mfma_f32_16x16x32_bf16 v[100:103], a[236:239], v[140:143], v[100:103]\n\t"
      "ds_read_b64_tr_b16 v[170:171], v221 offset:136\n\t"
      "ds_read_b128 v[156:159], v196 offset:12800\n\t"
      "s_waitcnt lgkmcnt(14)\n\t"
      "v_mfma_f32_16x16x32_bf16 v[104:107], a[216:219], v[152:155], v[104:107]\n\t"
      "s_cmp_eq_u32 s94, 1\n\t"
      "s_cbranch_scc0 .Lc4_%=\n\t"
      "s_add_u32 m0, s41, 0xf000\n\t"
      "s_add_u32 s51, s40, 0xc00000\n\t"
      "buffer_load_dwordx4 v209, s[20:23], s51 offen lds\n\t"
      "buffer_load_dwordx4 v209, s[20:23], s51 offen offset:1024 lds\n\t"
      "buffer_load_dwordx4 v209, s[20:23], s51 offen offset:2048 lds\n\t"
      ".Lc4_%=:\n\t"
      "v_mfma_f32_16x16x32_bf16 v[108:111], a[220:223], v[152:155], v[108:111]\n\t"
      "v_mfma_f32_16x16x32_bf16 v[112:115], a[224:227], v[152:155], v[112:115]\n\t"
      "s_waitcnt vmcnt(9)\n\t"
      "v_add_u32_e32 v221, 0x21000, v214\n\t"
      "ds_read_b64_tr_b16 v[172:173], v221 offset:0\n\t"
      "v_mfma_f32_16x16x32_bf16 v[116:119], a[228:231], v[152:155], v[116:119]\n\t"
      "ds_read_b64_tr_b16 v[176:177], v221 offset:8\n\t"
      "ds_read_b64_tr_b16 v[180:181], v221 offset:64\n\t"
      "ds_read_b64_tr_b16 v[184:185], v221 offset:72\n\t"
      "v_mfma_f32_16x16x32_bf16 v[120:123], a[232:235], v[152:155], v[120:123]\n\t"
      "ds_read_b64_tr_b16 v[188:189], v221 offset:128\n\t"
      "ds_read_b64_tr_b16 v[192:193], v221 offset:136\n\t"
      "v_mfma_f32_16x16x32_bf16 v[132:135], a[236:239], v[152:155], v[132:135]\n\t"
      "ds_read_b128 v[148:151], v197 offset:512\n\t"
      "ds_read_b128 v[160:163], v197 offset:12800\n\t"
      "s_waitcnt lgkmcnt(9)\n\t"
      "v_mfma_f32_16x16x32_bf16 v[80:83], a[240:243], v[144:147], v[80:83]\n\t"
      "v_mfma_f32_16x16x32_bf16 v[84:87], a[244:247], v[144:147], v[84:87]\n\t"
      "v_mfma_f32_16x16x32_bf16 v[88:91], a[248:251], v[144:147], v[88:91]\n\t"
      "s_waitcnt vmcnt(6)\n\t"
      "v_add_u32_e32 v221, 0x24000, v214\n\t"
      "ds_read_b64_tr_b16 v[174:175], v221 offset:0\n\t"
      "v_mfma_f32_16x16x32_bf16 v[92:95], a[252:255], v[144:147], v[92:95]\n\t"
      "ds_read_b64_tr_b16 v[178:179], v221 offset:8\n\t"
      "ds_read_b64_tr_b16 v[182:183], v221 offset:64\n\t"
      "v_mfma_f32_16x16x32_bf16 v[96:99], v[164:167], v[144:147], v[96:99]\n\t"
      "ds_read_b64_tr_b16 v[186:187], v221 offset:72\n\t"
      "ds_read_b64_tr_b16 v[190:191], v221 offset:128\n\t"
      "v_mfma_f32_16x16x32_bf16 v[100:103], v[168:171], v[144:147], v[100:103]\n\t"
      "ds_read_b64_tr_b16 v[194:195], v221 offset:136\n\t"
      ".Lcq_%=:\n\t"
      "s_waitcnt lgkmcnt(14)\n\t"
      "v_mfma_f32_16x16x32_bf16 v[104:107], a[240:243], v[156:159], v[104:107]\n\t"
      "v_add_u32_e32 v196, s46, v200\n\t"
      "v_add_u32_e32 v197, s46, v201\n\t"
      "v_mfma_f32_16x16x32_bf16 v[108:111], a[244:247], v[156:159], v[108:111]\n\t"
      "v_mfma_f32_16x16x32_bf16 v[112:115], a[248:251], v[156:159], v[112:115]\n\t"
      "v_mfma_f32_16x16x32_bf16 v[116:119], a[252:255], v[156:159], v[116:119]\n\t"
      "v_add_u32_e32 v198, s46, v202\n\t"
      "v_add_u32_e32 v199, s46, v203\n\t"
      "v_mfma_f32_16x16x32_bf16 v[120:123], v[164:167], v[156:159], v[120:123]\n\t"
      "v_mfma_f32_16x16x32_bf16 v[132:135], v[168:171], v[156:159], v[132:135]\n\t"
      "s_waitcnt lgkmcnt(0)\n\t"
      "v_mfma_f32_16x16x32_bf16 v[80:83], v[172:175], v[148:151], v[80:83]\n\t"
      "s_cmp_eq_u32 s94, 0\n\t"
      "s_cbranch_scc0 .Lc5_%=\n\t"
      "s_add_u32 m0, s41, 0xc000\n\t"
      "s_add_u32 s51, s40, 0xc00000\n\t"
      "buffer_load_dwordx4 v208, s[20:23], s51 offen lds\n\t"
      "buffer_load_dwordx4 v208, s[20:23], s51 offen offset:1024 lds\n\t"
      "buffer_load_dwordx4 v208, s[20:23], s51 offen offset:2048 lds\n\t"
      ".Lc5_%=:\n\t"
      "v_mfma_f32_16x16x32_bf16 v[84:87], v[176:179], v[148:151], v[84:87]\n\t"
      "v_mfma_f32_16x16x32_bf16 v[88:91], v[180:183], v[148:151], v[88:91]\n\t"
      "v_mfma_f32_16x16x32_bf16 v[92:95], v[184:187], v[148:151], v[92:95]\n\t"
      "v_mfma_f32_16x16x32_bf16 v[96:99], v[188:191], v[148:151], v[96:99]\n\t"
      "v_mfma_f32_16x16x32_bf16 v[100:103], v[192:195], v[148:151], v[100:103]\n\t"
      "s_cmp_eq_u32 s94, 0\n\t"
      "s_cbranch_scc0 .Lc6_%=\n\t"
      "s_add_u32 m0, s41, 0xf000\n\t"
      "s_add_u32 s51, s40, 0xc00000\n\t"
      "buffer_load_dwordx4 v209, s[20:23], s51 offen lds\n\t"
      "buffer_load_dwordx4 v209, s[20:23], s51 offen offset:1024 lds\n\t"
      "buffer_load_dwordx4 v209, s[20:23], s51 offen offset:2048 lds\n\t"
      ".Lc6_%=:\n\t"
      "s_mov_b64 s[96:97], s[34:35]\n\t"
      "s_movk_i32 s98, 0x148\n\t"
      "s_mov_b32 s99, s23\n\t"
      "buffer_load_dwordx3 v[252:254], v207, s[96:99], 0 offen offset:40\n\t"
      "buffer_load_dwordx2 v[240:241], v245, s[24:27], 0 offen sc1\n\t"
      "s_cmp_eq_u32 s94, 0\n\t"
      "s_cbranch_scc0 .Lwpx2_0_%=\n\t"
      "s_waitcnt vmcnt(11)\n\t"
      ".Lwpx2_0_%=:\n\t"
      "s_barrier\n\t"
      "ds_read_b128 v[32:35], v196 offset:0\n\t"
      "ds_read_b128 v[36:39], v197 offset:0\n\t"
      "v_mfma_f32_16x16x32_bf16 v[104:107], v[172:175], v[160:163], v[104:107]\n\t"
      "ds_read_b128 v[40:43], v198 offset:0\n\t"
      "ds_read_b128 v[44:47], v199 offset:0\n\t"
      "v_mfma_f32_16x16x32_bf16 v[108:111], v[176:179], v[160:163], v[108:111]\n\t"
      "ds_read_b128 v[48:51], v197 offset:256\n\t"
      "ds_read_b128 v[52:55], v198 offset:256\n\t"
      "v_mfma_f32_16x16x32_bf16 v[112:115], v[180:183], v[160:163], v[112:115]\n\t"
      "ds_read_b128 v[56:59], v199 offset:256\n\t"
      "ds_read_b128 v[60:63], v196 offset:256\n\t"
      "v_mfma_f32_16x16x32_bf16 v[116:119], v[184:187], v[160:163], v[116:119]\n\t"
      "ds_read_b128 v[64:67], v198 offset:512\n\t"
      "ds_read_b128 v[68:71], v199 offset:512\n\t"
      "v_mfma_f32_16x16x32_bf16 v[120:123], v[188:191], v[160:163], v[120:123]\n\t"
      "ds_read_b128 v[72:75], v196 offset:512\n\t"
      "ds_read_b128 v[76:79], v197 offset:512\n\t"
      "v_mfma_f32_16x16x32_bf16 v[132:135], v[192:195], v[160:163], v[132:135]\n\t"
      "s_waitcnt lgkmcnt(8)\n\t"
      "v_mfma_f32_16x16x32_bf16 v[128:131], a[0:3], v[32:35], v[8:11]\n\t"
      "v_cvt_pk_bf16_f32 v80, v80, v81\n\t"
      "v_cvt_pk_bf16_f32 v81, v82, v83\n\t"
      "v_mfma_f32_16x16x32_bf16 v[128:131], a[24:27], v[36:39], v[128:131]\n\t"
      "v_cvt_pk_bf16_f32 v136, v120, v121\n\t"
      "v_cvt_pk_bf16_f32 v137, v122, v123\n\t"
      "v_mfma_f32_16x16x32_bf16 v[128:131], a[48:51], v[40:43], v[128:131]\n\t"
      "v_cvt_pk_bf16_f32 v138, v132, v133\n\t"
      "v_mfma_f32_16x16x32_bf16 v[128:131], a[72:75], v[44:47], v[128:131]\n\t"
      "v_cvt_pk_bf16_f32 v139, v134, v135\n\t"
      "s_waitcnt lgkmcnt(4)\n\t"
      "v_mfma_f32_16x16x32_bf16 v[128:131], a[96:99], v[48:51], v[128:131]\n\t"
      "ds_write_b128 v219, v[136:139] offset:12288\n\t"
      "v_mfma_f32_16x16x32_bf16 v[128:131], a[120:123], v[52:55], v[128:131]\n\t"
      "v_cvt_pk_bf16_f32 v82, v84, v85\n\t"
      "v_cvt_pk_bf16_f32 v83, v86, v87\n\t"
      "v_mfma_f32_16x16x32_bf16 v[128:131], a[144:147], v[56:59], v[128:131]\n\t"
      "v_cvt_pk_bf16_f32 v88, v88, v89\n\t"
      "v_cvt_pk_bf16_f32 v89, v90, v91\n\t"
      "v_mfma_f32_16x16x32_bf16 v[128:131], a[168:171], v[60:63], v[128:131]\n\t"
      "ds_write_b128 v217, v[80:83] offset:0\n\t"
      "v_cvt_pk_bf16_f32 v90, v92, v93\n\t"
      "s_waitcnt lgkmcnt(2)\n\t"
      "v_mfma_f32_16x16x32_bf16 v[128:131], a[192:195], v[64:67], v[128:131]\n\t"
      "v_cvt_pk_bf16_f32 v91, v94, v95\n\t"
      "v_cvt_pk_bf16_f32 v96, v96, v97\n\t"
      "v_mfma_f32_16x16x32_bf16 v[128:131], a[216:219], v[68:71], v[128:131]\n\t"
      "ds_write_b128 v218, v[88:91] offset:0\n\t"
      "v_cvt_pk_bf16_f32 v97, v98, v99\n\t"
      "v_mfma_f32_16x16x32_bf16 v[128:131], a[240:243], v[72:75], v[128:131]\n\t"
      "v_cvt_pk_bf16_f32 v98, v100, v101\n\t"
      "v_cvt_pk_bf16_f32 v99, v102, v103\n\t"
      "v_mfma_f32_16x16x32_bf16 v[128:131], v[172:175], v[76:79], v[128:131]\n\t"
      "v_cvt_pk_bf16_f32 v104, v104, v105\n\t"
      "v_cvt_pk_bf16_f32 v105, v106, v107\n\t"
      "v_mfma_f32_16x16x32_bf16 v[132:135], a[4:7], v[32:35], v[12:15]\n\t"
      "ds_write_b128 v219, v[96:99] offset:0\n\t"
      "v_cvt_pk_bf16_f32 v106, v108, v109\n\t"
      "v_mfma_f32_16x16x32_bf16 v[132:135], a[28:31], v[36:39], v[132:135]\n\t"
      "v_cvt_pk_bf16_f32 v107, v110, v111\n\t"
      "v_cvt_pk_bf16_f32 v112, v112, v113\n\t"
      "v_mfma_f32_16x16x32_bf16 v[132:135], a[52:55], v[40:43], v[132:135]\n\t"
      "v_cvt_pk_bf16_f32 v136, v128, v129\n\t"
      "v_mfma_f32_16x16x32_bf16 v[132:135], a[76:79], v[44:47], v[132:135]\n\t"
      "v_cvt_pk_bf16_f32 v137, v130, v131\n\t"
      "v_mfma_f32_16x16x32_bf16 v[132:135], a[100:103], v[48:51], v[132:135]\n\t"
      "ds_write_b128 v217, v[104:107] offset:12288\n\t"
      "v_cvt_pk_bf16_f32 v113, v114, v115\n\t"
      "s_add_u32 s48, s47, 0x6000\n\t"
      "v_mfma_f32_16x16x32_bf16 v[132:135], a[124:127], v[52:55], v[132:135]\n\t"
      "v_cvt_pk_bf16_f32 v114, v116, v117\n\t"
      "v_cvt_pk_bf16_f32 v115, v118, v119\n\t"
      "s_cmp_eq_u32 s48, s50\n\t"
      "s_cselect_b32 s48, s41, s48\n\t"
      "v_mfma_f32_16x16x32_bf16 v[132:135], a[148:151], v[56:59], v[132:135]\n\t"
      "ds_write_b128 v218, v[112:115] offset:12288\n\t"
      "v_mfma_f32_16x16x32_bf16 v[132:135], a[172:175], v[60:63], v[132:135]\n\t"
      "v_add_u32_e32 v217, s46, v204\n\t"
      "v_mfma_f32_16x16x32_bf16 v[132:135], a[196:199], v[64:67], v[132:135]\n\t"
      "v_add_u32_e32 v218, s46, v205\n\t"
      "v_mfma_f32_16x16x32_bf16 v[132:135], a[220:223], v[68:71], v[132:135]\n\t"
      "v_readlane_b32 s73, v248, 3\n\t"
      "v_add_u32_e32 v219, s46, v206\n\t"
      "v_mfma_f32_16x16x32_bf16 v[132:135], a[244:247], v[72:75], v[132:135]\n\t"
      "v_mfma_f32_16x16x32_bf16 v[132:135], v[176:179], v[76:79], v[132:135]\n\t"
      "s_waitcnt lgkmcnt(0)\n\t"
      "s_barrier\n\t"
      "v_mfma_f32_16x16x32_bf16 v[128:131], a[8:11], v[32:35], v[16:19]\n\t"
      "ds_read_b128 v[140:143], v220 offset:0\n\t"
      "v_mfma_f32_16x16x32_bf16 v[128:131], a[32:35], v[36:39], v[128:131]\n\t"
      "ds_read_b128 v[144:147], v220 offset:1024\n\t"
      "s_memrealtime s[80:81]\n\t"
      "v_mfma_f32_16x16x32_bf16 v[128:131], a[56:59], v[40:43], v[128:131]\n\t"
      "v_cvt_pk_bf16_f32 v138, v132, v133\n\t"
      "s_mov_b32 m0, s45\n\t"
      "v_mfma_f32_16x16x32_bf16 v[128:131], a[80:83], v[44:47], v[128:131]\n\t"
      "v_cvt_pk_bf16_f32 v139, v134, v135\n\t"
      "v_mfma_f32_16x16x32_bf16 v[128:131], a[104:107], v[48:51], v[128:131]\n\t"
      "ds_write_b128 v217, v[136:139] offset:0\n\t"
      "v_mfma_f32_16x16x32_bf16 v[128:131], a[128:131], v[52:55], v[128:131]\n\t"
      "s_waitcnt lgkmcnt(2)\n\t"
      "buffer_store_dwordx4 v[140:143], v208, s[28:31], s40 offen sc1\n\t"
      "v_mfma_f32_16x16x32_bf16 v[128:131], a[152:155], v[56:59], v[128:131]\n\t"
      "ds_read_b128 v[148:151], v220 offset:2048\n\t"
      "v_mfma_f32_16x16x32_bf16 v[128:131], a[176:179], v[60:63], v[128:131]\n\t"
      "ds_read_b128 v[152:155], v220 offset:12288\n\t"
      "v_mfma_f32_16x16x32_bf16 v[128:131], a[200:203], v[64:67], v[128:131]\n\t"
      "ds_read_b128 v[156:159], v220 offset:13312\n\t"
      "v_mfma_f32_16x16x32_bf16 v[128:131], a[224:227], v[68:71], v[128:131]\n\t"
      "s_waitcnt lgkmcnt(4)\n\t"
      "buffer_store_dwordx4 v[144:147], v208, s[28:31], s40 offen offset:1024 sc1\n\t"
      "v_mfma_f32_16x16x32_bf16 v[128:131], a[248:251], v[72:75], v[128:131]\n\t"
      "ds_read_b128 v[160:163], v220 offset:14336\n\t"
      "v_mfma_f32_16x16x32_bf16 v[128:131], v[180:183], v[76:79], v[128:131]\n\t"
      "v_mfma_f32_16x16x32_bf16 v[132:135], a[12:15], v[32:35], v[20:23]\n\t"
      "v_mfma_f32_16x16x32_bf16 v[132:135], a[36:39], v[36:39], v[132:135]\n\t"
      "s_waitcnt lgkmcnt(3)\n\t"
      "buffer_store_dwordx4 v[148:151], v208, s[28:31], s40 offen offset:2048 sc1\n\t"
      "v_mfma_f32_16x16x32_bf16 v[132:135], a[60:63], v[40:43], v[132:135]\n\t"
      "v_cvt_pk_bf16_f32 v136, v128, v129\n\t"
      "v_mfma_f32_16x16x32_bf16 v[132:135], a[84:87], v[44:47], v[132:135]\n\t"
      "v_cvt_pk_bf16_f32 v137, v130, v131\n\t"
      "v_mfma_f32_16x16x32_bf16 v[132:135], a[108:111], v[48:51], v[132:135]\n\t"
      "v_mov_b32_e32 v6, 0\n\t"
      "v_mfma_f32_16x16x32_bf16 v[132:135], a[132:135], v[52:55], v[132:135]\n\t"
      "buffer_load_dwordx4 v208, s[20:23], s73 offen lds\n\t"
      "v_mfma_f32_16x16x32_bf16 v[132:135], a[156:159], v[56:59], v[132:135]\n\t"
      "ds_write_b32 v238, v6\n\t"
      "v_mfma_f32_16x16x32_bf16 v[132:135], a[180:183], v[60:63], v[132:135]\n\t"
      "v_mfma_f32_16x16x32_bf16 v[132:135], a[204:207], v[64:67], v[132:135]\n\t"
      "v_mfma_f32_16x16x32_bf16 v[132:135], a[228:231], v[68:71], v[132:135]\n\t"
      "buffer_load_dwordx4 v208, s[20:23], s73 offen offset:1024 lds\n\t"
      "v_mfma_f32_16x16x32_bf16 v[132:135], a[252:255], v[72:75], v[132:135]\n\t"
      "v_mfma_f32_16x16x32_bf16 v[132:135], v[184:187], v[76:79], v[132:135]\n\t"
      "s_waitcnt vmcnt(13)\n\t"
      "s_barrier\n\t"
      "v_mfma_f32_16x16x32_bf16 v[128:131], a[16:19], v[32:35], v[24:27]\n\t"
      "ds_read_b128 v[80:83], v196 offset:12288\n\t"
      "v_mfma_f32_16x16x32_bf16 v[128:131], a[40:43], v[36:39], v[128:131]\n\t"
      "buffer_load_dwordx4 v208, s[20:23], s73 offen offset:2048 lds\n\t"
      "v_mfma_f32_16x16x32_bf16 v[128:131], a[64:67], v[40:43], v[128:131]\n\t"
      "ds_read_b128 v[84:87], v197 offset:12288\n\t"
      "v_cvt_pk_bf16_f32 v138, v132, v133\n\t"
      "s_add_u32 m0, s45, 0x3000\n\t"
      "v_mfma_f32_16x16x32_bf16 v[128:131], a[88:91], v[44:47], v[128:131]\n\t"
      "ds_read_b128 v[88:91], v198 offset:12288\n\t"
      "v_cvt_pk_bf16_f32 v139, v134, v135\n\t"
      "v_mfma_f32_16x16x32_bf16 v[128:131], a[112:115], v[48:51], v[128:131]\n\t"
      "ds_write_b128 v218, v[136:139] offset:0\n\t"
      "v_mfma_f32_16x16x32_bf16 v[128:131], a[136:139], v[52:55], v[128:131]\n\t"
      "s_waitcnt lgkmcnt(7)\n\t"
      "buffer_store_dwordx4 v[152:155], v209, s[28:31], s40 offen sc1\n\t"
      "s_waitcnt vmcnt(7)\n\t"
      "v_subrev_u32_e32 v242, s67, v240\n\t"
      "v_xor_b32_e32 v241, s63, v241\n\t"
      "v_mfma_f32_16x16x32_bf16 v[128:131], a[160:163], v[56:59], v[128:131]\n\t"
      "ds_read_b128 v[92:95], v199 offset:12288\n\t"
      "v_lshl_or_b32 v240, v242, 20, v241\n\t"
      "v_mfma_f32_16x16x32_bf16 v[128:131], a[184:187], v[60:63], v[128:131]\n\t"
      "ds_read_b128 v[96:99], v197 offset:12544\n\t"
      "v_mfma_f32_16x16x32_bf16 v[128:131], a[208:211], v[64:67], v[128:131]\n\t"
      "v_lshrrev_b32_e32 v242, 12, v242\n\t"
      "v_cmp_ne_u32_e32 vcc, 0, v240\n\t"
      "v_mfma_f32_16x16x32_bf16 v[128:131], a[232:235], v[68:71], v[128:131]\n\t"
      "s_waitcnt lgkmcnt(8)\n\t"
      "buffer_store_dwordx4 v[156:159], v209, s[28:31], s40 offen offset:1024 sc1\n\t"
      "v_mfma_f32_16x16x32_bf16 v[128:131], v[164:167], v[72:75], v[128:131]\n\t"
      "ds_read_b128 v[100:103], v198 offset:12544\n\t"
      "s_cbranch_vccnz .Lgs_%=\n\t"
      ".Lgr_%=:\n\t"
      "ds_write_b32 v246, v242\n\t"
      "v_mfma_f32_16x16x32_bf16 v[128:131], v[188:191], v[76:79], v[128:131]\n\t"
      "ds_read_b128 v[104:107], v199 offset:12544\n\t"
      "v_mfma_f32_16x16x32_bf16 v[132:135], a[20:23], v[32:35], v[28:31]\n\t"
      "ds_read_b128 v[108:111], v196 offset:12544\n\t"
      "v_mfma_f32_16x16x32_bf16 v[132:135], a[44:47], v[36:39], v[132:135]\n\t"
      "s_waitcnt lgkmcnt(11)\n\t"
      "buffer_store_dwordx4 v[160:163], v209, s[28:31], s40 offen offset:2048 sc1\n\t"
      "v_mfma_f32_16x16x32_bf16 v[132:135], a[68:71], v[40:43], v[132:135]\n\t"
      "ds_read_b128 v[112:115], v198 offset:12800\n\t"
      "v_cvt_pk_bf16_f32 v136, v128, v129\n\t"
      "v_add_u32_e32 v220, s46, v207\n\t"
      "v_mfma_f32_16x16x32_bf16 v[132:135], a[92:95], v[44:47], v[132:135]\n\t"
      "ds_read_b128 v[116:119], v199 offset:12800\n\t"
      "v_cvt_pk_bf16_f32 v137, v130, v131\n\t"
      "v_mfma_f32_16x16x32_bf16 v[132:135], a[116:119], v[48:51], v[132:135]\n\t"
      "s_sub_u32 s87, s80, s52\n\t"
      "v_mfma_f32_16x16x32_bf16 v[132:135], a[140:143], v[52:55], v[132:135]\n\t"
      "buffer_load_dwordx4 v209, s[20:23], s73 offen lds\n\t"
      "v_mfma_f32_16x16x32_bf16 v[132:135], a[164:167], v[56:59], v[132:135]\n\t"
      "ds_read_b128 v[120:123], v196 offset:12800\n\t"
      "v_mfma_f32_16x16x32_bf16 v[132:135], a[188:191], v[60:63], v[132:135]\n\t"
      "ds_read_b128 v[124:127], v197 offset:12800\n\t"
      "v_mfma_f32_16x16x32_bf16 v[132:135], a[212:215], v[64:67], v[132:135]\n\t"
      "s_add_u32 s44, s44, 1\n\t"
      "v_mfma_f32_16x16x32_bf16 v[132:135], a[236:239], v[68:71], v[132:135]\n\t"
      "buffer_load_dwordx4 v209, s[20:23], s73 offen offset:1024 lds\n\t"
      "v_mfma_f32_16x16x32_bf16 v[132:135], v[168:171], v[72:75], v[132:135]\n\t"
      "s_add_u32 s85, s85, 1\n\t"
      "s_add_u32 s82, s87, s84\n\t"
      "v_mfma_f32_16x16x32_bf16 v[132:135], v[192:195], v[76:79], v[132:135]\n\t"
      "s_waitcnt lgkmcnt(9)\n\t"
      "v_mfma_f32_16x16x32_bf16 v[128:131], a[0:3], v[80:83], v[8:11]\n\t"
      "v_mfma_f32_16x16x32_bf16 v[128:131], a[24:27], v[84:87], v[128:131]\n\t"
      "v_readlane_b32 s72, v248, s44\n\t"
      "v_mfma_f32_16x16x32_bf16 v[128:131], a[48:51], v[88:91], v[128:131]\n\t"
      "v_cvt_pk_bf16_f32 v138, v132, v133\n\t"
      "v_mfma_f32_16x16x32_bf16 v[128:131], a[72:75], v[92:95], v[128:131]\n\t"
      "v_cvt_pk_bf16_f32 v139, v134, v135\n\t"
      "s_waitcnt lgkmcnt(4)\n\t"
      "v_mfma_f32_16x16x32_bf16 v[128:131], a[96:99], v[96:99], v[128:131]\n\t"
      "ds_write_b128 v219, v[136:139] offset:0\n\t"
      "v_mfma_f32_16x16x32_bf16 v[128:131], a[120:123], v[100:103], v[128:131]\n\t"
      "v_mfma_f32_16x16x32_bf16 v[128:131], a[144:147], v[104:107], v[128:131]\n\t"
      "buffer_load_dwordx4 v209, s[20:23], s73 offen offset:2048 lds\n\t"
      "v_mfma_f32_16x16x32_bf16 v[128:131], a[168:171], v[108:111], v[128:131]\n\t"
      "v_add_u32_e32 v196, s47, v200\n\t"
      "s_waitcnt lgkmcnt(1)\n\t"
      "v_mfma_f32_16x16x32_bf16 v[128:131], a[192:195], v[112:115], v[128:131]\n\t"
      "v_add_u32_e32 v197, s47, v201\n\t"
      "v_mfma_f32_16x16x32_bf16 v[128:131], a[216:219], v[116:119], v[128:131]\n\t"
      "v_add_u32_e32 v198, s47, v202\n\t"
      "v_mfma_f32_16x16x32_bf16 v[128:131], a[240:243], v[120:123], v[128:131]\n\t"
      "v_add_u32_e32 v199, s47, v203\n\t"
      "v_mfma_f32_16x16x32_bf16 v[128:131], v[172:175], v[124:127], v[128:131]\n\t"
      "s_waitcnt vmcnt(17) lgkmcnt(0)\n\t"
      "s_barrier\n\t"
      "v_mfma_f32_16x16x32_bf16 v[132:135], a[4:7], v[80:83], v[12:15]\n\t"
      "ds_read_b128 v[140:143], v220 offset:0\n\t"
      "v_mfma_f32_16x16x32_bf16 v[132:135], a[28:31], v[84:87], v[132:135]\n\t"
      "ds_read_b128 v[240:243], v247 offset:0\n\t"
      "v_mfma_f32_16x16x32_bf16 v[132:135], a[52:55], v[88:91], v[132:135]\n\t"
      "v_cvt_pk_bf16_f32 v136, v128, v129\n\t"
      "v_mfma_f32_16x16x32_bf16 v[132:135], a[76:79], v[92:95], v[132:135]\n\t"
      "ds_read_b128 v[32:35], v196 offset:0\n\t"
      "v_cvt_pk_bf16_f32 v137, v130, v131\n\t"
      "v_mfma_f32_16x16x32_bf16 v[132:135], a[100:103], v[96:99], v[132:135]\n\t"
      "v_mfma_f32_16x16x32_bf16 v[132:135], a[124:127], v[100:103], v[132:135]\n\t"
      "v_mfma_f32_16x16x32_bf16 v[132:135], a[148:151], v[104:107], v[132:135]\n\t"
      "ds_read_b128 v[36:39], v197 offset:0\n\t"
      "v_mfma_f32_16x16x32_bf16 v[132:135], a[172:175], v[108:111], v[132:135]\n\t"
      "v_mfma_f32_16x16x32_bf16 v[132:135], a[196:199], v[112:115], v[132:135]\n\t"
      "s_waitcnt lgkmcnt(2)\n\t"
      "v_add3_u32 v240, v240, v241, v242\n\t"
      "v_mfma_f32_16x16x32_bf16 v[132:135], a[220:223], v[116:119], v[132:135]\n\t"
      "ds_read_b128 v[40:43], v198 offset:0\n\t"
      "v_mfma_f32_16x16x32_bf16 v[132:135], a[244:247], v[120:123], v[132:135]\n\t"
      "v_add_u32_e32 v240, v240, v243\n\t"
      "v_mfma_f32_16x16x32_bf16 v[132:135], v[176:179], v[124:127], v[132:135]\n\t"
      "buffer_store_dwordx4 v[140:143], v208, s[28:31], s72 offen sc1\n\t"
      "v_mfma_f32_16x16x32_bf16 v[128:131], a[8:11], v[80:83], v[16:19]\n\t"
      "ds_read_b128 v[44:47], v199 offset:0\n\t"
      "v_mfma_f32_16x16x32_bf16 v[128:131], a[32:35], v[84:87], v[128:131]\n\t"
      "v_add_u32_dpp v241, v240, v240 quad_perm:[1,0,3,2] row_mask:0xf bank_mask:0xf\n\t"
      "s_memrealtime s[80:81]\n\t"
      "v_mfma_f32_16x16x32_bf16 v[128:131], a[56:59], v[88:91], v[128:131]\n\t"
      "v_cvt_pk_bf16_f32 v138, v132, v133\n\t"
      "v_mfma_f32_16x16x32_bf16 v[128:131], a[80:83], v[92:95], v[128:131]\n\t"
      "v_cvt_pk_bf16_f32 v139, v134, v135\n\t"
      "v_mfma_f32_16x16x32_bf16 v[128:131], a[104:107], v[96:99], v[128:131]\n\t"
      "ds_write_b128 v217, v[136:139] offset:12288\n\t"
      "v_add_u32_dpp v242, v241, v241 quad_perm:[2,3,0,1] row_mask:0xf bank_mask:0xf\n\t"
      "v_mfma_f32_16x16x32_bf16 v[128:131], a[128:131], v[100:103], v[128:131]\n\t"
      "s_mov_b32 m0, s46\n\t"
      "v_and_b32_e32 v240, 0xff, v242\n\t"
      "v_mfma_f32_16x16x32_bf16 v[128:131], a[152:155], v[104:107], v[128:131]\n\t"
      "v_cmp_le_u32_e64 s[76:77], 19, v240\n\t"
      "v_cmp_ge_u32_e64 s[78:79], 13, v240\n\t"
      "v_mfma_f32_16x16x32_bf16 v[128:131], a[176:179], v[108:111], v[128:131]\n\t"
      "ds_read_b128 v[48:51], v197 offset:256\n\t"
      "v_readlane_b32 s70, v242, 0\n\t"
      "v_mfma_f32_16x16x32_bf16 v[128:131], a[200:203], v[112:115], v[128:131]\n\t"
      "v_cmp_lt_u32_e32 vcc, 32, v240\n\t"
      "s_mov_b32 s66, s76\n\t"
      "v_mfma_f32_16x16x32_bf16 v[128:131], a[224:227], v[116:119], v[128:131]\n\t"
      "ds_read_b128 v[52:55], v198 offset:256\n\t"
      "v_mov_b32_e32 v242, s62\n\t"
      "v_mfma_f32_16x16x32_bf16 v[128:131], a[248:251], v[120:123], v[128:131]\n\t"
      "s_and_b32 s65, s76, s77\n\t"
      "s_and_b32 s78, s78, s79\n\t"
      "v_mfma_f32_16x16x32_bf16 v[128:131], v[180:183], v[124:127], v[128:131]\n\t"
      "v_mov_b32_e32 v243, s63\n\t"
      "s_or_b32 s65, s65, s78\n\t"
      "v_mfma_f32_16x16x32_bf16 v[132:135], a[12:15], v[80:83], v[20:23]\n\t"
      "ds_read_b128 v[56:59], v199 offset:256\n\t"
      "s_cmp_lg_u64 vcc, 0\n\t"
      "s_cselect_b32 s65, 0, s65\n\t"
      "s_cselect_b32 s64, 0xfffc0000, 0\n\t"
      "s_cselect_b32 s49, 31, 17\n\t"
      "v_mfma_f32_16x16x32_bf16 v[132:135], a[36:39], v[84:87], v[132:135]\n\t"
      "v_and_b32_e32 v240, s66, v250\n\t"
      "v_and_b32_e32 v241, s65, v250\n\t"
      "v_mfma_f32_16x16x32_bf16 v[132:135], a[60:63], v[88:91], v[132:135]\n\t"
      "v_cvt_pk_bf16_f32 v136, v128, v129\n\t"
      "s_andn2_b32 s67, 0x40000000, s64\n\t"
      "v_mfma_f32_16x16x32_bf16 v[132:135], a[84:87], v[92:95], v[132:135]\n\t"
      "v_cvt_pk_bf16_f32 v137, v130, v131\n\t"
      "ds_read_b128 v[144:147], v220 offset:1024\n\t"
      "v_mfma_f32_16x16x32_bf16 v[132:135], a[108:111], v[96:99], v[132:135]\n\t"
      "v_cmp_ne_u32_e64 s[76:77], 0, v240\n\t"
      "v_cmp_ne_u32_e64 s[78:79], 0, v241\n\t"
      "v_mfma_f32_16x16x32_bf16 v[132:135], a[132:135], v[100:103], v[132:135]\n\t"
      "buffer_store_dwordx2 v[242:243], v210, s[24:27], 0 offen sc1\n\t"
      "v_mfma_f32_16x16x32_bf16 v[132:135], a[156:159], v[104:107], v[132:135]\n\t"
      "ds_read_b128 v[60:63], v196 offset:256\n\t"
      "s_lshr_b32 s70, s70, 10\n\t"
      "v_mfma_f32_16x16x32_bf16 v[132:135], a[180:183], v[108:111], v[132:135]\n\t"
      "s_xor_b32 s75, s75, s76\n\t"
      "s_xor_b32 s74, s74, s76\n\t"
      "v_mfma_f32_16x16x32_bf16 v[132:135], a[204:207], v[112:115], v[132:135]\n\t"
      "s_and_b32 s76, s75, s78\n\t"
      "s_or_b32 s76, s76, s64\n\t"
      "v_cndmask_b32_e64 v249, v248, v251, s[76:77]\n\t"
      "v_mfma_f32_16x16x32_bf16 v[132:135], a[228:231], v[116:119], v[132:135]\n\t"
      "ds_read_b128 v[64:67], v198 offset:512\n\t"
      "s_add_u32 s70, s70, s68\n\t"
      "v_mfma_f32_16x16x32_bf16 v[132:135], a[252:255], v[120:123], v[132:135]\n\t"
      "s_and_b32 s76, s74, s78\n\t"
      "s_or_b32 s76, s76, s64\n\t"
      "v_cndmask_b32_e64 v248, v248, v251, s[76:77]\n\t"
      "v_mfma_f32_16x16x32_bf16 v[132:135], v[184:187], v[124:127], v[132:135]\n\t"
      "s_waitcnt lgkmcnt(2)\n\t"
      "buffer_store_dwordx4 v[144:147], v208, s[28:31], s72 offen offset:1024 sc1\n\t"
      "v_mfma_f32_16x16x32_bf16 v[128:131], a[16:19], v[80:83], v[24:27]\n\t"
      "ds_read_b128 v[68:71], v199 offset:512\n\t"
      "v_mfma_f32_16x16x32_bf16 v[128:131], a[40:43], v[84:87], v[128:131]\n\t"
      "s_lshr_b32 s74, s78, 3\n\t"
      "s_or_b32 s76, s74, s78\n\t"
      "v_mfma_f32_16x16x32_bf16 v[128:131], a[64:67], v[88:91], v[128:131]\n\t"
      "v_cvt_pk_bf16_f32 v138, v132, v133\n\t"
      "v_readlane_b32 s73, v248, s85\n\t"
      "v_mfma_f32_16x16x32_bf16 v[128:131], a[88:91], v[92:95], v[128:131]\n\t"
      "v_cvt_pk_bf16_f32 v139, v134, v135\n\t"
      "s_sub_u32 s87, s80, s52\n\t"
      "v_mfma_f32_16x16x32_bf16 v[128:131], a[112:115], v[96:99], v[128:131]\n\t"
      "ds_write_b128 v218, v[136:139] offset:12288\n\t"
      "s_sub_u32 s83, s82, s87\n\t"
      "v_mfma_f32_16x16x32_bf16 v[128:131], a[136:139], v[100:103], v[128:131]\n\t"
      "v_cndmask_b32_e64 v252, v252, v253, s[76:77]\n\t"
      "s_and_b32 s78, s74, s78\n\t"
      "v_mfma_f32_16x16x32_bf16 v[128:131], a[160:163], v[104:107], v[128:131]\n\t"
      "v_cndmask_b32_e64 v252, v252, v254, s[78:79]\n\t"
      "v_mfma_f32_16x16x32_bf16 v[128:131], a[184:187], v[108:111], v[128:131]\n\t"
      "ds_read_b128 v[148:151], v220 offset:2048\n\t"
      "s_add_u32 s70, s70, -1572\n\t"
      "v_mfma_f32_16x16x32_bf16 v[128:131], a[208:211], v[112:115], v[128:131]\n\t"
      "buffer_load_dwordx4 v208, s[20:23], s73 offen lds\n\t"
      "v_mfma_f32_16x16x32_bf16 v[128:131], a[232:235], v[116:119], v[128:131]\n\t"
      "ds_read_b128 v[72:75], v196 offset:512\n\t"
      "v_readlane_b32 s71, v252, s44\n\t"
      "s_max_i32 s82, s82, s87\n\t"
      "v_mfma_f32_16x16x32_bf16 v[128:131], v[164:167], v[120:123], v[128:131]\n\t"
      "s_sub_u32 s61, s70, s82\n\t"
      "v_mfma_f32_16x16x32_bf16 v[128:131], v[188:191], v[124:127], v[128:131]\n\t"
      "v_subrev_u32_e32 v240, 0x9e3779b9, v243\n\t"
      "s_sub_u32 s84, s84, 19\n\t"
      "v_mfma_f32_16x16x32_bf16 v[132:135], a[20:23], v[80:83], v[28:31]\n\t"
      "ds_read_b128 v[76:79], v197 offset:512\n\t"
      "s_lshr_b32 s60, s71, 16\n\t"
      "v_mfma_f32_16x16x32_bf16 v[132:135], a[44:47], v[84:87], v[132:135]\n\t"
      "buffer_load_dwordx4 v208, s[20:23], s73 offen offset:1024 lds\n\t"
      "s_and_b32 s86, s71, 0xff\n\t"
      "v_mfma_f32_16x16x32_bf16 v[132:135], a[68:71], v[88:91], v[132:135]\n\t"
      "v_cvt_pk_bf16_f32 v136, v128, v129\n\t"
      "s_bfe_u32 s71, s71, 0x80008\n\t"
      "v_mfma_f32_16x16x32_bf16 v[132:135], a[92:95], v[92:95], v[132:135]\n\t"
      "v_cvt_pk_bf16_f32 v137, v130, v131\n\t"
      "s_max_i32 s84, s84, s86\n\t"
      "v_mfma_f32_16x16x32_bf16 v[132:135], a[116:119], v[96:99], v[132:135]\n\t"
      "s_mul_i32 s61, s61, s60\n\t"
      "s_ashr_i32 s61, s61, 8\n\t"
      "v_mfma_f32_16x16x32_bf16 v[132:135], a[140:143], v[100:103], v[132:135]\n\t"
      "buffer_store_dword v240, v244, s[24:27], s67 offen offset:4 sc1\n\t"
      "v_mfma_f32_16x16x32_bf16 v[132:135], a[164:167], v[104:107], v[132:135]\n\t"
      "s_add_u32 s61, s61, s71\n\t"
      "s_mov_b32 s45, s46\n\t"
      "v_mfma_f32_16x16x32_bf16 v[132:135], a[188:191], v[108:111], v[132:135]\n\t"
      "s_max_i32 s61, s61, s84\n\t"
      "s_mov_b32 s46, s47\n\t"
      "v_mfma_f32_16x16x32_bf16 v[132:135], a[212:215], v[112:115], v[132:135]\n\t"
      "s_add_u32 s70, s70, s71\n\t"
      "s_mov_b32 s47, s48\n\t"
      "v_mfma_f32_16x16x32_bf16 v[132:135], a[236:239], v[116:119], v[132:135]\n\t"
      "s_add_u32 s82, s82, s61\n\t"
      "ds_write_b64 v219, v[136:137] offset:12288\n\t"
      "v_mfma_f32_16x16x32_bf16 v[132:135], v[168:171], v[120:123], v[132:135]\n\t"
      "v_add_u32_e32 v6, 1, v6\n\t"
      "v_mfma_f32_16x16x32_bf16 v[132:135], v[192:195], v[124:127], v[132:135]\n\t"
      "s_waitcnt lgkmcnt(3)\n\t"
      "buffer_store_dwordx4 v[148:151], v208, s[28:31], s72 offen offset:2048 sc1\n\t"
      ".p2align 6\n\t"
      ".Lloop_%=:\n\t"
      "v_mfma_f32_16x16x32_bf16 v[128:131], a[0:3], v[32:35], v[8:11]\n\t"
      "s_waitcnt vmcnt(17)\n\t"
      "v_mfma_f32_16x16x32_bf16 v[128:131], a[24:27], v[36:39], v[128:131]\n\t"
      "v_readlane_b32 s72, v249, s44\n\t"
      "v_mfma_f32_16x16x32_bf16 v[128:131], a[48:51], v[40:43], v[128:131]\n\t"
      "v_cvt_pk_bf16_f32 v138, v132, v133\n\t"
      "v_cvt_pk_bf16_f32 v139, v134, v135\n\t"
      "v_mfma_f32_16x16x32_bf16 v[128:131], a[72:75], v[44:47], v[128:131]\n\t"
      "ds_write_b32 v219, v138 offset:12296\n\t"
      "v_mfma_f32_16x16x32_bf16 v[128:131], a[96:99], v[48:51], v[128:131]\n\t"
      "ds_write_b32 v219, v139 offset:12300\n\t"
      "v_mfma_f32_16x16x32_bf16 v[128:131], a[120:123], v[52:55], v[128:131]\n\t"
      "ds_write_b32 v238, v6\n\t"
      "v_mfma_f32_16x16x32_bf16 v[128:131], a[144:147], v[56:59], v[128:131]\n\t"
      "buffer_load_dwordx4 v208, s[20:23], s73 offen offset:2048 lds\n\t"
      "v_mfma_f32_16x16x32_bf16 v[128:131], a[168:171], v[60:63], v[128:131]\n\t"
      "s_waitcnt lgkmcnt(4)\n\t"
      "v_add_u32_e32 v217, s46, v204\n\t"
      "v_mfma_f32_16x16x32_bf16 v[128:131], a[192:195], v[64:67], v[128:131]\n\t"
      "v_add_u32_e32 v218, s46, v205\n\t"
      "v_mfma_f32_16x16x32_bf16 v[128:131], a[216:219], v[68:71], v[128:131]\n\t"
      "v_add_u32_e32 v219, s46, v206\n\t"
      "v_mfma_f32_16x16x32_bf16 v[128:131], a[240:243], v[72:75], v[128:131]\n\t"
      "v_mfma_f32_16x16x32_bf16 v[128:131], v[172:175], v[76:79], v[128:131]\n\t"
      "s_cmp_gt_i32 s83, 1\n\t"
      "s_cbranch_scc1 .LslpA_%=\n\t"
      ".LretA_%=:\n\t"
      "v_mfma_f32_16x16x32_bf16 v[132:135], a[4:7], v[32:35], v[12:15]\n\t"
      "ds_read_b32 v7, v239\n\t"
      "v_mfma_f32_16x16x32_bf16 v[132:135], a[28:31], v[36:39], v[132:135]\n\t"
      "v_mfma_f32_16x16x32_bf16 v[132:135], a[52:55], v[40:43], v[132:135]\n\t"
      "v_cvt_pk_bf16_f32 v136, v128, v129\n\t"
      "v_cvt_pk_bf16_f32 v137, v130, v131\n\t"
      "v_mfma_f32_16x16x32_bf16 v[132:135], a[76:79], v[44:47], v[132:135]\n\t"
      "v_mfma_f32_16x16x32_bf16 v[132:135], a[100:103], v[48:51], v[132:135]\n\t"
      "s_waitcnt lgkmcnt(0)\n\t"
      "v_cmp_gt_u32_e32 vcc, v6, v7\n\t"
      "v_mfma_f32_16x16x32_bf16 v[132:135], a[124:127], v[52:55], v[132:135]\n\t"
      "s_cbranch_vccnz .LsbA_%=\n\t"
      ".LsrA_%=:\n\t"
      "v_mfma_f32_16x16x32_bf16 v[132:135], a[148:151], v[56:59], v[132:135]\n\t"
      "ds_read_b128 v[140:143], v220 offset:12288\n\t"
      "v_mfma_f32_16x16x32_bf16 v[132:135], a[172:175], v[60:63], v[132:135]\n\t"
      "ds_write_b32 v217, v136 offset:0\n\t"
      "v_mfma_f32_16x16x32_bf16 v[132:135], a[196:199], v[64:67], v[132:135]\n\t"
      "ds_write_b32 v217, v137 offset:4\n\t"
      "v_mfma_f32_16x16x32_bf16 v[132:135], a[220:223], v[68:71], v[132:135]\n\t"
      "ds_read_b128 v[80:83], v196 offset:12288\n\t"
      "v_mfma_f32_16x16x32_bf16 v[132:135], a[244:247], v[72:75], v[132:135]\n\t"
      "v_readlane_b32 s73, v249, s85\n\t"
      "v_mfma_f32_16x16x32_bf16 v[132:135], v[176:179], v[76:79], v[132:135]\n\t"
      "s_waitcnt lgkmcnt(3)\n\t"
      "buffer_store_dwordx4 v[140:143], v209, s[28:31], s72 offen sc1\n\t"
      "v_mfma_f32_16x16x32_bf16 v[128:131], a[8:11], v[32:35], v[16:19]\n\t"
      "ds_read_b128 v[84:87], v197 offset:12288\n\t"
      "v_mfma_f32_16x16x32_bf16 v[128:131], a[32:35], v[36:39], v[128:131]\n\t"
      "s_memrealtime s[80:81]\n\t"
      "v_mfma_f32_16x16x32_bf16 v[128:131], a[56:59], v[40:43], v[128:131]\n\t"
      "v_cvt_pk_bf16_f32 v138, v132, v133\n\t"
      "v_cvt_pk_bf16_f32 v139, v134, v135\n\t"
      "v_mfma_f32_16x16x32_bf16 v[128:131], a[80:83], v[44:47], v[128:131]\n\t"
      "ds_write_b32 v217, v138 offset:8\n\t"
      "v_mfma_f32_16x16x32_bf16 v[128:131], a[104:107], v[48:51], v[128:131]\n\t"
      "ds_write_b32 v217, v139 offset:12\n\t"
      "v_mfma_f32_16x16x32_bf16 v[128:131], a[128:131], v[52:55], v[128:131]\n\t"
      "ds_read_b128 v[88:91], v198 offset:12288\n\t"
      "v_mfma_f32_16x16x32_bf16 v[128:131], a[152:155], v[56:59], v[128:131]\n\t"
      "s_add_u32 m0, s45, 0x3000\n\t"
      "v_mfma_f32_16x16x32_bf16 v[128:131], a[176:179], v[60:63], v[128:131]\n\t"
      "ds_read_b128 v[92:95], v199 offset:12288\n\t"
      "v_mfma_f32_16x16x32_bf16 v[128:131], a[200:203], v[64:67], v[128:131]\n\t"
      "s_add_u32 s48, s47, 0x6000\n\t"
      "v_mfma_f32_16x16x32_bf16 v[128:131], a[224:227], v[68:71], v[128:131]\n\t"
      "ds_read_b128 v[96:99], v197 offset:12544\n\t"
      "v_mfma_f32_16x16x32_bf16 v[128:131], a[248:251], v[72:75], v[128:131]\n\t"
      "s_cmp_eq_u32 s48, s50\n\t"
      "s_cselect_b32 s48, s41, s48\n\t"
      "v_mfma_f32_16x16x32_bf16 v[128:131], v[180:183], v[76:79], v[128:131]\n\t"
      "buffer_load_dwordx4 v209, s[20:23], s73 offen lds\n\t"
      "v_mfma_f32_16x16x32_bf16 v[132:135], a[12:15], v[32:35], v[20:23]\n\t"
      "ds_read_b128 v[100:103], v198 offset:12544\n\t"
      "v_mfma_f32_16x16x32_bf16 v[132:135], a[36:39], v[36:39], v[132:135]\n\t"
      "v_mfma_f32_16x16x32_bf16 v[132:135], a[60:63], v[40:43], v[132:135]\n\t"
      "v_cvt_pk_bf16_f32 v136, v128, v129\n\t"
      "v_cvt_pk_bf16_f32 v137, v130, v131\n\t"
      "v_mfma_f32_16x16x32_bf16 v[132:135], a[84:87], v[44:47], v[132:135]\n\t"
      "ds_write_b32 v218, v136 offset:0\n\t"
      "v_mfma_f32_16x16x32_bf16 v[132:135], a[108:111], v[48:51], v[132:135]\n\t"
      "ds_write_b32 v218, v137 offset:4\n\t"
      "v_mfma_f32_16x16x32_bf16 v[132:135], a[132:135], v[52:55], v[132:135]\n\t"
      "ds_read_b128 v[144:147], v220 offset:13312\n\t"
      "v_mfma_f32_16x16x32_bf16 v[132:135], a[156:159], v[56:59], v[132:135]\n\t"
      "ds_read_b128 v[104:107], v199 offset:12544\n\t"
      "v_mfma_f32_16x16x32_bf16 v[132:135], a[180:183], v[60:63], v[132:135]\n\t"
      "s_sub_u32 s84, s84, 19\n\t"
      "v_mfma_f32_16x16x32_bf16 v[132:135], a[204:207], v[64:67], v[132:135]\n\t"
      "s_max_i32 s84, s84, s86\n\t"
      "v_mfma_f32_16x16x32_bf16 v[132:135], a[228:231], v[68:71], v[132:135]\n\t"
      "ds_read_b128 v[108:111], v196 offset:12544\n\t"
      "v_mfma_f32_16x16x32_bf16 v[132:135], a[252:255], v[72:75], v[132:135]\n\t"
      "v_mfma_f32_16x16x32_bf16 v[132:135], v[184:187], v[76:79], v[132:135]\n\t"
      "s_waitcnt lgkmcnt(2)\n\t"
      "buffer_store_dwordx4 v[144:147], v209, s[28:31], s72 offen offset:1024 sc1\n\t"
      "v_mfma_f32_16x16x32_bf16 v[128:131], a[16:19], v[32:35], v[24:27]\n\t"
      "ds_read_b128 v[112:115], v198 offset:12800\n\t"
      "v_mfma_f32_16x16x32_bf16 v[128:131], a[40:43], v[36:39], v[128:131]\n\t"
      "v_mfma_f32_16x16x32_bf16 v[128:131], a[64:67], v[40:43], v[128:131]\n\t"
      "v_cvt_pk_bf16_f32 v138, v132, v133\n\t"
      "v_cvt_pk_bf16_f32 v139, v134, v135\n\t"
      "v_mfma_f32_16x16x32_bf16 v[128:131], a[88:91], v[44:47], v[128:131]\n\t"
      "ds_write_b32 v218, v138 offset:8\n\t"
      "v_mfma_f32_16x16x32_bf16 v[128:131], a[112:115], v[48:51], v[128:131]\n\t"
      "ds_write_b32 v218, v139 offset:12\n\t"
      "v_mfma_f32_16x16x32_bf16 v[128:131], a[136:139], v[52:55], v[128:131]\n\t"
      "s_sub_u32 s87, s80, s52\n\t"
      "v_mfma_f32_16x16x32_bf16 v[128:131], a[160:163], v[56:59], v[128:131]\n\t"
      "s_sub_u32 s83, s82, s87\n\t"
      "ds_read_b128 v[116:119], v199 offset:12800\n\t"
      "v_mfma_f32_16x16x32_bf16 v[128:131], a[184:187], v[60:63], v[128:131]\n\t"
      "s_max_i32 s82, s82, s87\n\t"
      "v_mfma_f32_16x16x32_bf16 v[128:131], a[208:211], v[64:67], v[128:131]\n\t"
      "s_sub_u32 s61, s70, s82\n\t"
      "v_mfma_f32_16x16x32_bf16 v[128:131], a[232:235], v[68:71], v[128:131]\n\t"
      "s_mul_i32 s61, s61, s60\n\t"
      "ds_read_b128 v[120:123], v196 offset:12800\n\t"
      "v_mfma_f32_16x16x32_bf16 v[128:131], v[164:167], v[72:75], v[128:131]\n\t"
      "s_ashr_i32 s61, s61, 8\n\t"
      "v_mfma_f32_16x16x32_bf16 v[128:131], v[188:191], v[76:79], v[128:131]\n\t"
      "buffer_load_dwordx4 v209, s[20:23], s73 offen offset:1024 lds\n\t"
      "v_mfma_f32_16x16x32_bf16 v[132:135], a[20:23], v[32:35], v[28:31]\n\t"
      "s_add_u32 s61, s61, s71\n\t"
      "ds_read_b128 v[124:127], v197 offset:12800\n\t"
      "v_mfma_f32_16x16x32_bf16 v[132:135], a[44:47], v[36:39], v[132:135]\n\t"
      "s_max_i32 s61, s61, s84\n\t"
      "ds_read_b128 v[148:151], v220 offset:14336\n\t"
      "v_mfma_f32_16x16x32_bf16 v[132:135], a[68:71], v[40:43], v[132:135]\n\t"
      "s_add_u32 s82, s82, s61\n\t"
      "v_cvt_pk_bf16_f32 v136, v128, v129\n\t"
      "v_cvt_pk_bf16_f32 v137, v130, v131\n\t"
      "v_mfma_f32_16x16x32_bf16 v[132:135], a[92:95], v[44:47], v[132:135]\n\t"
      "ds_write_b32 v219, v136 offset:0\n\t"
      "v_mfma_f32_16x16x32_bf16 v[132:135], a[116:119], v[48:51], v[132:135]\n\t"
      "ds_write_b32 v219, v137 offset:4\n\t"
      "v_mfma_f32_16x16x32_bf16 v[132:135], a[140:143], v[52:55], v[132:135]\n\t"
      "s_add_u32 s70, s70, s71\n\t"
      "v_add_u32_e32 v220, s46, v207\n\t"
      "v_mfma_f32_16x16x32_bf16 v[132:135], a[164:167], v[56:59], v[132:135]\n\t"
      "v_add_u32_e32 v6, 1, v6\n\t"
      "v_mfma_f32_16x16x32_bf16 v[132:135], a[188:191], v[60:63], v[132:135]\n\t"
      "v_mfma_f32_16x16x32_bf16 v[132:135], a[212:215], v[64:67], v[132:135]\n\t"
      "s_add_u32 s44, s44, 1\n\t"
      "v_mfma_f32_16x16x32_bf16 v[132:135], a[236:239], v[68:71], v[132:135]\n\t"
      "s_add_u32 s85, s85, 1\n\t"
      "v_mfma_f32_16x16x32_bf16 v[132:135], v[168:171], v[72:75], v[132:135]\n\t"
      "v_mfma_f32_16x16x32_bf16 v[132:135], v[192:195], v[76:79], v[132:135]\n\t"
      "s_waitcnt lgkmcnt(2)\n\t"
      "buffer_store_dwordx4 v[148:151], v209, s[28:31], s72 offen offset:2048 sc1\n\t"
      "v_mfma_f32_16x16x32_bf16 v[128:131], a[0:3], v[80:83], v[8:11]\n\t"
      "s_waitcnt vmcnt(17)\n\t"
      "v_mfma_f32_16x16x32_bf16 v[128:131], a[24:27], v[84:87], v[128:131]\n\t"
      "v_readlane_b32 s72, v248, s44\n\t"
      "v_mfma_f32_16x16x32_bf16 v[128:131], a[48:51], v[88:91], v[128:131]\n\t"
      "v_cvt_pk_bf16_f32 v138, v132, v133\n\t"
      "v_cvt_pk_bf16_f32 v139, v134, v135\n\t"
      "v_mfma_f32_16x16x32_bf16 v[128:131], a[72:75], v[92:95], v[128:131]\n\t"
      "ds_write_b32 v219, v138 offset:8\n\t"
      "v_mfma_f32_16x16x32_bf16 v[128:131], a[96:99], v[96:99], v[128:131]\n\t"
      "ds_write_b32 v219, v139 offset:12\n\t"
      "v_mfma_f32_16x16x32_bf16 v[128:131], a[120:123], v[100:103], v[128:131]\n\t"
      "ds_write_b32 v238, v6\n\t"
      "v_mfma_f32_16x16x32_bf16 v[128:131], a[144:147], v[104:107], v[128:131]\n\t"
      "buffer_load_dwordx4 v209, s[20:23], s73 offen offset:2048 lds\n\t"
      "v_mfma_f32_16x16x32_bf16 v[128:131], a[168:171], v[108:111], v[128:131]\n\t"
      "v_add_u32_e32 v196, s47, v200\n\t"
      "v_mfma_f32_16x16x32_bf16 v[128:131], a[192:195], v[112:115], v[128:131]\n\t"
      "v_add_u32_e32 v197, s47, v201\n\t"
      "v_mfma_f32_16x16x32_bf16 v[128:131], a[216:219], v[116:119], v[128:131]\n\t"
      "v_add_u32_e32 v198, s47, v202\n\t"
      "v_mfma_f32_16x16x32_bf16 v[128:131], a[240:243], v[120:123], v[128:131]\n\t"
      "v_add_u32_e32 v199, s47, v203\n\t"
      "v_mfma_f32_16x16x32_bf16 v[128:131], v[172:175], v[124:127], v[128:131]\n\t"
      "s_cmp_gt_i32 s83, 1\n\t"
      "s_cbranch_scc1 .LslpB_%=\n\t"
      ".LretB_%=:\n\t"
      "v_mfma_f32_16x16x32_bf16 v[132:135], a[4:7], v[80:83], v[12:15]\n\t"
      "ds_read_b32 v7, v239\n\t"
      "v_mfma_f32_16x16x32_bf16 v[132:135], a[28:31], v[84:87], v[132:135]\n\t"
      "v_mfma_f32_16x16x32_bf16 v[132:135], a[52:55], v[88:91], v[132:135]\n\t"
      "v_cvt_pk_bf16_f32 v136, v128, v129\n\t"
      "v_cvt_pk_bf16_f32 v137, v130, v131\n\t"
      "v_mfma_f32_16x16x32_bf16 v[132:135], a[76:79], v[92:95], v[132:135]\n\t"
      "v_mfma_f32_16x16x32_bf16 v[132:135], a[100:103], v[96:99], v[132:135]\n\t"
      "s_waitcnt lgkmcnt(0)\n\t"
      "v_cmp_gt_u32_e32 vcc, v6, v7\n\t"
      "v_mfma_f32_16x16x32_bf16 v[132:135], a[124:127], v[100:103], v[132:135]\n\t"
      "s_cbranch_vccnz .LsbB_%=\n\t"
      ".LsrB_%=:\n\t"
      "v_mfma_f32_16x16x32_bf16 v[132:135], a[148:151], v[104:107], v[132:135]\n\t"
      "ds_read_b128 v[140:143], v220 offset:0\n\t"
      "v_mfma_f32_16x16x32_bf16 v[132:135], a[172:175], v[108:111], v[132:135]\n\t"
      "ds_write_b32 v217, v136 offset:12288\n\t"
      "v_mfma_f32_16x16x32_bf16 v[132:135], a[196:199], v[112:115], v[132:135]\n\t"
      "ds_write_b32 v217, v137 offset:12292\n\t"
      "v_mfma_f32_16x16x32_bf16 v[132:135], a[220:223], v[116:119], v[132:135]\n\t"
      "ds_read_b128 v[32:35], v196 offset:0\n\t"
      "v_mfma_f32_16x16x32_bf16 v[132:135], a[244:247], v[120:123], v[132:135]\n\t"
      "v_readlane_b32 s73, v248, s85\n\t"
      "v_mfma_f32_16x16x32_bf16 v[132:135], v[176:179], v[124:127], v[132:135]\n\t"
      "s_waitcnt lgkmcnt(3)\n\t"
      "buffer_store_dwordx4 v[140:143], v208, s[28:31], s72 offen sc1\n\t"
      "v_mfma_f32_16x16x32_bf16 v[128:131], a[8:11], v[80:83], v[16:19]\n\t"
      "ds_read_b128 v[36:39], v197 offset:0\n\t"
      "v_mfma_f32_16x16x32_bf16 v[128:131], a[32:35], v[84:87], v[128:131]\n\t"
      "s_memrealtime s[80:81]\n\t"
      "v_mfma_f32_16x16x32_bf16 v[128:131], a[56:59], v[88:91], v[128:131]\n\t"
      "v_cvt_pk_bf16_f32 v138, v132, v133\n\t"
      "v_cvt_pk_bf16_f32 v139, v134, v135\n\t"
      "v_mfma_f32_16x16x32_bf16 v[128:131], a[80:83], v[92:95], v[128:131]\n\t"
      "ds_write_b32 v217, v138 offset:12296\n\t"
      "v_mfma_f32_16x16x32_bf16 v[128:131], a[104:107], v[96:99], v[128:131]\n\t"
      "ds_write_b32 v217, v139 offset:12300\n\t"
      "v_mfma_f32_16x16x32_bf16 v[128:131], a[128:131], v[100:103], v[128:131]\n\t"
      "ds_read_b128 v[40:43], v198 offset:0\n\t"
      "v_mfma_f32_16x16x32_bf16 v[128:131], a[152:155], v[104:107], v[128:131]\n\t"
      "s_mov_b32 m0, s46\n\t"
      "v_mfma_f32_16x16x32_bf16 v[128:131], a[176:179], v[108:111], v[128:131]\n\t"
      "ds_read_b128 v[44:47], v199 offset:0\n\t"
      "v_mfma_f32_16x16x32_bf16 v[128:131], a[200:203], v[112:115], v[128:131]\n\t"
      "v_mfma_f32_16x16x32_bf16 v[128:131], a[224:227], v[116:119], v[128:131]\n\t"
      "ds_read_b128 v[48:51], v197 offset:256\n\t"
      "v_mfma_f32_16x16x32_bf16 v[128:131], a[248:251], v[120:123], v[128:131]\n\t"
      "v_mfma_f32_16x16x32_bf16 v[128:131], v[180:183], v[124:127], v[128:131]\n\t"
      "buffer_load_dwordx4 v208, s[20:23], s73 offen lds\n\t"
      "v_mfma_f32_16x16x32_bf16 v[132:135], a[12:15], v[80:83], v[20:23]\n\t"
      "ds_read_b128 v[52:55], v198 offset:256\n\t"
      "v_mfma_f32_16x16x32_bf16 v[132:135], a[36:39], v[84:87], v[132:135]\n\t"
      "v_readlane_b32 s71, v252, s44\n\t"
      "v_mfma_f32_16x16x32_bf16 v[132:135], a[60:63], v[88:91], v[132:135]\n\t"
      "v_cvt_pk_bf16_f32 v136, v128, v129\n\t"
      "v_cvt_pk_bf16_f32 v137, v130, v131\n\t"
      "v_mfma_f32_16x16x32_bf16 v[132:135], a[84:87], v[92:95], v[132:135]\n\t"
      "ds_write_b32 v218, v136 offset:12288\n\t"
      "v_mfma_f32_16x16x32_bf16 v[132:135], a[108:111], v[96:99], v[132:135]\n\t"
      "s_lshr_b32 s60, s71, 16\n\t"
      "ds_write_b32 v218, v137 offset:12292\n\t"
      "v_mfma_f32_16x16x32_bf16 v[132:135], a[132:135], v[100:103], v[132:135]\n\t"
      "s_and_b32 s86, s71, 0xff\n\t"
      "ds_read_b128 v[144:147], v220 offset:1024\n\t"
      "v_mfma_f32_16x16x32_bf16 v[132:135], a[156:159], v[104:107], v[132:135]\n\t"
      "s_bfe_u32 s71, s71, 0x80008\n\t"
      "ds_read_b128 v[56:59], v199 offset:256\n\t"
      "v_mfma_f32_16x16x32_bf16 v[132:135], a[180:183], v[108:111], v[132:135]\n\t"
      "s_sub_u32 s84, s84, 19\n\t"
      "v_mfma_f32_16x16x32_bf16 v[132:135], a[204:207], v[112:115], v[132:135]\n\t"
      "s_max_i32 s84, s84, s86\n\t"
      "v_mfma_f32_16x16x32_bf16 v[132:135], a[228:231], v[116:119], v[132:135]\n\t"
      "ds_read_b128 v[60:63], v196 offset:256\n\t"
      "v_mfma_f32_16x16x32_bf16 v[132:135], a[252:255], v[120:123], v[132:135]\n\t"
      "v_mfma_f32_16x16x32_bf16 v[132:135], v[184:187], v[124:127], v[132:135]\n\t"
      "s_waitcnt lgkmcnt(2)\n\t"
      "buffer_store_dwordx4 v[144:147], v208, s[28:31], s72 offen offset:1024 sc1\n\t"
      "v_mfma_f32_16x16x32_bf16 v[128:131], a[16:19], v[80:83], v[24:27]\n\t"
      "ds_read_b128 v[64:67], v198 offset:512\n\t"
      "v_mfma_f32_16x16x32_bf16 v[128:131], a[40:43], v[84:87], v[128:131]\n\t"
      "v_mfma_f32_16x16x32_bf16 v[128:131], a[64:67], v[88:91], v[128:131]\n\t"
      "v_cvt_pk_bf16_f32 v138, v132, v133\n\t"
      "v_cvt_pk_bf16_f32 v139, v134, v135\n\t"
      "v_mfma_f32_16x16x32_bf16 v[128:131], a[88:91], v[92:95], v[128:131]\n\t"
      "ds_write_b32 v218, v138 offset:12296\n\t"
      "v_mfma_f32_16x16x32_bf16 v[128:131], a[112:115], v[96:99], v[128:131]\n\t"
      "ds_write_b32 v218, v139 offset:12300\n\t"
      "v_mfma_f32_16x16x32_bf16 v[128:131], a[136:139], v[100:103], v[128:131]\n\t"
      "s_sub_u32 s87, s80, s52\n\t"
      "v_mfma_f32_16x16x32_bf16 v[128:131], a[160:163], v[104:107], v[128:131]\n\t"
      "s_sub_u32 s83, s82, s87\n\t"
      "ds_read_b128 v[68:71], v199 offset:512\n\t"
      "v_mfma_f32_16x16x32_bf16 v[128:131], a[184:187], v[108:111], v[128:131]\n\t"
      "s_max_i32 s82, s82, s87\n\t"
      "v_mfma_f32_16x16x32_bf16 v[128:131], a[208:211], v[112:115], v[128:131]\n\t"
      "s_sub_u32 s61, s70, s82\n\t"
      "v_mfma_f32_16x16x32_bf16 v[128:131], a[232:235], v[116:119], v[128:131]\n\t"
      "s_mul_i32 s61, s61, s60\n\t"
      "ds_read_b128 v[72:75], v196 offset:512\n\t"
      "v_mfma_f32_16x16x32_bf16 v[128:131], v[164:167], v[120:123], v[128:131]\n\t"
      "s_ashr_i32 s61, s61, 8\n\t"
      "v_mfma_f32_16x16x32_bf16 v[128:131], v[188:191], v[124:127], v[128:131]\n\t"
      "buffer_load_dwordx4 v208, s[20:23], s73 offen offset:1024 lds\n\t"
      "v_mfma_f32_16x16x32_bf16 v[132:135], a[20:23], v[80:83], v[28:31]\n\t"
      "s_add_u32 s61, s61, s71\n\t"
      "ds_read_b128 v[76:79], v197 offset:512\n\t"
      "v_mfma_f32_16x16x32_bf16 v[132:135], a[44:47], v[84:87], v[132:135]\n\t"
      "s_max_i32 s61, s61, s84\n\t"
      "ds_read_b128 v[148:151], v220 offset:2048\n\t"
      "v_mfma_f32_16x16x32_bf16 v[132:135], a[68:71], v[88:91], v[132:135]\n\t"
      "s_add_u32 s82, s82, s61\n\t"
      "v_cvt_pk_bf16_f32 v136, v128, v129\n\t"
      "v_cvt_pk_bf16_f32 v137, v130, v131\n\t"
      "v_mfma_f32_16x16x32_bf16 v[132:135], a[92:95], v[92:95], v[132:135]\n\t"
      "ds_write_b32 v219, v136 offset:12288\n\t"
      "v_mfma_f32_16x16x32_bf16 v[132:135], a[116:119], v[96:99], v[132:135]\n\t"
      "ds_write_b32 v219, v137 offset:12292\n\t"
      "v_mfma_f32_16x16x32_bf16 v[132:135], a[140:143], v[100:103], v[132:135]\n\t"
      "s_add_u32 s70, s70, s71\n\t"
      "v_mfma_f32_16x16x32_bf16 v[132:135], a[164:167], v[104:107], v[132:135]\n\t"
      "s_mov_b32 s45, s46\n\t"
      "v_add_u32_e32 v6, 1, v6\n\t"
      "v_mfma_f32_16x16x32_bf16 v[132:135], a[188:191], v[108:111], v[132:135]\n\t"
      "s_mov_b32 s46, s47\n\t"
      "v_mfma_f32_16x16x32_bf16 v[132:135], a[212:215], v[112:115], v[132:135]\n\t"
      "s_mov_b32 s47, s48\n\t"
      "v_mfma_f32_16x16x32_bf16 v[132:135], a[236:239], v[116:119], v[132:135]\n\t"
      "v_mfma_f32_16x16x32_bf16 v[132:135], v[168:171], v[120:123], v[132:135]\n\t"
      "s_cmp_lg_u32 s44, s49\n\t"
      "v_mfma_f32_16x16x32_bf16 v[132:135], v[192:195], v[124:127], v[132:135]\n\t"
      "s_waitcnt lgkmcnt(2)\n\t"
      "buffer_store_dwordx4 v[148:151], v208, s[28:31], s72 offen offset:2048 sc1\n\t"
      "s_cbranch_scc1 .Lloop_%=\n\t"
      "v_readlane_b32 s72, v249, s44\n\t"
      "s_nop 6\n\t"
      "v_cvt_pk_bf16_f32 v138, v132, v133\n\t"
      "v_cvt_pk_bf16_f32 v139, v134, v135\n\t"
      "ds_write_b64 v219, v[138:139] offset:12296\n\t"
      "s_waitcnt lgkmcnt(0)\n\t"
      "s_barrier\n\t"
      "ds_read_b128 v[32:35], v220 offset:12288\n\t"
      "ds_read_b128 v[36:39], v220 offset:13312\n\t"
      "ds_read_b128 v[40:43], v220 offset:14336\n\t"
      "s_waitcnt lgkmcnt(2)\n\t"
      "buffer_store_dwordx4 v[32:35], v209, s[28:31], s72 offen sc1\n\t"
      "s_waitcnt lgkmcnt(1)\n\t"
      "buffer_store_dwordx4 v[36:39], v209, s[28:31], s72 offen offset:1024 sc1\n\t"
      "s_waitcnt lgkmcnt(0)\n\t"
      "buffer_store_dwordx4 v[40:43], v209, s[28:31], s72 offen offset:2048 sc1\n\t"
      "s_endpgm\n\t"
      ".LslpA_%=:\n\t"
      "s_min_i32 s83, s83, 0x190\n\t"
      ".Lsl2A_%=:\n\t"
      "s_sleep 1\n\t"
      "s_sub_u32 s83, s83, 3\n\t"
      "s_cmp_gt_i32 s83, 1\n\t"
      "s_cbranch_scc1 .Lsl2A_%=\n\t"
      "s_branch .LretA_%=\n\t"
      ".LslpB_%=:\n\t"
      "s_min_i32 s83, s83, 0x190\n\t"
      ".Lsl2B_%=:\n\t"
      "s_sleep 1\n\t"
      "s_sub_u32 s83, s83, 3\n\t"
      "s_cmp_gt_i32 s83, 1\n\t"
      "s_cbranch_scc1 .Lsl2B_%=\n\t"
      "s_branch .LretB_%=\n\t"
      ".Lgs_%=:\n\t"
      "s_sleep 8\n\t"
      "buffer_load_dwordx2 v[240:241], v245, s[24:27], 0 offen sc1\n\t"
      "s_memrealtime s[76:77]\n\t"
      "s_waitcnt vmcnt(0) lgkmcnt(0)\n\t"
      "v_subrev_u32_e32 v242, s67, v240\n\t"
      "v_xor_b32_e32 v241, s63, v241\n\t"
      "v_lshl_or_b32 v240, v242, 20, v241\n\t"
      "v_lshrrev_b32_e32 v242, 12, v242\n\t"
      "v_cmp_ne_u32_e32 vcc, 0, v240\n\t"
      "s_nop 0\n\t"
      "s_cbranch_vccz .Lgr_%=\n\t"
      "s_sub_u32 s76, s76, s52\n\t"
      "s_cmp_lt_u32 s76, 2000\n\t"
      "s_cbranch_scc1 .Lgs_%=\n\t"
      "v_mov_b32_e32 v242, 10\n\t"
      "s_branch .Lgr_%=\n\t"
      ".LsbA_%=:\n\t"
      "ds_read_b32 v7, v239\n\t"
      "s_waitcnt lgkmcnt(0)\n\t"
      "v_cmp_gt_u32_e32 vcc, v6, v7\n\t"
      "s_nop 0\n\t"
      "s_cbranch_vccnz .LsbA_%=\n\t"
      "s_branch .LsrA_%=\n\t"
      ".LsbB_%=:\n\t"
      "ds_read_b32 v7, v239\n\t"
      "s_waitcnt lgkmcnt(0)\n\t"
      "v_cmp_gt_u32_e32 vcc, v6, v7\n\t"
      "s_nop 0\n\t"
      "s_cbranch_vccnz .LsbB_%=\n\t"
      "s_branch .LsrB_%=\n\t"
      ".Lend_%=:\n\t"
      :
      : "{s[20:21]}"(xa), "{s[16:17]}"(wa), "{s[36:37]}"(ba), "{s[28:29]}"(oa), "{s[24:25]}"(sa), "{s[52:53]}"(rs),
        "{s[22:23]}"(0x0002000006c00000ull), "{s[26:27]}"(0x0002000000000808ull), "{s[30:31]}"(0x0002000006c00000ull),
        "{s[18:19]}"(0x0002000000048000ull), "{s[38:39]}"(0x0002000000000f00ull),
        "{s40}"(t0), "{s41}"(p0), "{s43}"(lds0), "{s94}"(ty),
        "{v208}"(ga), "{v209}"(gb),
        "{v211}"(woff[0]), "{v212}"(woff[1]), "{v213}"(woff[2]), "{v215}"(boff),
        "{s95}"(tres), "{s92}"(pre1), "{s[60:61]}"(pres), "{s[62:63]}"(ep), "{s77}"(pfire), "{s98}"(((tidx >> 3) * 4u + (tidx & 3u)) * 128u), "{v238}"(vfw), "{v239}"(vfr),
        "{v248}"(ta), "{v249}"(ta), "{v251}"(t1), "{v250}"(mk), "{s74}"(plm), "{s75}"(plb),
        "{s[34:35]}"(__builtin_amdgcn_kernarg_segment_ptr()),
        "{v244}"(vpub), "{v210}"(vbmp), "{v245}"(vgv), "{v246}"(vxw), "{v247}"(vxr),
        "{v200}"(cfa[0]), "{v201}"(cfa[1]), "{v202}"(cfa[2]), "{v203}"(cfa[3]),
        "{v204}"(csw[0]), "{v205}"(csw[1]), "{v206}"(csw[2]), "{v207}"(csr),
        "{v214}"(tbase)
      : "memory", "scc", "vcc",
      "s42", "s44", "s45", "s46", "s47", "s48", "s49", "s50", "s51", "s54", "s55", "s56", "s57", "s58", "s59", "s64", "s65", "s66", "s67", "s68",
      "s69", "s70", "s71", "s72", "s73", "s76", "s78", "s79", "s80", "s81", "s82", "s83", "s84", "s85", "s86", "s87", "s88", "s89", "s90", "s91",
      "s93", "s96", "s97", "s99",
      "v1", "v2", "v3", "v4", "v5", "v6", "v7", "v8", "v9", "v10", "v11", "v12", "v13", "v14", "v15", "v16", "v17", "v18", "v19", "v20", "v21", "v22",
      "v23", "v24", "v25", "v26", "v27", "v28", "v29", "v30", "v31", "v32", "v33", "v34", "v35", "v36", "v37", "v38", "v39", "v40", "v41", "v42",
      "v43", "v44", "v45", "v46", "v47", "v48", "v49", "v50", "v51", "v52", "v53", "v54", "v55", "v56", "v57", "v58", "v59", "v60", "v61", "v62",
      "v63", "v64", "v65", "v66", "v67", "v68", "v69", "v70", "v71", "v72", "v73", "v74", "v75", "v76", "v77", "v78", "v79", "v80", "v81", "v82",
      "v83", "v84", "v85", "v86", "v87", "v88", "v89", "v90", "v91", "v92", "v93", "v94", "v95", "v96", "v97", "v98", "v99", "v100", "v101", "v102",
      "v103", "v104", "v105", "v106", "v107", "v108", "v109", "v110", "v111", "v112", "v113", "v114", "v115", "v116", "v117", "v118", "v119", "v120",
      "v121", "v122", "v123", "v124", "v125", "v126", "v127", "v128", "v129", "v130", "v131", "v132", "v133", "v134", "v135", "v136", "v137", "v138",
      "v139", "v140", "v141", "v142", "v143", "v144", "v145", "v146", "v147", "v148", "v149", "v150", "v151", "v152", "v153", "v154", "v155", "v156",
      "v157", "v158", "v159", "v160", "v161", "v162", "v163", "v164", "v165", "v166", "v167", "v168", "v169", "v170", "v171", "v172", "v173", "v174",
      "v175", "v176", "v177", "v178", "v179", "v180", "v181", "v182", "v183", "v184", "v185", "v186", "v187", "v188", "v189", "v190", "v191", "v192",
      "v193", "v194", "v195", "v196", "v197", "v198", "v199", "v216", "v217", "v218", "v219", "v220", "v221", "v222", "v223", "v224", "v225", "v226",
      "v227", "v228", "v229", "v230", "v231", "v232", "v233", "v234", "v235", "v236", "v237", "v240", "v241", "v242", "v243", "v252", "v253", "v254",
      "v255",
      "a0", "a1", "a2", "a3", "a4", "a5", "a6", "a7", "a8", "a9", "a10", "a11", "a12", "a13", "a14", "a15", "a16", "a17", "a18", "a19", "a20", "a21",
      "a22", "a23", "a24", "a25", "a26", "a27", "a28", "a29", "a30", "a31", "a32", "a33", "a34", "a35", "a36", "a37", "a38", "a39", "a40", "a41",
      "a42", "a43", "a44", "a45", "a46", "a47", "a48", "a49", "a50", "a51", "a52", "a53", "a54", "a55", "a56", "a57", "a58", "a59", "a60", "a61",
      "a62", "a63", "a64", "a65", "a66", "a67", "a68", "a69", "a70", "a71", "a72", "a73", "a74", "a75", "a76", "a77", "a78", "a79", "a80", "a81",
      "a82", "a83", "a84", "a85", "a86", "a87", "a88", "a89", "a90", "a91", "a92", "a93", "a94", "a95", "a96", "a97", "a98", "a99", "a100", "a101",
      "a102", "a103", "a104", "a105", "a106", "a107", "a108", "a109", "a110", "a111", "a112", "a113", "a114", "a115", "a116", "a117", "a118", "a119",
      "a120", "a121", "a122", "a123", "a124", "a125", "a126", "a127", "a128", "a129", "a130", "a131", "a132", "a133", "a134", "a135", "a136", "a137",
      "a138", "a139", "a140", "a141", "a142", "a143", "a144", "a145", "a146", "a147", "a148", "a149", "a150", "a151", "a152", "a153", "a154", "a155",
      "a156", "a157", "a158", "a159", "a160", "a161", "a162", "a163", "a164", "a165", "a166", "a167", "a168", "a169", "a170", "a171", "a172", "a173",
      "a174", "a175", "a176", "a177", "a178", "a179", "a180", "a181", "a182", "a183", "a184", "a185", "a186", "a187", "a188", "a189", "a190", "a191",
      "a192", "a193", "a194", "a195", "a196", "a197", "a198", "a199", "a200", "a201", "a202", "a203", "a204", "a205", "a206", "a207", "a208", "a209",
      "a210", "a211", "a212", "a213", "a214", "a215", "a216", "a217", "a218", "a219", "a220", "a221", "a222", "a223", "a224", "a225", "a226", "a227",
      "a228", "a229", "a230", "a231", "a232", "a233", "a234", "a235", "a236", "a237", "a238", "a239", "a240", "a241", "a242", "a243", "a244", "a245",
      "a246", "a247", "a248", "a249", "a250", "a251", "a252", "a253", "a254", "a255");
}

extern "C" void kernel_launch(void* const* d_in, const int* in_sizes, int n_in,
                              void* d_out, int out_size, void* d_ws, size_t ws_size,
                              hipStream_t stream) {
  const uint16_t* x = (const uint16_t*)d_in[0];
  const uint16_t* w = (const uint16_t*)d_in[1];
  const uint16_t* b = (const uint16_t*)d_in[2];
  uint16_t* out = (uint16_t*)d_out;
  const PaceTab pt = {{
    0x205f4eu, 0x205c4cu, 0x184944u, 0u,
    0x205f4eu, 0x205c4cu, 0x184944u, 0u,
    0x20584eu, 0x20554cu, 0x184944u, 0u,
    0x204f4eu, 0x204b48u, 0x184944u, 0u,
    0x184f4eu, 0x184b48u, 0x184944u, 0u,
    0x184f4eu, 0x184b48u, 0x184944u, 0u,
    0x184f4eu, 0x184b48u, 0x184944u, 0u,
    0x11514eu, 0x114e48u, 0x114a44u, 0u,
    0x13534eu, 0x135049u, 0x134b44u, 0u,
    0x16544eu, 0x16514au, 0x164c46u, 0u,
    0x1b554eu, 0x1b524au, 0x1b4e46u, 0u,
    0x22574eu, 0x22544au, 0x225046u, 0u,
    0x2f574eu, 0x2f544au, 0x2f5146u, 0u,
    0x49564eu, 0x49534au, 0x495046u, 0u,
    0xab4d4du, 0xab4b4au, 0xab4846u, 0u,
    0x1004400u, 0x1004400u, 0x1004400u, 0u,
    0x1004400u, 0x1004400u, 0x1004400u, 0u,
    0x1004400u, 0x1004400u, 0x1004400u, 0u}};
  conv1x1_kernel<<<256, 256, 0, stream>>>(x, w, b, out, (unsigned long long*)d_ws, pt);
}
```
